```python
import torch

def _softmax(x, axis=-1):
    return torch.softmax(x, dim=axis)
def _gelu(x):
    return torch.nn.functional.gelu(x, approximate="tanh")
def _silu(x):
    return torch.nn.functional.silu(x)
def _sigmoid(x):
    return torch.sigmoid(x)
def _layernorm(x, axis=-1, eps=1e-5):
    m = x.mean(dim=axis, keepdim=True)
    v = x.var(dim=axis, keepdim=True, unbiased=False)
    return (x - m) / torch.sqrt(v + eps)
def _scatter_add(tgt, idx, src, axis=0):
    return tgt.clone().index_add_(axis, idx, src)
def _segment_sum(src, seg, n):
    out = torch.zeros((n,) + src.shape[1:], dtype=src.dtype)
    return out.index_add_(0, seg, src)
def _pad(x, pw):
    flat = tuple(v for pair in reversed(pw) for v in pair)
    return torch.nn.functional.pad(x, flat)

kernel_name = 'sequential_preLN__softmax_full_mha__geglu_ffn__prompt'

def setup_inputs():

    _w = (lambda a: (lambda t: t.to(torch.bfloat16) if t.is_floating_point() else t)(torch.as_tensor(a)))
    def _A_setup():
        B, S, D = 1, 2048, 576
        return {
            "x": _w((lambda *s: torch.randn(*s, dtype=torch.bfloat16) * 0.05)(B, S, D)),
            "wqkv": _w((lambda *s: torch.randn(*s, dtype=torch.bfloat16) * 0.05)(D, 3 * D)),
            "wo": _w((lambda *s: torch.randn(*s, dtype=torch.bfloat16) * 0.05)(D, D)),
        }
    _A_in = _A_setup()

    def _B_setup():
        B, S, D, F = 1, 2048, 576, 4608
        return {
            "x": _w((lambda *s: torch.randn(*s, dtype=torch.bfloat16) * 0.05)(B, S, D)),
            "w_gate": _w((lambda *s: torch.randn(*s, dtype=torch.bfloat16) * 0.05)(D, F)),
            "w_up": _w((lambda *s: torch.randn(*s, dtype=torch.bfloat16) * 0.05)(D, F)),
            "w_down": _w((lambda *s: torch.randn(*s, dtype=torch.bfloat16) * 0.05)(F, D)),
        }
    _B_in = _B_setup()

    return {
        "A_x": _A_in["x"],
        "A_wqkv": _A_in["wqkv"],
        "A_wo": _A_in["wo"],
        "B_x": _B_in["x"],
        "B_w_gate": _B_in["w_gate"],
        "B_w_up": _B_in["w_up"],
        "B_w_down": _B_in["w_down"],
    }

def reference(A_x, A_wqkv, A_wo, B_x, B_w_gate, B_w_up, B_w_down):
    def _A_ref(x, wqkv, wo):
        B, S, D = x.shape
        H = 32; Dh = D // H
        qkv = (lambda x, *d: x.permute(*d))((x @ wqkv).reshape(B, S, 3, H, Dh), 2, 0, 3, 1, 4)
        q, k, v = qkv[0], qkv[1], qkv[2]
        scale = Dh ** -0.5
        scores = torch.einsum("bhqd,bhkd->bhqk", q, k) * scale
        p = _softmax(scores, axis=-1)
        o = torch.einsum("bhqk,bhkd->bhqd", p, v)
        return (lambda x, *d: x.permute(*d))(o, 0, 2, 1, 3).reshape(B, S, D) @ wo

    def _B_ref(x, w_gate, w_up, w_down):
        return (_gelu(x @ w_gate) * (x @ w_up)) @ w_down

    x = A_x
    h = x + _A_ref(_layernorm(x, axis=-1), A_wqkv, A_wo)
    h2 = h + _B_ref(_layernorm(h, axis=-1), B_w_gate, B_w_up, B_w_down)
    return h2
```

```cpp
#include <hip/hip_runtime.h>
#include <hip/hip_bf16.h>
#include <stdint.h>

namespace {

typedef __bf16 bf16x2 __attribute__((ext_vector_type(2)));
typedef __bf16 bf16x8 __attribute__((ext_vector_type(8)));
typedef __bf16 bf16x16 __attribute__((ext_vector_type(16)));
typedef short s16x4 __attribute__((ext_vector_type(4)));
typedef float f32x2 __attribute__((ext_vector_type(2)));
typedef float f32x4 __attribute__((ext_vector_type(4)));
typedef float f32x8 __attribute__((ext_vector_type(8)));
typedef float f32x16 __attribute__((ext_vector_type(16)));
typedef unsigned u32x2 __attribute__((ext_vector_type(2)));
typedef unsigned u32x4 __attribute__((ext_vector_type(4)));
#define LAS __attribute__((address_space(3)))
#define DI __device__ __forceinline__

constexpr int S_ = 2048, D_ = 576, H_ = 32, F_ = 4608;
constexpr float EPS = 1e-5f;
constexpr float QSCALE = 0.23570226039551584f * 1.4426950408889634f;
constexpr float SAFE_B = 48.f;

constexpr int KIMG = 81920, VIMG = 77824;
constexpr size_t WS_QM   = 0;
constexpr size_t WS_QT   = WS_QM + (size_t)32 * 2048 * 32;
constexpr size_t WS_KIMG = WS_QT + (size_t)32 * 2048 * 4;
constexpr size_t WS_VIMG = WS_KIMG + (size_t)32 * KIMG;
constexpr size_t WS_KMAX = WS_VIMG + (size_t)32 * VIMG;
constexpr size_t WS_MUX  = WS_KMAX + 32 * 16 * 4;
constexpr size_t WS_WBAR = WS_MUX + 2048 * 4;
constexpr size_t WS_SSQ  = WS_WBAR + 4096;
constexpr size_t WS_OF   = WS_SSQ + (size_t)2048 * 64;
constexpr size_t WS_HC   = WS_OF + (size_t)128 * 18 * 1024;
constexpr size_t WS_HCF  = WS_HC + (size_t)128 * 36 * 512;
constexpr size_t WS_W1   = WS_HCF + (size_t)128 * 18 * 1024;
constexpr size_t WS_W2   = WS_W1 + (size_t)32 * 324 * 1024;
constexpr size_t WS_WO   = WS_W2 + (size_t)16 * 324 * 1024;
constexpr size_t WS_ACT  = WS_WO + (size_t)6 * 108 * 1024;
constexpr size_t WS_PART = WS_ACT + (size_t)128 * 144 * 1024;
constexpr size_t WS_END  = WS_PART + (size_t)4 * 128 * 36 * 1024;

DI unsigned cvtpk(float lo, float hi) { f32x2 v = {lo, hi}; bf16x2 b = __builtin_convertvector(v, bf16x2); return __builtin_bit_cast(unsigned, b); }
DI float bflo(unsigned w) { return __uint_as_float(w << 16); }
DI float bfhi(unsigned w) { return __uint_as_float(w & 0xffff0000u); }
DI float dot2(unsigned a, unsigned b, float c) { return __builtin_amdgcn_fdot2_f32_bf16(__builtin_bit_cast(bf16x2, a), __builtin_bit_cast(bf16x2, b), c, false); }
DI int rfl(int x) { return __builtin_amdgcn_readfirstlane(x); }
DI float xor32(float v) { auto r = __builtin_amdgcn_permlane32_swap(__float_as_uint(v), __float_as_uint(v), false, false); return __uint_as_float((threadIdx.x & 32) ? r[0] : r[1]); }
DI f32x4 mfma16(bf16x8 a, bf16x8 b, f32x4 c) { return __builtin_amdgcn_mfma_f32_16x16x32_bf16(a, b, c, 0, 0, 0); }
DI void glds16(const void* sbase, unsigned voff, unsigned lds_dst) {
    asm volatile("s_mov_b32 m0, %2\n\ts_nop 0\n\tglobal_load_lds_dwordx4 %0, %1" :: "v"(voff), "s"(sbase), "s"(lds_dst) : "memory");
}
DI void gload16(bf16x8& dst, const void* sbase, unsigned voff) { asm volatile("global_load_dwordx4 %0, %1, %2" : "=v"(dst) : "v"(voff), "s"(sbase)); }
DI void gload16f(f32x4& dst, const void* sbase, unsigned voff) { asm volatile("global_load_dwordx4 %0, %1, %2" : "=v"(dst) : "v"(voff), "s"(sbase)); }
DI void gload8(u32x2& dst, const void* sbase, unsigned voff) { asm volatile("global_load_dwordx2 %0, %1, %2" : "=v"(dst) : "v"(voff), "s"(sbase)); }
DI void gload4(float& dst, const void* sbase, unsigned voff) { asm volatile("global_load_dword %0, %1, %2" : "=v"(dst) : "v"(voff), "s"(sbase)); }
#define PINV(x) asm volatile("" : "+v"(x))
#define WAITV(n) asm volatile("s_waitcnt vmcnt(%0)" :: "n"(n) : "memory")
#define WAIT_BAR(n) asm volatile("s_waitcnt vmcnt(%0) lgkmcnt(0)\n\ts_barrier" :: "n"(n) : "memory")
#define SBAR() __builtin_amdgcn_sched_barrier(0)
#define LBAR() asm volatile("s_waitcnt lgkmcnt(0)\n\ts_barrier" ::: "memory")
DI bf16x8 lds_frag(LAS const char* p) { return *(LAS const bf16x8*)p; }
typedef short v4i16_t __attribute__((ext_vector_type(4)));
DI u32x2 lds_tr(LAS const char* p) { return __builtin_bit_cast(u32x2, __builtin_amdgcn_ds_read_tr16_b64_v4i16((LAS v4i16_t*)p)); }
DI bf16x8 tr_frag(LAS const char* p, int off2) { u32x2 a = lds_tr(p), b = lds_tr(p + off2); u32x4 r = {a[0], a[1], b[0], b[1]}; return __builtin_bit_cast(bf16x8, r); }

DI void ftile_to_kstep(int T, int& ks, int& half) {
    const int j = T / 9, tt = T - 9 * j, a = j >> 1;
    const int idx = (tt == 8) ? 8 : 4 * (j & 1) + ((tt < 2) ? 0 : (tt == 3 || tt == 4) ? 1 : (tt == 2 || tt == 5) ? 2 : 3);
    half = (tt == 8) ? (j & 1) : ((tt == 1 || tt == 4 || tt == 5 || tt == 7) ? 1 : 0);
    ks = 9 * a + idx;
}
DI int kstep_to_ftile(int ks, int half) {
    const int a = ks / 9, idx = ks - 9 * a;
    if (idx == 8) return 9 * (2 * a + half) + 8;
    const int j = 2 * a + (idx >> 2), p = idx & 3;
    const int tt = (p == 0) ? half : (p == 1) ? 3 + half : (p == 2) ? (half ? 5 : 2) : 6 + half;
    return 9 * j + tt;
}

struct RetileJob { const char* src; unsigned voff; unsigned dst[4]; bool live; };
DI void retile_setup(RetileJob& J, int b, int lane, const void* wo, const void* wg, const void* wu, const void* wd) {
    J.live = b < 4050;
    const int r8 = lane >> 3, ch = lane & 7;
    if (b < 2592) {
        const int gu = b >= 1296, bb = b - 1296 * gu, s = bb / 72, cb = bb - 72 * s;
        J.src = (const char*)(gu ? wu : wg) + (size_t)s * 32 * (F_ * 2) + cb * 128;
        J.voff = r8 * (F_ * 2) + ch * 16;
#pragma unroll
        for (int u = 0; u < 4; ++u) { const int T = 4 * cb + u, j = T / 9, tt = T - 9 * j, grp = tt / 3, tig = tt - 3 * grp;
            J.dst[u] = (unsigned)(WS_W1 + (size_t)(j * 324 + ((grp * 18 + s) * 3 + tig) * 2 + gu) * 1024); }
    } else if (b < 3888) {
        const int bb = b - 2592, ks = bb / 9, cb = bb - 9 * ks;
        J.src = (const char*)wd + cb * 128;
        J.voff = (unsigned)ks;
        const int ksl = ks / 36, kap = ks - 36 * ksl, kj = kap / 18, r = kap - 18 * kj, st = r >> 1, s2 = r & 1;
#pragma unroll
        for (int u = 0; u < 4; ++u) { const int T = 4 * cb + u, n4 = T / 9, tt = T - 9 * n4;
            J.dst[u] = (unsigned)(WS_W2 + (size_t)((ksl * 4 + n4) * 324 + ((st * 2 + kj) * 2 + s2) * 9 + tt) * 1024); }
    } else {
        const int bb = (b < 4050 ? b : 4049) - 3888, s = bb / 9, cb = bb - 9 * s;
        J.src = (const char*)wo + (size_t)s * 32 * (D_ * 2) + cb * 128;
        J.voff = ch * 16;
#pragma unroll
        for (int u = 0; u < 4; ++u) { const int T = 4 * cb + u, n6 = T / 6, tt = T - 6 * n6;
            J.dst[u] = (unsigned)(WS_WO + (size_t)(n6 * 108 + s * 6 + tt) * 1024); }
    }
}
DI void retile_issue(const RetileJob& J, int b, int lane, unsigned lds_scratch) {
    const int r8 = lane >> 3, ch = lane & 7;
#pragma unroll
    for (int p = 0; p < 4; ++p) {
        unsigned voff;
        if (b < 2592) voff = J.voff + p * 8 * (F_ * 2);
        else if (b < 3888) { const int rho = 8 * p + r8, T = kstep_to_ftile((int)J.voff, rho >> 4); voff = (16 * T + (rho & 15)) * (D_ * 2) + ch * 16; }
        else { const int rho = 8 * p + r8, k = 8 * ((rho >> 2) & 3) + 4 * (rho >> 4) + (rho & 3); voff = J.voff + k * (D_ * 2); }
        glds16(J.src, voff, lds_scratch + p * 1024);
    }
}
DI void retile_finish(const RetileJob& J, int lane, LAS const char* scratch, char* ws) {
    const int g = lane >> 4, q = (lane >> 2) & 3, p = lane & 3;
    LAS const char* base = scratch + (4 * g + q) * 128 + p * 8;
#pragma unroll
    for (int u = 0; u < 4; ++u) { const bf16x8 f = tr_frag(base + 32 * u, 16 * 128); if (J.live) *(bf16x8*)(ws + J.dst[u] + lane * 16) = f; }
}
__global__ __launch_bounds__(512) void prep_kernel(const void* wo, const void* wg, const void* wu, const void* wd, char* ws) {
    extern __shared__ __attribute__((aligned(16))) char lds[];
    const int lane = threadIdx.x & 63, wid = rfl(threadIdx.x >> 6), b = blockIdx.x * 8 + wid;
    RetileJob J; retile_setup(J, b, lane, wo, wg, wu, wd);
    retile_issue(J, b, lane, (unsigned)(uintptr_t)lds + wid * 4096);
    WAITV(0);
    retile_finish(J, lane, (LAS const char*)lds + wid * 4096, ws);
}

constexpr int K1_SLOT = 54 * 1024, K1_STG = 73728, K1_SMALL = 118784, K1_LDS = 122880;
template <bool VPART>
DI void k1_body(const char* x, const char* wqkv, char* ws, char* lds_generic, int mt, int nt) {
    LAS char* lds = (LAS char*)lds_generic; const unsigned ldsb = (unsigned)(uintptr_t)lds_generic;
    const int tid = threadIdx.x, lane = tid & 63, wid = rfl(tid >> 6), mi = wid & 3, kj = wid >> 2, m = lane & 15, g = lane >> 4;
    const int part = nt >> 2, hg = nt & 3;
    const char* xb = x + (size_t)(128 * mt + 32 * mi) * (D_ * 2) + kj * 576;
    const unsigned xoff = m * (D_ * 2) + g * 16;
    const char* wb = wqkv + (size_t)nt * 288;
    bf16x8 xf[2][9];
    auto issue_x = [&](int s0) {
#pragma unroll
        for (int s = s0; s < s0 + 3; ++s) { gload16(xf[0][s], xb + s * 64, xoff); gload16(xf[1][s], xb + 16 * (D_ * 2) + s * 64, xoff); } };
    auto issue_w = [&](int st, int slot) {
#pragma unroll
        for (int i = 0; i < 7; ++i) { int pc = wid + 8 * i; pc = pc > 53 ? 53 : pc;
            const int kk = pc / 27, r = pc - 27 * kk, s3 = r / 9, p9 = r - 9 * s3;
            const int idx = p9 * 64 + lane, rho = (idx * 3641) >> 16, ch = idx - 18 * rho, k = 8 * ((rho >> 2) & 3) + 4 * (rho >> 4) + (rho & 3);
            glds16(wb + (size_t)(288 * kk + 32 * (3 * st + s3)) * 3456, (unsigned)(k * 3456 + ch * 16), ldsb + slot * K1_SLOT + pc * 1024); } };
    issue_x(0); issue_w(0, 0); issue_x(3); issue_w(1, 1); issue_x(6);
    f32x4 acc[9][2];
#pragma unroll
    for (int t = 0; t < 9; ++t) { acc[t][0] = (f32x4){0, 0, 0, 0}; acc[t][1] = (f32x4){0, 0, 0, 0}; }
    float sx[2] = {0.f, 0.f}, sq[2] = {0.f, 0.f}, cs[3] = {0.f, 0.f, 0.f};
    const int q4 = (lane >> 2) & 3, p4 = lane & 3;
    LAS const char* trb = lds + kj * (27 * 1024) + (4 * g + q4) * 288 + p4 * 8;
    const int t2 = (mi + 8 > 8) ? 8 : mi + 8;
    auto stage = [&](int st, int slot) {
#pragma unroll
        for (int s3 = 0; s3 < 3; ++s3) { const int s = 3 * st + s3;
            PINV(xf[0][s]); PINV(xf[1][s]);
            LAS const char* sb = trb + slot * K1_SLOT + s3 * 9216;
#pragma unroll
            for (int t = 0; t < 9; ++t) { const bf16x8 w = tr_frag(sb + 32 * t, 16 * 288);
#pragma unroll
                for (int u = 0; u < 2; ++u) acc[t][u] = VPART ? mfma16(xf[u][s], w, acc[t][u]) : mfma16(w, xf[u][s], acc[t][u]); }
#pragma unroll
            for (int u = 0; u < 2; ++u) { const u32x4 xv = __builtin_bit_cast(u32x4, xf[u][s]);
#pragma unroll
                for (int e = 0; e < 4; ++e) { sx[u] = dot2(xv[e], 0x3f803f80u, sx[u]); sq[u] = dot2(xv[e], xv[e], sq[u]); } }
            { const u32x4 a = __builtin_bit_cast(u32x4, tr_frag(sb + 32 * mi, 16 * 288)), b = __builtin_bit_cast(u32x4, tr_frag(sb + 32 * (mi + 4), 16 * 288)),
                          c = __builtin_bit_cast(u32x4, tr_frag(sb + 32 * t2, 16 * 288));
#pragma unroll
              for (int e = 0; e < 4; ++e) { cs[0] = dot2(a[e], 0x3f803f80u, cs[0]); cs[1] = dot2(b[e], 0x3f803f80u, cs[1]); cs[2] = dot2(c[e], 0x3f803f80u, cs[2]); } }
        } };
    WAIT_BAR(19); stage(0, 0);
    LBAR(); issue_w(2, 0);
    WAIT_BAR(13); stage(1, 1);
    WAIT_BAR(0); stage(2, 0);
    LAS float* sm = (LAS float*)(lds + K1_SMALL);
#pragma unroll
    for (int u = 0; u < 2; ++u) { sx[u] += __shfl_xor(sx[u], 16); sx[u] += xor32(sx[u]); sq[u] += __shfl_xor(sq[u], 16); sq[u] += xor32(sq[u]);
        if (g == 0) { sm[kj * 128 + 32 * mi + 16 * u + m] = sx[u]; sm[256 + kj * 128 + 32 * mi + 16 * u + m] = sq[u]; } }
#pragma unroll
    for (int i = 0; i < 3; ++i) { cs[i] += __shfl_xor(cs[i], 16); cs[i] += xor32(cs[i]); }
    if (g == 0) { sm[512 + kj * 144 + 16 * mi + m] = cs[0]; sm[512 + kj * 144 + 16 * (mi + 4) + m] = cs[1]; sm[512 + kj * 144 + 16 * t2 + m] = cs[2]; }
    LBAR();
    LAS char* ex = lds + mi * 18432 + lane * 16;
    if (kj == 0) {
#pragma unroll
        for (int t = 5; t < 9; ++t) { *(LAS f32x4*)(ex + (2 * t) * 1024) = acc[t][0]; *(LAS f32x4*)(ex + (2 * t + 1) * 1024) = acc[t][1]; }
    } else {
#pragma unroll
        for (int t = 0; t < 5; ++t) { *(LAS f32x4*)(ex + (2 * t) * 1024) = acc[t][0]; *(LAS f32x4*)(ex + (2 * t + 1) * 1024) = acc[t][1]; }
    }
    LBAR();
    LAS char* stg = lds + K1_STG;
    const float osc = (part == 0) ? QSCALE : 1.f;
    auto finish = [&](int t) {
#pragma unroll
        for (int u = 0; u < 2; ++u) {
            f32x4 a = acc[t][u] + *(LAS const f32x4*)(ex + (2 * t + u) * 1024);
            const int tl0 = 32 * mi + 16 * u;
            if (!VPART) {
                const int tl = tl0 + m;
                const float mean = (sm[tl] + sm[128 + tl]) * (1.f / D_), var = (sm[256 + tl] + sm[384 + tl]) * (1.f / D_) - mean * mean;
                const float rs = rsqrtf(fmaxf(var, 0.f) + EPS) * osc;
                const f32x4 c4 = *(LAS const f32x4*)(sm + 512 + 16 * t + 4 * g) + *(LAS const f32x4*)(sm + 512 + 144 + 16 * t + 4 * g);
                a = (a - c4 * mean) * rs;
#pragma unroll
                for (int p = 0; p < 2; ++p) { const int n = 16 * t + 4 * g + 2 * p, hl = (n * 3641) >> 16, d = n - 18 * hl; const unsigned w = cvtpk(a[2 * p], a[2 * p + 1]);
                    if (part == 0) { const int o = d < 16 ? hl * 4096 + tl * 32 + d * 2 : 32768 + hl * 512 + tl * 4; *(LAS unsigned*)(stg + o) = w; }
                    else if (d < 16) *(LAS unsigned*)(stg + hl * 5120 + (d >> 3) * 2048 + tl * 16 + (d & 7) * 2) = w;
                    else *(LAS u32x2*)(stg + hl * 5120 + 4096 + tl * 8) = (u32x2){w, 0x00003f80u}; }
            } else {
                const int tl = tl0 + 4 * g;
                const f32x4 mean = (*(LAS const f32x4*)(sm + tl) + *(LAS const f32x4*)(sm + 128 + tl)) * (1.f / D_);
                const f32x4 var = (*(LAS const f32x4*)(sm + 256 + tl) + *(LAS const f32x4*)(sm + 384 + tl)) * (1.f / D_) - mean * mean;
                const int n = 16 * t + m, hl = (n * 3641) >> 16, d = n - 18 * hl;
                const float c1 = sm[512 + n] + sm[512 + 144 + n];
                float o[4];
#pragma unroll
                for (int r = 0; r < 4; ++r) o[r] = (a[r] - c1 * mean[r]) * rsqrtf(fmaxf(var[r], 0.f) + EPS);
                *(LAS u32x2*)(stg + hl * 4864 + (2 * mi + u) * 608 + (g >> 1) * 304 + d * 16 + (g & 1) * 8) = (u32x2){cvtpk(o[0], o[1]), cvtpk(o[2], o[3])};
            }
        } };
    if (kj == 0) { finish(0); finish(1); finish(2); finish(3); finish(4); } else { finish(5); finish(6); finish(7); finish(8); }
    if (VPART && tid < 128) *(LAS u32x4*)(stg + (tid >> 4) * 4864 + ((tid >> 1) & 7) * 608 + (tid & 1) * 304 + 18 * 16) = (u32x4){0x3f803f80u, 0x3f803f80u, 0x3f803f80u, 0x3f803f80u};
    if (nt == 0 && tid < 128) ((float*)(ws + WS_MUX))[128 * mt + tid] = (sm[tid] + sm[128 + tid]) * (1.f / D_);
    LBAR();
    const int head0 = 8 * hg, tok0 = 128 * mt;
    if (VPART) {
        for (int c = tid; c < 2432; c += 512) { const int hl = c / 304, r = c - 304 * hl;
            *(u32x4*)(ws + WS_VIMG + (size_t)(head0 + hl) * VIMG + (tok0 >> 4) * 608 + r * 16) = *(LAS const u32x4*)(stg + c * 16); }
    } else if (part == 0) {
        for (int c = tid; c < 2048; c += 512) { const int hl = c >> 8, r = c & 255;
            *(u32x4*)(ws + WS_QM + ((size_t)(head0 + hl) * S_ + tok0) * 32 + r * 16) = *(LAS const u32x4*)(stg + c * 16); }
        if (tid < 256) { const int hl = tid >> 5, r = tid & 31;
            *(u32x4*)(ws + WS_QT + ((size_t)(head0 + hl) * S_ + tok0) * 4 + r * 16) = *(LAS const u32x4*)(stg + 32768 + tid * 16); }
    } else {
        for (int c = tid; c < 2560; c += 512) { const int hl = c / 320, r = c - 320 * hl;
            const size_t o = r < 128 ? (size_t)tok0 * 16 + r * 16 : r < 256 ? 32768 + (size_t)tok0 * 16 + (r - 128) * 16 : 65536 + (size_t)tok0 * 8 + (r - 256) * 16;
            *(u32x4*)(ws + WS_KIMG + (size_t)(head0 + hl) * KIMG + o) = *(LAS const u32x4*)(stg + c * 16); }
        float mx = 0.f;
#pragma unroll
        for (int i = 0; i < 2; ++i) { const int tl = lane + 64 * i; LAS const char* hb = stg + wid * 5120;
            const u32x4 a = *(LAS const u32x4*)(hb + tl * 16), b = *(LAS const u32x4*)(hb + 2048 + tl * 16); const unsigned c = *(LAS const unsigned*)(hb + 4096 + tl * 8);
            float s2 = dot2(c, c, 0.f);
#pragma unroll
            for (int e = 0; e < 4; ++e) { s2 = dot2(a[e], a[e], s2); s2 = dot2(b[e], b[e], s2); }
            mx = fmaxf(mx, s2); }
#pragma unroll
        for (int o = 1; o < 64; o <<= 1) mx = fmaxf(mx, __shfl_xor(mx, o));
        if (lane == 0) ((float*)(ws + WS_KMAX))[(head0 + wid) * 16 + mt] = mx;
    }
}
__global__ __launch_bounds__(512, 2) void k1_qkv(const char* x, const char* wqkv, const char* wo, char* ws) {
    extern __shared__ __attribute__((aligned(16))) char lds[];
    const int b = blockIdx.x;
    if (b >= 192) {
        const int lane = threadIdx.x & 63, gw = (b - 192) * 8 + (threadIdx.x >> 6);
        for (int c = gw; c < D_; c += 512) {
            const char* row = wo + (size_t)c * (D_ * 2);
            const u32x4 a = *(const u32x4*)(row + lane * 16); float s = 0.f;
#pragma unroll
            for (int e = 0; e < 4; ++e) s = dot2(a[e], 0x3f803f80u, s);
            if (lane < 8) { const u32x4 bq = *(const u32x4*)(row + 1024 + lane * 16);
#pragma unroll
                for (int e = 0; e < 4; ++e) s = dot2(bq[e], 0x3f803f80u, s); }
#pragma unroll
            for (int o = 1; o < 64; o <<= 1) s += __shfl_xor(s, o);
            if (lane == 0) { const float w = s * (1.f / D_); const unsigned hi = cvtpk(w, 0.f) & 0xffffu; const unsigned lo = cvtpk(w - bflo(hi), 0.f) & 0xffffu;
                ((unsigned short*)(ws + WS_WBAR))[c] = (unsigned short)hi; ((unsigned short*)(ws + WS_WBAR))[D_ + c] = (unsigned short)lo; }
        }
        return;
    }
    const int xcd = b & 7, idx = b >> 3, mt = (xcd & 3) * 4 + (idx & 3), nt = (xcd >> 2) * 6 + (idx >> 2);
    if (nt >= 8) k1_body<true>(x, wqkv, ws, lds, mt, nt); else k1_body<false>(x, wqkv, ws, lds, mt, nt);
}

constexpr int K2_LDS = 160 * 1024;
template <bool SAFE>
DI void attn_stage(LAS const char* kp, LAS const char* tp, LAS const char* vp, const bf16x8 (&qm)[2], const s16x4 (&qt)[2], f32x16 (&o)[2], float (&mrun)[2]) {
#pragma unroll 1
    for (int it = 0; it < 8; ++it) {
        const bf16x8 kf = *(LAS const bf16x8*)(kp + it * 512);
        const s16x4 kt = *(LAS const s16x4*)(tp + it * 256);
        const bf16x8 v0 = *(LAS const bf16x8*)(vp + it * 1216), v1 = *(LAS const bf16x8*)(vp + it * 1216 + 608);
#pragma unroll
        for (int u = 0; u < 2; ++u) {
            f32x16 s = __builtin_amdgcn_mfma_f32_32x32x16_bf16(kf, qm[u], (f32x16){}, 0, 0, 0);
            s = __builtin_amdgcn_mfma_f32_32x32x8bf16_1k(kt, qt[u], s, 0, 0, 0);
            if (!SAFE) {
                float mx = s[0];
#pragma unroll
                for (int r = 1; r < 16; ++r) mx = fmaxf(mx, s[r]);
                mx = fmaxf(mx, xor32(mx));
                const float mn = fmaxf(mrun[u], mx), al = __builtin_amdgcn_exp2f(mrun[u] - mn);
                mrun[u] = mn;
#pragma unroll
                for (int r = 0; r < 16; ++r) { s[r] -= mn; o[u][r] *= al; }
            }
#pragma unroll
            for (int r = 0; r < 16; ++r) s[r] = __builtin_amdgcn_exp2f(s[r]);
            const f32x8 lo = __builtin_shufflevector(s, s, 0, 1, 2, 3, 4, 5, 6, 7), hi = __builtin_shufflevector(s, s, 8, 9, 10, 11, 12, 13, 14, 15);
            o[u] = __builtin_amdgcn_smfmac_f32_32x32x32_bf16(v0, __builtin_bit_cast(bf16x16, lo), o[u], 0xDDDD, 0, 0);
            o[u] = __builtin_amdgcn_smfmac_f32_32x32x32_bf16(v1, __builtin_bit_cast(bf16x16, hi), o[u], 0xDDDD, 0, 0);
        }
    }
}
__global__ __launch_bounds__(1024, 4) void k2_attn(char* ws) {
    extern __shared__ __attribute__((aligned(16))) char lds_generic[];
    LAS char* lds = (LAS char*)lds_generic; const unsigned ldsb = (unsigned)(uintptr_t)lds_generic;
    const int tid = threadIdx.x, lane = tid & 63, wid = rfl(tid >> 6), qg = wid & 3, kq = wid >> 2, r32 = lane & 31, h = lane >> 5;
    const int b = blockIdx.x, xcd = b & 7, idx = b >> 3, head = xcd * 4 + (idx & 3), qb = idx >> 2;
    const char* kimg = ws + WS_KIMG + (size_t)head * KIMG; const char* vimg = ws + WS_VIMG + (size_t)head * VIMG;
    const int q0 = 256 * qb + 64 * qg;
    bf16x8 qm[2]; float qtl[2]; float km;
    gload16(qm[0], ws + WS_QM + ((size_t)head * S_ + q0) * 32, r32 * 32 + h * 16); gload16(qm[1], ws + WS_QM + ((size_t)head * S_ + q0 + 32) * 32, r32 * 32 + h * 16);
    gload4(qtl[0], ws + WS_QT + ((size_t)head * S_ + q0) * 4, r32 * 4); gload4(qtl[1], ws + WS_QT + ((size_t)head * S_ + q0 + 32) * 4, r32 * 4);
    gload4(km, ws + WS_KMAX + head * 64, (lane & 15) * 4);
    auto piece = [&](int st, int i) {
        if (i < 10) { const int o = i < 4 ? (512 * kq + 256 * st) * 16 + i * 1024 : i < 8 ? 32768 + (512 * kq + 256 * st) * 16 + (i - 4) * 1024 : 65536 + (512 * kq + 256 * st) * 8 + (i - 8) * 1024;
            glds16(kimg + o, lane * 16, ldsb + o); }
        else { const int o = kq * 19456 + (st * 10 + i - 10) * 1024; glds16(vimg + o, lane * 16, ldsb + KIMG + o); } };
#pragma unroll
    for (int u = 0; u < 5; ++u) piece(0, 5 * qg + u);
#pragma unroll
    for (int u = 0; u < 5; ++u) { const int i = 5 * qg + u; piece(1, i > 18 ? 18 : i); }
    WAITV(10);
    PINV(qm[0]); PINV(qm[1]); PINV(qtl[0]); PINV(qtl[1]); PINV(km);
#pragma unroll
    for (int o = 1; o < 16; o <<= 1) km = fmaxf(km, __shfl_xor(km, o));
    s16x4 qt[2]; bool unsafe = false;
#pragma unroll
    for (int u = 0; u < 2; ++u) { const u32x4 qv = __builtin_bit_cast(u32x4, qm[u]); const unsigned tl = __float_as_uint(qtl[u]);
        float n2 = h ? 0.f : dot2(tl, tl, 0.f);
#pragma unroll
        for (int e = 0; e < 4; ++e) n2 = dot2(qv[e], qv[e], n2);
        n2 += xor32(n2);
        const float bound = sqrtf(n2 * km) * 1.0001f + 1e-6f;
        unsafe |= !(bound <= SAFE_B);
        const unsigned cb = (__float_as_uint(bound) + 0xffffu) >> 16;
        const u32x2 w = h ? (u32x2){0u, 0u} : (u32x2){tl, cb | 0x8000u};
        qt[u] = __builtin_bit_cast(s16x4, w); }
    const bool slow = __any(unsafe);
    if (slow) {
#pragma unroll
        for (int u = 0; u < 2; ++u) { u32x2 w = __builtin_bit_cast(u32x2, qt[u]); w[1] = 0u; qt[u] = __builtin_bit_cast(s16x4, w); } }
    f32x16 o[2] = {(f32x16){}, (f32x16){}}; float mrun[2] = {-INFINITY, -INFINITY};
    const int vrow = r32 <= 18 ? r32 : r32 == 19 ? 18 : r32 < 24 ? r32 - 20 : r32 < 28 ? r32 - 12 : 16;
    LAS const char* kp = lds + h * 32768 + (512 * kq + r32) * 16;
    LAS const char* tp = lds + 65536 + (512 * kq + r32) * 8;
    LAS const char* vp = lds + KIMG + kq * 19456 + h * 304 + vrow * 16;
    WAIT_BAR(5);
    if (slow) attn_stage<false>(kp, tp, vp, qm, qt, o, mrun); else attn_stage<true>(kp, tp, vp, qm, qt, o, mrun);
    WAIT_BAR(0);
    if (slow) attn_stage<false>(kp + 4096, tp + 2048, vp + 9728, qm, qt, o, mrun); else attn_stage<true>(kp + 4096, tp + 2048, vp + 9728, qm, qt, o, mrun);
    LBAR();
    LAS float* mx = (LAS float*)(lds + 16 * 6144);
    if (h == 0) { mx[wid * 64 + r32] = slow ? mrun[0] : 0.f; mx[wid * 64 + 32 + r32] = slow ? mrun[1] : 0.f; }
    LBAR();
#pragma unroll
    for (int u = 0; u < 2; ++u) {
        float f = 1.f;
        if (slow) { float M = mx[qg * 64 + 32 * u + r32];
#pragma unroll
            for (int k = 1; k < 4; ++k) M = fmaxf(M, mx[(4 * k + qg) * 64 + 32 * u + r32]);
            f = __builtin_amdgcn_exp2f(mrun[u] - M); }
#pragma unroll
        for (int qd = 0; qd < 3; ++qd) *(LAS f32x4*)(lds + wid * 6144 + (u * 3 + qd) * 1024 + lane * 16) = (f32x4){o[u][4 * qd] * f, o[u][4 * qd + 1] * f, o[u][4 * qd + 2] * f, o[u][4 * qd + 3] * f};
    }
    LBAR();
    { const int u = kq & 1, hf = kq >> 1;
      LAS const char* pb = lds + qg * 6144 + u * 3072;
      float l = 0.f;
#pragma unroll
      for (int k = 0; k < 4; ++k) l += *(LAS const float*)(pb + k * 24576 + 2048 + r32 * 16 + 8);
      const float rl = 1.f / l;
      const int token = q0 + 32 * u + r32; char* ob = ws + WS_OF + (size_t)(token >> 4) * 18 * 1024 + (token & 15) * 16;
      auto put = [&](int d, float a, float bb) { const int c = 18 * head + d; *(unsigned*)(ob + (c >> 5) * 1024 + ((c & 31) >> 3) * 256 + (c & 7) * 2) = cvtpk(a * rl, bb * rl); };
      f32x4 a = {0, 0, 0, 0};
#pragma unroll
      for (int k = 0; k < 4; ++k) a += *(LAS const f32x4*)(pb + k * 24576 + hf * 1024 + lane * 16);
      put(8 * hf + 4 * h, a[0], a[1]); put(8 * hf + 4 * h + 2, a[2], a[3]);
      if (hf == 0) { f32x4 c = {0, 0, 0, 0};
#pragma unroll
          for (int k = 0; k < 4; ++k) c += *(LAS const f32x4*)(pb + k * 24576 + 2048 + lane * 16);
          if (h == 0) put(16, c[0], c[1]); }
    }
}

constexpr int K3_LDS = 112 * 1024;
__global__ __launch_bounds__(512, 2) void k3_oproj(const char* x, char* ws) {
    extern __shared__ __attribute__((aligned(16))) char lds_generic[];
    LAS char* lds = (LAS char*)lds_generic; const unsigned ldsb = (unsigned)(uintptr_t)lds_generic;
    const int tid = threadIdx.x, lane = tid & 63, wid = rfl(tid >> 6), mi = wid & 3, kj = wid >> 2, m = lane & 15, g = lane >> 4;
    const int b = blockIdx.x, xcd = b & 7, idx = b >> 3, mt32 = xcd * 4 + (idx & 3), n6 = idx >> 2, mt16 = 4 * mt32 + mi;
    const char* wof = ws + WS_WO + (size_t)n6 * 108 * 1024;
    bf16x8 of[9], wf[9];
    const char* ob = ws + WS_OF + ((size_t)mt16 * 18 + 9 * kj) * 1024;
    const char* wbp = ws + WS_WBAR + kj * 576; const unsigned wboff = (m == 1 ? D_ * 2 : 0) + g * 16;
#pragma unroll
    for (int s = 0; s < 4; ++s) { gload16(of[s], ob + s * 1024, lane * 16); gload16(wf[s], wbp + s * 64, wboff); }
#pragma unroll
    for (int i = 0; i < 6; ++i) { const int pc = wid + 8 * i, fr = pc < 24 ? pc : pc + 30; glds16(wof + fr * 1024, lane * 16, ldsb + fr * 1024); }
#pragma unroll
    for (int s = 4; s < 9; ++s) { gload16(of[s], ob + s * 1024, lane * 16); gload16(wf[s], wbp + s * 64, wboff); }
#pragma unroll
    for (int i = 0; i < 8; ++i) { int pc = wid + 8 * i; pc = pc > 59 ? 59 : pc; const int fr = pc < 30 ? pc + 24 : pc + 48; glds16(wof + fr * 1024, lane * 16, ldsb + fr * 1024); }
    const int token = 16 * mt16 + m;
    u32x2 xr[4]; float mux;
    const char* xp = x + (size_t)(16 * mt16) * (D_ * 2) + (96 * n6 + 64 * kj) * 2; const unsigned xo = m * (D_ * 2) + g * 8;
#pragma unroll
    for (int t = 0; t < 4; ++t) gload8(xr[t], xp + (kj ? (t & 1) : t) * 32, xo);
    gload4(mux, ws + WS_MUX + (size_t)16 * mt16 * 4, m * 4);
    f32x4 acc[6], accw = {0, 0, 0, 0};
#pragma unroll
    for (int t = 0; t < 6; ++t) acc[t] = (f32x4){0, 0, 0, 0};
    LAS const char* ab = lds + kj * (54 * 1024) + lane * 16;
    auto step = [&](int s) { PINV(of[s]); PINV(wf[s]);
#pragma unroll
        for (int t = 0; t < 6; ++t) acc[t] = mfma16(lds_frag(ab + (s * 6 + t) * 1024), of[s], acc[t]);
        u32x4 w = __builtin_bit_cast(u32x4, wf[s]); if (m >= 2) w = (u32x4){0, 0, 0, 0};
        accw = mfma16(__builtin_bit_cast(bf16x8, w), of[s], accw); };
    WAIT_BAR(23);
#pragma unroll
    for (int s = 0; s < 4; ++s) step(s);
    WAIT_BAR(0);
#pragma unroll
    for (int s = 4; s < 9; ++s) step(s);
#pragma unroll
    for (int t = 0; t < 4; ++t) PINV(xr[t]);
    PINV(mux);
    LBAR();
    LAS char* ex = lds + mi * 8192 + lane * 16;
    if (kj == 0) { *(LAS f32x4*)(ex + 4 * 1024) = acc[4]; *(LAS f32x4*)(ex + 5 * 1024) = acc[5]; *(LAS f32x4*)(ex + 6 * 1024) = accw; }
    else {
#pragma unroll
        for (int t = 0; t < 4; ++t) *(LAS f32x4*)(ex + t * 1024) = acc[t];
        *(LAS f32x4*)(ex + 7 * 1024) = accw; }
    LBAR();
    accw += *(LAS const f32x4*)(ex + (kj ? 6 : 7) * 1024);
    const float mu = mux + __shfl(accw[0] + accw[1], m);
    float ssq = 0.f;
    auto fin = [&](int t, u32x2 xv, unsigned (&pk)[2]) {
        const f32x4 a = acc[t] + *(LAS const f32x4*)(ex + t * 1024);
        const unsigned h0 = cvtpk(a[0] + bflo(xv[0]), a[1] + bfhi(xv[0])), h1 = cvtpk(a[2] + bflo(xv[1]), a[3] + bfhi(xv[1]));
        *(u32x2*)(ws + WS_HC + ((size_t)mt16 * 36 + 6 * n6 + t) * 512 + lane * 8) = (u32x2){h0, h1};
        pk[0] = cvtpk(bflo(h0) - mu, bfhi(h0) - mu); pk[1] = cvtpk(bflo(h1) - mu, bfhi(h1) - mu);
        ssq = dot2(pk[0], pk[0], ssq); ssq = dot2(pk[1], pk[1], ssq); };
    auto pair = [&](int t, u32x2 xa, u32x2 xb2) { unsigned p0[2], p1[2]; fin(t, xa, p0); fin(t + 1, xb2, p1);
        *(u32x4*)(ws + WS_HCF + ((size_t)mt16 * 18 + 3 * n6 + (t >> 1)) * 1024 + lane * 16) = (u32x4){p0[0], p0[1], p1[0], p1[1]}; };
    if (kj == 0) { pair(0, xr[0], xr[1]); pair(2, xr[2], xr[3]); } else pair(4, xr[0], xr[1]);
    ssq += __shfl_xor(ssq, 16); ssq += xor32(ssq);
    if (g == 0) ((float*)(ws + WS_SSQ))[token * 16 + 2 * n6 + kj] = ssq;
}

constexpr int RING_SLOT = 36 * 1024, G_LDS = 4 * RING_SLOT;
DI void ring_issue(const char* stream, int st, int wid, int lane, unsigned ldsb) {
    const char* sb = stream + (size_t)st * RING_SLOT + wid * 1024; const unsigned db = ldsb + (st & 3) * RING_SLOT + wid * 1024;
#pragma unroll
    for (int i = 0; i < 4; ++i) glds16(sb + i * 8192, lane * 16, db + i * 8192);
    if (wid < 4) glds16(sb + 32768, lane * 16, db + 32768);
}
DI float geglu(float g, float u) {
    const float z = g * __builtin_fmaf(g * g, -0.10294324f, -2.30220818f);
    return g * u * __builtin_amdgcn_rcpf(1.f + __builtin_amdgcn_exp2f(z));
}
__global__ __launch_bounds__(512, 2) void g1_gateup(char* ws) {
    extern __shared__ __attribute__((aligned(16))) char lds_generic[];
    LAS char* lds = (LAS char*)lds_generic; const unsigned ldsb = (unsigned)(uintptr_t)lds_generic;
    const int tid = threadIdx.x, lane = tid & 63, wid = rfl(tid >> 6), g = lane >> 4, m = lane & 15;
    const int b = blockIdx.x, xcd = b & 7, idx = b >> 3, j = (xcd >> 1) * 8 + (idx & 7), mt8 = (xcd & 1) * 4 + (idx >> 3);
    const int mt16 = 16 * mt8 + 2 * wid;
    const char* stream = ws + WS_W1 + (size_t)j * 324 * 1024;
    const char* hb = ws + WS_HCF + (size_t)mt16 * 18 * 1024;
    bf16x8 hf[2][18]; f32x4 sq[2];
    gload16f(sq[0], ws + WS_SSQ + (size_t)16 * mt16 * 64, m * 64 + g * 16); gload16f(sq[1], ws + WS_SSQ + (size_t)16 * (mt16 + 1) * 64, m * 64 + g * 16);
#pragma unroll
    for (int st = 0; st < 3; ++st) { ring_issue(stream, st, wid, lane, ldsb);
#pragma unroll
        for (int s = 6 * st; s < 6 * st + 6; ++s) { gload16(hf[0][s], hb + s * 1024, lane * 16); gload16(hf[1][s], hb + (18 + s) * 1024, lane * 16); } }
    f32x4 acc[3][2][2]; float rs[2]; unsigned keep[2][2];
    char* ab = ws + WS_ACT + (size_t)mt16 * 144 * 1024 + lane * 16;
    const int ksb = 9 * (j >> 1) + 4 * (j & 1);
#pragma unroll
    for (int st = 0; st < 9; ++st) {
        if (st == 0) WAIT_BAR(2 * 4 + 24); else if (st == 1) WAIT_BAR(2 * 4 + 12); else if (st <= 6) WAIT_BAR(8); else if (st == 7) WAIT_BAR(4); else WAIT_BAR(0);
        if (st + 3 < 9) ring_issue(stream, st + 3, wid, lane, ldsb);
        if (st % 3 == 0) {
#pragma unroll
            for (int t = 0; t < 3; ++t)
#pragma unroll
                for (int c = 0; c < 2; ++c) { acc[t][c][0] = (f32x4){0, 0, 0, 0}; acc[t][c][1] = (f32x4){0, 0, 0, 0}; } }
        if (st == 0) { PINV(sq[0]); PINV(sq[1]);
#pragma unroll
            for (int u = 0; u < 2; ++u) { float t = g < 3 ? (sq[u][0] + sq[u][1]) + (sq[u][2] + sq[u][3]) : 0.f; t += __shfl_xor(t, 16); t += xor32(t); rs[u] = rsqrtf(t * (1.f / D_) + EPS); } }
        LAS const char* sb = lds + (st & 3) * RING_SLOT + lane * 16;
        bf16x8 ar[3];
        ar[0] = lds_frag(sb); ar[1] = lds_frag(sb + 1024);
#pragma unroll
        for (int i = 0; i < 36; ++i) { const int s6 = i / 6, t = (i % 6) >> 1, c = i & 1, s = 6 * (st % 3) + s6;
            if (st < 3 && i % 6 == 0) { PINV(hf[0][s]); PINV(hf[1][s]); }
            if (i + 2 < 36) ar[(i + 2) % 3] = lds_frag(sb + (i + 2) * 1024);
            SBAR();
            acc[t][c][0] = mfma16(ar[i % 3], hf[0][s], acc[t][c][0]); acc[t][c][1] = mfma16(ar[i % 3], hf[1][s], acc[t][c][1]);
            SBAR(); }
        if (st % 3 == 2) { const int grp = st / 3;
#pragma unroll
            for (int u = 0; u < 2; ++u) { unsigned pk[3][2];
#pragma unroll
                for (int t = 0; t < 3; ++t) { float v[4];
#pragma unroll
                    for (int r = 0; r < 4; ++r) v[r] = geglu(acc[t][0][u][r] * rs[u], acc[t][1][u][r] * rs[u]);
                    pk[t][0] = cvtpk(v[0], v[1]); pk[t][1] = cvtpk(v[2], v[3]); }
                char* au = ab + (size_t)u * 144 * 1024;
                if (grp == 0) { *(u32x4*)(au + (ksb + 0) * 1024) = (u32x4){pk[0][0], pk[0][1], pk[1][0], pk[1][1]}; keep[u][0] = pk[2][0]; keep[u][1] = pk[2][1]; }
                else if (grp == 1) { *(u32x4*)(au + (ksb + 1) * 1024) = (u32x4){pk[0][0], pk[0][1], pk[1][0], pk[1][1]};
                                     *(u32x4*)(au + (ksb + 2) * 1024) = (u32x4){keep[u][0], keep[u][1], pk[2][0], pk[2][1]}; }
                else { *(u32x4*)(au + (ksb + 3) * 1024) = (u32x4){pk[0][0], pk[0][1], pk[1][0], pk[1][1]};
                       *(u32x2*)(au + (9 * (j >> 1) + 8) * 1024 + (j & 1) * 8) = (u32x2){pk[2][0], pk[2][1]}; }
            } }
    }
}

__global__ __launch_bounds__(512, 2) void g2_down(char* ws) {
    extern __shared__ __attribute__((aligned(16))) char lds_generic[];
    LAS char* lds = (LAS char*)lds_generic; const unsigned ldsb = (unsigned)(uintptr_t)lds_generic;
    const int tid = threadIdx.x, lane = tid & 63, wid = rfl(tid >> 6), mi = wid & 3, kj = wid >> 2;
    const int b = blockIdx.x, xcd = b & 7, idx = b >> 3, ksl = xcd >> 1, mt = (xcd & 1) * 8 + (idx & 7), n4 = idx >> 3;
    const int mt16 = 8 * mt + 2 * mi;
    const char* stream = ws + WS_W2 + (size_t)(ksl * 4 + n4) * 324 * 1024;
    const char* ap = ws + WS_ACT + ((size_t)mt16 * 144 + 36 * ksl + 18 * kj) * 1024;
    bf16x8 af[4][2][2];
    auto issue = [&](int st) { ring_issue(stream, st, wid, lane, ldsb);
#pragma unroll
        for (int s2 = 0; s2 < 2; ++s2) { gload16(af[st & 3][s2][0], ap + (2 * st + s2) * 1024, lane * 16); gload16(af[st & 3][s2][1], ap + (144 + 2 * st + s2) * 1024, lane * 16); } };
    issue(0); issue(1); issue(2);
    f32x4 acc[9][2];
#pragma unroll
    for (int t = 0; t < 9; ++t) { acc[t][0] = (f32x4){0, 0, 0, 0}; acc[t][1] = (f32x4){0, 0, 0, 0}; }
#pragma unroll
    for (int st = 0; st < 9; ++st) {
        if (st <= 6) WAIT_BAR(16); else if (st == 7) WAIT_BAR(8); else WAIT_BAR(0);
        if (st + 3 < 9) issue(st + 3);
        LAS const char* sb = lds + (st & 3) * RING_SLOT + kj * (18 * 1024) + lane * 16;
#pragma unroll
        for (int s2 = 0; s2 < 2; ++s2) { PINV(af[st & 3][s2][0]); PINV(af[st & 3][s2][1]);
#pragma unroll
            for (int t = 0; t < 9; ++t) { const bf16x8 a = lds_frag(sb + (s2 * 9 + t) * 1024);
                acc[t][0] = mfma16(a, af[st & 3][s2][0], acc[t][0]); acc[t][1] = mfma16(a, af[st & 3][s2][1], acc[t][1]); } }
    }
    LBAR();
    LAS char* ex = lds + mi * 18432 + lane * 16;
    char* pb = ws + WS_PART + (((size_t)ksl * 128 + mt16) * 36 + 9 * n4) * 1024 + lane * 16;
    if (kj == 0) {
#pragma unroll
        for (int t = 5; t < 9; ++t) { *(LAS f32x4*)(ex + (2 * t) * 1024) = acc[t][0]; *(LAS f32x4*)(ex + (2 * t + 1) * 1024) = acc[t][1]; }
    } else {
#pragma unroll
        for (int t = 0; t < 5; ++t) { *(LAS f32x4*)(ex + (2 * t) * 1024) = acc[t][0]; *(LAS f32x4*)(ex + (2 * t + 1) * 1024) = acc[t][1]; }
    }
    LBAR();
    auto fin = [&](int t) {
#pragma unroll
        for (int u = 0; u < 2; ++u) *(f32x4*)(pb + ((size_t)u * 36 + t) * 1024) = acc[t][u] + *(LAS const f32x4*)(ex + (2 * t + u) * 1024); };
    if (kj == 0) { fin(0); fin(1); fin(2); fin(3); fin(4); } else { fin(5); fin(6); fin(7); fin(8); }
}

__global__ __launch_bounds__(256) void red_kernel(const char* ws, char* out) {
    const int lane = threadIdx.x & 63, w = blockIdx.x * 4 + (threadIdx.x >> 6);
    const int mt16 = w / 9, tq = w - 9 * mt16, m = lane & 15, g = lane >> 4;
#pragma unroll
    for (int i = 0; i < 4; ++i) { const int T = 4 * tq + i; const size_t o = ((size_t)mt16 * 36 + T);
        f32x4 a = *(const f32x4*)(ws + WS_PART + o * 1024 + lane * 16);
#pragma unroll
        for (int k = 1; k < 4; ++k) a += *(const f32x4*)(ws + WS_PART + ((size_t)k * 128 * 36 + o) * 1024 + lane * 16);
        const u32x2 hv = *(const u32x2*)(ws + WS_HC + o * 512 + lane * 8);
        *(u32x2*)(out + (size_t)(16 * mt16 + m) * (D_ * 2) + (16 * T + 4 * g) * 2) = (u32x2){cvtpk(a[0] + bflo(hv[0]), a[1] + bfhi(hv[0])), cvtpk(a[2] + bflo(hv[1]), a[3] + bfhi(hv[1]))}; }
}

}

extern "C" void kernel_launch(void* const* d_in, const int* in_sizes, int n_in, void* d_out, int out_size, void* d_ws, size_t ws_size, hipStream_t stream) {
    const char* x = (const char*)d_in[0]; const char* wqkv = (const char*)d_in[1]; const char* wo = (const char*)d_in[2];
    const char* wg = (const char*)d_in[4]; const char* wu = (const char*)d_in[5]; const char* wd = (const char*)d_in[6];
    char* ws = (char*)d_ws;
    (void)hipFuncSetAttribute((const void*)k1_qkv, hipFuncAttributeMaxDynamicSharedMemorySize, K1_LDS);
    (void)hipFuncSetAttribute((const void*)k2_attn, hipFuncAttributeMaxDynamicSharedMemorySize, K2_LDS);
    (void)hipFuncSetAttribute((const void*)k3_oproj, hipFuncAttributeMaxDynamicSharedMemorySize, K3_LDS);
    (void)hipFuncSetAttribute((const void*)g1_gateup, hipFuncAttributeMaxDynamicSharedMemorySize, G_LDS);
    (void)hipFuncSetAttribute((const void*)g2_down, hipFuncAttributeMaxDynamicSharedMemorySize, G_LDS);
    prep_kernel<<<507, 512, 32768, stream>>>(wo, wg, wu, wd, ws);
    k1_qkv<<<256, 512, K1_LDS, stream>>>(x, wqkv, wo, ws);
    k2_attn<<<256, 1024, K2_LDS, stream>>>(ws);
    k3_oproj<<<192, 512, K3_LDS, stream>>>(x, ws);
    g1_gateup<<<256, 512, G_LDS, stream>>>(ws);
    g2_down<<<256, 512, G_LDS, stream>>>(ws);
    red_kernel<<<288, 256, 0, stream>>>(ws, (char*)d_out);
}
```

```cpp
#include <hip/hip_runtime.h>
#include <hip/hip_bf16.h>
#include <stdint.h>
#include <utility>
#define K1_TOUCH 0
#define K1_ROT 0
#define G1PD 3
#define ACT_AUX 0
#define G1_PRIO 1

namespace {

typedef __bf16 bf16x2 __attribute__((ext_vector_type(2)));
typedef __bf16 bf16x8 __attribute__((ext_vector_type(8)));
typedef __bf16 bf16x16 __attribute__((ext_vector_type(16)));
typedef short s16x4 __attribute__((ext_vector_type(4)));
typedef float f32x2 __attribute__((ext_vector_type(2)));
typedef float f32x4 __attribute__((ext_vector_type(4)));
typedef float f32x8 __attribute__((ext_vector_type(8)));
typedef float f32x16 __attribute__((ext_vector_type(16)));
typedef unsigned u32x2 __attribute__((ext_vector_type(2)));
typedef unsigned u32x4 __attribute__((ext_vector_type(4)));
#define LAS __attribute__((address_space(3)))
#define DI __device__ __forceinline__

constexpr int S_ = 2048, D_ = 576, H_ = 32, F_ = 4608;
constexpr float EPS = 1e-5f;
constexpr float QSCALE = 0.23570226039551584f * 1.4426950408889634f;
constexpr float SAFE_B = 48.f;

constexpr int KVCHUNK = 19456, KVIMG = 8 * KVCHUNK;
constexpr size_t WS_QM   = 0;
constexpr size_t WS_QT   = WS_QM + (size_t)32 * 2048 * 32;
constexpr size_t WS_KV   = WS_QT + (size_t)32 * 2048 * 4;
constexpr size_t WS_KMAX = WS_KV + (size_t)32 * KVIMG;
constexpr size_t WS_MUX  = WS_KMAX + 32 * 16 * 4;
constexpr size_t WS_WBAR = WS_MUX + 2048 * 4;
constexpr size_t WS_SSQ  = WS_WBAR + 4096;
constexpr size_t WS_OF   = WS_SSQ + (size_t)2048 * 64;
constexpr size_t WS_HC   = WS_OF + (size_t)128 * 18 * 1024;
constexpr size_t WS_HCF  = WS_HC + (size_t)128 * 36 * 512;
constexpr size_t WS_W1   = WS_HCF + (size_t)128 * 18 * 1024;
constexpr size_t WS_W2   = WS_W1 + (size_t)32 * 324 * 1024;
constexpr size_t WS_WO   = WS_W2 + (size_t)16 * 324 * 1024;
constexpr size_t WS_ACT  = WS_WO + (size_t)6 * 108 * 1024;
constexpr size_t WS_PART = WS_ACT + (size_t)128 * 144 * 1024;
constexpr size_t WS_END  = WS_PART + (size_t)4 * 128 * 36 * 512;

#define STAMP_DECL
#define STAMP(kid, i)
#define RSTAMP(kid, i)
#define STAMP_FLUSH(kid)
DI unsigned cvtpk(float lo, float hi) { f32x2 v = {lo, hi}; bf16x2 b = __builtin_convertvector(v, bf16x2); return __builtin_bit_cast(unsigned, b); }
DI float bflo(unsigned w) { return __uint_as_float(w << 16); }
DI float bfhi(unsigned w) { return __uint_as_float(w & 0xffff0000u); }
DI float dot2(unsigned a, unsigned b, float c) { return __builtin_amdgcn_fdot2_f32_bf16(__builtin_bit_cast(bf16x2, a), __builtin_bit_cast(bf16x2, b), c, false); }
DI int rfl(int x) { return __builtin_amdgcn_readfirstlane(x); }
DI float xor32(float v) { auto r = __builtin_amdgcn_permlane32_swap(__float_as_uint(v), __float_as_uint(v), false, false); return __uint_as_float((threadIdx.x & 32) ? r[0] : r[1]); }
DI float xor16(float v) { auto r = __builtin_amdgcn_permlane16_swap(__float_as_uint(v), __float_as_uint(v), false, false); return __uint_as_float((threadIdx.x & 16) ? r[0] : r[1]); }
DI f32x4 mfma16(bf16x8 a, bf16x8 b, f32x4 c) { return __builtin_amdgcn_mfma_f32_16x16x32_bf16(a, b, c, 0, 0, 0); }
DI void mfma16_acc(f32x4& c, bf16x8 a, bf16x8 b) { asm volatile("v_mfma_f32_16x16x32_bf16 %0, %1, %2, %0" : "+v"(c) : "v"(a), "v"(b)); }
DI void mfma16_new(f32x4& c, bf16x8 a, bf16x8 b) { asm volatile("v_mfma_f32_16x16x32_bf16 %0, %1, %2, 0" : "=&v"(c) : "v"(a), "v"(b)); }
DI void glds16(const void* sbase, unsigned voff, unsigned lds_dst) {
    asm volatile("s_mov_b32 m0, %2\n\ts_nop 0\n\tglobal_load_lds_dwordx4 %0, %1" :: "v"(voff), "s"(sbase), "s"(lds_dst) : "memory");
}
DI void gload16(bf16x8& dst, const void* sbase, unsigned voff) { asm volatile("global_load_dwordx4 %0, %1, %2" : "=v"(dst) : "v"(voff), "s"(sbase)); }
DI void gload16f(f32x4& dst, const void* sbase, unsigned voff) { asm volatile("global_load_dwordx4 %0, %1, %2" : "=v"(dst) : "v"(voff), "s"(sbase)); }
DI void gload8(u32x2& dst, const void* sbase, unsigned voff) { asm volatile("global_load_dwordx2 %0, %1, %2" : "=v"(dst) : "v"(voff), "s"(sbase)); }
DI void gload4(float& dst, const void* sbase, unsigned voff) { asm volatile("global_load_dword %0, %1, %2" : "=v"(dst) : "v"(voff), "s"(sbase)); }
typedef __amdgpu_buffer_rsrc_t srd_t;
DI srd_t make_srd(const void* p, unsigned bytes) { return __builtin_amdgcn_make_buffer_rsrc((void*)p, (short)0, (int)bytes, 0x00020000); }
template <int IMM> DI void bload16(bf16x8& dst, srd_t r, unsigned voff, unsigned soff) {
    asm volatile("buffer_load_dwordx4 %0, %1, %2, %3 offen offset:%4" : "=v"(dst) : "v"(voff), "s"(r), "s"(soff), "n"(IMM)); }
template <int IMM> DI void bload16f(f32x4& dst, srd_t r, unsigned voff, unsigned soff) {
    asm volatile("buffer_load_dwordx4 %0, %1, %2, %3 offen offset:%4" : "=v"(dst) : "v"(voff), "s"(r), "s"(soff), "n"(IMM)); }
template <int IMM> DI void bload4(float& dst, srd_t r, unsigned voff, unsigned soff) {
    asm volatile("buffer_load_dword %0, %1, %2, %3 offen offset:%4" : "=v"(dst) : "v"(voff), "s"(r), "s"(soff), "n"(IMM)); }
DI void bdma4(srd_t r, unsigned voff, unsigned soff, unsigned lds_dst) {
    asm volatile("s_mov_b32 m0, %3\n\ts_nop 0\n\tbuffer_load_dwordx4 %0, %1, %2 offen lds\n\tbuffer_load_dwordx4 %0, %1, %2 offen offset:1024 lds\n\t"
                 "buffer_load_dwordx4 %0, %1, %2 offen offset:2048 lds\n\tbuffer_load_dwordx4 %0, %1, %2 offen offset:3072 lds" :: "v"(voff), "s"(r), "s"(soff), "s"(lds_dst) : "memory"); }
DI void bdma1(srd_t r, unsigned voff, unsigned soff, unsigned lds_dst) {
    asm volatile("s_mov_b32 m0, %3\n\ts_nop 0\n\tbuffer_load_dwordx4 %0, %1, %2 offen lds" :: "v"(voff), "s"(r), "s"(soff), "s"(lds_dst) : "memory"); }
template <int IMM> DI void bdma_next(srd_t r, unsigned voff, unsigned soff, unsigned lds_dst) {
    asm volatile("s_mov_b32 m0, %3\n\ts_nop 0\n\tbuffer_load_dwordx4 %0, %1, %2 offen offset:%4 lds" :: "v"(voff), "s"(r), "s"(soff), "s"(lds_dst), "n"(IMM) : "memory"); }
#define ACT_POL ""
DI void bstore16(u32x4 d, srd_t r, unsigned voff, unsigned soff) { asm volatile("buffer_store_dwordx4 %0, %1, %2, %3 offen" ACT_POL "\n\ts_nop 1" :: "v"(d), "v"(voff), "s"(r), "s"(soff) : "memory"); }
DI void bstore8(u32x2 d, srd_t r, unsigned voff, unsigned soff) { asm volatile("buffer_store_dwordx2 %0, %1, %2, %3 offen" ACT_POL "\n\ts_nop 1" :: "v"(d), "v"(voff), "s"(r), "s"(soff) : "memory"); }
#define PINV(x) asm volatile("" : "+v"(x))
#define WAITV(n) asm volatile("s_waitcnt vmcnt(%0)" :: "n"(n) : "memory")
#define WAIT_BAR(n) asm volatile("s_waitcnt vmcnt(%0) lgkmcnt(0)\n\ts_barrier" :: "n"(n) : "memory")
#define SBAR() __builtin_amdgcn_sched_barrier(0)
#define LBAR() asm volatile("s_waitcnt lgkmcnt(0)\n\ts_barrier" ::: "memory")
DI bf16x8 lds_frag(LAS const char* p) { return *(LAS const bf16x8*)p; }
typedef short v4i16_t __attribute__((ext_vector_type(4)));
DI u32x2 lds_tr(LAS const char* p) { return __builtin_bit_cast(u32x2, __builtin_amdgcn_ds_read_tr16_b64_v4i16((LAS v4i16_t*)p)); }
DI bf16x8 tr_frag(LAS const char* p, int off2) { u32x2 a = lds_tr(p), b = lds_tr(p + off2); u32x4 r = {a[0], a[1], b[0], b[1]}; return __builtin_bit_cast(bf16x8, r); }

DI int kstep_to_ftile(int ks, int half) {
    const int a = ks / 9, idx = ks - 9 * a;
    if (idx == 8) return 9 * (2 * a + half) + 8;
    return 9 * (2 * a + (idx >> 2)) + 2 * (idx & 3) + half;
}

struct RetileJob { const char* src; unsigned voff; unsigned dst[4]; bool live; };
DI void retile_setup(RetileJob& J, int b, int lane, const void* wo, const void* wg, const void* wu, const void* wd) {
    J.live = b < 4050;
    const int r8 = lane >> 3, ch = lane & 7;
    if (b < 2592) {
        const int gu = b >= 1296, bb = b - 1296 * gu, s = bb / 72, cb = bb - 72 * s;
        J.src = (const char*)(gu ? wu : wg) + (size_t)s * 32 * (F_ * 2) + cb * 128;
        J.voff = r8 * (F_ * 2) + ch * 16;
#pragma unroll
        for (int u = 0; u < 4; ++u) { const int T = 4 * cb + u, j = T / 9, tt = T - 9 * j;
            J.dst[u] = (unsigned)(WS_W1 + (size_t)(j * 324 + (tt < 8 ? (tt >> 1) * 72 + s * 4 + (tt & 1) * 2 : 288 + s * 2) + gu) * 1024); }
    } else if (b < 3888) {
        const int bb = b - 2592, ks = bb / 9, cb = bb - 9 * ks;
        J.src = (const char*)wd + cb * 128;
        J.voff = (unsigned)ks;
        const int ksl = ks / 36, kap = ks - 36 * ksl, kj = kap / 18, r = kap - 18 * kj, st = r >> 1, s2 = r & 1;
#pragma unroll
        for (int u = 0; u < 4; ++u) { const int T = 4 * cb + u, n4 = T / 9, tt = T - 9 * n4;
            J.dst[u] = (unsigned)(WS_W2 + (size_t)((ksl * 4 + n4) * 324 + ((st * 2 + kj) * 2 + s2) * 9 + tt) * 1024); }
    } else {
        const int bb = (b < 4050 ? b : 4049) - 3888, s = bb / 9, cb = bb - 9 * s;
        J.src = (const char*)wo + (size_t)s * 32 * (D_ * 2) + cb * 128;
        J.voff = ch * 16;
#pragma unroll
        for (int u = 0; u < 4; ++u) { const int T = 4 * cb + u, n6 = T / 6, tt = T - 6 * n6;
            J.dst[u] = (unsigned)(WS_WO + (size_t)(n6 * 108 + s * 6 + tt) * 1024); }
    }
}
DI void retile_issue(const RetileJob& J, int b, int lane, unsigned lds_scratch) {
    const int r8 = lane >> 3, ch = lane & 7;
#pragma unroll
    for (int p = 0; p < 4; ++p) {
        unsigned voff;
        if (b < 2592) voff = J.voff + p * 8 * (F_ * 2);
        else if (b < 3888) { const int rho = 8 * p + r8, T = kstep_to_ftile((int)J.voff, rho >> 4); voff = (16 * T + (rho & 15)) * (D_ * 2) + ch * 16; }
        else { const int rho = 8 * p + r8, k = 8 * ((rho >> 2) & 3) + 4 * (rho >> 4) + (rho & 3); voff = J.voff + k * (D_ * 2); }
        glds16(J.src, voff, lds_scratch + p * 1024);
    }
}
DI void retile_finish(const RetileJob& J, int lane, LAS const char* scratch, char* ws) {
    const int g = lane >> 4, q = (lane >> 2) & 3, p = lane & 3;
    LAS const char* base = scratch + (4 * g + q) * 128 + p * 8;
#pragma unroll
    for (int u = 0; u < 4; ++u) { const bf16x8 f = tr_frag(base + 32 * u, 16 * 128); if (J.live) *(bf16x8*)(ws + J.dst[u] + lane * 16) = f; }
}

constexpr int K1_SLOT = 34 * 1024, K1_STG = 0, K1_SMALL = 4 * K1_SLOT, K1_LDS = K1_SMALL + 4096;
template <int PART, int NJ>
DI void k1_wave(const char* x, const char* wqkv, char* ws, char* lds_generic, int mt, int nt, int rot, int shx, int shw) {
    constexpr bool VPART = PART == 2; constexpr int NT = NJ ? 4 : 5, T0 = NJ ? 5 : 0;
    LAS char* lds = (LAS char*)lds_generic; const unsigned ldsb = (unsigned)(uintptr_t)lds_generic;
    const int tid = threadIdx.x, lane = tid & 63, wid = rfl(tid >> 6), mi = wid & 3, m = lane & 15, g = lane >> 4;
    STAMP_DECL; RSTAMP(1, 14); STAMP(1, 0);
    const srd_t sx = make_srd(x, S_ * D_ * 2), sw = make_srd(wqkv, D_ * 3 * D_ * 2);
    const unsigned xv = (lane >> 3) * (D_ * 2) + (((lane & 7) ^ ((4 * wid + (lane >> 4)) & 7)) << 4);
    unsigned wv[2];
#pragma unroll
    for (int i = 0; i < 2; ++i) { const int idx = (i ? 8 : wid) * 64 + lane, rho = (idx * 3641) >> 16, ch = idx - 18 * rho;
        wv[i] = (8 * ((rho >> 2) & 3) + 4 * (rho >> 4) + (rho & 3)) * 3456 + ch * 16; }
    const unsigned xs = (128 * mt + 8 * wid) * (D_ * 2), wsb = nt * 288;
    auto issue = [&](int st, int p) {
        int k = st + rot; k = k >= 9 ? k - 9 : k; const unsigned dst = ldsb + (st & 3) * K1_SLOT;
        if (p < 2) bdma1(sx, xv, xs + p * 64 * (D_ * 2) + k * 128, dst + (wid + 8 * p) * 1024);
        else if (p < 4) bdma1(sw, wv[0], wsb + (64 * k + 32 * (p - 2)) * 3456, dst + 16384 + ((p - 2) * 9 + wid) * 1024);
        else if (wid < 2) bdma1(sw, wv[1], wsb + (64 * k + 32 * wid) * 3456, dst + 16384 + (wid * 9 + 8) * 1024); };
#pragma unroll
    for (int st = 0; st < 3; ++st)
#pragma unroll
        for (int p = 0; p < 5; ++p) issue(st, p);
    float tch[2] = {0.f, 0.f};
#pragma unroll
    for (int i = 0; i < 2 * K1_TOUCH; ++i) { int t = wid + 8 * i; t = t >= 12 ? t - 4 : t;
        if (t < 3) { const int L = shx * 192 + t * 64 + lane, row = (L * 7282) >> 16; bload4<0>(tch[i], sx, (128 * mt + row) * (D_ * 2) + (L - 9 * row) * 128, 0); }
        else { const int q = (t - 3) * 64 + lane; bload4<0>(tch[i], sw, (shw * 144 + (q >> 2)) * 3456 + ((nt * 288) & ~127) + (q & 3) * 128, 0); } }
    f32x4 acc[NT][2];
    f32x4 accs = {0, 0, 0, 0}, accg = {0, 0, 0, 0}, accc[NT];
#pragma unroll
    for (int t = 0; t < NT; ++t) accc[t] = (f32x4){0, 0, 0, 0};
    bf16x8 ones = __builtin_bit_cast(bf16x8, (u32x4){0x3f803f80u, 0x3f803f80u, 0x3f803f80u, 0x3f803f80u}); PINV(ones);
    asm volatile("s_nop 1" : "+v"(accs), "+v"(accg));
#pragma unroll
    for (int t = 0; t < NT; ++t) PINV(accc[t]);
    const unsigned xa0 = (32 * mi + m) * 128 + ((g ^ ((m >> 1) & 7)) << 4), xa1 = (32 * mi + m) * 128 + (((4 + g) ^ ((m >> 1) & 7)) << 4);
    const unsigned wa = 16384 + (4 * g + ((lane >> 2) & 3)) * 288 + (lane & 3) * 8 + T0 * 32;
    bf16x8 xq[2][2][2], wr[2][2][NT];
    auto rd = [&](int st, int i) {
        LAS const char* sb = lds + (st & 3) * K1_SLOT;
        if (i < 4) xq[st & 1][i >> 1][i & 1] = lds_frag(sb + ((i >> 1) ? xa1 : xa0) + (i & 1) * 2048);
        else { const int s2 = (i - 4) / NT, t = (i - 4) % NT; wr[st & 1][s2][t] = tr_frag(sb + wa + s2 * 9216 + 32 * t, 16 * 288); } };
    STAMP(1, 1);
    WAIT_BAR(8 + K1_TOUCH * 2);
    STAMP(1, 2);
#pragma unroll
    for (int i = 0; i < 4 + 2 * NT; ++i) rd(0, i);
#pragma unroll
    for (int st = 0; st < 9; ++st) {
        if (st < 8) { if (st < 2) WAIT_BAR(4 + K1_TOUCH * 2); else if (st < 6) WAIT_BAR(4); else if (st == 6) WAIT_BAR(4); else WAIT_BAR(0); }
        if (st == 2) { PINV(tch[0]); PINV(tch[1]); }
#pragma unroll
        for (int i = 0; i < 2 * NT; ++i) { const int s2 = i / NT, t = i % NT; const bf16x8 w = wr[st & 1][s2][t], x0 = xq[st & 1][s2][0], x1 = xq[st & 1][s2][1];
            if (st == 0 && s2 == 0) { if (VPART) { mfma16_new(acc[t][0], x0, w); mfma16_new(acc[t][1], x1, w); } else { mfma16_new(acc[t][0], w, x0); mfma16_new(acc[t][1], w, x1); } }
            else { if (VPART) { mfma16_acc(acc[t][0], x0, w); mfma16_acc(acc[t][1], x1, w); } else { mfma16_acc(acc[t][0], w, x0); mfma16_acc(acc[t][1], w, x1); } }
            if (st + 1 < 9) { if (i < 4) { rd(st + 1, i); rd(st + 1, 4 + i); } else if (4 + i < 4 + 2 * NT) rd(st + 1, 4 + i); }
            if (st + 3 < 9 && (i & 1) == 0 && i / 2 < 4) issue(st + 3, i / 2 == 0 ? 0 : i / 2 == 1 ? 2 : i / 2 == 2 ? 1 : 3);
            if (st + 3 < 9 && i == 7) issue(st + 3, 4);
            if (t == 1) mfma16_acc(accs, ones, NJ ? x1 : x0);
            if (t == 2) mfma16_acc(accg, NJ ? x1 : x0, NJ ? x1 : x0);
            if (t == NT - 1 && ((2 * st + s2) & 3) == mi) {
#pragma unroll
                for (int tt = 0; tt < NT; ++tt) { if (VPART) mfma16_acc(accc[tt], ones, wr[st & 1][s2][tt]); else mfma16_acc(accc[tt], wr[st & 1][s2][tt], ones); } }
            SBAR(); }
    }
    STAMP(1, 3);
    LAS float* sm = (LAS float*)(lds + K1_SMALL);
    asm volatile("s_nop 7\n\ts_nop 7" : "+v"(acc[NT - 1][0]), "+v"(acc[NT - 1][1]), "+v"(accs), "+v"(accg));
#pragma unroll
    for (int t = 0; t < NT; ++t) { PINV(accc[t]); PINV(acc[t][0]); PINV(acc[t][1]); }
    if (g == 0) sm[32 * mi + 16 * NJ + m] = accs[0];
    if (g == (m >> 2)) sm[128 + 32 * mi + 16 * NJ + m] = (m & 2) ? ((m & 1) ? accg[3] : accg[2]) : ((m & 1) ? accg[1] : accg[0]);
#pragma unroll
    for (int t = 0; t < NT; ++t) { if (VPART) { if (g == 0) sm[256 + mi * 144 + 16 * (T0 + t) + m] = accc[t][0]; }
        else if (m == 0) *(LAS f32x4*)(sm + 256 + mi * 144 + 16 * (T0 + t) + 4 * g) = accc[t]; }
    LBAR();
    STAMP(1, 4);
    LAS char* stg = lds + K1_STG;
    if (VPART && tid < 256) { const int hs = tid >> 1;
        *(LAS u32x4*)(stg + hs * 320 + ((tid & 1) ? 19 : 4) * 16) = (tid & 1) ? (u32x4){0x3f803f80u, 0x3f803f80u, 0x3f803f80u, 0x3f803f80u} : (u32x4){0, 0, 0, 0}; }
    f32x4 mean[2], rstd[2];
#pragma unroll
    for (int u = 0; u < 2; ++u) { const int tl = 32 * mi + 16 * u + (VPART ? 4 * g : m);
        if (VPART) { mean[u] = *(LAS const f32x4*)(sm + tl) * (1.f / D_);
            const f32x4 var = *(LAS const f32x4*)(sm + 128 + tl) * (1.f / D_) - mean[u] * mean[u];
#pragma unroll
            for (int r = 0; r < 4; ++r) rstd[u][r] = __builtin_amdgcn_rsqf(fmaxf(var[r], 0.f) + EPS); }
        else { mean[u][0] = sm[tl] * (1.f / D_);
            rstd[u][0] = __builtin_amdgcn_rsqf(fmaxf(sm[128 + tl] * (1.f / D_) - mean[u][0] * mean[u][0], 0.f) + EPS) * (PART == 0 ? QSCALE : 1.f); } }
#pragma unroll
    for (int t = 0; t < NT; ++t) { const int tt = T0 + t;
        if (VPART) { const int n = 16 * tt + m, hl = (n * 3641) >> 16, d = n - 18 * hl, row = d < 4 ? d : d < 12 ? d + 5 : d < 16 ? d - 7 : d + 1;
            const float c1 = (sm[256 + n] + sm[400 + n]) + (sm[544 + n] + sm[688 + n]);
            LAS char* p = stg + hl * 5120 + 2 * mi * 640 + (g >> 1) * 320 + row * 16 + (g & 1) * 8;
#pragma unroll
            for (int u = 0; u < 2; ++u) { const f32x4 a = (acc[t][u] - mean[u] * c1) * rstd[u];
                *(LAS u32x2*)(p + u * 640) = (u32x2){cvtpk(a[0], a[1]), cvtpk(a[2], a[3])}; }
        } else {
            const int n0 = 16 * tt + 4 * g;
            const f32x4 c4 = (*(LAS const f32x4*)(sm + 256 + n0) + *(LAS const f32x4*)(sm + 400 + n0)) + (*(LAS const f32x4*)(sm + 544 + n0) + *(LAS const f32x4*)(sm + 688 + n0));
            int o[2], ts[2];
#pragma unroll
            for (int p = 0; p < 2; ++p) { const int n = n0 + 2 * p, hl = (n * 3641) >> 16, d = n - 18 * hl; const bool tail = d == 16;
                if (PART == 0) { o[p] = tail ? 32768 + hl * 512 : hl * 4096 + d * 2; ts[p] = tail ? 4 : 32; }
                else { o[p] = hl * 4608 + (tail ? 4096 : (d >> 3) * 2048 + (d & 7) * 2); ts[p] = tail ? 4 : 16; } }
#pragma unroll
            for (int u = 0; u < 2; ++u) { const f32x4 a = (acc[t][u] - c4 * mean[u][0]) * rstd[u][0]; const int tl = 32 * mi + 16 * u + m;
                *(LAS unsigned*)(stg + o[0] + tl * ts[0]) = cvtpk(a[0], a[1]); *(LAS unsigned*)(stg + o[1] + tl * ts[1]) = cvtpk(a[2], a[3]); }
        } }
    if (nt == 0 && tid < 128) ((float*)(ws + WS_MUX))[128 * mt + tid] = sm[tid] * (1.f / D_);
    LBAR();
    STAMP(1, 5);
    const int head = 8 * (nt & 3) + wid;
    if (PART == 2) { char* dst = ws + WS_KV + (size_t)head * KVIMG + (mt >> 1) * KVCHUNK + 9216 + (mt & 1) * 5120 + lane * 16;
#pragma unroll
        for (int i = 0; i < 5; ++i) *(u32x4*)(dst + i * 1024) = *(LAS const u32x4*)(stg + wid * 5120 + i * 1024 + lane * 16);
    } else if (PART == 0) { char* dst = ws + WS_QM + ((size_t)head * S_ + 128 * mt) * 32 + lane * 16;
#pragma unroll
        for (int i = 0; i < 4; ++i) *(u32x4*)(dst + i * 1024) = *(LAS const u32x4*)(stg + wid * 4096 + i * 1024 + lane * 16);
        if (lane < 32) *(u32x4*)(ws + WS_QT + ((size_t)head * S_ + 128 * mt) * 4 + lane * 16) = *(LAS const u32x4*)(stg + 32768 + wid * 512 + lane * 16);
    } else { char* dst = ws + WS_KV + (size_t)head * KVIMG + (mt >> 1) * KVCHUNK; LAS const char* hb = stg + wid * 4608;
#pragma unroll
        for (int i = 0; i < 4; ++i) *(u32x4*)(dst + (i >> 1) * 4096 + (mt & 1) * 2048 + (i & 1) * 1024 + lane * 16) = *(LAS const u32x4*)(hb + i * 1024 + lane * 16);
        if (lane < 32) *(u32x4*)(dst + 8192 + (mt & 1) * 512 + lane * 16) = *(LAS const u32x4*)(hb + 4096 + lane * 16);
        float mx = 0.f;
#pragma unroll
        for (int i = 0; i < 2; ++i) { const int tl = lane + 64 * i;
            const u32x4 a = *(LAS const u32x4*)(hb + tl * 16), bq = *(LAS const u32x4*)(hb + 2048 + tl * 16); const unsigned c = *(LAS const unsigned*)(hb + 4096 + tl * 4);
            float s2 = dot2(c, c, 0.f);
#pragma unroll
            for (int e = 0; e < 4; ++e) { s2 = dot2(a[e], a[e], s2); s2 = dot2(bq[e], bq[e], s2); }
            mx = fmaxf(mx, s2); }
        mx = fmaxf(mx, xor16(mx)); mx = fmaxf(mx, xor32(mx));
#pragma unroll
        for (int o = 1; o < 16; o <<= 1) mx = fmaxf(mx, __shfl_xor(mx, o));
        if (lane == 0) ((float*)(ws + WS_KMAX))[head * 16 + mt] = mx;
    }
    STAMP(1, 6); RSTAMP(1, 15); STAMP_FLUSH(1);
}
template <int PART> DI void k1_body(const char* x, const char* wqkv, char* ws, char* lds, int mt, int nt, int rot, int shx, int shw) {
    if (threadIdx.x >= 256) k1_wave<PART, 1>(x, wqkv, ws, lds, mt, nt, rot, shx, shw); else k1_wave<PART, 0>(x, wqkv, ws, lds, mt, nt, rot, shx, shw);
}
__global__ __launch_bounds__(512, 2) void k1_qkv(const char* x, const char* wqkv, const char* wo, char* ws) {
    extern __shared__ __attribute__((aligned(16))) char lds[];
    const int b = blockIdx.x;
    if (b >= 192) {
        const int lane = threadIdx.x & 63, wv = rfl(threadIdx.x >> 6), gw = (b - 192) * 8 + wv;
        if (gw < 162) { RetileJob J; retile_setup(J, 3888 + gw, lane, wo, nullptr, nullptr, nullptr);
            retile_issue(J, 3888 + gw, lane, (unsigned)(uintptr_t)lds + wv * 4096); WAITV(0); retile_finish(J, lane, (LAS const char*)lds + wv * 4096, ws); }
        for (int c = gw; c < D_; c += 512) {
            const char* row = wo + (size_t)c * (D_ * 2);
            const u32x4 a = *(const u32x4*)(row + lane * 16); float s = 0.f;
#pragma unroll
            for (int e = 0; e < 4; ++e) s = dot2(a[e], 0x3f803f80u, s);
            if (lane < 8) { const u32x4 bq = *(const u32x4*)(row + 1024 + lane * 16);
#pragma unroll
                for (int e = 0; e < 4; ++e) s = dot2(bq[e], 0x3f803f80u, s); }
#pragma unroll
            for (int o = 1; o < 64; o <<= 1) s += __shfl_xor(s, o);
            if (lane == 0) { const float w = s * (1.f / D_); const unsigned hi = cvtpk(w, 0.f) & 0xffffu; const unsigned lo = cvtpk(w - bflo(hi), 0.f) & 0xffffu;
                ((unsigned short*)(ws + WS_WBAR))[c] = (unsigned short)hi; ((unsigned short*)(ws + WS_WBAR))[D_ + c] = (unsigned short)lo; }
        }
        return;
    }
    const int xcd = b & 7, idx = b >> 3, mt = (xcd & 3) * 4 + (idx & 3), nt = (xcd >> 2) * 6 + (idx >> 2);
    const int rot = K1_ROT ? (2 * (idx & 3) + ((idx >> 2) * 3 >> 1)) % 9 : 0;
    const int shx = idx >> 2, shw = idx & 3;
    if (nt >= 8) k1_body<2>(x, wqkv, ws, lds, mt, nt, rot, shx, shw); else if (nt >= 4) k1_body<1>(x, wqkv, ws, lds, mt, nt, rot, shx, shw); else k1_body<0>(x, wqkv, ws, lds, mt, nt, rot, shx, shw);
}

constexpr int K2_LDS = 160 * 1024;
#define EXPS(S, a, b) _Pragma("unroll") for (int r_ = a; r_ < b; ++r_) S[r_] = __builtin_amdgcn_exp2f(S[r_]); SBAR()
#define PLO(S) __builtin_bit_cast(bf16x16, (f32x8)__builtin_shufflevector(S, S, 0, 1, 2, 3, 4, 5, 6, 7))
#define PHI(S) __builtin_bit_cast(bf16x16, (f32x8)__builtin_shufflevector(S, S, 8, 9, 10, 11, 12, 13, 14, 15))
#define KTAIL(p) __builtin_bit_cast(s16x4, (u32x2){*(LAS const unsigned*)(p), kc})
#define QK1(q) __builtin_amdgcn_mfma_f32_32x32x16_bf16(kf, q, (f32x16){}, 0, 0, 0)
#define QK2(q, s) __builtin_amdgcn_mfma_f32_32x32x8bf16_1k(kt, q, s, 0, 0, 0)
#define PV(v, p, o) __builtin_amdgcn_smfmac_f32_32x32x32_bf16(v, p, o, 0xDDDD, 0, 0)
DI void attn_fast(LAS const char* kp, LAS const char* tp, LAS const char* vp, const bf16x8 (&qm)[2], const s16x4 (&qt)[2], f32x16 (&o)[2], unsigned kc) {
    bf16x8 kf = *(LAS const bf16x8*)kp; s16x4 kt = KTAIL(tp); bf16x8 v0, v1;
    f32x16 s0 = QK1(qm[0]), s1; s0 = QK2(qt[0], s0);
    SBAR();
#pragma unroll
    for (int it = 0; it < 8; ++it) {
        EXPS(s0, 0, 2);
        if (it > 0) { o[1] = PV(v0, PLO(s1), o[1]); SBAR(); }
        EXPS(s0, 2, 6);
        if (it > 0) { o[1] = PV(v1, PHI(s1), o[1]); SBAR(); }
        v0 = *(LAS const bf16x8*)(vp + it * 1280); v1 = *(LAS const bf16x8*)(vp + it * 1280 + 640); SBAR();
        EXPS(s0, 6, 9);
        s1 = QK1(qm[1]); SBAR();
        EXPS(s0, 9, 12);
        s1 = QK2(qt[1], s1); SBAR();
        if (it + 1 < 8) { kf = *(LAS const bf16x8*)(kp + (it + 1) * 512); kt = KTAIL(tp + (it + 1) * 128); SBAR(); }
        EXPS(s0, 12, 16);
        EXPS(s1, 0, 2);
        o[0] = PV(v0, PLO(s0), o[0]); SBAR();
        EXPS(s1, 2, 6);
        o[0] = PV(v1, PHI(s0), o[0]); SBAR();
        EXPS(s1, 6, 9);
        if (it + 1 < 8) { s0 = QK1(qm[0]); SBAR(); }
        EXPS(s1, 9, 12);
        if (it + 1 < 8) { s0 = QK2(qt[0], s0); SBAR(); }
        EXPS(s1, 12, 16);
    }
    o[1] = PV(v0, PLO(s1), o[1]); o[1] = PV(v1, PHI(s1), o[1]);
}
DI void attn_slow(LAS const char* kp, LAS const char* tp, LAS const char* vp, const bf16x8 (&qm)[2], const s16x4 (&qt)[2], f32x16 (&o)[2], float (&mrun)[2], unsigned kc) {
#pragma unroll 1
    for (int it = 0; it < 8; ++it) {
        const bf16x8 kf = *(LAS const bf16x8*)(kp + it * 512); const s16x4 kt = KTAIL(tp + it * 128);
        const bf16x8 v0 = *(LAS const bf16x8*)(vp + it * 1280), v1 = *(LAS const bf16x8*)(vp + it * 1280 + 640);
#pragma unroll
        for (int u = 0; u < 2; ++u) {
            f32x16 s = QK1(qm[u]); s = QK2(qt[u], s);
            float mx = s[0];
#pragma unroll
            for (int r = 1; r < 16; ++r) mx = fmaxf(mx, s[r]);
            mx = fmaxf(mx, xor32(mx));
            const float mn = fmaxf(mrun[u], mx), al = __builtin_amdgcn_exp2f(mrun[u] - mn);
            mrun[u] = mn;
#pragma unroll
            for (int r = 0; r < 16; ++r) { s[r] = __builtin_amdgcn_exp2f(s[r] - mn); o[u][r] *= al; }
            o[u] = PV(v0, PLO(s), o[u]); o[u] = PV(v1, PHI(s), o[u]);
        }
    }
}
__global__ __launch_bounds__(1024, 4) void k2_attn(const char* wg, const char* wu, char* ws) {
    extern __shared__ __attribute__((aligned(16))) char lds_generic[];
    LAS char* lds = (LAS char*)lds_generic; const unsigned ldsb = (unsigned)(uintptr_t)lds_generic;
    const int tid = threadIdx.x, lane = tid & 63, wid = rfl(tid >> 6), qg = wid & 3, kq = wid >> 2, r32 = lane & 31, h = lane >> 5;
    const int b = blockIdx.x, xcd = b & 7, idx = b >> 3, head = xcd * 4 + (idx & 3), qb = idx >> 2;
    STAMP_DECL; RSTAMP(2, 14); STAMP(2, 0);
    const srd_t srd = make_srd(ws, (unsigned)WS_END);
    const int c0 = (2 * kq + (qb & 1)) * KVCHUNK, c1 = (2 * kq + ((qb & 1) ^ 1)) * KVCHUNK;
    const unsigned kvb = (unsigned)WS_KV + head * KVIMG, l16 = lane * 16;
    bdma4(srd, l16, kvb + c0 + qg * 5120, ldsb + c0 + qg * 5120); if (qg < 3) bdma1(srd, l16, kvb + c0 + qg * 5120 + 4096, ldsb + c0 + qg * 5120 + 4096);
    const int q0 = 256 * qb + 64 * qg;
    bf16x8 qm[2]; float qtl[2]; float km;
    { const unsigned so = (unsigned)WS_QM + (head * S_ + q0) * 32, vo = r32 * 32 + h * 16; bload16<0>(qm[0], srd, vo, so); bload16<1024>(qm[1], srd, vo, so);
      const unsigned st = (unsigned)WS_QT + (head * S_ + q0) * 4; bload4<0>(qtl[0], srd, r32 * 4, st); bload4<128>(qtl[1], srd, r32 * 4, st);
      bload4<0>(km, srd, (lane & 15) * 4, (unsigned)WS_KMAX + head * 64); }
    asm volatile("s_waitcnt vmcnt(0)" : "+v"(qm[0]), "+v"(qm[1]), "+v"(qtl[0]), "+v"(qtl[1]), "+v"(km) :: "memory");
#pragma unroll
    for (int o = 1; o < 16; o <<= 1) km = fmaxf(km, __shfl_xor(km, o));
    s16x4 qt[2]; bool unsafe = false;
#pragma unroll
    for (int u = 0; u < 2; ++u) { const u32x4 qv = __builtin_bit_cast(u32x4, qm[u]); const unsigned tl = __float_as_uint(qtl[u]);
        float n2 = h ? 0.f : dot2(tl, tl, 0.f);
#pragma unroll
        for (int e = 0; e < 4; ++e) n2 = dot2(qv[e], qv[e], n2);
        n2 += xor32(n2);
        const float bound = __builtin_amdgcn_sqrtf(n2 * km) * 1.0001f + 1e-6f;
        unsafe |= !(bound <= SAFE_B);
        const unsigned cb = (__float_as_uint(bound) + 0xffffu) >> 16;
        const u32x2 w = h ? (u32x2){0u, 0u} : (u32x2){tl, cb | 0x8000u};
        qt[u] = __builtin_bit_cast(s16x4, w); }
    const bool slow = __any(unsafe);
    if (slow) {
#pragma unroll
        for (int u = 0; u < 2; ++u) { u32x2 w = __builtin_bit_cast(u32x2, qt[u]); w[1] = 0u; qt[u] = __builtin_bit_cast(s16x4, w); } }
    f32x16 o[2] = {(f32x16){}, (f32x16){}}; float mrun[2] = {-INFINITY, -INFINITY};
    const int vrow = r32 < 4 ? r32 : r32 < 12 ? r32 + 5 : r32 < 16 ? r32 - 7 : r32 < 19 ? r32 + 1 : 4;
    const unsigned kc = h ? 0u : 0x00003f80u;
    unsigned kp = ldsb + c0 + h * 4096 + r32 * 16, tp = ldsb + c0 + 8192 + r32 * 4, vp = ldsb + c0 + 9216 + h * 320 + vrow * 16;
#define KP(o) ((LAS const char*)(uintptr_t)kp + (o))
#define TP(o) ((LAS const char*)(uintptr_t)tp + (o))
#define VP(o) ((LAS const char*)(uintptr_t)vp + (o))
    STAMP(2, 1);
    asm volatile("s_barrier" ::: "memory");
    STAMP(2, 2);
    bdma4(srd, l16, kvb + c1 + qg * 5120, ldsb + c1 + qg * 5120); if (qg < 3) bdma1(srd, l16, kvb + c1 + qg * 5120 + 4096, ldsb + c1 + qg * 5120 + 4096);
    const int rb = wid * 256 + b; const bool rt = rb < 2592;
    const int gu = rb >= 1296, rs_ = (rb - 1296 * gu) / 72, rcb = (rb - 1296 * gu) - 72 * rs_;
    bf16x8 wb[4];
    if (rt) { const srd_t sw = make_srd(gu ? wu : wg, D_ * F_ * 2); const unsigned vo = (lane >> 3) * (F_ * 2) + (lane & 7) * 16, so = rs_ * 32 * (F_ * 2) + rcb * 128;
#pragma unroll
        for (int p = 0; p < 4; ++p) bload16<0>(wb[p], sw, vo, so + p * 8 * (F_ * 2)); }
    if (slow) attn_slow(KP(0), TP(0), VP(0), qm, qt, o, mrun, kc); else attn_fast(KP(0), TP(0), VP(0), qm, qt, o, kc);
    STAMP(2, 3);
    if (rt) WAIT_BAR(4); else WAIT_BAR(0);
    STAMP(2, 4);
    if (rt) { asm volatile("s_waitcnt vmcnt(0)" : "+v"(wb[0]), "+v"(wb[1]), "+v"(wb[2]), "+v"(wb[3]) :: "memory");
        int ty = threadIdx.x; asm volatile("" : "+v"(ty)); const int ln = ty & 63;
        LAS char* scr = lds + c0 + qg * 4096;
#pragma unroll
        for (int p = 0; p < 4; ++p) *(LAS bf16x8*)(scr + p * 1024 + ln * 16) = wb[p];
        LAS const char* base = scr + (4 * (ln >> 4) + ((ln >> 2) & 3)) * 128 + (ln & 3) * 8;
#pragma unroll
        for (int u = 0; u < 4; ++u) { const int T = 4 * rcb + u, j = T / 9, tt = T - 9 * j;
            bstore16(__builtin_bit_cast(u32x4, tr_frag(base + 32 * u, 16 * 128)), srd, ln * 16,
                     (unsigned)WS_W1 + (j * 324 + (tt < 8 ? (tt >> 1) * 72 + rs_ * 4 + (tt & 1) * 2 : 288 + rs_ * 2) + gu) * 1024); } }
    STAMP(2, 5);
    { const int dc = c1 - c0; kp += dc; tp += dc; vp += dc; PINV(kp); PINV(tp); PINV(vp); }
    if (slow) attn_slow(KP(0), TP(0), VP(0), qm, qt, o, mrun, kc); else attn_fast(KP(0), TP(0), VP(0), qm, qt, o, kc);
    STAMP(2, 6);
    int tz = threadIdx.x; asm volatile("" : "+v"(tz));
    const int lane2 = tz & 63, r2 = tz & 31, h2 = (tz >> 5) & 1;
    LAS float* mx = (LAS float*)(lds + KVIMG);
#pragma unroll
    for (int u = 0; u < 2; ++u) { LAS char* pb = lds + c0 + (qg * 2 + u) * 2432;
        *(LAS f32x4*)(pb + lane2 * 16) = (f32x4){o[u][0], o[u][1], o[u][2], o[u][3]};
        *(LAS f32x4*)(pb + 1024 + lane2 * 16) = (f32x4){o[u][4], o[u][5], o[u][6], o[u][7]};
        if (h2 == 0) { *(LAS f32x2*)(pb + 2048 + r2 * 8) = (f32x2){o[u][8], o[u][9]}; *(LAS float*)(pb + 2304 + r2 * 4) = o[u][10];
                      mx[wid * 64 + 32 * u + r2] = slow ? mrun[u] : 0.f; } }
    LBAR();
    { const int u = kq & 1, hf = kq >> 1;
      LAS const char* pb = lds + (qb & 1) * KVCHUNK + (qg * 2 + u) * 2432;
      float f[4] = {1.f, 1.f, 1.f, 1.f};
      if (slow) { float mk[4], M = -INFINITY;
#pragma unroll
          for (int k = 0; k < 4; ++k) { mk[k] = mx[(4 * k + qg) * 64 + 32 * u + r2]; M = fmaxf(M, mk[k]); }
#pragma unroll
          for (int k = 0; k < 4; ++k) f[k] = __builtin_amdgcn_exp2f(mk[k] - M); }
      float l = 0.f; f32x4 a = {0, 0, 0, 0}; f32x2 c = {0, 0};
#pragma unroll
      for (int k = 0; k < 4; ++k) { LAS const char* p = pb + k * 2 * KVCHUNK;
          l += f[k] * *(LAS const float*)(p + 2304 + r2 * 4); a += *(LAS const f32x4*)(p + hf * 1024 + lane2 * 16) * f[k]; c += *(LAS const f32x2*)(p + 2048 + r2 * 8) * f[k]; }
      const float rl = __builtin_amdgcn_rcpf(l);
      const int token = q0 + 32 * u + r2; char* ob = ws + WS_OF + (size_t)(token >> 4) * 18 * 1024 + (token & 15) * 16;
      auto put = [&](int d, float x0, float x1) { const int cc = 18 * head + d; *(unsigned*)(ob + (cc >> 5) * 1024 + ((cc & 31) >> 3) * 256 + (cc & 7) * 2) = cvtpk(x0 * rl, x1 * rl); };
      put(8 * hf + 4 * h2, a[0], a[1]); put(8 * hf + 4 * h2 + 2, a[2], a[3]);
      if (hf == 0 && h2 == 0) put(16, c[0], c[1]);
    }
    STAMP(2, 7); RSTAMP(2, 15); STAMP_FLUSH(2);
}

constexpr int K3_LDS = 112 * 1024;
__global__ __launch_bounds__(512, 2) void k3_oproj(const char* x, const char* wd, char* ws) {
    extern __shared__ __attribute__((aligned(16))) char lds_generic[];
    LAS char* lds = (LAS char*)lds_generic; const unsigned ldsb = (unsigned)(uintptr_t)lds_generic;
    const int tid = threadIdx.x, lane = tid & 63, wid = rfl(tid >> 6), mi = wid & 3, kj = wid >> 2, m = lane & 15, g = lane >> 4;
    if (blockIdx.x >= 192) {
        RetileJob J[3]; const int gw = (blockIdx.x - 192) * 8 + wid;
#pragma unroll
        for (int i = 0; i < 3; ++i) { const int rb = gw + 512 * i; if (rb < 1296) { retile_setup(J[i], 2592 + rb, lane, nullptr, nullptr, nullptr, wd); retile_issue(J[i], 2592 + rb, lane, ldsb + (wid * 3 + i) * 4096); } }
        WAITV(0);
#pragma unroll
        for (int i = 0; i < 3; ++i) if (gw + 512 * i < 1296) retile_finish(J[i], lane, lds + (wid * 3 + i) * 4096, ws);
        return;
    }
    const int b = blockIdx.x, xcd = b & 7, idx = b >> 3, mt32 = xcd * 4 + (idx & 3), n6 = idx >> 2, mt16 = 4 * mt32 + mi;
    STAMP_DECL; RSTAMP(3, 14); STAMP(3, 0);
    const char* wof = ws + WS_WO + (size_t)n6 * 108 * 1024;
    bf16x8 of[9], wf[9];
    const char* ob = ws + WS_OF + ((size_t)mt16 * 18 + 9 * kj) * 1024;
    const char* wbp = ws + WS_WBAR + kj * 576; const unsigned wboff = (m == 1 ? D_ * 2 : 0) + g * 16;
#pragma unroll
    for (int s = 0; s < 4; ++s) { gload16(of[s], ob + s * 1024, lane * 16); gload16(wf[s], wbp + s * 64, wboff); }
#pragma unroll
    for (int i = 0; i < 6; ++i) { const int pc = wid + 8 * i, fr = pc < 24 ? pc : pc + 30; glds16(wof + fr * 1024, lane * 16, ldsb + fr * 1024); }
#pragma unroll
    for (int s = 4; s < 9; ++s) { gload16(of[s], ob + s * 1024, lane * 16); gload16(wf[s], wbp + s * 64, wboff); }
#pragma unroll
    for (int i = 0; i < 8; ++i) { int pc = wid + 8 * i; pc = pc > 59 ? 59 : pc; const int fr = pc < 30 ? pc + 24 : pc + 48; glds16(wof + fr * 1024, lane * 16, ldsb + fr * 1024); }
    const int token = 16 * mt16 + m;
    u32x2 xr[4]; float mux;
    const char* xp = x + (size_t)(16 * mt16) * (D_ * 2) + (96 * n6 + 64 * kj) * 2; const unsigned xo = m * (D_ * 2) + g * 8;
#pragma unroll
    for (int t = 0; t < 4; ++t) gload8(xr[t], xp + (kj ? (t & 1) : t) * 32, xo);
    gload4(mux, ws + WS_MUX + (size_t)16 * mt16 * 4, m * 4);
    f32x4 acc[6], accw = {0, 0, 0, 0};
#pragma unroll
    for (int t = 0; t < 6; ++t) acc[t] = (f32x4){0, 0, 0, 0};
    LAS const char* ab = lds + kj * (54 * 1024) + lane * 16;
    auto step = [&](int s) { PINV(of[s]); PINV(wf[s]);
#pragma unroll
        for (int t = 0; t < 6; ++t) acc[t] = mfma16(lds_frag(ab + (s * 6 + t) * 1024), of[s], acc[t]);
        u32x4 w = __builtin_bit_cast(u32x4, wf[s]); if (m >= 2) w = (u32x4){0, 0, 0, 0};
        accw = mfma16(__builtin_bit_cast(bf16x8, w), of[s], accw); };
    STAMP(3, 1);
    WAIT_BAR(23);
    STAMP(3, 2);
#pragma unroll
    for (int s = 0; s < 4; ++s) step(s);
    STAMP(3, 3);
    WAIT_BAR(0);
    STAMP(3, 4);
#pragma unroll
    for (int s = 4; s < 9; ++s) step(s);
    STAMP(3, 5);
#pragma unroll
    for (int t = 0; t < 4; ++t) PINV(xr[t]);
    PINV(mux);
    LBAR();
    LAS char* ex = lds + mi * 8192 + lane * 16;
    if (kj == 0) { *(LAS f32x4*)(ex + 4 * 1024) = acc[4]; *(LAS f32x4*)(ex + 5 * 1024) = acc[5]; *(LAS f32x4*)(ex + 6 * 1024) = accw; }
    else {
#pragma unroll
        for (int t = 0; t < 4; ++t) *(LAS f32x4*)(ex + t * 1024) = acc[t];
        *(LAS f32x4*)(ex + 7 * 1024) = accw; }
    LBAR();
    accw += *(LAS const f32x4*)(ex + (kj ? 6 : 7) * 1024);
    const float mu = mux + __shfl(accw[0] + accw[1], m);
    float ssq = 0.f;
    auto fin = [&](int t, u32x2 xv, unsigned (&pk)[2]) {
        const f32x4 a = acc[t] + *(LAS const f32x4*)(ex + t * 1024);
        const unsigned h0 = cvtpk(a[0] + bflo(xv[0]), a[1] + bfhi(xv[0])), h1 = cvtpk(a[2] + bflo(xv[1]), a[3] + bfhi(xv[1]));
        *(u32x2*)(ws + WS_HC + ((size_t)mt16 * 36 + 6 * n6 + t) * 512 + lane * 8) = (u32x2){h0, h1};
        pk[0] = cvtpk(bflo(h0) - mu, bfhi(h0) - mu); pk[1] = cvtpk(bflo(h1) - mu, bfhi(h1) - mu);
        ssq = dot2(pk[0], pk[0], ssq); ssq = dot2(pk[1], pk[1], ssq); };
    auto pair = [&](int t, u32x2 xa, u32x2 xb2) { unsigned p0[2], p1[2]; fin(t, xa, p0); fin(t + 1, xb2, p1);
        *(u32x4*)(ws + WS_HCF + ((size_t)mt16 * 18 + 3 * n6 + (t >> 1)) * 1024 + lane * 16) = (u32x4){p0[0], p0[1], p1[0], p1[1]}; };
    if (kj == 0) { pair(0, xr[0], xr[1]); pair(2, xr[2], xr[3]); } else pair(4, xr[0], xr[1]);
    ssq += __shfl_xor(ssq, 16); ssq += xor32(ssq);
    if (g == 0) ((float*)(ws + WS_SSQ))[token * 16 + 2 * n6 + kj] = ssq;
    STAMP(3, 6); RSTAMP(3, 15); STAMP_FLUSH(3);
}

constexpr int RING_SLOT = 36 * 1024, G_LDS = 4 * RING_SLOT;
DI void ring_issue(const char* stream, int st, int wid, int lane, unsigned ldsb) {
    const char* sb = stream + (size_t)st * RING_SLOT + wid * 1024; const unsigned db = ldsb + (st & 3) * RING_SLOT + wid * 1024;
#pragma unroll
    for (int i = 0; i < 4; ++i) glds16(sb + i * 8192, lane * 16, db + i * 8192);
    if (wid < 4) glds16(sb + 32768, lane * 16, db + 32768);
}
DI float geglu(float g, float u) {
    const float z = g * __builtin_fmaf(g * g, -0.10294324f, -2.30220818f);
    return g * u * __builtin_amdgcn_rcpf(1.f + __builtin_amdgcn_exp2f(z));
}
struct Ev { int type, arg, n; };
enum { E_SSQ, E_A, E_B, E_WAITB, E_BAR, E_READ, E_MMA, E_GP, E_RS, E_STAMP, E_ST, E_NOP };
constexpr int G1_PD = G1PD;
constexpr int g1_grp(int f) { return f < 288 ? f / 72 : 4; }
constexpr int g1_ks(int f) { return f < 288 ? (f % 72) / 4 : (f - 288) / 2; }
constexpr int g1_tile(int f) { return f < 288 ? (f >> 1) & 1 : 0; }
template <bool LATE> struct G1Prog {
    Ev ev[1600]; int n;
    constexpr void add(int t, int a) { ev[n].type = t; ev[n].arg = a; ev[n].n = 0; ++n; }
    static constexpr int vm(const Ev& e) { return e.type == E_SSQ ? 2 : (e.type == E_A || e.type == E_B || e.type == E_ST) ? 1 : 0; }
    static constexpr int NP = LATE ? 4 : 5;
    constexpr void addB(int s, int u) { add(E_B, s * 2 + u); }
    constexpr G1Prog() : ev{}, n(0) {
        for (int p = 0; p < NP; ++p) add(E_A, p);
        addB(0, 0); addB(0, 1); addB(1, 0); addB(1, 1);
        add(E_BAR, 0); add(E_STAMP, 1);
        for (int f = 0; f < G1_PD; ++f) add(E_READ, f);
        for (int f = 0; f < 324; ++f) {
            const int st = f / 36, i = f % 36, g = g1_grp(f), r = f - 72 * g;
            if (g == 0) { const int s = f / 4, q = f % 4;
                if (q == 0) add(E_WAITB, s);
                if (f == 0) { addB(2, 0); addB(2, 1); add(E_SSQ, 0); }
                if (s + 3 < 18 && q == 1) addB(s + 3, 0);
                if (s + 3 < 18 && q == 3) addB(s + 3, 1);
                if (f == 40) add(E_RS, 0); }
            if (st == 0 && i >= 2 && i < 2 + 2 * NP && i % 2 == 0) add(E_A, 8 + (i - 2) / 2);
            if (st + 2 < 9) { const int i0 = st == 0 ? 14 : LATE ? 17 : 1, d = st == 0 ? 4 : 4 - (LATE ? 0 : 0);
                if (i >= i0 && (i - i0) % d == 0 && (i - i0) / d < NP) add(E_A, (st + 2) * 8 + (i - i0) / d); }
            if (i == 36 - G1_PD && st + 1 < 9) add(E_BAR, st + 1);
            if (f + G1_PD < 324) add(E_READ, f + G1_PD);
            add(E_MMA, f);
            if (g > 0) {
                const int step = g < 4 ? 2 : 1, r0 = 3, k = (r - r0) / step;
                if (r >= r0 && (r - r0) % step == 0 && k < 32) { add(E_GP, (g - 1) * 64 + k);
                    if (k == 15 || k == 31) add(E_ST, (g - 1) * 2 + (k == 31)); } }
            if (i == 35) add(E_STAMP, 2 + st);
            if (f == 107) add(E_STAMP, 12);
            if (f == 323) add(E_STAMP, 13);
        }
        add(E_NOP, 0);
        for (int k = 0; k < 16; ++k) { add(E_GP, 4 * 64 + k); if (k == 7 || k == 15) add(E_ST, 8 + (k == 15)); }
        for (int i = 0; i < n; ++i) if (ev[i].type == E_WAITB || ev[i].type == E_BAR || ev[i].type == E_RS) {
            int cnt = 0, j = i - 1;
            for (; j >= 0; --j) {
                if (ev[i].type == E_WAITB && ev[j].type == E_B && ev[j].arg == 2 * ev[i].arg + 1) break;
                if (ev[i].type == E_RS && ev[j].type == E_SSQ) break;
                if (ev[i].type == E_BAR && ev[j].type == E_A && ev[j].arg == 8 * ev[i].arg + NP - 1) break;
                cnt += vm(ev[j]); }
            ev[i].n = cnt > 63 ? 63 : cnt; }
    }
};
template <bool LATE> constexpr G1Prog<LATE> g1prog{};
DI float vmul(float a, float b) { float r; asm("v_mul_f32_e32 %0, %1, %2" : "=v"(r) : "v"(a), "v"(b)); return r; }
DI float vfma(float a, float b, float c) { float r; asm("v_fma_f32 %0, %1, %2, %3" : "=v"(r) : "v"(a), "v"(b), "v"(c)); return r; }
struct G1State {
    bf16x8 hf[2][18]; f32x4 acc[2][2][2][2];
    bf16x8 ar[G1_PD + 1]; f32x4 sq[2]; float a0[2], a1[2], kk[2]; f32x4 z, w; u32x4 o;
    unsigned lb0, lb1, lb2; unsigned l16, ldsb, soffA, soffH, soffS, soffAct, vq; int wid, j, g;
};
template <bool LATE, int PC> DI void g1_exec(G1State& c, const srd_t srd) {
    constexpr Ev e = g1prog<LATE>.ev[PC];
    if constexpr (e.type == E_SSQ) { bload16f<0>(c.sq[0], srd, c.vq, c.soffS); bload16f<1024>(c.sq[1], srd, c.vq, c.soffS); }
    else if constexpr (e.type == E_A) { constexpr int st = e.arg >> 3, p = e.arg & 7;
        const unsigned so = c.soffA + st * RING_SLOT, dst = c.ldsb + (st & 3) * RING_SLOT;
        if constexpr (p < 4) bdma_next<p * 1024>(srd, c.l16, so + c.wid * 4096, dst + c.wid * 4096);
        else bdma1(srd, c.l16, so + 32768 + c.wid * 1024, dst + 32768 + c.wid * 1024); }
    else if constexpr (e.type == E_B) { constexpr int s = e.arg >> 1, u = e.arg & 1;
        bload16<((18 * u + s) & 3) * 1024>(c.hf[u][s], srd, c.l16, c.soffH + ((18 * u + s) >> 2) * 4096); }
    else if constexpr (e.type == E_WAITB) { asm volatile("s_waitcnt vmcnt(%2)" : "+v"(c.hf[0][e.arg]), "+v"(c.hf[1][e.arg]) : "n"(e.n)); }
    else if constexpr (e.type == E_BAR) { asm volatile("s_waitcnt vmcnt(%0)\n\ts_barrier" :: "n"(e.n) : "memory"); }
    else if constexpr (e.type == E_RS) { asm volatile("s_waitcnt vmcnt(%2)" : "+v"(c.sq[0]), "+v"(c.sq[1]) : "n"(e.n));
#pragma unroll
        for (int u = 0; u < 2; ++u) { float t = c.g < 3 ? (c.sq[u][0] + c.sq[u][1]) + (c.sq[u][2] + c.sq[u][3]) : 0.f; t += __shfl_xor(t, 16); t += xor32(t);
            const float v = t * (1.f / D_) + EPS, rs = __builtin_amdgcn_rsqf(v);
            c.a0[u] = -2.30220818f * rs; c.a1[u] = -0.10294324f * rs * rs * rs; c.kk[u] = v; }
        SBAR(); }
    else if constexpr (e.type == E_STAMP) {
    }
    else if constexpr (e.type == E_NOP) {
        asm volatile("s_nop 7" : "+v"(c.acc[0][0][0][0]), "+v"(c.acc[0][0][0][1]), "+v"(c.acc[0][0][1][0]), "+v"(c.acc[0][0][1][1])); }
    else if constexpr (e.type == E_READ) { constexpr int f = e.arg, off = ((f / 36) & 3) * RING_SLOT + (f % 36) * 1024;
        if constexpr ((off >> 16) == 0) c.ar[f % (G1_PD + 1)] = lds_frag((LAS const char*)(uintptr_t)c.lb0 + (off & 65535));
        else if constexpr ((off >> 16) == 1) c.ar[f % (G1_PD + 1)] = lds_frag((LAS const char*)(uintptr_t)c.lb1 + (off & 65535));
        else c.ar[f % (G1_PD + 1)] = lds_frag((LAS const char*)(uintptr_t)c.lb2 + (off & 65535));
        SBAR(); }
    else if constexpr (e.type == E_MMA) { constexpr int f = e.arg, s = g1_ks(f), t = g1_tile(f), q = f & 1, set = g1_grp(f) & 1;
        if constexpr (s == 0) { mfma16_new(c.acc[set][t][q][0], c.ar[f % (G1_PD + 1)], c.hf[0][s]); mfma16_new(c.acc[set][t][q][1], c.ar[f % (G1_PD + 1)], c.hf[1][s]); }
        else { mfma16_acc(c.acc[set][t][q][0], c.ar[f % (G1_PD + 1)], c.hf[0][s]); mfma16_acc(c.acc[set][t][q][1], c.ar[f % (G1_PD + 1)], c.hf[1][s]); }
        SBAR(); }
    else if constexpr (e.type == E_GP) {
        constexpr int grp = e.arg >> 6, k = e.arg & 63, quad = k >> 3, ph = k & 7, set = grp & 1;
        constexpr int u = grp < 4 ? quad >> 1 : quad, t = grp < 4 ? quad & 1 : 0;
        const f32x4 g = c.acc[set][t][0][u];
#pragma unroll
        for (int r = 0; r < 4; ++r) {
            if constexpr (ph == 0) c.z[r] = vmul(g[r], g[r]);
            if constexpr (ph == 1) c.z[r] = vfma(c.z[r], c.a1[u], c.a0[u]);
            if constexpr (ph == 2) c.z[r] = vmul(c.z[r], g[r]);
            if constexpr (ph == 3) c.z[r] = __builtin_amdgcn_exp2f(c.z[r]);
            if constexpr (ph == 4) { c.z[r] = vfma(c.z[r], c.kk[u], c.kk[u]); }
            if constexpr (ph == 5) { c.z[r] = __builtin_amdgcn_rcpf(c.z[r]); }
            if constexpr (ph == 6) c.w[r] = vmul(vmul(g[r], c.acc[set][t][1][u][r]), c.z[r]); }
        if constexpr (ph == 7) { c.o[2 * t] = cvtpk(c.w[0], c.w[1]); c.o[2 * t + 1] = cvtpk(c.w[2], c.w[3]); }
        SBAR(); }
    else if constexpr (e.type == E_ST) { constexpr int grp = e.arg >> 1, u = e.arg & 1;
        const unsigned so = c.soffAct + u * (144 * 1024);
        if constexpr (grp < 4) bstore16(c.o, srd, c.l16, so + (9 * (c.j >> 1) + 4 * (c.j & 1) + grp) * 1024);
        else bstore8((u32x2){c.o[0], c.o[1]}, srd, c.l16 + (c.j & 1) * 8, so + (9 * (c.j >> 1) + 8) * 1024);
        SBAR(); }
}
template <bool LATE, int... I> DI void g1_run(G1State& c, const srd_t srd, std::integer_sequence<int, I...>) { (g1_exec<LATE, I>(c, srd), ...); }
__global__ __launch_bounds__(512, 2) void g1_gateup(char* ws) {
    extern __shared__ __attribute__((aligned(16))) char lds_generic[];
    const int tid = threadIdx.x, lane = tid & 63, wid = rfl(tid >> 6);
    const int b = blockIdx.x, xcd = b & 7, idx = b >> 3, j = (xcd >> 1) * 8 + (idx & 7), mt8 = (xcd & 1) * 4 + (idx >> 3);
    const int mt16 = 16 * mt8 + 2 * wid;
    G1State c;
    RSTAMP(4, 14); STAMP(4, 0);
    const srd_t srd = make_srd(ws, (unsigned)WS_END); c.l16 = lane * 16; c.ldsb = (unsigned)(uintptr_t)lds_generic; c.wid = wid; c.j = j; c.g = lane >> 4;
    c.lb0 = c.ldsb + lane * 16; c.lb1 = c.lb0 + 65536; c.lb2 = c.lb0 + 131072; PINV(c.lb0); PINV(c.lb1); PINV(c.lb2);
    c.soffA = (unsigned)WS_W1 + j * (324 * 1024); c.soffH = (unsigned)WS_HCF + mt16 * (18 * 1024); c.soffS = (unsigned)WS_SSQ + mt16 * 1024;
    c.soffAct = (unsigned)WS_ACT + mt16 * (144 * 1024); c.vq = (lane & 15) * 64 + (lane >> 4) * 16;
    if (wid >= 4) g1_run<true>(c, srd, std::make_integer_sequence<int, g1prog<true>.n>{});
    else g1_run<false>(c, srd, std::make_integer_sequence<int, g1prog<false>.n>{});
    STAMP(4, 11); RSTAMP(4, 15); STAMP_FLUSH(4);
}

constexpr int G2_PD = 4;
template <bool LATE> struct G2Prog {
    Ev ev[700]; int n;
    constexpr void add(int t, int a) { ev[n].type = t; ev[n].arg = a; ev[n].n = 0; ++n; }
    static constexpr int vm(const Ev& e) { return (e.type == E_A || e.type == E_B) ? 1 : 0; }
    static constexpr int NP = LATE ? 4 : 5;
    constexpr G2Prog() : ev{}, n(0) {
        for (int p = 0; p < NP; ++p) add(E_A, p);
        for (int q = 0; q < 4; ++q) add(E_B, q);
        for (int p = 0; p < NP; ++p) add(E_A, 8 + p);
        for (int q = 0; q < 4; ++q) add(E_B, 4 + q);
        add(E_BAR, 0); add(E_STAMP, 1);
        for (int q = 0; q < 4; ++q) add(E_B, 8 + q);
        for (int f = 0; f < G2_PD; ++f) add(E_READ, f);
        for (int f = 0; f < 162; ++f) {
            const int st = f / 18, i = f % 18;
            if (i % 9 == 0) add(E_WAITB, st * 2 + i / 9);
            const int ia = LATE ? 3 : 1, ib = LATE ? 1 : 3;
            if (st + 2 < 9 && i >= ia && (i - ia) % 4 == 0 && (i - ia) / 4 < 4) add(E_A, (st + 2) * 8 + (i - ia) / 4);
            if (st + 2 < 9 && NP == 5 && i == 12) add(E_A, (st + 2) * 8 + 4);
            if (st + 3 < 9 && i >= ib && (i - ib) % 4 == 0 && (i - ib) / 4 < 4) add(E_B, (st + 3) * 4 + (i - ib) / 4);
            if (i == 18 - G2_PD && st + 1 < 9) add(E_BAR, st + 1);
            if (f + G2_PD < 162) add(E_READ, f + G2_PD);
            add(E_MMA, f);
            if (i == 17) add(E_STAMP, 2 + st);
        }
        for (int i = 0; i < n; ++i) if (ev[i].type == E_WAITB || ev[i].type == E_BAR) {
            int cnt = 0, j = i - 1;
            for (; j >= 0; --j) {
                if (ev[i].type == E_WAITB && ev[j].type == E_B && ev[j].arg == 2 * ev[i].arg + 1) break;
                if (ev[i].type == E_BAR && ev[j].type == E_A && (ev[j].arg >> 3) == ev[i].arg) break;
                cnt += vm(ev[j]); }
            ev[i].n = cnt > 63 ? 63 : cnt; }
    }
};
template <bool LATE> constexpr G2Prog<LATE> g2prog{};
struct G2State {
    bf16x8 af[4][2][2]; f32x4 acc[9][2]; bf16x8 ar[G2_PD + 1];
    unsigned lb0, lb1, lb2; unsigned l16, ldsb, soffA, soffB; int wid;
};
template <bool LATE, int PC> DI void g2_exec(G2State& c, const srd_t srd) {
    constexpr Ev e = g2prog<LATE>.ev[PC];
    if constexpr (e.type == E_A) { constexpr int st = e.arg >> 3, p = e.arg & 7;
        const unsigned so = c.soffA + st * RING_SLOT, dst = c.ldsb + (st & 3) * RING_SLOT;
        if constexpr (p < 4) bdma_next<p * 1024>(srd, c.l16, so + c.wid * 4096, dst + c.wid * 4096);
        else bdma1(srd, c.l16, so + 32768 + c.wid * 1024, dst + 32768 + c.wid * 1024); }
    else if constexpr (e.type == E_B) { constexpr int st = e.arg >> 2, s2 = (e.arg >> 1) & 1, u = e.arg & 1;
        bload16<((2 * st + s2) & 3) * 1024>(c.af[st & 3][s2][u], srd, c.l16, c.soffB + u * (144 * 1024) + ((2 * st + s2) >> 2) * 4096); }
    else if constexpr (e.type == E_WAITB) { asm volatile("s_waitcnt vmcnt(%2)" : "+v"(c.af[(e.arg >> 1) & 3][e.arg & 1][0]), "+v"(c.af[(e.arg >> 1) & 3][e.arg & 1][1]) : "n"(e.n)); }
    else if constexpr (e.type == E_BAR) { asm volatile("s_waitcnt vmcnt(%0)\n\ts_barrier" :: "n"(e.n) : "memory"); }
    else if constexpr (e.type == E_STAMP) {
    }
    else if constexpr (e.type == E_READ) { constexpr int f = e.arg, st = f / 18, off = (st & 3) * RING_SLOT + (f % 18) * 1024;
        if constexpr ((off >> 16) == 0) c.ar[f % (G2_PD + 1)] = lds_frag((LAS const char*)(uintptr_t)c.lb0 + (off & 65535));
        else c.ar[f % (G2_PD + 1)] = lds_frag((LAS const char*)(uintptr_t)c.lb1 + (off & 65535));
        SBAR(); }
    else if constexpr (e.type == E_MMA) { constexpr int f = e.arg, st = f / 18, s2 = (f % 18) / 9, t = f % 9;
        if constexpr (f < 9) { mfma16_new(c.acc[t][0], c.ar[f % (G2_PD + 1)], c.af[st & 3][s2][0]); mfma16_new(c.acc[t][1], c.ar[f % (G2_PD + 1)], c.af[st & 3][s2][1]); }
        else { mfma16_acc(c.acc[t][0], c.ar[f % (G2_PD + 1)], c.af[st & 3][s2][0]); mfma16_acc(c.acc[t][1], c.ar[f % (G2_PD + 1)], c.af[st & 3][s2][1]); }
        SBAR(); }
}
template <bool LATE, int... I> DI void g2_run(G2State& c, const srd_t srd, std::integer_sequence<int, I...>) { (g2_exec<LATE, I>(c, srd), ...); }
__global__ __launch_bounds__(512, 2) void g2_down(char* ws) {
    extern __shared__ __attribute__((aligned(16))) char lds_generic[];
    LAS char* lds = (LAS char*)lds_generic;
    const int tid = threadIdx.x, lane = tid & 63, wid = rfl(tid >> 6), mi = wid & 3, kj = wid >> 2;
    const int b = blockIdx.x, xcd = b & 7, idx = b >> 3, ksl = xcd >> 1, mt = (xcd & 1) * 8 + (idx & 7), n4 = idx >> 3;
    const int mt16 = 8 * mt + 2 * mi;
    G2State c;
    RSTAMP(5, 14); STAMP(5, 0);
    const srd_t srd = make_srd(ws, (unsigned)WS_END); c.l16 = lane * 16; c.ldsb = (unsigned)(uintptr_t)lds_generic; c.wid = wid;
    c.lb0 = c.ldsb + kj * (18 * 1024) + lane * 16; c.lb1 = c.lb0 + 65536; PINV(c.lb0); PINV(c.lb1);
    c.soffA = (unsigned)WS_W2 + (ksl * 4 + n4) * (324 * 1024); c.soffB = (unsigned)WS_ACT + (mt16 * 144 + 36 * ksl + 18 * kj) * 1024;
    if (wid >= 4) g2_run<true>(c, srd, std::make_integer_sequence<int, g2prog<true>.n>{});
    else g2_run<false>(c, srd, std::make_integer_sequence<int, g2prog<false>.n>{});
    asm volatile("s_nop 7" : "+v"(c.acc[8][0]), "+v"(c.acc[8][1]), "+v"(c.acc[7][0]), "+v"(c.acc[7][1]));
    STAMP(5, 11);
    LBAR();
    LAS char* ex = lds + mi * 18432 + lane * 16;
    if (kj == 0) {
#pragma unroll
        for (int t = 5; t < 9; ++t) { *(LAS f32x4*)(ex + (2 * t) * 1024) = c.acc[t][0]; *(LAS f32x4*)(ex + (2 * t + 1) * 1024) = c.acc[t][1]; }
    } else {
#pragma unroll
        for (int t = 0; t < 5; ++t) { *(LAS f32x4*)(ex + (2 * t) * 1024) = c.acc[t][0]; *(LAS f32x4*)(ex + (2 * t + 1) * 1024) = c.acc[t][1]; }
    }
    LBAR();
    STAMP(5, 12);
    char* pb = ws + WS_PART + (((size_t)ksl * 128 + mt16) * 36 + 9 * n4) * 512 + lane * 8;
    auto fin = [&](int t) {
#pragma unroll
        for (int u = 0; u < 2; ++u) { const f32x4 a = c.acc[t][u] + *(LAS const f32x4*)(ex + (2 * t + u) * 1024);
            *(u32x2*)(pb + ((size_t)u * 36 + t) * 512) = (u32x2){cvtpk(a[0], a[1]), cvtpk(a[2], a[3])}; } };
    if (kj == 0) { fin(0); fin(1); fin(2); fin(3); fin(4); } else { fin(5); fin(6); fin(7); fin(8); }
    STAMP(5, 13); RSTAMP(5, 15); STAMP_FLUSH(5);
}

__global__ __launch_bounds__(256) void red_kernel(const char* ws, char* out) {
    __shared__ __attribute__((aligned(16))) char tr[4][16 * 144];
    const int lane = threadIdx.x & 63, wv = threadIdx.x >> 6, w = blockIdx.x * 4 + wv;
    const int mt16 = w / 9, tq = w - 9 * mt16, m = lane & 15, g = lane >> 4;
    u32x2 pv[4][4], hv[4];
#pragma unroll
    for (int i = 0; i < 4; ++i) { const size_t o = (size_t)mt16 * 36 + 4 * tq + i;
#pragma unroll
        for (int k = 0; k < 4; ++k) pv[i][k] = *(const u32x2*)(ws + WS_PART + ((size_t)k * 128 * 36 + o) * 512 + lane * 8);
        hv[i] = *(const u32x2*)(ws + WS_HC + o * 512 + lane * 8); }
#pragma unroll
    for (int i = 0; i < 4; ++i) { f32x4 a = {bflo(hv[i][0]), bfhi(hv[i][0]), bflo(hv[i][1]), bfhi(hv[i][1])};
#pragma unroll
        for (int k = 0; k < 4; ++k) a += (f32x4){bflo(pv[i][k][0]), bfhi(pv[i][k][0]), bflo(pv[i][k][1]), bfhi(pv[i][k][1])};
        *(u32x2*)(&tr[wv][m * 144 + i * 32 + g * 8]) = (u32x2){cvtpk(a[0], a[1]), cvtpk(a[2], a[3])}; }
#pragma unroll
    for (int h = 0; h < 2; ++h) { const int row = 8 * h + (lane >> 3), cq = lane & 7;
        *(u32x4*)(out + (size_t)(16 * mt16 + row) * (D_ * 2) + tq * 128 + cq * 16) = *(const u32x4*)(&tr[wv][row * 144 + cq * 16]); }
}

}

extern "C" void kernel_launch(void* const* d_in, const int* in_sizes, int n_in, void* d_out, int out_size, void* d_ws, size_t ws_size, hipStream_t stream) {
    const char* x = (const char*)d_in[0]; const char* wqkv = (const char*)d_in[1]; const char* wo = (const char*)d_in[2];
    const char* wg = (const char*)d_in[4]; const char* wu = (const char*)d_in[5]; const char* wd = (const char*)d_in[6];
    char* ws = (char*)d_ws;
    (void)hipFuncSetAttribute((const void*)k1_qkv, hipFuncAttributeMaxDynamicSharedMemorySize, K1_LDS);
    (void)hipFuncSetAttribute((const void*)k2_attn, hipFuncAttributeMaxDynamicSharedMemorySize, K2_LDS);
    (void)hipFuncSetAttribute((const void*)k3_oproj, hipFuncAttributeMaxDynamicSharedMemorySize, K3_LDS);
    (void)hipFuncSetAttribute((const void*)g1_gateup, hipFuncAttributeMaxDynamicSharedMemorySize, G_LDS);
    (void)hipFuncSetAttribute((const void*)g2_down, hipFuncAttributeMaxDynamicSharedMemorySize, G_LDS);
    k1_qkv<<<256, 512, K1_LDS, stream>>>(x, wqkv, wo, ws);
    k2_attn<<<256, 1024, K2_LDS, stream>>>(wg, wu, ws);
    k3_oproj<<<256, 512, K3_LDS, stream>>>(x, wd, ws);
    g1_gateup<<<256, 512, G_LDS, stream>>>(ws);
    g2_down<<<256, 512, G_LDS, stream>>>(ws);
    red_kernel<<<288, 256, 0, stream>>>(ws, (char*)d_out);
}
```

```cpp
#include <hip/hip_runtime.h>
#include <hip/hip_bf16.h>
#include <stdint.h>
#include <utility>
#define K1_TOUCH 0
#define TOUCH 0
#define K2_RT 1
#define K2_PRIO 1
#define K1_ROT 0
#define G1PD 4
#define ACT_POL 1
#define RT_POL 1
#define PART_POL 1
#define H_POL 1
#define QKV_POL 1
#define G1_PRIO 1

namespace {

typedef __bf16 bf16x2 __attribute__((ext_vector_type(2)));
typedef __bf16 bf16x8 __attribute__((ext_vector_type(8)));
typedef __bf16 bf16x16 __attribute__((ext_vector_type(16)));
typedef short s16x4 __attribute__((ext_vector_type(4)));
typedef float f32x2 __attribute__((ext_vector_type(2)));
typedef float f32x4 __attribute__((ext_vector_type(4)));
typedef float f32x8 __attribute__((ext_vector_type(8)));
typedef float f32x16 __attribute__((ext_vector_type(16)));
typedef unsigned u32x2 __attribute__((ext_vector_type(2)));
typedef unsigned u32x4 __attribute__((ext_vector_type(4)));
#define LAS __attribute__((address_space(3)))
#define DI __device__ __forceinline__

constexpr int S_ = 2048, D_ = 576, H_ = 32, F_ = 4608;
constexpr float EPS = 1e-5f;
constexpr float QSCALE = 0.23570226039551584f * 1.4426950408889634f;
constexpr float SAFE_B = 48.f;

constexpr int KVCHUNK = 19456, KVIMG = 8 * KVCHUNK;
constexpr size_t WS_QM   = 0;
constexpr size_t WS_QT   = WS_QM + (size_t)32 * 2048 * 32;
constexpr size_t WS_KV   = WS_QT + (size_t)32 * 2048 * 4;
constexpr size_t WS_KMAX = WS_KV + (size_t)32 * KVIMG;
constexpr size_t WS_MUX  = WS_KMAX + 32 * 16 * 4;
constexpr size_t WS_WBAR = WS_MUX + 2048 * 4;
constexpr size_t WS_SSQ  = WS_WBAR + 4096;
constexpr size_t WS_OF   = WS_SSQ + (size_t)2048 * 64;
constexpr size_t WS_HC   = WS_OF + (size_t)128 * 18 * 1024;
constexpr size_t WS_HCF  = WS_HC + (size_t)128 * 36 * 512;
constexpr size_t WS_W1   = WS_HCF + (size_t)128 * 18 * 1024;
constexpr size_t WS_W2   = WS_W1 + (size_t)32 * 324 * 1024;
constexpr size_t WS_WO   = WS_W2 + (size_t)16 * 324 * 1024;
constexpr size_t WS_ACT  = WS_WO + (size_t)6 * 108 * 1024;
constexpr size_t WS_PART = WS_ACT + (size_t)128 * 144 * 1024;
constexpr size_t WS_CNT  = WS_PART + (size_t)64 * 16 * 18 * 512;
constexpr size_t WS_END  = WS_CNT + 65 * 256;

#define STAMP_DECL
#define STAMP(kid, i)
#define RSTAMP(kid, i)
#define STAMP_FLUSH(kid)
DI unsigned cvtpk(float lo, float hi) { f32x2 v = {lo, hi}; bf16x2 b = __builtin_convertvector(v, bf16x2); return __builtin_bit_cast(unsigned, b); }
DI float bflo(unsigned w) { return __uint_as_float(w << 16); }
DI float bfhi(unsigned w) { return __uint_as_float(w & 0xffff0000u); }
DI float dot2(unsigned a, unsigned b, float c) { return __builtin_amdgcn_fdot2_f32_bf16(__builtin_bit_cast(bf16x2, a), __builtin_bit_cast(bf16x2, b), c, false); }
DI int rfl(int x) { return __builtin_amdgcn_readfirstlane(x); }
DI float xor32(float v) { auto r = __builtin_amdgcn_permlane32_swap(__float_as_uint(v), __float_as_uint(v), false, false); return __uint_as_float((threadIdx.x & 32) ? r[0] : r[1]); }
DI float xor16(float v) { auto r = __builtin_amdgcn_permlane16_swap(__float_as_uint(v), __float_as_uint(v), false, false); return __uint_as_float((threadIdx.x & 16) ? r[0] : r[1]); }
DI f32x4 mfma16(bf16x8 a, bf16x8 b, f32x4 c) { return __builtin_amdgcn_mfma_f32_16x16x32_bf16(a, b, c, 0, 0, 0); }
DI void mfma16_acc(f32x4& c, bf16x8 a, bf16x8 b) { asm volatile("v_mfma_f32_16x16x32_bf16 %0, %1, %2, %0" : "+v"(c) : "v"(a), "v"(b)); }
DI void mfma16_new(f32x4& c, bf16x8 a, bf16x8 b) { asm volatile("v_mfma_f32_16x16x32_bf16 %0, %1, %2, 0" : "=&v"(c) : "v"(a), "v"(b)); }
DI void glds16(const void* sbase, unsigned voff, unsigned lds_dst) {
    asm volatile("s_mov_b32 m0, %2\n\ts_nop 0\n\tglobal_load_lds_dwordx4 %0, %1" :: "v"(voff), "s"(sbase), "s"(lds_dst) : "memory");
}
DI void gload16(bf16x8& dst, const void* sbase, unsigned voff) { asm volatile("global_load_dwordx4 %0, %1, %2" : "=v"(dst) : "v"(voff), "s"(sbase)); }
DI void gload16f(f32x4& dst, const void* sbase, unsigned voff) { asm volatile("global_load_dwordx4 %0, %1, %2" : "=v"(dst) : "v"(voff), "s"(sbase)); }
DI void gload8(u32x2& dst, const void* sbase, unsigned voff) { asm volatile("global_load_dwordx2 %0, %1, %2" : "=v"(dst) : "v"(voff), "s"(sbase)); }
DI void gload4(float& dst, const void* sbase, unsigned voff) { asm volatile("global_load_dword %0, %1, %2" : "=v"(dst) : "v"(voff), "s"(sbase)); }
typedef __amdgpu_buffer_rsrc_t srd_t;
DI srd_t make_srd(const void* p, unsigned bytes) { return __builtin_amdgcn_make_buffer_rsrc((void*)p, (short)0, (int)bytes, 0x00020000); }
template <int IMM> DI void bload16(bf16x8& dst, srd_t r, unsigned voff, unsigned soff) {
    asm volatile("buffer_load_dwordx4 %0, %1, %2, %3 offen offset:%4" : "=v"(dst) : "v"(voff), "s"(r), "s"(soff), "n"(IMM)); }
template <int IMM> DI void bload16f(f32x4& dst, srd_t r, unsigned voff, unsigned soff) {
    asm volatile("buffer_load_dwordx4 %0, %1, %2, %3 offen offset:%4" : "=v"(dst) : "v"(voff), "s"(r), "s"(soff), "n"(IMM)); }
template <int IMM> DI void bload4(float& dst, srd_t r, unsigned voff, unsigned soff) {
    asm volatile("buffer_load_dword %0, %1, %2, %3 offen offset:%4" : "=v"(dst) : "v"(voff), "s"(r), "s"(soff), "n"(IMM)); }
template <int IMM> DI void bload16u_sc1(u32x4& dst, srd_t r, unsigned voff, unsigned soff) {
    asm volatile("buffer_load_dwordx4 %0, %1, %2, %3 offen offset:%4 sc1" : "=v"(dst) : "v"(voff), "s"(r), "s"(soff), "n"(IMM)); }
template <int IMM> DI void bload8_sc1(u32x2& dst, srd_t r, unsigned voff, unsigned soff) {
    asm volatile("buffer_load_dwordx2 %0, %1, %2, %3 offen offset:%4 sc1" : "=v"(dst) : "v"(voff), "s"(r), "s"(soff), "n"(IMM)); }
template <int IMM> DI void bload8(u32x2& dst, srd_t r, unsigned voff, unsigned soff) {
    asm volatile("buffer_load_dwordx2 %0, %1, %2, %3 offen offset:%4" : "=v"(dst) : "v"(voff), "s"(r), "s"(soff), "n"(IMM)); }
DI void touch64(float& sink, srd_t r, unsigned lane128, unsigned soff) { asm volatile("buffer_load_dword %0, %1, %2, %3 offen" : "+v"(sink) : "v"(lane128), "s"(r), "s"(soff)); }
DI void bdma4(srd_t r, unsigned voff, unsigned soff, unsigned lds_dst) {
    asm volatile("s_mov_b32 m0, %3\n\ts_nop 0\n\tbuffer_load_dwordx4 %0, %1, %2 offen lds\n\tbuffer_load_dwordx4 %0, %1, %2 offen offset:1024 lds\n\t"
                 "buffer_load_dwordx4 %0, %1, %2 offen offset:2048 lds\n\tbuffer_load_dwordx4 %0, %1, %2 offen offset:3072 lds" :: "v"(voff), "s"(r), "s"(soff), "s"(lds_dst) : "memory"); }
DI void bdma1(srd_t r, unsigned voff, unsigned soff, unsigned lds_dst) {
    asm volatile("s_mov_b32 m0, %3\n\ts_nop 0\n\tbuffer_load_dwordx4 %0, %1, %2 offen lds" :: "v"(voff), "s"(r), "s"(soff), "s"(lds_dst) : "memory"); }
template <int IMM> DI void bdma_next(srd_t r, unsigned voff, unsigned soff, unsigned lds_dst) {
    asm volatile("s_mov_b32 m0, %3\n\ts_nop 0\n\tbuffer_load_dwordx4 %0, %1, %2 offen offset:%4 lds" :: "v"(voff), "s"(r), "s"(soff), "s"(lds_dst), "n"(IMM) : "memory"); }
template <int POL> DI void bstore16(u32x4 d, srd_t r, unsigned voff, unsigned soff) {
    if constexpr (POL == 1) asm volatile("buffer_store_dwordx4 %0, %1, %2, %3 offen sc1\n\ts_nop 1" :: "v"(d), "v"(voff), "s"(r), "s"(soff) : "memory");
    else if constexpr (POL == 2) asm volatile("buffer_store_dwordx4 %0, %1, %2, %3 offen nt\n\ts_nop 1" :: "v"(d), "v"(voff), "s"(r), "s"(soff) : "memory");
    else asm volatile("buffer_store_dwordx4 %0, %1, %2, %3 offen\n\ts_nop 1" :: "v"(d), "v"(voff), "s"(r), "s"(soff) : "memory"); }
template <int POL> DI void bstore8(u32x2 d, srd_t r, unsigned voff, unsigned soff) {
    if constexpr (POL == 1) asm volatile("buffer_store_dwordx2 %0, %1, %2, %3 offen sc1\n\ts_nop 1" :: "v"(d), "v"(voff), "s"(r), "s"(soff) : "memory");
    else if constexpr (POL == 2) asm volatile("buffer_store_dwordx2 %0, %1, %2, %3 offen nt\n\ts_nop 1" :: "v"(d), "v"(voff), "s"(r), "s"(soff) : "memory");
    else asm volatile("buffer_store_dwordx2 %0, %1, %2, %3 offen\n\ts_nop 1" :: "v"(d), "v"(voff), "s"(r), "s"(soff) : "memory"); }
#define PINV(x) asm volatile("" : "+v"(x))
#define WAITV(n) asm volatile("s_waitcnt vmcnt(%0)" :: "n"(n) : "memory")
#define WAIT_BAR(n) asm volatile("s_waitcnt vmcnt(%0) lgkmcnt(0)\n\ts_barrier" :: "n"(n) : "memory")
#define SBAR() __builtin_amdgcn_sched_barrier(0)
#define LBAR() asm volatile("s_waitcnt lgkmcnt(0)\n\ts_barrier" ::: "memory")
DI bf16x8 lds_frag(LAS const char* p) { return *(LAS const bf16x8*)p; }
typedef short v4i16_t __attribute__((ext_vector_type(4)));
DI u32x2 lds_tr(LAS const char* p) { return __builtin_bit_cast(u32x2, __builtin_amdgcn_ds_read_tr16_b64_v4i16((LAS v4i16_t*)p)); }
DI bf16x8 tr_frag(LAS const char* p, int off2) { u32x2 a = lds_tr(p), b = lds_tr(p + off2); u32x4 r = {a[0], a[1], b[0], b[1]}; return __builtin_bit_cast(bf16x8, r); }

DI int kstep_to_ftile(int ks, int half) {
    const int a = ks / 9, idx = ks - 9 * a;
    if (idx == 8) return 9 * (2 * a + half) + 8;
    return 9 * (2 * a + (idx >> 2)) + 2 * (idx & 3) + half;
}

struct RetileJob { const char* src; unsigned voff; unsigned dst[4]; bool live; };
DI void retile_setup(RetileJob& J, int b, int lane, const void* wo, const void* wg, const void* wu, const void* wd) {
    J.live = b < 4050;
    const int r8 = lane >> 3, ch = lane & 7;
    if (b < 2592) {
        const int gu = b >= 1296, bb = b - 1296 * gu, s = bb / 72, cb = bb - 72 * s;
        J.src = (const char*)(gu ? wu : wg) + (size_t)s * 32 * (F_ * 2) + cb * 128;
        J.voff = r8 * (F_ * 2) + ch * 16;
#pragma unroll
        for (int u = 0; u < 4; ++u) { const int T = 4 * cb + u, j = T / 9, tt = T - 9 * j;
            J.dst[u] = (unsigned)(WS_W1 + (size_t)(j * 324 + (tt < 8 ? (tt >> 1) * 72 + s * 4 + (tt & 1) * 2 : 288 + s * 2) + gu) * 1024); }
    } else if (b < 3888) {
        const int bb = b - 2592, ks = bb / 9, cb = bb - 9 * ks;
        J.src = (const char*)wd + cb * 128;
        J.voff = (unsigned)ks;
        const int ksl = ks / 36, kap = ks - 36 * ksl, kj = kap / 18, r = kap - 18 * kj, st = r >> 1, s2 = r & 1;
#pragma unroll
        for (int u = 0; u < 4; ++u) { const int T = 4 * cb + u, n4 = T / 9, tt = T - 9 * n4;
            J.dst[u] = (unsigned)(WS_W2 + (size_t)((ksl * 4 + n4) * 324 + ((st * 2 + kj) * 2 + s2) * 9 + tt) * 1024); }
    } else {
        const int bb = (b < 4050 ? b : 4049) - 3888, s = bb / 9, cb = bb - 9 * s;
        J.src = (const char*)wo + (size_t)s * 32 * (D_ * 2) + cb * 128;
        J.voff = ch * 16;
#pragma unroll
        for (int u = 0; u < 4; ++u) { const int T = 4 * cb + u, n6 = T / 6, tt = T - 6 * n6;
            J.dst[u] = (unsigned)(WS_WO + (size_t)(n6 * 108 + s * 6 + tt) * 1024); }
    }
}
DI void retile_issue(const RetileJob& J, int b, int lane, unsigned lds_scratch) {
    const int r8 = lane >> 3, ch = lane & 7;
#pragma unroll
    for (int p = 0; p < 4; ++p) {
        unsigned voff;
        if (b < 2592) voff = J.voff + p * 8 * (F_ * 2);
        else if (b < 3888) { const int rho = 8 * p + r8, T = kstep_to_ftile((int)J.voff, rho >> 4); voff = (16 * T + (rho & 15)) * (D_ * 2) + ch * 16; }
        else { const int rho = 8 * p + r8, k = 8 * ((rho >> 2) & 3) + 4 * (rho >> 4) + (rho & 3); voff = J.voff + k * (D_ * 2); }
        glds16(J.src, voff, lds_scratch + p * 1024);
    }
}
DI void retile_finish(const RetileJob& J, int lane, LAS const char* scratch, char* ws) {
    const int g = lane >> 4, q = (lane >> 2) & 3, p = lane & 3;
    LAS const char* base = scratch + (4 * g + q) * 128 + p * 8;
#pragma unroll
    for (int u = 0; u < 4; ++u) { const bf16x8 f = tr_frag(base + 32 * u, 16 * 128); if (J.live) *(bf16x8*)(ws + J.dst[u] + lane * 16) = f; }
}

constexpr int K1_SLOT = 34 * 1024, K1_STG = 0, K1_SMALL = 4 * K1_SLOT, K1_LDS = K1_SMALL + 4096;
template <int PART, int NJ>
DI void k1_wave(const char* x, const char* wqkv, char* ws, char* lds_generic, int mt, int nt, int rot, int shx, int shw) {
    constexpr bool VPART = PART == 2; constexpr int NT = NJ ? 4 : 5, T0 = NJ ? 5 : 0;
    LAS char* lds = (LAS char*)lds_generic; const unsigned ldsb = (unsigned)(uintptr_t)lds_generic;
    const int tid = threadIdx.x, lane = tid & 63, wid = rfl(tid >> 6), mi = wid & 3, m = lane & 15, g = lane >> 4;
    STAMP_DECL; RSTAMP(1, 14); STAMP(1, 0);
    const srd_t sx = make_srd(x, S_ * D_ * 2), sw = make_srd(wqkv, D_ * 3 * D_ * 2);
    const unsigned xv = (lane >> 3) * (D_ * 2) + (((lane & 7) ^ ((4 * wid + (lane >> 4)) & 7)) << 4);
    unsigned wv[2];
#pragma unroll
    for (int i = 0; i < 2; ++i) { const int idx = (i ? 8 : wid) * 64 + lane, rho = (idx * 3641) >> 16, ch = idx - 18 * rho;
        wv[i] = (8 * ((rho >> 2) & 3) + 4 * (rho >> 4) + (rho & 3)) * 3456 + ch * 16; }
    const unsigned xs = (128 * mt + 8 * wid) * (D_ * 2), wsb = nt * 288;
    auto issue = [&](int st, int p) {
        int k = st + rot; k = k >= 9 ? k - 9 : k; const unsigned dst = ldsb + (st & 3) * K1_SLOT;
        if (p < 2) bdma1(sx, xv, xs + p * 64 * (D_ * 2) + k * 128, dst + (wid + 8 * p) * 1024);
        else if (p < 4) bdma1(sw, wv[0], wsb + (64 * k + 32 * (p - 2)) * 3456, dst + 16384 + ((p - 2) * 9 + wid) * 1024);
        else if (wid < 2) bdma1(sw, wv[1], wsb + (64 * k + 32 * wid) * 3456, dst + 16384 + (wid * 9 + 8) * 1024); };
#pragma unroll
    for (int st = 0; st < 3; ++st)
#pragma unroll
        for (int p = 0; p < 5; ++p) issue(st, p);
    float tch[2] = {0.f, 0.f};
#pragma unroll
    for (int i = 0; i < 2 * K1_TOUCH; ++i) { int t = wid + 8 * i; t = t >= 12 ? t - 4 : t;
        if (t < 3) { const int L = shx * 192 + t * 64 + lane, row = (L * 7282) >> 16; bload4<0>(tch[i], sx, (128 * mt + row) * (D_ * 2) + (L - 9 * row) * 128, 0); }
        else { const int q = (t - 3) * 64 + lane; bload4<0>(tch[i], sw, (shw * 144 + (q >> 2)) * 3456 + ((nt * 288) & ~127) + (q & 3) * 128, 0); } }
    f32x4 acc[NT][2];
    f32x4 accs = {0, 0, 0, 0}, accg = {0, 0, 0, 0}, accc[NT];
#pragma unroll
    for (int t = 0; t < NT; ++t) accc[t] = (f32x4){0, 0, 0, 0};
    bf16x8 ones = __builtin_bit_cast(bf16x8, (u32x4){0x3f803f80u, 0x3f803f80u, 0x3f803f80u, 0x3f803f80u}); PINV(ones);
    asm volatile("s_nop 1" : "+v"(accs), "+v"(accg));
#pragma unroll
    for (int t = 0; t < NT; ++t) PINV(accc[t]);
    const unsigned xa0 = (32 * mi + m) * 128 + ((g ^ ((m >> 1) & 7)) << 4), xa1 = (32 * mi + m) * 128 + (((4 + g) ^ ((m >> 1) & 7)) << 4);
    const unsigned wa = 16384 + (4 * g + ((lane >> 2) & 3)) * 288 + (lane & 3) * 8 + T0 * 32;
    bf16x8 xq[2][2][2], wr[2][2][NT];
    auto rd = [&](int st, int i) {
        LAS const char* sb = lds + (st & 3) * K1_SLOT;
        if (i < 4) xq[st & 1][i >> 1][i & 1] = lds_frag(sb + ((i >> 1) ? xa1 : xa0) + (i & 1) * 2048);
        else { const int s2 = (i - 4) / NT, t = (i - 4) % NT; wr[st & 1][s2][t] = tr_frag(sb + wa + s2 * 9216 + 32 * t, 16 * 288); } };
    STAMP(1, 1);
    WAIT_BAR(8 + K1_TOUCH * 2);
    STAMP(1, 2);
#pragma unroll
    for (int i = 0; i < 4 + 2 * NT; ++i) rd(0, i);
#pragma unroll
    for (int st = 0; st < 9; ++st) {
        if (st < 8) { if (st < 2) WAIT_BAR(4 + K1_TOUCH * 2); else if (st < 6) WAIT_BAR(4); else if (st == 6) WAIT_BAR(4); else WAIT_BAR(0); }
        if (st == 2) { PINV(tch[0]); PINV(tch[1]); }
#pragma unroll
        for (int i = 0; i < 2 * NT; ++i) { const int s2 = i / NT, t = i % NT; const bf16x8 w = wr[st & 1][s2][t], x0 = xq[st & 1][s2][0], x1 = xq[st & 1][s2][1];
            if (st == 0 && s2 == 0) { if (VPART) { mfma16_new(acc[t][0], x0, w); mfma16_new(acc[t][1], x1, w); } else { mfma16_new(acc[t][0], w, x0); mfma16_new(acc[t][1], w, x1); } }
            else { if (VPART) { mfma16_acc(acc[t][0], x0, w); mfma16_acc(acc[t][1], x1, w); } else { mfma16_acc(acc[t][0], w, x0); mfma16_acc(acc[t][1], w, x1); } }
            if (st + 1 < 9) { if (i < 4) { rd(st + 1, i); rd(st + 1, 4 + i); } else if (4 + i < 4 + 2 * NT) rd(st + 1, 4 + i); }
            if (st + 3 < 9 && (i & 1) == 0 && i / 2 < 4) issue(st + 3, i / 2 == 0 ? 0 : i / 2 == 1 ? 2 : i / 2 == 2 ? 1 : 3);
            if (st + 3 < 9 && i == 7) issue(st + 3, 4);
            if (t == 1) mfma16_acc(accs, ones, NJ ? x1 : x0);
            if (t == 2) mfma16_acc(accg, NJ ? x1 : x0, NJ ? x1 : x0);
            if (t == NT - 1 && ((2 * st + s2) & 3) == mi) {
#pragma unroll
                for (int tt = 0; tt < NT; ++tt) { if (VPART) mfma16_acc(accc[tt], ones, wr[st & 1][s2][tt]); else mfma16_acc(accc[tt], wr[st & 1][s2][tt], ones); } }
            SBAR(); }
    }
    STAMP(1, 3);
    LAS float* sm = (LAS float*)(lds + K1_SMALL);
    asm volatile("s_nop 7\n\ts_nop 7" : "+v"(acc[NT - 1][0]), "+v"(acc[NT - 1][1]), "+v"(accs), "+v"(accg));
#pragma unroll
    for (int t = 0; t < NT; ++t) { PINV(accc[t]); PINV(acc[t][0]); PINV(acc[t][1]); }
    if (g == 0) sm[32 * mi + 16 * NJ + m] = accs[0];
    if (g == (m >> 2)) sm[128 + 32 * mi + 16 * NJ + m] = (m & 2) ? ((m & 1) ? accg[3] : accg[2]) : ((m & 1) ? accg[1] : accg[0]);
#pragma unroll
    for (int t = 0; t < NT; ++t) { if (VPART) { if (g == 0) sm[256 + mi * 144 + 16 * (T0 + t) + m] = accc[t][0]; }
        else if (m == 0) *(LAS f32x4*)(sm + 256 + mi * 144 + 16 * (T0 + t) + 4 * g) = accc[t]; }
    LBAR();
    STAMP(1, 4);
    LAS char* stg = lds + K1_STG;
    if (VPART && tid < 256) { const int hs = tid >> 1;
        *(LAS u32x4*)(stg + hs * 320 + ((tid & 1) ? 19 : 4) * 16) = (tid & 1) ? (u32x4){0x3f803f80u, 0x3f803f80u, 0x3f803f80u, 0x3f803f80u} : (u32x4){0, 0, 0, 0}; }
    f32x4 mean[2], rstd[2];
#pragma unroll
    for (int u = 0; u < 2; ++u) { const int tl = 32 * mi + 16 * u + (VPART ? 4 * g : m);
        if (VPART) { mean[u] = *(LAS const f32x4*)(sm + tl) * (1.f / D_);
            const f32x4 var = *(LAS const f32x4*)(sm + 128 + tl) * (1.f / D_) - mean[u] * mean[u];
#pragma unroll
            for (int r = 0; r < 4; ++r) rstd[u][r] = __builtin_amdgcn_rsqf(fmaxf(var[r], 0.f) + EPS); }
        else { mean[u][0] = sm[tl] * (1.f / D_);
            rstd[u][0] = __builtin_amdgcn_rsqf(fmaxf(sm[128 + tl] * (1.f / D_) - mean[u][0] * mean[u][0], 0.f) + EPS) * (PART == 0 ? QSCALE : 1.f); } }
#pragma unroll
    for (int t = 0; t < NT; ++t) { const int tt = T0 + t;
        if (VPART) { const int n = 16 * tt + m, hl = (n * 3641) >> 16, d = n - 18 * hl, row = d + (d >= 4 ? 5 : 0) - (d >= 12 ? 12 : 0) + (d >= 16 ? 8 : 0);
            const float c1 = (sm[256 + n] + sm[400 + n]) + (sm[544 + n] + sm[688 + n]);
            LAS char* p = stg + hl * 5120 + 2 * mi * 640 + (g >> 1) * 320 + row * 16 + (g & 1) * 8;
#pragma unroll
            for (int u = 0; u < 2; ++u) { const f32x4 a = (acc[t][u] - mean[u] * c1) * rstd[u];
                *(LAS u32x2*)(p + u * 640) = (u32x2){cvtpk(a[0], a[1]), cvtpk(a[2], a[3])}; }
        } else {
            const int n0 = 16 * tt + 4 * g;
            const f32x4 c4 = (*(LAS const f32x4*)(sm + 256 + n0) + *(LAS const f32x4*)(sm + 400 + n0)) + (*(LAS const f32x4*)(sm + 544 + n0) + *(LAS const f32x4*)(sm + 688 + n0));
            int o[2], ts[2];
#pragma unroll
            for (int p = 0; p < 2; ++p) { const int n = n0 + 2 * p, hl = (n * 3641) >> 16, d = n - 18 * hl; const bool tail = d == 16;
                if (PART == 0) { o[p] = tail ? 32768 + hl * 512 : hl * 4096 + d * 2; ts[p] = tail ? 4 : 32; }
                else { o[p] = hl * 4608 + (tail ? 4096 : (d >> 3) * 2048 + (d & 7) * 2); ts[p] = tail ? 4 : 16; } }
#pragma unroll
            for (int u = 0; u < 2; ++u) { const f32x4 a = (acc[t][u] - c4 * mean[u][0]) * rstd[u][0]; const int tl = 32 * mi + 16 * u + m;
                *(LAS unsigned*)(stg + o[0] + tl * ts[0]) = cvtpk(a[0], a[1]); *(LAS unsigned*)(stg + o[1] + tl * ts[1]) = cvtpk(a[2], a[3]); }
        } }
    if (nt == 0 && tid < 128) ((float*)(ws + WS_MUX))[128 * mt + tid] = sm[tid] * (1.f / D_);
    LBAR();
    STAMP(1, 5);
    const int head = 8 * (nt & 3) + wid; const srd_t sws = make_srd(ws, (unsigned)WS_END); const unsigned l16 = lane * 16;
    if (PART == 2) { const unsigned dst = (unsigned)WS_KV + head * KVIMG + (mt >> 1) * KVCHUNK + 9216 + (mt & 1) * 5120;
#pragma unroll
        for (int i = 0; i < 5; ++i) bstore16<QKV_POL>(*(LAS const u32x4*)(stg + wid * 5120 + i * 1024 + l16), sws, l16, dst + i * 1024);
    } else if (PART == 0) { const unsigned dst = (unsigned)WS_QM + (head * S_ + 128 * mt) * 32;
#pragma unroll
        for (int i = 0; i < 4; ++i) bstore16<QKV_POL>(*(LAS const u32x4*)(stg + wid * 4096 + i * 1024 + l16), sws, l16, dst + i * 1024);
        if (lane < 32) bstore16<QKV_POL>(*(LAS const u32x4*)(stg + 32768 + wid * 512 + l16), sws, l16, (unsigned)WS_QT + (head * S_ + 128 * mt) * 4);
    } else { const unsigned dst = (unsigned)WS_KV + head * KVIMG + (mt >> 1) * KVCHUNK; LAS const char* hb = stg + wid * 4608;
#pragma unroll
        for (int i = 0; i < 4; ++i) bstore16<QKV_POL>(*(LAS const u32x4*)(hb + i * 1024 + l16), sws, l16, dst + (i >> 1) * 4096 + (mt & 1) * 2048 + (i & 1) * 1024);
        if (lane < 32) bstore16<QKV_POL>(*(LAS const u32x4*)(hb + 4096 + l16), sws, l16, dst + 8192 + (mt & 1) * 512);
        float mx = 0.f;
#pragma unroll
        for (int i = 0; i < 2; ++i) { const int tl = lane + 64 * i;
            const u32x4 a = *(LAS const u32x4*)(hb + tl * 16), bq = *(LAS const u32x4*)(hb + 2048 + tl * 16); const unsigned c = *(LAS const unsigned*)(hb + 4096 + tl * 4);
            float s2 = dot2(c, c, 0.f);
#pragma unroll
            for (int e = 0; e < 4; ++e) { s2 = dot2(a[e], a[e], s2); s2 = dot2(bq[e], bq[e], s2); }
            mx = fmaxf(mx, s2); }
        mx = fmaxf(mx, xor16(mx)); mx = fmaxf(mx, xor32(mx));
#pragma unroll
        for (int o = 1; o < 16; o <<= 1) mx = fmaxf(mx, __shfl_xor(mx, o));
        if (lane == 0) ((float*)(ws + WS_KMAX))[head * 16 + mt] = mx;
    }
    STAMP(1, 6); RSTAMP(1, 15); STAMP_FLUSH(1);
}
template <int PART> DI void k1_body(const char* x, const char* wqkv, char* ws, char* lds, int mt, int nt, int rot, int shx, int shw) {
    if (threadIdx.x >= 256) k1_wave<PART, 1>(x, wqkv, ws, lds, mt, nt, rot, shx, shw); else k1_wave<PART, 0>(x, wqkv, ws, lds, mt, nt, rot, shx, shw);
}
__global__ __launch_bounds__(512, 2) void k1_qkv(const char* x, const char* wqkv, const char* wo, char* ws) {
    extern __shared__ __attribute__((aligned(16))) char lds[];
    const int b = blockIdx.x;
    if (b >= 192) {
        const int lane = threadIdx.x & 63, wv = rfl(threadIdx.x >> 6), gw = (b - 192) * 8 + wv;
        if (threadIdx.x == 511) ((unsigned*)(ws + WS_CNT))[(b - 192) * 64] = 0u;
        if (gw < 162) { RetileJob J; retile_setup(J, 3888 + gw, lane, wo, nullptr, nullptr, nullptr);
            retile_issue(J, 3888 + gw, lane, (unsigned)(uintptr_t)lds + wv * 4096); WAITV(0); retile_finish(J, lane, (LAS const char*)lds + wv * 4096, ws); }
        for (int c = gw; c < D_; c += 512) {
            const char* row = wo + (size_t)c * (D_ * 2);
            const u32x4 a = *(const u32x4*)(row + lane * 16); float s = 0.f;
#pragma unroll
            for (int e = 0; e < 4; ++e) s = dot2(a[e], 0x3f803f80u, s);
            if (lane < 8) { const u32x4 bq = *(const u32x4*)(row + 1024 + lane * 16);
#pragma unroll
                for (int e = 0; e < 4; ++e) s = dot2(bq[e], 0x3f803f80u, s); }
#pragma unroll
            for (int o = 1; o < 64; o <<= 1) s += __shfl_xor(s, o);
            if (lane == 0) { const float w = s * (1.f / D_); const unsigned hi = cvtpk(w, 0.f) & 0xffffu; const unsigned lo = cvtpk(w - bflo(hi), 0.f) & 0xffffu;
                ((unsigned short*)(ws + WS_WBAR))[c] = (unsigned short)hi; ((unsigned short*)(ws + WS_WBAR))[D_ + c] = (unsigned short)lo; }
        }
        return;
    }
    const int xcd = b & 7, idx = b >> 3, mt = (xcd & 3) * 4 + (idx & 3), nt = (xcd >> 2) * 6 + (idx >> 2);
    const int rot = K1_ROT ? (2 * (idx & 3) + ((idx >> 2) * 3 >> 1)) % 9 : 0;
    const int shx = idx >> 2, shw = idx & 3;
    if (nt >= 8) k1_body<2>(x, wqkv, ws, lds, mt, nt, rot, shx, shw); else if (nt >= 4) k1_body<1>(x, wqkv, ws, lds, mt, nt, rot, shx, shw); else k1_body<0>(x, wqkv, ws, lds, mt, nt, rot, shx, shw);
}

constexpr int K2_LDS = 160 * 1024;
#define EXPS(S, a, b) _Pragma("unroll") for (int r_ = a; r_ < b; ++r_) S[r_] = __builtin_amdgcn_exp2f(S[r_]); SBAR()
#define PLO(S) __builtin_bit_cast(bf16x16, (f32x8)__builtin_shufflevector(S, S, 0, 1, 2, 3, 4, 5, 6, 7))
#define PHI(S) __builtin_bit_cast(bf16x16, (f32x8)__builtin_shufflevector(S, S, 8, 9, 10, 11, 12, 13, 14, 15))
#define KTAIL(p) __builtin_bit_cast(s16x4, (u32x2){*(LAS const unsigned*)(p), kc})
#define QK1(q) __builtin_amdgcn_mfma_f32_32x32x16_bf16(kf, q, (f32x16){}, 0, 0, 0)
#define QK2(q, s) __builtin_amdgcn_mfma_f32_32x32x8bf16_1k(kt, q, s, 0, 0, 0)
#define PV(v, p, o) __builtin_amdgcn_smfmac_f32_32x32x32_bf16(v, p, o, 0xDDDD, 0, 0)
DI void attn_fast(LAS const char* kp, LAS const char* tp, LAS const char* vp, const bf16x8 (&qm)[2], const s16x4 (&qt)[2], f32x16 (&o)[2], unsigned kc) {
    bf16x8 kf = *(LAS const bf16x8*)kp; s16x4 kt = KTAIL(tp); bf16x8 v0, v1;
    f32x16 s0 = QK1(qm[0]), s1; s0 = QK2(qt[0], s0);
    SBAR();
#pragma unroll
    for (int it = 0; it < 8; ++it) {
        if (K2_PRIO) { if (it == 0) __builtin_amdgcn_s_setprio(3); if (it == 2) __builtin_amdgcn_s_setprio(2); if (it == 4) __builtin_amdgcn_s_setprio(1); if (it == 6) __builtin_amdgcn_s_setprio(0); }
        EXPS(s0, 0, 2);
        if (it > 0) { o[1] = PV(v0, PLO(s1), o[1]); SBAR(); }
        EXPS(s0, 2, 6);
        if (it > 0) { o[1] = PV(v1, PHI(s1), o[1]); SBAR(); }
        v0 = *(LAS const bf16x8*)(vp + it * 1280); v1 = *(LAS const bf16x8*)(vp + it * 1280 + 640); SBAR();
        EXPS(s0, 6, 9);
        s1 = QK1(qm[1]); SBAR();
        EXPS(s0, 9, 12);
        s1 = QK2(qt[1], s1); SBAR();
        if (it + 1 < 8) { kf = *(LAS const bf16x8*)(kp + (it + 1) * 512); kt = KTAIL(tp + (it + 1) * 128); SBAR(); }
        EXPS(s0, 12, 16);
        EXPS(s1, 0, 2);
        o[0] = PV(v0, PLO(s0), o[0]); SBAR();
        EXPS(s1, 2, 6);
        o[0] = PV(v1, PHI(s0), o[0]); SBAR();
        EXPS(s1, 6, 9);
        if (it + 1 < 8) { s0 = QK1(qm[0]); SBAR(); }
        EXPS(s1, 9, 12);
        if (it + 1 < 8) { s0 = QK2(qt[0], s0); SBAR(); }
        EXPS(s1, 12, 16);
    }
    o[1] = PV(v0, PLO(s1), o[1]); o[1] = PV(v1, PHI(s1), o[1]);
}
DI void attn_slow(LAS const char* kp, LAS const char* tp, LAS const char* vp, const bf16x8 (&qm)[2], const s16x4 (&qt)[2], f32x16 (&o)[2], float (&mrun)[2], unsigned kc) {
#pragma unroll 1
    for (int it = 0; it < 8; ++it) {
        const bf16x8 kf = *(LAS const bf16x8*)(kp + it * 512); const s16x4 kt = KTAIL(tp + it * 128);
        const bf16x8 v0 = *(LAS const bf16x8*)(vp + it * 1280), v1 = *(LAS const bf16x8*)(vp + it * 1280 + 640);
#pragma unroll
        for (int u = 0; u < 2; ++u) {
            f32x16 s = QK1(qm[u]); s = QK2(qt[u], s);
            float mx = s[0];
#pragma unroll
            for (int r = 1; r < 16; ++r) mx = fmaxf(mx, s[r]);
            mx = fmaxf(mx, xor32(mx));
            const float mn = fmaxf(mrun[u], mx), al = __builtin_amdgcn_exp2f(mrun[u] - mn);
            mrun[u] = mn;
#pragma unroll
            for (int r = 0; r < 16; ++r) { s[r] = __builtin_amdgcn_exp2f(s[r] - mn); o[u][r] *= al; }
            o[u] = PV(v0, PLO(s), o[u]); o[u] = PV(v1, PHI(s), o[u]);
        }
    }
}
__global__ __launch_bounds__(1024, 4) void k2_attn(const char* x, const char* wg, const char* wu, const char* wd, char* ws) {
    extern __shared__ __attribute__((aligned(16))) char lds_generic[];
    LAS char* lds = (LAS char*)lds_generic; const unsigned ldsb = (unsigned)(uintptr_t)lds_generic;
    const int tid = threadIdx.x, lane = tid & 63, wid = rfl(tid >> 6), qg = wid & 3, kq = wid >> 2, r32 = lane & 31, h = lane >> 5;
    const int b = blockIdx.x, xcd = b & 7, idx = b >> 3, head = xcd * 4 + (idx & 3), qb = idx >> 2;
    STAMP_DECL; RSTAMP(2, 14); STAMP(2, 0);
    const srd_t srd = make_srd(ws, (unsigned)WS_END);
    const int c0 = (2 * kq + (qb & 1)) * KVCHUNK, c1 = (2 * kq + ((qb & 1) ^ 1)) * KVCHUNK;
    const unsigned kvb = (unsigned)WS_KV + head * KVIMG, l16 = lane * 16;
    const int q0 = 256 * qb + 64 * qg;
    bf16x8 qm[2]; float qtl[2]; float km;
    { const unsigned so = (unsigned)WS_QM + (head * S_ + q0) * 32, vo = r32 * 32 + h * 16; bload16<0>(qm[0], srd, vo, so); bload16<1024>(qm[1], srd, vo, so);
      const unsigned st = (unsigned)WS_QT + (head * S_ + q0) * 4; bload4<0>(qtl[0], srd, r32 * 4, st); bload4<128>(qtl[1], srd, r32 * 4, st);
      bload4<0>(km, srd, (lane & 15) * 4, (unsigned)WS_KMAX + head * 64); }
    bdma4(srd, l16, kvb + c0 + qg * 5120, ldsb + c0 + qg * 5120); if (qg < 3) bdma1(srd, l16, kvb + c0 + qg * 5120 + 4096, ldsb + c0 + qg * 5120 + 4096);
    const int slot = wid * 32 + idx; const bool rt = slot < (K2_RT ? 486 : 0);
    bf16x8 wb[4]; unsigned rdst[4], rso[4], rvo; const char* rsrc;
    if (slot < 324) { const int gu = slot >= 162, r = slot - 162 * gu, ks = (r * 57) >> 9, cb = 9 * xcd + r - 9 * ks, T0 = 4 * cb, j0 = (T0 * 7282) >> 16, t0 = T0 - 9 * j0;
        rsrc = gu ? wu : wg; rvo = (lane >> 3) * (F_ * 2) + (lane & 7) * 16;
#pragma unroll
        for (int p = 0; p < 4; ++p) { rso[p] = (ks * 32 + p * 8) * (F_ * 2) + cb * 128;
            const int w = t0 + p >= 9, tt = t0 + p - 9 * w, m4 = tt >= 4, m8 = tt >= 8;
            rdst[p] = (unsigned)WS_W1 + ((j0 + w) * 324 + 2 * tt + m4 * 34 * (tt - (tt & 1)) + (ks << (3 - m4 - m8)) + gu) * 1024; }
    } else { const int r2 = slot < 486 ? slot - 324 : 0, q = (r2 * 57) >> 9, cb = r2 - 9 * q, hi = q >= 9, qi = q - 9 * hi, a = 2 * xcd + hi;
        rsrc = wd; rvo = (lane >> 3) * (D_ * 2) + (lane & 7) * 16;
        const int e8 = qi == 8, t0 = 18 * a + 9 * (qi >> 2) + 2 * (qi & 3) - 10 * e8, t1 = t0 + 1 + 8 * e8, T0 = 4 * cb, n0 = (T0 * 7282) >> 16, u0 = T0 - 9 * n0;
#pragma unroll
        for (int p = 0; p < 4; ++p) { rso[p] = (16 * (p >> 1 ? t1 : t0) + 8 * (p & 1)) * (D_ * 2) + cb * 128;
            const int w = u0 + p >= 9, tt = u0 + p - 9 * w;
            rdst[p] = (unsigned)WS_W2 + (((xcd >> 1) * 4 + n0 + w) * 324 + (((q >> 1) * 2 + (xcd & 1)) * 2 + (q & 1)) * 9 + tt) * 1024; }
    }
#pragma unroll
    for (int p = 0; p < 4; ++p) { rso[p] = rfl(rso[p]); rdst[p] = rfl(rdst[p]); }
    asm volatile("s_waitcnt vmcnt(4)" : "+v"(qm[0]), "+v"(qm[1]), "+v"(qtl[0]), "+v"(qtl[1]), "+v"(km) :: "memory");
    km = fmaxf(km, __shfl_xor(km, 1)); km = fmaxf(km, __shfl_xor(km, 2)); km = fmaxf(km, __shfl_xor(km, 4)); km = fmaxf(km, __shfl_xor(km, 8));
    s16x4 qt[2]; bool unsafe = false;
#pragma unroll
    for (int u = 0; u < 2; ++u) { const u32x4 qv = __builtin_bit_cast(u32x4, qm[u]); const unsigned tl = __float_as_uint(qtl[u]);
        float n2 = h ? 0.f : dot2(tl, tl, 0.f);
#pragma unroll
        for (int e = 0; e < 4; ++e) n2 = dot2(qv[e], qv[e], n2);
        n2 += xor32(n2);
        const float bound = __builtin_amdgcn_sqrtf(n2 * km) * 1.0001f + 1e-6f;
        unsafe |= !(bound <= SAFE_B);
        const unsigned cb = (__float_as_uint(bound) + 0xffffu) >> 16;
        const u32x2 w = h ? (u32x2){0u, 0u} : (u32x2){tl, cb | 0x8000u};
        qt[u] = __builtin_bit_cast(s16x4, w); }
    const bool slow = __any(unsafe);
    if (slow) {
#pragma unroll
        for (int u = 0; u < 2; ++u) { u32x2 w = __builtin_bit_cast(u32x2, qt[u]); w[1] = 0u; qt[u] = __builtin_bit_cast(s16x4, w); } }
    f32x16 o[2] = {(f32x16){}, (f32x16){}}; float mrun[2] = {-INFINITY, -INFINITY};
    const int vrow = r32 >= 19 ? 4 : r32 + (r32 >= 4 ? 5 : 0) - (r32 >= 12 ? 12 : 0) + (r32 >= 16 ? 8 : 0);
    const unsigned kc = h ? 0u : 0x00003f80u;
    unsigned kp = ldsb + c0 + h * 4096 + r32 * 16, tp = ldsb + c0 + 8192 + r32 * 4, vp = ldsb + c0 + 9216 + h * 320 + vrow * 16;
#define KP(o) ((LAS const char*)(uintptr_t)kp + (o))
#define TP(o) ((LAS const char*)(uintptr_t)tp + (o))
#define VP(o) ((LAS const char*)(uintptr_t)vp + (o))
    STAMP(2, 1);
    WAIT_BAR(0);
    STAMP(2, 2);
    bdma4(srd, l16, kvb + c1 + qg * 5120, ldsb + c1 + qg * 5120); if (qg < 3) bdma1(srd, l16, kvb + c1 + qg * 5120 + 4096, ldsb + c1 + qg * 5120 + 4096);
    if (rt) { const srd_t sw = make_srd(rsrc, D_ * F_ * 2);
#pragma unroll
        for (int p = 0; p < 4; ++p) bload16<0>(wb[p], sw, rvo, rso[p]); }
    if (slow) attn_slow(KP(0), TP(0), VP(0), qm, qt, o, mrun, kc); else attn_fast(KP(0), TP(0), VP(0), qm, qt, o, kc);
    STAMP(2, 3);
    if (rt) WAIT_BAR(4); else WAIT_BAR(0);
    STAMP(2, 4);
    if (rt) { asm volatile("s_waitcnt vmcnt(0)" : "+v"(wb[0]), "+v"(wb[1]), "+v"(wb[2]), "+v"(wb[3]) :: "memory");
        int ty = threadIdx.x; asm volatile("" : "+v"(ty)); const int ln = ty & 63;
        LAS char* scr = lds + c0 + qg * 4096;
#pragma unroll
        for (int p = 0; p < 4; ++p) *(LAS bf16x8*)(scr + p * 1024 + ln * 16) = wb[p];
        LAS const char* base = scr + (4 * (ln >> 4) + ((ln >> 2) & 3)) * 128 + (ln & 3) * 8;
#pragma unroll
        for (int u = 0; u < 4; ++u) bstore16<RT_POL>(__builtin_bit_cast(u32x4, tr_frag(base + 32 * u, 16 * 128)), srd, ln * 16, rdst[u]); }
    STAMP(2, 5);
    float sink = 0.f;
    if (TOUCH && wid < 3) touch64(sink, srd, lane * 128, (unsigned)WS_WO + idx * (162 * 128) + wid * 8192);
    if (TOUCH && (wid == 3 || wid == 4)) touch64(sink, make_srd(x, S_ * D_ * 2), lane * 128, (256 * xcd) * (D_ * 2) + idx * (72 * 128) + (wid - 3) * 8192);
    { const int dc = c1 - c0; kp += dc; tp += dc; vp += dc; PINV(kp); PINV(tp); PINV(vp); }
    if (slow) attn_slow(KP(0), TP(0), VP(0), qm, qt, o, mrun, kc); else attn_fast(KP(0), TP(0), VP(0), qm, qt, o, kc);
    STAMP(2, 6);
    int tz = threadIdx.x; asm volatile("" : "+v"(tz));
    const int lane2 = tz & 63, r2 = tz & 31, h2 = (tz >> 5) & 1;
    LAS float* mx = (LAS float*)(lds + KVIMG);
#pragma unroll
    for (int u = 0; u < 2; ++u) { LAS char* pb = lds + c0 + (qg * 2 + u) * 2432;
        *(LAS f32x4*)(pb + lane2 * 16) = (f32x4){o[u][0], o[u][1], o[u][2], o[u][3]};
        *(LAS f32x4*)(pb + 1024 + lane2 * 16) = (f32x4){o[u][4], o[u][5], o[u][6], o[u][7]};
        if (h2 == 0) { *(LAS f32x2*)(pb + 2048 + r2 * 8) = (f32x2){o[u][8], o[u][9]}; *(LAS float*)(pb + 2304 + r2 * 4) = o[u][10];
                      mx[wid * 64 + 32 * u + r2] = slow ? mrun[u] : 0.f; } }
    LBAR();
    { const int u = kq & 1, hf = kq >> 1;
      LAS const char* pb = lds + (qb & 1) * KVCHUNK + (qg * 2 + u) * 2432;
      float f[4] = {1.f, 1.f, 1.f, 1.f};
      if (slow) { float mk[4], M = -INFINITY;
#pragma unroll
          for (int k = 0; k < 4; ++k) { mk[k] = mx[(4 * k + qg) * 64 + 32 * u + r2]; M = fmaxf(M, mk[k]); }
#pragma unroll
          for (int k = 0; k < 4; ++k) f[k] = __builtin_amdgcn_exp2f(mk[k] - M); }
      float l = 0.f; f32x4 a = {0, 0, 0, 0}; f32x2 c = {0, 0};
#pragma unroll
      for (int k = 0; k < 4; ++k) { LAS const char* p = pb + k * 2 * KVCHUNK;
          l += f[k] * *(LAS const float*)(p + 2304 + r2 * 4); a += *(LAS const f32x4*)(p + hf * 1024 + lane2 * 16) * f[k]; c += *(LAS const f32x2*)(p + 2048 + r2 * 8) * f[k]; }
      const float rl = __builtin_amdgcn_rcpf(l);
      const int token = q0 + 32 * u + r2; char* ob = ws + WS_OF + (size_t)(token >> 4) * 18 * 1024 + (token & 15) * 16;
      auto put = [&](int d, float x0, float x1) { const int cc = 18 * head + d; *(unsigned*)(ob + (cc >> 5) * 1024 + ((cc & 31) >> 3) * 256 + (cc & 7) * 2) = cvtpk(x0 * rl, x1 * rl); };
      put(8 * hf + 4 * h2, a[0], a[1]); put(8 * hf + 4 * h2 + 2, a[2], a[3]);
      if (hf == 0 && h2 == 0) put(16, c[0], c[1]);
    }
    PINV(sink);
    STAMP(2, 7); RSTAMP(2, 15); STAMP_FLUSH(2);
}

constexpr int K3_X = 108 * 1024, K3_WB = K3_X + 12 * 1024, K3_Z = K3_WB + 3 * 1024, K3_LDS = K3_Z + 2048;
__global__ __launch_bounds__(512, 2) void k3_oproj(const char* x, char* ws) {
    extern __shared__ __attribute__((aligned(16))) char lds_generic[];
    LAS char* lds = (LAS char*)lds_generic; const unsigned ldsb = (unsigned)(uintptr_t)lds_generic;
    const int tid = threadIdx.x, lane = tid & 63, wid = rfl(tid >> 6), mi = wid & 3, kj = wid >> 2, m = lane & 15, g = lane >> 4;
    const int b = blockIdx.x, xcd = b & 7, idx = b >> 3, mt32 = xcd * 4 + (idx & 3), n6 = idx >> 2, mt16 = 4 * mt32 + mi;
    STAMP_DECL; RSTAMP(3, 14); STAMP(3, 0);
    const srd_t srd = make_srd(ws, (unsigned)WS_END), sx = make_srd(x, S_ * D_ * 2);
    const unsigned l16 = lane * 16, wo0 = (unsigned)WS_WO + n6 * (108 * 1024), ob = (unsigned)WS_OF + (mt16 * 18 + 9 * kj) * 1024;
    bf16x8 of[9];
    { const unsigned f0 = (54 * kj + 6 * mi) * 1024;
      bload16<0>(of[0], srd, l16, ob); bload16<1024>(of[1], srd, l16, ob); bload16<2048>(of[2], srd, l16, ob); bload16<3072>(of[3], srd, l16, ob);
      bdma4(srd, l16, wo0 + f0, ldsb + f0); bdma1(srd, l16, wo0 + f0 + 4096, ldsb + f0 + 4096); bdma1(srd, l16, wo0 + f0 + 5120, ldsb + f0 + 5120); }
    if (tid < 72) *(LAS u32x4*)(lds + K3_Z + tid * 16) = (u32x4){0, 0, 0, 0};
    f32x4 acc[6], accw = {0, 0, 0, 0}; float mux, sink = 0.f;
    asm volatile("s_nop 1" : "+v"(accw));
    LAS const char* ab = lds + kj * (54 * 1024) + lane * 16;
    LAS const char* wbp = lds + (m < 2 ? K3_WB + m * (D_ * 2) + kj * 576 + g * 16 : K3_Z);
    bf16x8 wr[2][6];
    auto rd = [&](int s) {
#pragma unroll
        for (int t = 0; t < 6; ++t) wr[s & 1][t] = lds_frag(ab + (s * 6 + t) * 1024);
        SBAR(); };
    auto step = [&](int s) { PINV(of[s]);
#pragma unroll
        for (int t = 0; t < 6; ++t) { if (s == 0) mfma16_new(acc[t], wr[s & 1][t], of[s]); else mfma16_acc(acc[t], wr[s & 1][t], of[s]); }
        SBAR(); };
    STAMP(3, 1);
    WAIT_BAR(0);
    STAMP(3, 2);
    { const unsigned f1 = (54 * kj + 24 + (mi < 3 ? 8 * mi : 22)) * 1024;
      rd(0); rd(1);
      bload16<0>(of[4], srd, l16, ob + 4096); bload16<1024>(of[5], srd, l16, ob + 4096); bdma4(srd, l16, wo0 + f1, ldsb + f1);
      step(0); rd(2);
      bload16<2048>(of[6], srd, l16, ob + 4096); bload16<3072>(of[7], srd, l16, ob + 4096); bdma4(srd, l16, wo0 + f1 + 4096, ldsb + f1 + 4096);
      step(1); rd(3);
      bload16<0>(of[8], srd, l16, ob + 8192);
#pragma unroll
      for (int i = 0; i < 2; ++i) { int pc = wid + 8 * i; pc = pc > 11 ? 11 : pc; const int q = pc * 64 + lane, row = (q * 5462) >> 16;
          bdma1(sx, row * (D_ * 2) + (q - 12 * row) * 16, (64 * mt32) * (D_ * 2) + n6 * 192, ldsb + K3_X + pc * 1024); }
      { const int pc = wid > 2 ? 2 : wid; bdma1(srd, l16, (unsigned)WS_WBAR + pc * 1024, ldsb + K3_WB + pc * 1024); }
      bload4<0>(mux, srd, m * 4, (unsigned)WS_MUX + mt16 * 64);
      if (TOUCH && wid < 5) touch64(sink, srd, lane * 128, (unsigned)WS_W1 + ((xcd >> 1) * 8 + idx / 3) * (324 * 1024) + (idx % 3) * (36 * 1024) + wid * 8192);
      step(2); step(3); }
    STAMP(3, 3);
    WAIT_BAR(0);
    STAMP(3, 4);
    bf16x8 wbf[9];
    rd(4); rd(5);
#pragma unroll
    for (int s = 0; s < 9; ++s) wbf[s] = lds_frag(wbp + s * 64);
    SBAR();
    step(4); rd(6); step(5); rd(7); step(6); rd(8); step(7); step(8);
#pragma unroll
    for (int s = 0; s < 9; ++s) mfma16_acc(accw, wbf[s], of[s]);
    PINV(mux);
    const int token = 16 * mt16 + m;
    u32x2 xr[4];
#pragma unroll
    for (int t = 0; t < 4; ++t) xr[t] = *(LAS const u32x2*)(lds + K3_X + (16 * mi + m) * 192 + (4 * kj + (kj ? (t & 1) : t)) * 32 + g * 8);
    asm volatile("s_nop 7" : "+v"(acc[5]), "+v"(acc[4]), "+v"(accw));
    STAMP(3, 5);
    LBAR();
    LAS char* ex = lds + mi * 8192 + lane * 16;
    if (kj == 0) { *(LAS f32x4*)(ex + 4 * 1024) = acc[4]; *(LAS f32x4*)(ex + 5 * 1024) = acc[5]; *(LAS f32x4*)(ex + 6 * 1024) = accw; }
    else {
#pragma unroll
        for (int t = 0; t < 4; ++t) *(LAS f32x4*)(ex + t * 1024) = acc[t];
        *(LAS f32x4*)(ex + 7 * 1024) = accw; }
    LBAR();
    accw += *(LAS const f32x4*)(ex + (kj ? 6 : 7) * 1024);
    const float mu = mux + __shfl(accw[0] + accw[1], m);
    float ssq = 0.f;
    auto fin = [&](int t, u32x2 xv, unsigned (&pk)[2]) {
        const f32x4 a = acc[t] + *(LAS const f32x4*)(ex + t * 1024);
        const unsigned h0 = cvtpk(a[0] + bflo(xv[0]), a[1] + bfhi(xv[0])), h1 = cvtpk(a[2] + bflo(xv[1]), a[3] + bfhi(xv[1]));
        bstore8<H_POL>((u32x2){h0, h1}, srd, lane * 8, (unsigned)WS_HC + (mt16 * 36 + 6 * n6 + t) * 512);
        pk[0] = cvtpk(bflo(h0) - mu, bfhi(h0) - mu); pk[1] = cvtpk(bflo(h1) - mu, bfhi(h1) - mu);
        ssq = dot2(pk[0], pk[0], ssq); ssq = dot2(pk[1], pk[1], ssq); };
    auto pair = [&](int t, u32x2 xa, u32x2 xb2) { unsigned p0[2], p1[2]; fin(t, xa, p0); fin(t + 1, xb2, p1);
        bstore16<H_POL>((u32x4){p0[0], p0[1], p1[0], p1[1]}, srd, l16, (unsigned)WS_HCF + (mt16 * 18 + 3 * n6 + (t >> 1)) * 1024); };
    if (kj == 0) { pair(0, xr[0], xr[1]); pair(2, xr[2], xr[3]); } else pair(4, xr[0], xr[1]);
    ssq += xor16(ssq); ssq += xor32(ssq);
    if (g == 0) ((float*)(ws + WS_SSQ))[token * 16 + 2 * n6 + kj] = ssq;
    PINV(sink);
    STAMP(3, 6); RSTAMP(3, 15); STAMP_FLUSH(3);
}

constexpr int RING_SLOT = 36 * 1024, G_LDS = 4 * RING_SLOT;
DI void ring_issue(const char* stream, int st, int wid, int lane, unsigned ldsb) {
    const char* sb = stream + (size_t)st * RING_SLOT + wid * 1024; const unsigned db = ldsb + (st & 3) * RING_SLOT + wid * 1024;
#pragma unroll
    for (int i = 0; i < 4; ++i) glds16(sb + i * 8192, lane * 16, db + i * 8192);
    if (wid < 4) glds16(sb + 32768, lane * 16, db + 32768);
}
DI float geglu(float g, float u) {
    const float z = g * __builtin_fmaf(g * g, -0.10294324f, -2.30220818f);
    return g * u * __builtin_amdgcn_rcpf(1.f + __builtin_amdgcn_exp2f(z));
}
struct Ev { int type, arg, n; };
enum { E_SSQ, E_A, E_B, E_WAITB, E_BAR, E_READ, E_MMA, E_GP, E_RS, E_STAMP, E_ST, E_NOP, E_TOUCH };
constexpr int G1_PD = G1PD;
constexpr int g1_grp(int f) { return f < 144 ? 0 : f < 216 ? 1 : f < 288 ? 2 : 3; }
constexpr int g1_ks(int f) { return f < 144 ? f / 8 : f < 288 ? ((f - 144) % 72) / 4 : (f - 288) / 2; }
constexpr int g1_tile(int f) { return f < 288 ? (f >> 1) & 1 : 0; }
constexpr int g1_set(int f) { return f < 144 ? (f >> 2) & 1 : f < 216 ? 0 : f < 288 ? 1 : 0; }
template <bool LATE> struct G1Prog {
    Ev ev[1600]; int n;
    constexpr void add(int t, int a) { ev[n].type = t; ev[n].arg = a; ev[n].n = 0; ++n; }
    static constexpr int vm(const Ev& e) { return e.type == E_SSQ ? 2 : (e.type == E_A || e.type == E_B || e.type == E_ST || e.type == E_TOUCH) ? 1 : 0; }
    static constexpr int NP = LATE ? 4 : 5;
    constexpr void addB(int s, int u) { add(E_B, s * 2 + u); }
    static constexpr int jset(int q) { return q == 0 ? 0 : q == 1 ? 1 : q == 2 ? 0 : q == 3 ? 1 : 0; }
    constexpr G1Prog() : ev{}, n(0) {
        for (int p = 0; p < NP; ++p) add(E_A, p);
        addB(0, 0); addB(0, 1); addB(1, 0); addB(1, 1);
        add(E_BAR, 0); add(E_STAMP, 1);
        for (int f = 0; f < G1_PD; ++f) add(E_READ, f);
        for (int f = 0; f < 324; ++f) {
            const int st = f / 36, i = f % 36, g = g1_grp(f);
            if (g == 0) { const int s = f / 8, q = f % 8;
                if (q == 0) add(E_WAITB, s);
                if (f == 0) { addB(2, 0); addB(2, 1); }
                if (s + 3 < 18 && q == 2) addB(s + 3, 0);
                if (s + 3 < 18 && q == 6) addB(s + 3, 1);
                if (f == 100) add(E_SSQ, 0);
                if (f == 136) add(E_RS, 0); }
            if (st == 0 && i >= 1 && i < 1 + 3 * NP && (i - 1) % 3 == 0) add(E_A, 8 + (i - 1) / 3);
            if (st + 2 < 9) { const int i0 = st == 0 ? 17 : LATE ? 17 : 1, d = 4;
                if (i >= i0 && (i - i0) % d == 0 && (i - i0) / d < NP) add(E_A, (st + 2) * 8 + (i - i0) / d); }
            if (i == 36 - G1_PD && st + 1 < 9) add(E_BAR, st + 1);
            if (f + G1_PD < 324) add(E_READ, f + G1_PD);
            add(E_MMA, f);
            if (f == 143) { add(E_NOP, 0);
                for (int k = 0; k < 32; ++k) { add(E_GP, 0 * 64 + k); if (k == 15 || k == 31) add(E_ST, 0 * 2 + (k == 31)); } }
            if (g > 0) {
                const int r = f - (g == 1 ? 144 : g == 2 ? 216 : 288), step = g < 3 ? 2 : 1, r0 = 3, k = (r - r0) / step;
                if (r >= r0 && (r - r0) % step == 0 && k < 32) { add(E_GP, g * 64 + k);
                    if (k == 15 || k == 31) add(E_ST, g * 2 + (k == 31)); } }
            if (TOUCH && f == 7 * 36 + 8) add(E_TOUCH, 0);
            if (i == 35) add(E_STAMP, 2 + st);
            if (f == 107) add(E_STAMP, 12);
            if (f == 323) add(E_STAMP, 13);
        }
        add(E_NOP, 1);
        for (int k = 0; k < 16; ++k) { add(E_GP, 4 * 64 + k); if (k == 7 || k == 15) add(E_ST, 8 + (k == 15)); }
        for (int i = 0; i < n; ++i) if (ev[i].type == E_WAITB || ev[i].type == E_BAR || ev[i].type == E_RS) {
            int cnt = 0, j = i - 1;
            for (; j >= 0; --j) {
                if (ev[i].type == E_WAITB && ev[j].type == E_B && ev[j].arg == 2 * ev[i].arg + 1) break;
                if (ev[i].type == E_RS && ev[j].type == E_SSQ) break;
                if (ev[i].type == E_BAR && ev[j].type == E_A && ev[j].arg == 8 * ev[i].arg + NP - 1) break;
                cnt += vm(ev[j]); }
            ev[i].n = cnt > 63 ? 63 : cnt; }
    }
};
template <bool LATE> constexpr G1Prog<LATE> g1prog{};
DI float vmul(float a, float b) { float r; asm("v_mul_f32_e32 %0, %1, %2" : "=v"(r) : "v"(a), "v"(b)); return r; }
DI float vfma(float a, float b, float c) { float r; asm("v_fma_f32 %0, %1, %2, %3" : "=v"(r) : "v"(a), "v"(b), "v"(c)); return r; }
struct G1State {
    bf16x8 hf[2][18]; f32x4 acc[2][2][2][2];
    bf16x8 ar[G1_PD + 1]; f32x4 sq[2]; float a0[2], a1[2], kk[2], sink; f32x4 z, w; u32x4 o; unsigned soffT;
    unsigned lb0, lb1, lb2; unsigned l16, ldsb, soffA, soffH, soffS, soffAct, vq; int wid, j, g;
};
template <bool LATE, int PC> DI void g1_exec(G1State& c, const srd_t srd) {
    constexpr Ev e = g1prog<LATE>.ev[PC];
    if constexpr (e.type == E_SSQ) { bload16f<0>(c.sq[0], srd, c.vq, c.soffS); bload16f<1024>(c.sq[1], srd, c.vq, c.soffS); }
    else if constexpr (e.type == E_A) { constexpr int st = e.arg >> 3, p = e.arg & 7;
        const unsigned so = c.soffA + st * RING_SLOT, dst = c.ldsb + (st & 3) * RING_SLOT;
        if constexpr (p < 4) bdma_next<p * 1024>(srd, c.l16, so + c.wid * 4096, dst + c.wid * 4096);
        else bdma1(srd, c.l16, so + 32768 + c.wid * 1024, dst + 32768 + c.wid * 1024); }
    else if constexpr (e.type == E_B) { constexpr int s = e.arg >> 1, u = e.arg & 1;
        bload16<((18 * u + s) & 3) * 1024>(c.hf[u][s], srd, c.l16, c.soffH + ((18 * u + s) >> 2) * 4096); }
    else if constexpr (e.type == E_WAITB) { asm volatile("s_waitcnt vmcnt(%2)" : "+v"(c.hf[0][e.arg]), "+v"(c.hf[1][e.arg]) : "n"(e.n)); }
    else if constexpr (e.type == E_BAR) { asm volatile("s_waitcnt vmcnt(%0)\n\ts_barrier" :: "n"(e.n) : "memory"); }
    else if constexpr (e.type == E_RS) { asm volatile("s_waitcnt vmcnt(%2)" : "+v"(c.sq[0]), "+v"(c.sq[1]) : "n"(e.n));
#pragma unroll
        for (int u = 0; u < 2; ++u) { float t = c.g < 3 ? (c.sq[u][0] + c.sq[u][1]) + (c.sq[u][2] + c.sq[u][3]) : 0.f; t += __shfl_xor(t, 16); t += xor32(t);
            const float v = t * (1.f / D_) + EPS, rs = __builtin_amdgcn_rsqf(v);
            c.a0[u] = -2.30220818f * rs; c.a1[u] = -0.10294324f * rs * rs * rs; c.kk[u] = v; }
        SBAR(); }
    else if constexpr (e.type == E_STAMP) {
    }
    else if constexpr (e.type == E_TOUCH) { touch64(c.sink, srd, c.l16 * 8, c.soffT); }
    else if constexpr (e.type == E_NOP) {
        asm volatile("s_nop 7" : "+v"(c.acc[0][0][0][0]), "+v"(c.acc[0][0][0][1]), "+v"(c.acc[0][0][1][0]), "+v"(c.acc[0][0][1][1]));
        if constexpr (e.arg == 0) asm volatile("" : "+v"(c.acc[0][1][0][0]), "+v"(c.acc[0][1][0][1]), "+v"(c.acc[0][1][1][0]), "+v"(c.acc[0][1][1][1])); }
    else if constexpr (e.type == E_READ) { constexpr int f = e.arg, off = ((f / 36) & 3) * RING_SLOT + (f % 36) * 1024;
        if constexpr ((off >> 16) == 0) c.ar[f % (G1_PD + 1)] = lds_frag((LAS const char*)(uintptr_t)c.lb0 + (off & 65535));
        else if constexpr ((off >> 16) == 1) c.ar[f % (G1_PD + 1)] = lds_frag((LAS const char*)(uintptr_t)c.lb1 + (off & 65535));
        else c.ar[f % (G1_PD + 1)] = lds_frag((LAS const char*)(uintptr_t)c.lb2 + (off & 65535));
        SBAR(); }
    else if constexpr (e.type == E_MMA) { constexpr int f = e.arg, s = g1_ks(f), t = g1_tile(f), q = f & 1, set = g1_set(f);
        if constexpr (s == 0) { mfma16_new(c.acc[set][t][q][0], c.ar[f % (G1_PD + 1)], c.hf[0][s]); mfma16_new(c.acc[set][t][q][1], c.ar[f % (G1_PD + 1)], c.hf[1][s]); }
        else { mfma16_acc(c.acc[set][t][q][0], c.ar[f % (G1_PD + 1)], c.hf[0][s]); mfma16_acc(c.acc[set][t][q][1], c.ar[f % (G1_PD + 1)], c.hf[1][s]); }
        SBAR(); }
    else if constexpr (e.type == E_GP) {
        constexpr int grp = e.arg >> 6, k = e.arg & 63, quad = k >> 3, ph = k & 7, set = G1Prog<LATE>::jset(grp);
        constexpr int u = grp < 4 ? quad >> 1 : quad, t = grp < 4 ? quad & 1 : 0;
        const f32x4 g = c.acc[set][t][0][u];
#pragma unroll
        for (int r = 0; r < 4; ++r) {
            if constexpr (ph == 0) c.z[r] = vmul(g[r], g[r]);
            if constexpr (ph == 1) c.z[r] = vfma(c.z[r], c.a1[u], c.a0[u]);
            if constexpr (ph == 2) c.z[r] = vmul(c.z[r], g[r]);
            if constexpr (ph == 3) c.z[r] = __builtin_amdgcn_exp2f(c.z[r]);
            if constexpr (ph == 4) { c.z[r] = vfma(c.z[r], c.kk[u], c.kk[u]); }
            if constexpr (ph == 5) { c.z[r] = __builtin_amdgcn_rcpf(c.z[r]); }
            if constexpr (ph == 6) c.w[r] = vmul(vmul(g[r], c.acc[set][t][1][u][r]), c.z[r]); }
        if constexpr (ph == 7) { c.o[2 * t] = cvtpk(c.w[0], c.w[1]); c.o[2 * t + 1] = cvtpk(c.w[2], c.w[3]); }
        SBAR(); }
    else if constexpr (e.type == E_ST) { constexpr int grp = e.arg >> 1, u = e.arg & 1;
        const unsigned so = c.soffAct + u * (144 * 1024);
        if constexpr (grp < 4) bstore16<ACT_POL>(c.o, srd, c.l16, so + (9 * (c.j >> 1) + 4 * (c.j & 1) + grp) * 1024);
        else bstore8<ACT_POL>((u32x2){c.o[0], c.o[1]}, srd, c.l16 + (c.j & 1) * 8, so + (9 * (c.j >> 1) + 8) * 1024);
        SBAR(); }
}
template <bool LATE, int... I> DI void g1_run(G1State& c, const srd_t srd, std::integer_sequence<int, I...>) { (g1_exec<LATE, I>(c, srd), ...); }
__global__ __launch_bounds__(512, 2) void g1_gateup(char* ws) {
    extern __shared__ __attribute__((aligned(16))) char lds_generic[];
    const int tid = threadIdx.x, lane = tid & 63, wid = rfl(tid >> 6);
    const int b = blockIdx.x, xcd = b & 7, idx = b >> 3, j = (xcd >> 1) * 8 + (idx & 7), mt8 = (xcd & 1) * 4 + (idx >> 3);
    const int mt16 = 16 * mt8 + 2 * wid;
    G1State c;
    RSTAMP(4, 14); STAMP(4, 0);
    const srd_t srd = make_srd(ws, (unsigned)WS_END); c.l16 = lane * 16; c.ldsb = (unsigned)(uintptr_t)lds_generic; c.wid = wid; c.j = j; c.g = lane >> 4;
    c.lb0 = c.ldsb + lane * 16; c.lb1 = c.lb0 + 65536; c.lb2 = c.lb0 + 131072; PINV(c.lb0); PINV(c.lb1); PINV(c.lb2);
    c.soffA = (unsigned)WS_W1 + j * (324 * 1024); c.soffH = (unsigned)WS_HCF + mt16 * (18 * 1024); c.soffS = (unsigned)WS_SSQ + mt16 * 1024;
    c.soffAct = (unsigned)WS_ACT + mt16 * (144 * 1024); c.vq = (lane & 15) * 64 + (lane >> 4) * 16;
    c.sink = 0.f; c.soffT = (unsigned)WS_W2 + (xcd >> 1) * (4 * 324 * 1024) + (idx * 8 + wid) * (41 * 128);
    if (wid >= 4) g1_run<true>(c, srd, std::make_integer_sequence<int, g1prog<true>.n>{});
    else g1_run<false>(c, srd, std::make_integer_sequence<int, g1prog<false>.n>{});
    PINV(c.sink);
    STAMP(4, 11); RSTAMP(4, 15); STAMP_FLUSH(4);
}

constexpr int G2_PD = 4;
template <bool LATE> struct G2Prog {
    Ev ev[700]; int n;
    constexpr void add(int t, int a) { ev[n].type = t; ev[n].arg = a; ev[n].n = 0; ++n; }
    static constexpr int vm(const Ev& e) { return (e.type == E_A || e.type == E_B) ? 1 : 0; }
    static constexpr int NP = LATE ? 4 : 5;
    constexpr G2Prog() : ev{}, n(0) {
        for (int p = 0; p < NP; ++p) add(E_A, p);
        add(E_B, 0); add(E_B, 1);
        add(E_BAR, 0); add(E_STAMP, 1);
        for (int f = 0; f < G2_PD; ++f) add(E_READ, f);
        add(E_B, 2); add(E_B, 3);
        for (int f = 0; f < 162; ++f) {
            const int st = f / 18, i = f % 18;
            if (i % 9 == 0) add(E_WAITB, st * 2 + i / 9);
            const int ia = LATE ? 3 : 1, ib = LATE ? 1 : 3;
            if (st == 0) {
                if (i < NP) add(E_A, 8 + i);
                if (i >= 5 && i < 9) add(E_B, 4 + i - 5);
                if (i >= 9 && i < 9 + NP) add(E_A, 16 + i - 9);
                if (i >= 14) add(E_B, 8 + i - 14);
            } else {
                if (st == 1 && i >= 14) add(E_B, 16 + i - 14);
                if (st + 2 < 9 && i >= ia && (i - ia) % 4 == 0 && (i - ia) / 4 < 4) add(E_A, (st + 2) * 8 + (i - ia) / 4);
                if (st + 2 < 9 && NP == 5 && i == 12) add(E_A, (st + 2) * 8 + 4);
                { const int la = st == 1 ? 2 : 3; if (st + la < 9 && (st == 1 || st + la > 4) && i >= ib && (i - ib) % 4 == 0 && (i - ib) / 4 < 4) add(E_B, (st + la) * 4 + (i - ib) / 4); }
            }
            if (i == 18 - G2_PD && st + 1 < 9) add(E_BAR, st + 1);
            if (f + G2_PD < 162) add(E_READ, f + G2_PD);
            add(E_MMA, f);
            if (i == 17) add(E_STAMP, 2 + st);
        }
        for (int i = 0; i < n; ++i) if (ev[i].type == E_WAITB || ev[i].type == E_BAR) {
            int cnt = 0, j = i - 1;
            for (; j >= 0; --j) {
                if (ev[i].type == E_WAITB && ev[j].type == E_B && ev[j].arg == 2 * ev[i].arg + 1) break;
                if (ev[i].type == E_BAR && ev[j].type == E_A && (ev[j].arg >> 3) == ev[i].arg) break;
                cnt += vm(ev[j]); }
            ev[i].n = cnt > 63 ? 63 : cnt; }
    }
};
template <bool LATE> constexpr G2Prog<LATE> g2prog{};
struct G2State {
    bf16x8 af[4][2][2]; f32x4 acc[9][2]; bf16x8 ar[G2_PD + 1];
    unsigned lb0, lb1, lb2; unsigned l16, ldsb, soffA, soffB; int wid;
};
template <bool LATE, int PC> DI void g2_exec(G2State& c, const srd_t srd) {
    constexpr Ev e = g2prog<LATE>.ev[PC];
    if constexpr (e.type == E_A) { constexpr int st = e.arg >> 3, p = e.arg & 7;
        const unsigned so = c.soffA + st * RING_SLOT, dst = c.ldsb + (st & 3) * RING_SLOT;
        if constexpr (p < 4) bdma_next<p * 1024>(srd, c.l16, so + c.wid * 4096, dst + c.wid * 4096);
        else bdma1(srd, c.l16, so + 32768 + c.wid * 1024, dst + 32768 + c.wid * 1024); }
    else if constexpr (e.type == E_B) { constexpr int st = e.arg >> 2, s2 = (e.arg >> 1) & 1, u = e.arg & 1;
        bload16<((2 * st + s2) & 3) * 1024>(c.af[st & 3][s2][u], srd, c.l16, c.soffB + u * (144 * 1024) + ((2 * st + s2) >> 2) * 4096); }
    else if constexpr (e.type == E_WAITB) { asm volatile("s_waitcnt vmcnt(%2)" : "+v"(c.af[(e.arg >> 1) & 3][e.arg & 1][0]), "+v"(c.af[(e.arg >> 1) & 3][e.arg & 1][1]) : "n"(e.n)); }
    else if constexpr (e.type == E_BAR) { asm volatile("s_waitcnt vmcnt(%0)\n\ts_barrier" :: "n"(e.n) : "memory"); }
    else if constexpr (e.type == E_STAMP) {
    }
    else if constexpr (e.type == E_READ) { constexpr int f = e.arg, st = f / 18, off = (st & 3) * RING_SLOT + (f % 18) * 1024;
        if constexpr ((off >> 16) == 0) c.ar[f % (G2_PD + 1)] = lds_frag((LAS const char*)(uintptr_t)c.lb0 + (off & 65535));
        else c.ar[f % (G2_PD + 1)] = lds_frag((LAS const char*)(uintptr_t)c.lb1 + (off & 65535));
        SBAR(); }
    else if constexpr (e.type == E_MMA) { constexpr int f = e.arg, st = f / 18, s2 = (f % 18) / 9, t = f % 9;
        if constexpr (f < 9) { mfma16_new(c.acc[t][0], c.ar[f % (G2_PD + 1)], c.af[st & 3][s2][0]); mfma16_new(c.acc[t][1], c.ar[f % (G2_PD + 1)], c.af[st & 3][s2][1]); }
        else { mfma16_acc(c.acc[t][0], c.ar[f % (G2_PD + 1)], c.af[st & 3][s2][0]); mfma16_acc(c.acc[t][1], c.ar[f % (G2_PD + 1)], c.af[st & 3][s2][1]); }
        SBAR(); }
}
template <bool LATE, int... I> DI void g2_run(G2State& c, const srd_t srd, std::integer_sequence<int, I...>) { (g2_exec<LATE, I>(c, srd), ...); }
__global__ __launch_bounds__(512, 2) void g2_down(char* ws, char* out) {
    extern __shared__ __attribute__((aligned(16))) char lds_generic[];
    LAS char* lds = (LAS char*)lds_generic;
    const int tid = threadIdx.x, lane = tid & 63, wid = rfl(tid >> 6), mi = wid & 3, kj = wid >> 2;
    const int b = blockIdx.x, xcd = b & 7, idx = b >> 3, ksl = xcd >> 1, mt = (xcd & 1) * 8 + (idx & 7), n4 = idx >> 3;
    const int mt16 = 8 * mt + 2 * mi;
    G2State c;
    RSTAMP(5, 14); STAMP(5, 0);
    const srd_t srd = make_srd(ws, (unsigned)WS_END); c.l16 = lane * 16; c.ldsb = (unsigned)(uintptr_t)lds_generic; c.wid = wid;
    c.lb0 = c.ldsb + kj * (18 * 1024) + lane * 16; c.lb1 = c.lb0 + 65536; PINV(c.lb0); PINV(c.lb1);
    c.soffA = (unsigned)WS_W2 + (ksl * 4 + n4) * (324 * 1024); c.soffB = (unsigned)WS_ACT + (mt16 * 144 + 36 * ksl + 18 * kj) * 1024;
    if (wid >= 4) g2_run<true>(c, srd, std::make_integer_sequence<int, g2prog<true>.n>{});
    else g2_run<false>(c, srd, std::make_integer_sequence<int, g2prog<false>.n>{});
    asm volatile("s_nop 7" : "+v"(c.acc[8][0]), "+v"(c.acc[8][1]), "+v"(c.acc[7][0]), "+v"(c.acc[7][1]));
    STAMP(5, 11);
    LBAR();
    LAS char* ex = lds + mi * 18432 + lane * 16;
    if (kj == 0) {
#pragma unroll
        for (int t = 5; t < 9; ++t) { *(LAS f32x4*)(ex + (2 * t) * 1024) = c.acc[t][0]; *(LAS f32x4*)(ex + (2 * t + 1) * 1024) = c.acc[t][1]; }
    } else {
#pragma unroll
        for (int t = 0; t < 5; ++t) { *(LAS f32x4*)(ex + (2 * t) * 1024) = c.acc[t][0]; *(LAS f32x4*)(ex + (2 * t + 1) * 1024) = c.acc[t][1]; }
    }
    LBAR();
    const int tile = mt * 4 + n4; const unsigned xb = (unsigned)WS_PART + tile * (16 * 9 * 1024);
    auto fin = [&](int t) {
        const f32x4 a0 = c.acc[t][0] + *(LAS const f32x4*)(ex + (2 * t) * 1024), a1 = c.acc[t][1] + *(LAS const f32x4*)(ex + (2 * t + 1) * 1024);
        if (mi == ksl) { *(LAS f32x4*)(ex + (2 * t) * 1024) = a0; *(LAS f32x4*)(ex + (2 * t + 1) * 1024) = a1; }
        else bstore16<1>((u32x4){cvtpk(a0[0], a0[1]), cvtpk(a0[2], a0[3]), cvtpk(a1[0], a1[1]), cvtpk(a1[2], a1[3])}, srd, lane * 16, xb + ((mi * 4 + ksl) * 9 + t) * 1024); };
    if (kj == 0) { fin(0); fin(1); fin(2); fin(3); fin(4); } else { fin(5); fin(6); fin(7); fin(8); }
    u32x2 hv[3]; u32x4 pv[3]; u32x2 pw[3];
    const unsigned hb = (unsigned)WS_HC + ((8 * mt + 2 * ksl) * 36 + 9 * n4) * 512;
    bload8<0>(hv[0], srd, lane * 8, hb + wid * 512); bload8<0>(hv[1], srd, lane * 8, hb + (36 + wid) * 512); bload8<0>(hv[2], srd, lane * 8, hb + ((wid & 1) * 36 + 8) * 512);
    asm volatile("s_waitcnt vmcnt(0) lgkmcnt(0)\n\ts_barrier" : "+v"(hv[0]), "+v"(hv[1]), "+v"(hv[2]) :: "memory");
    STAMP(5, 12);
    LAS unsigned* flag = (LAS unsigned*)(lds + 4 * 18432);
    if (wid == 0) {
        unsigned* cnt = (unsigned*)(ws + WS_CNT) + tile * 64; unsigned bad = 0;
        if (lane == 0) {
            __hip_atomic_fetch_add(cnt, 1u, __ATOMIC_RELAXED, __HIP_MEMORY_SCOPE_AGENT);
            const unsigned long long t0 = __builtin_amdgcn_s_memrealtime();
            while (__hip_atomic_load(cnt, __ATOMIC_RELAXED, __HIP_MEMORY_SCOPE_AGENT) < 4u) {
                if (__builtin_amdgcn_s_memrealtime() - t0 > 2000000ull) { bad = 1; break; }
                __builtin_amdgcn_s_sleep(1); }
            *flag = bad; }
    }
    LBAR();
    const bool bad = *flag != 0;
#pragma unroll
    for (int k = 0; k < 3; ++k) { const unsigned sb = xb + (ksl * 4 + (k + (k >= ksl))) * (9 * 1024);
        bload16u_sc1<0>(pv[k], srd, lane * 16, sb + wid * 1024); bload8_sc1<0>(pw[k], srd, lane * 16 + (wid & 1) * 8, sb + 8 * 1024); }
    asm volatile("s_waitcnt vmcnt(0)" : "+v"(pv[0]), "+v"(pv[1]), "+v"(pv[2]), "+v"(pw[0]), "+v"(pw[1]), "+v"(pw[2]) :: "memory");
    const int m = lane & 15, g = lane >> 4;
    auto put = [&](int t, int u, u32x2 h2, u32x2 p0, u32x2 p1, u32x2 p2) {
        f32x4 a = *(LAS const f32x4*)(lds + ksl * 18432 + (2 * t + u) * 1024 + lane * 16);
        a += (f32x4){bflo(h2[0]), bfhi(h2[0]), bflo(h2[1]), bfhi(h2[1])}; a += (f32x4){bflo(p0[0]), bfhi(p0[0]), bflo(p0[1]), bfhi(p0[1])};
        a += (f32x4){bflo(p1[0]), bfhi(p1[0]), bflo(p1[1]), bfhi(p1[1])}; a += (f32x4){bflo(p2[0]), bfhi(p2[0]), bflo(p2[1]), bfhi(p2[1])};
        u32x2 o = {cvtpk(a[0], a[1]), cvtpk(a[2], a[3])};
        if (bad) o = (u32x2){0x7fc07fc0u, 0x7fc07fc0u};
        *(u32x2*)(out + (size_t)(128 * mt + 32 * ksl + 16 * u + m) * (D_ * 2) + (144 * n4 + 16 * t + 4 * g) * 2) = o; };
    put(wid, 0, hv[0], (u32x2){pv[0][0], pv[0][1]}, (u32x2){pv[1][0], pv[1][1]}, (u32x2){pv[2][0], pv[2][1]});
    put(wid, 1, hv[1], (u32x2){pv[0][2], pv[0][3]}, (u32x2){pv[1][2], pv[1][3]}, (u32x2){pv[2][2], pv[2][3]});
    if (wid < 2) put(8, wid, hv[2], pw[0], pw[1], pw[2]);
    STAMP(5, 13); RSTAMP(5, 15); STAMP_FLUSH(5);
}

}

extern "C" void kernel_launch(void* const* d_in, const int* in_sizes, int n_in, void* d_out, int out_size, void* d_ws, size_t ws_size, hipStream_t stream) {
    const char* x = (const char*)d_in[0]; const char* wqkv = (const char*)d_in[1]; const char* wo = (const char*)d_in[2];
    const char* wg = (const char*)d_in[4]; const char* wu = (const char*)d_in[5]; const char* wd = (const char*)d_in[6];
    char* ws = (char*)d_ws;
    (void)hipFuncSetAttribute((const void*)k1_qkv, hipFuncAttributeMaxDynamicSharedMemorySize, K1_LDS);
    (void)hipFuncSetAttribute((const void*)k2_attn, hipFuncAttributeMaxDynamicSharedMemorySize, K2_LDS);
    (void)hipFuncSetAttribute((const void*)k3_oproj, hipFuncAttributeMaxDynamicSharedMemorySize, K3_LDS);
    (void)hipFuncSetAttribute((const void*)g1_gateup, hipFuncAttributeMaxDynamicSharedMemorySize, G_LDS);
    (void)hipFuncSetAttribute((const void*)g2_down, hipFuncAttributeMaxDynamicSharedMemorySize, G_LDS);
    k1_qkv<<<256, 512, K1_LDS, stream>>>(x, wqkv, wo, ws);
    k2_attn<<<256, 1024, K2_LDS, stream>>>(x, wg, wu, wd, ws);
    k3_oproj<<<192, 512, K3_LDS, stream>>>(x, ws);
    g1_gateup<<<256, 512, G_LDS, stream>>>(ws);
    g2_down<<<256, 512, G_LDS, stream>>>(ws, (char*)d_out);
}
```

```cpp
#include <hip/hip_runtime.h>
#include <hip/hip_bf16.h>
#include <stdint.h>
#include <utility>
#define K1_TOUCH 0
#define TOUCH 1
#define K2_RT 1
#define K2_PRIO 1
#define K1_ROT 0
#define G1PD 4
#define ACT_POL 1
#define RT_POL 1
#define PART_POL 1
#define H_POL 1
#define QKV_POL 1
#define G1_PRIO 1

namespace {

typedef __bf16 bf16x2 __attribute__((ext_vector_type(2)));
typedef __bf16 bf16x8 __attribute__((ext_vector_type(8)));
typedef __bf16 bf16x16 __attribute__((ext_vector_type(16)));
typedef short s16x4 __attribute__((ext_vector_type(4)));
typedef float f32x2 __attribute__((ext_vector_type(2)));
typedef float f32x4 __attribute__((ext_vector_type(4)));
typedef float f32x8 __attribute__((ext_vector_type(8)));
typedef float f32x16 __attribute__((ext_vector_type(16)));
typedef unsigned u32x2 __attribute__((ext_vector_type(2)));
typedef unsigned u32x4 __attribute__((ext_vector_type(4)));
#define LAS __attribute__((address_space(3)))
#define DI __device__ __forceinline__

constexpr int S_ = 2048, D_ = 576, H_ = 32, F_ = 4608;
constexpr float EPS = 1e-5f;
constexpr float QSCALE = 0.23570226039551584f * 1.4426950408889634f;
constexpr float SAFE_B = 48.f;

constexpr int KVCHUNK = 19456, KVIMG = 8 * KVCHUNK;
constexpr bool OVERLAY = true;
constexpr size_t WS_QM   = 0;
constexpr size_t WS_QT   = WS_QM + (size_t)32 * 2048 * 32;
constexpr size_t WS_KV   = WS_QT + (size_t)32 * 2048 * 4;
constexpr size_t WS_OF   = WS_KV + (size_t)32 * KVIMG;
constexpr size_t WS_WO   = WS_OF + (size_t)128 * 18 * 1024;
constexpr size_t WS_A_END = WS_WO + (size_t)6 * 108 * 1024;
constexpr size_t WS_ACT  = OVERLAY ? 0 : WS_A_END;
constexpr size_t WS_KMAX = WS_ACT + (size_t)128 * 144 * 1024;
constexpr size_t WS_MUX  = WS_KMAX + 32 * 16 * 4;
constexpr size_t WS_WBAR = WS_MUX + 2048 * 4;
constexpr size_t WS_SSQ  = WS_WBAR + 4096;
constexpr size_t WS_CNT  = WS_SSQ + (size_t)2048 * 64;
constexpr size_t WS_HC   = WS_CNT + 96 * 256;
constexpr size_t WS_HCF  = WS_HC + (size_t)128 * 36 * 512;
constexpr size_t WS_W2   = WS_HCF + (size_t)128 * 18 * 1024;
constexpr size_t WS_W1   = WS_W2 + (size_t)16 * 324 * 1024;
constexpr size_t WS_PART = WS_W1 + (size_t)32 * 324 * 1024;
static_assert(WS_A_END <= (size_t)128 * 144 * 1024, "overlay fits");
constexpr size_t WS_END  = WS_PART + (size_t)64 * 16 * 9 * 1024;

#define STAMP_DECL
#define STAMP(kid, i)
#define RSTAMP(kid, i)
#define STAMP_FLUSH(kid)
DI unsigned cvtpk(float lo, float hi) { f32x2 v = {lo, hi}; bf16x2 b = __builtin_convertvector(v, bf16x2); return __builtin_bit_cast(unsigned, b); }
DI float bflo(unsigned w) { return __uint_as_float(w << 16); }
DI float bfhi(unsigned w) { return __uint_as_float(w & 0xffff0000u); }
DI float dot2(unsigned a, unsigned b, float c) { return __builtin_amdgcn_fdot2_f32_bf16(__builtin_bit_cast(bf16x2, a), __builtin_bit_cast(bf16x2, b), c, false); }
DI int rfl(int x) { return __builtin_amdgcn_readfirstlane(x); }
DI float xor32(float v) { auto r = __builtin_amdgcn_permlane32_swap(__float_as_uint(v), __float_as_uint(v), false, false); return __uint_as_float((threadIdx.x & 32) ? r[0] : r[1]); }
DI float xor16(float v) { auto r = __builtin_amdgcn_permlane16_swap(__float_as_uint(v), __float_as_uint(v), false, false); return __uint_as_float((threadIdx.x & 16) ? r[0] : r[1]); }
DI f32x4 mfma16(bf16x8 a, bf16x8 b, f32x4 c) { return __builtin_amdgcn_mfma_f32_16x16x32_bf16(a, b, c, 0, 0, 0); }
DI void mfma16_acc(f32x4& c, bf16x8 a, bf16x8 b) { asm volatile("v_mfma_f32_16x16x32_bf16 %0, %1, %2, %0" : "+v"(c) : "v"(a), "v"(b)); }
DI void mfma16_new(f32x4& c, bf16x8 a, bf16x8 b) { asm volatile("v_mfma_f32_16x16x32_bf16 %0, %1, %2, 0" : "=&v"(c) : "v"(a), "v"(b)); }
DI void glds16(const void* sbase, unsigned voff, unsigned lds_dst) {
    asm volatile("s_mov_b32 m0, %2\n\ts_nop 0\n\tglobal_load_lds_dwordx4 %0, %1" :: "v"(voff), "s"(sbase), "s"(lds_dst) : "memory");
}
DI void gload16(bf16x8& dst, const void* sbase, unsigned voff) { asm volatile("global_load_dwordx4 %0, %1, %2" : "=v"(dst) : "v"(voff), "s"(sbase)); }
DI void gload16f(f32x4& dst, const void* sbase, unsigned voff) { asm volatile("global_load_dwordx4 %0, %1, %2" : "=v"(dst) : "v"(voff), "s"(sbase)); }
DI void gload8(u32x2& dst, const void* sbase, unsigned voff) { asm volatile("global_load_dwordx2 %0, %1, %2" : "=v"(dst) : "v"(voff), "s"(sbase)); }
DI void gload4(float& dst, const void* sbase, unsigned voff) { asm volatile("global_load_dword %0, %1, %2" : "=v"(dst) : "v"(voff), "s"(sbase)); }
typedef __amdgpu_buffer_rsrc_t srd_t;
DI srd_t make_srd(const void* p, unsigned bytes) { return __builtin_amdgcn_make_buffer_rsrc((void*)p, (short)0, (int)bytes, 0x00020000); }
template <int IMM> DI void bload16(bf16x8& dst, srd_t r, unsigned voff, unsigned soff) {
    asm volatile("buffer_load_dwordx4 %0, %1, %2, %3 offen offset:%4" : "=v"(dst) : "v"(voff), "s"(r), "s"(soff), "n"(IMM)); }
template <int IMM> DI void bload16f(f32x4& dst, srd_t r, unsigned voff, unsigned soff) {
    asm volatile("buffer_load_dwordx4 %0, %1, %2, %3 offen offset:%4" : "=v"(dst) : "v"(voff), "s"(r), "s"(soff), "n"(IMM)); }
template <int IMM> DI void bload4(float& dst, srd_t r, unsigned voff, unsigned soff) {
    asm volatile("buffer_load_dword %0, %1, %2, %3 offen offset:%4" : "=v"(dst) : "v"(voff), "s"(r), "s"(soff), "n"(IMM)); }
template <int IMM> DI void bload16_sc1(bf16x8& dst, srd_t r, unsigned voff, unsigned soff) {
    asm volatile("buffer_load_dwordx4 %0, %1, %2, %3 offen offset:%4 sc1" : "=v"(dst) : "v"(voff), "s"(r), "s"(soff), "n"(IMM)); }
template <int IMM> DI void bload16u_sc1(u32x4& dst, srd_t r, unsigned voff, unsigned soff) {
    asm volatile("buffer_load_dwordx4 %0, %1, %2, %3 offen offset:%4 sc1" : "=v"(dst) : "v"(voff), "s"(r), "s"(soff), "n"(IMM)); }
template <int IMM> DI void bload8_sc1(u32x2& dst, srd_t r, unsigned voff, unsigned soff) {
    asm volatile("buffer_load_dwordx2 %0, %1, %2, %3 offen offset:%4 sc1" : "=v"(dst) : "v"(voff), "s"(r), "s"(soff), "n"(IMM)); }
template <int IMM> DI void bload8(u32x2& dst, srd_t r, unsigned voff, unsigned soff) {
    asm volatile("buffer_load_dwordx2 %0, %1, %2, %3 offen offset:%4" : "=v"(dst) : "v"(voff), "s"(r), "s"(soff), "n"(IMM)); }
DI void touch64(float& sink, srd_t r, unsigned lane128, unsigned soff) { asm volatile("buffer_load_dword %0, %1, %2, %3 offen" : "+v"(sink) : "v"(lane128), "s"(r), "s"(soff)); }
DI void bdma4(srd_t r, unsigned voff, unsigned soff, unsigned lds_dst) {
    asm volatile("s_mov_b32 m0, %3\n\ts_nop 0\n\tbuffer_load_dwordx4 %0, %1, %2 offen lds\n\tbuffer_load_dwordx4 %0, %1, %2 offen offset:1024 lds\n\t"
                 "buffer_load_dwordx4 %0, %1, %2 offen offset:2048 lds\n\tbuffer_load_dwordx4 %0, %1, %2 offen offset:3072 lds" :: "v"(voff), "s"(r), "s"(soff), "s"(lds_dst) : "memory"); }
DI void bdma1(srd_t r, unsigned voff, unsigned soff, unsigned lds_dst) {
    asm volatile("s_mov_b32 m0, %3\n\ts_nop 0\n\tbuffer_load_dwordx4 %0, %1, %2 offen lds" :: "v"(voff), "s"(r), "s"(soff), "s"(lds_dst) : "memory"); }
template <int IMM> DI void bdma_next(srd_t r, unsigned voff, unsigned soff, unsigned lds_dst) {
    asm volatile("s_mov_b32 m0, %3\n\ts_nop 0\n\tbuffer_load_dwordx4 %0, %1, %2 offen offset:%4 lds" :: "v"(voff), "s"(r), "s"(soff), "s"(lds_dst), "n"(IMM) : "memory"); }
template <int POL> DI void bstore16(u32x4 d, srd_t r, unsigned voff, unsigned soff) {
    if constexpr (POL == 1) asm volatile("buffer_store_dwordx4 %0, %1, %2, %3 offen sc1\n\ts_nop 1" :: "v"(d), "v"(voff), "s"(r), "s"(soff) : "memory");
    else if constexpr (POL == 2) asm volatile("buffer_store_dwordx4 %0, %1, %2, %3 offen nt\n\ts_nop 1" :: "v"(d), "v"(voff), "s"(r), "s"(soff) : "memory");
    else asm volatile("buffer_store_dwordx4 %0, %1, %2, %3 offen\n\ts_nop 1" :: "v"(d), "v"(voff), "s"(r), "s"(soff) : "memory"); }
template <int POL> DI void bstore8(u32x2 d, srd_t r, unsigned voff, unsigned soff) {
    if constexpr (POL == 1) asm volatile("buffer_store_dwordx2 %0, %1, %2, %3 offen sc1\n\ts_nop 1" :: "v"(d), "v"(voff), "s"(r), "s"(soff) : "memory");
    else if constexpr (POL == 2) asm volatile("buffer_store_dwordx2 %0, %1, %2, %3 offen nt\n\ts_nop 1" :: "v"(d), "v"(voff), "s"(r), "s"(soff) : "memory");
    else asm volatile("buffer_store_dwordx2 %0, %1, %2, %3 offen\n\ts_nop 1" :: "v"(d), "v"(voff), "s"(r), "s"(soff) : "memory"); }
#define PINV(x) asm volatile("" : "+v"(x))
#define WAITV(n) asm volatile("s_waitcnt vmcnt(%0)" :: "n"(n) : "memory")
#define WAIT_BAR(n) asm volatile("s_waitcnt vmcnt(%0) lgkmcnt(0)\n\ts_barrier" :: "n"(n) : "memory")
#define SBAR() __builtin_amdgcn_sched_barrier(0)
#define LBAR() asm volatile("s_waitcnt lgkmcnt(0)\n\ts_barrier" ::: "memory")
DI bf16x8 lds_frag(LAS const char* p) { return *(LAS const bf16x8*)p; }
typedef short v4i16_t __attribute__((ext_vector_type(4)));
DI u32x2 lds_tr(LAS const char* p) { return __builtin_bit_cast(u32x2, __builtin_amdgcn_ds_read_tr16_b64_v4i16((LAS v4i16_t*)p)); }
DI bf16x8 tr_frag(LAS const char* p, int off2) { u32x2 a = lds_tr(p), b = lds_tr(p + off2); u32x4 r = {a[0], a[1], b[0], b[1]}; return __builtin_bit_cast(bf16x8, r); }

DI int kstep_to_ftile(int ks, int half) {
    const int a = ks / 9, idx = ks - 9 * a;
    if (idx == 8) return 9 * (2 * a + half) + 8;
    return 9 * (2 * a + (idx >> 2)) + 2 * (idx & 3) + half;
}

struct RetileJob { const char* src; unsigned voff; unsigned dst[4]; bool live; };
DI void retile_setup(RetileJob& J, int b, int lane, const void* wo, const void* wg, const void* wu, const void* wd) {
    J.live = b < 4050;
    const int r8 = lane >> 3, ch = lane & 7;
    if (b < 2592) {
        const int gu = b >= 1296, bb = b - 1296 * gu, s = bb / 72, cb = bb - 72 * s;
        J.src = (const char*)(gu ? wu : wg) + (size_t)s * 32 * (F_ * 2) + cb * 128;
        J.voff = r8 * (F_ * 2) + ch * 16;
#pragma unroll
        for (int u = 0; u < 4; ++u) { const int T = 4 * cb + u, j = T / 9, tt = T - 9 * j;
            J.dst[u] = (unsigned)(WS_W1 + (size_t)(j * 324 + (tt < 8 ? (tt >> 1) * 72 + s * 4 + (tt & 1) * 2 : 288 + s * 2) + gu) * 1024); }
    } else if (b < 3888) {
        const int bb = b - 2592, ks = bb / 9, cb = bb - 9 * ks;
        J.src = (const char*)wd + cb * 128;
        J.voff = (unsigned)ks;
        const int ksl = ks / 36, kap = ks - 36 * ksl, kj = kap / 18, r = kap - 18 * kj, st = r >> 1, s2 = r & 1;
#pragma unroll
        for (int u = 0; u < 4; ++u) { const int T = 4 * cb + u, n4 = T / 9, tt = T - 9 * n4;
            J.dst[u] = (unsigned)(WS_W2 + (size_t)((ksl * 4 + n4) * 324 + ((st * 2 + kj) * 2 + s2) * 9 + tt) * 1024); }
    } else {
        const int bb = (b < 4050 ? b : 4049) - 3888, s = bb / 9, cb = bb - 9 * s;
        J.src = (const char*)wo + (size_t)s * 32 * (D_ * 2) + cb * 128;
        J.voff = ch * 16;
#pragma unroll
        for (int u = 0; u < 4; ++u) { const int T = 4 * cb + u, n6 = T / 6, tt = T - 6 * n6;
            J.dst[u] = (unsigned)(WS_WO + (size_t)(n6 * 108 + s * 6 + tt) * 1024); }
    }
}
DI void retile_issue(const RetileJob& J, int b, int lane, unsigned lds_scratch) {
    const int r8 = lane >> 3, ch = lane & 7;
#pragma unroll
    for (int p = 0; p < 4; ++p) {
        unsigned voff;
        if (b < 2592) voff = J.voff + p * 8 * (F_ * 2);
        else if (b < 3888) { const int rho = 8 * p + r8, T = kstep_to_ftile((int)J.voff, rho >> 4); voff = (16 * T + (rho & 15)) * (D_ * 2) + ch * 16; }
        else { const int rho = 8 * p + r8, k = 8 * ((rho >> 2) & 3) + 4 * (rho >> 4) + (rho & 3); voff = J.voff + k * (D_ * 2); }
        glds16(J.src, voff, lds_scratch + p * 1024);
    }
}
DI void retile_finish(const RetileJob& J, int lane, LAS const char* scratch, char* ws) {
    const int g = lane >> 4, q = (lane >> 2) & 3, p = lane & 3;
    LAS const char* base = scratch + (4 * g + q) * 128 + p * 8;
#pragma unroll
    for (int u = 0; u < 4; ++u) { const bf16x8 f = tr_frag(base + 32 * u, 16 * 128); if (J.live) *(bf16x8*)(ws + J.dst[u] + lane * 16) = f; }
}

constexpr int K1_SLOT = 34 * 1024, K1_STG = 0, K1_SMALL = 4 * K1_SLOT, K1_LDS = K1_SMALL + 4096;
template <int PART, int NJ>
DI void k1_wave(const char* x, const char* wqkv, char* ws, char* lds_generic, int mt, int nt, int rot, int shx, int shw) {
    constexpr bool VPART = PART == 2; constexpr int NT = NJ ? 4 : 5, T0 = NJ ? 5 : 0;
    LAS char* lds = (LAS char*)lds_generic; const unsigned ldsb = (unsigned)(uintptr_t)lds_generic;
    const int tid = threadIdx.x, lane = tid & 63, wid = rfl(tid >> 6), mi = wid & 3, m = lane & 15, g = lane >> 4;
    STAMP_DECL; RSTAMP(1, 14); STAMP(1, 0);
    const srd_t sx = make_srd(x, S_ * D_ * 2), sw = make_srd(wqkv, D_ * 3 * D_ * 2);
    const unsigned xv = (lane >> 3) * (D_ * 2) + (((lane & 7) ^ ((4 * wid + (lane >> 4)) & 7)) << 4);
    unsigned wv[2];
#pragma unroll
    for (int i = 0; i < 2; ++i) { const int idx = (i ? 8 : wid) * 64 + lane, rho = (idx * 3641) >> 16, ch = idx - 18 * rho;
        wv[i] = (8 * ((rho >> 2) & 3) + 4 * (rho >> 4) + (rho & 3)) * 3456 + ch * 16; }
    const unsigned xs = (128 * mt + 8 * wid) * (D_ * 2), wsb = nt * 288;
    auto issue = [&](int st, int p) {
        int k = st + rot; k = k >= 9 ? k - 9 : k; const unsigned dst = ldsb + (st & 3) * K1_SLOT;
        if (p < 2) bdma1(sx, xv, xs + p * 64 * (D_ * 2) + k * 128, dst + (wid + 8 * p) * 1024);
        else if (p < 4) bdma1(sw, wv[0], wsb + (64 * k + 32 * (p - 2)) * 3456, dst + 16384 + ((p - 2) * 9 + wid) * 1024);
        else if (wid < 2) bdma1(sw, wv[1], wsb + (64 * k + 32 * wid) * 3456, dst + 16384 + (wid * 9 + 8) * 1024); };
#pragma unroll
    for (int st = 0; st < 3; ++st)
#pragma unroll
        for (int p = 0; p < 5; ++p) issue(st, p);
    float tch[2] = {0.f, 0.f};
#pragma unroll
    for (int i = 0; i < 2 * K1_TOUCH; ++i) { int t = wid + 8 * i; t = t >= 12 ? t - 4 : t;
        if (t < 3) { const int L = shx * 192 + t * 64 + lane, row = (L * 7282) >> 16; bload4<0>(tch[i], sx, (128 * mt + row) * (D_ * 2) + (L - 9 * row) * 128, 0); }
        else { const int q = (t - 3) * 64 + lane; bload4<0>(tch[i], sw, (shw * 144 + (q >> 2)) * 3456 + ((nt * 288) & ~127) + (q & 3) * 128, 0); } }
    f32x4 acc[NT][2];
    f32x4 accs = {0, 0, 0, 0}, accg = {0, 0, 0, 0}, accc[NT];
#pragma unroll
    for (int t = 0; t < NT; ++t) accc[t] = (f32x4){0, 0, 0, 0};
    bf16x8 ones = __builtin_bit_cast(bf16x8, (u32x4){0x3f803f80u, 0x3f803f80u, 0x3f803f80u, 0x3f803f80u}); PINV(ones);
    asm volatile("s_nop 1" : "+v"(accs), "+v"(accg));
#pragma unroll
    for (int t = 0; t < NT; ++t) PINV(accc[t]);
    const unsigned xa0 = (32 * mi + m) * 128 + ((g ^ ((m >> 1) & 7)) << 4), xa1 = (32 * mi + m) * 128 + (((4 + g) ^ ((m >> 1) & 7)) << 4);
    const unsigned wa = 16384 + (4 * g + ((lane >> 2) & 3)) * 288 + (lane & 3) * 8 + T0 * 32;
    bf16x8 xq[2][2][2], wr[2][2][NT];
    auto rd = [&](int st, int i) {
        LAS const char* sb = lds + (st & 3) * K1_SLOT;
        if (i < 4) xq[st & 1][i >> 1][i & 1] = lds_frag(sb + ((i >> 1) ? xa1 : xa0) + (i & 1) * 2048);
        else { const int s2 = (i - 4) / NT, t = (i - 4) % NT; wr[st & 1][s2][t] = tr_frag(sb + wa + s2 * 9216 + 32 * t, 16 * 288); } };
    STAMP(1, 1);
    WAIT_BAR(8 + K1_TOUCH * 2);
    STAMP(1, 2);
#pragma unroll
    for (int i = 0; i < 4 + 2 * NT; ++i) rd(0, i);
#pragma unroll
    for (int st = 0; st < 9; ++st) {
        if (st < 8) { if (st < 2) WAIT_BAR(4 + K1_TOUCH * 2); else if (st < 6) WAIT_BAR(4); else if (st == 6) WAIT_BAR(4); else WAIT_BAR(0); }
        if (st == 2) { PINV(tch[0]); PINV(tch[1]); }
#pragma unroll
        for (int i = 0; i < 2 * NT; ++i) { const int s2 = i / NT, t = i % NT; const bf16x8 w = wr[st & 1][s2][t], x0 = xq[st & 1][s2][0], x1 = xq[st & 1][s2][1];
            if (st == 0 && s2 == 0) { if (VPART) { mfma16_new(acc[t][0], x0, w); mfma16_new(acc[t][1], x1, w); } else { mfma16_new(acc[t][0], w, x0); mfma16_new(acc[t][1], w, x1); } }
            else { if (VPART) { mfma16_acc(acc[t][0], x0, w); mfma16_acc(acc[t][1], x1, w); } else { mfma16_acc(acc[t][0], w, x0); mfma16_acc(acc[t][1], w, x1); } }
            if (st + 1 < 9) { if (i < 4) { rd(st + 1, i); rd(st + 1, 4 + i); } else if (4 + i < 4 + 2 * NT) rd(st + 1, 4 + i); }
            if (st + 3 < 9 && (i & 1) == 0 && i / 2 < 4) issue(st + 3, i / 2 == 0 ? 0 : i / 2 == 1 ? 2 : i / 2 == 2 ? 1 : 3);
            if (st + 3 < 9 && i == 7) issue(st + 3, 4);
            if (t == 1) mfma16_acc(accs, ones, NJ ? x1 : x0);
            if (t == 2) mfma16_acc(accg, NJ ? x1 : x0, NJ ? x1 : x0);
            if (t == NT - 1 && ((2 * st + s2) & 3) == mi) {
#pragma unroll
                for (int tt = 0; tt < NT; ++tt) { if (VPART) mfma16_acc(accc[tt], ones, wr[st & 1][s2][tt]); else mfma16_acc(accc[tt], wr[st & 1][s2][tt], ones); } }
            SBAR(); }
    }
    STAMP(1, 3);
    LAS float* sm = (LAS float*)(lds + K1_SMALL);
    asm volatile("s_nop 7\n\ts_nop 7" : "+v"(acc[NT - 1][0]), "+v"(acc[NT - 1][1]), "+v"(accs), "+v"(accg));
#pragma unroll
    for (int t = 0; t < NT; ++t) { PINV(accc[t]); PINV(acc[t][0]); PINV(acc[t][1]); }
    if (g == 0) sm[32 * mi + 16 * NJ + m] = accs[0];
    if (g == (m >> 2)) sm[128 + 32 * mi + 16 * NJ + m] = (m & 2) ? ((m & 1) ? accg[3] : accg[2]) : ((m & 1) ? accg[1] : accg[0]);
#pragma unroll
    for (int t = 0; t < NT; ++t) { if (VPART) { if (g == 0) sm[256 + mi * 144 + 16 * (T0 + t) + m] = accc[t][0]; }
        else if (m == 0) *(LAS f32x4*)(sm + 256 + mi * 144 + 16 * (T0 + t) + 4 * g) = accc[t]; }
    LBAR();
    STAMP(1, 4);
    LAS char* stg = lds + K1_STG;
    if (VPART && tid < 256) { const int hs = tid >> 1;
        *(LAS u32x4*)(stg + hs * 320 + ((tid & 1) ? 19 : 4) * 16) = (tid & 1) ? (u32x4){0x3f803f80u, 0x3f803f80u, 0x3f803f80u, 0x3f803f80u} : (u32x4){0, 0, 0, 0}; }
    f32x4 mean[2], rstd[2];
#pragma unroll
    for (int u = 0; u < 2; ++u) { const int tl = 32 * mi + 16 * u + (VPART ? 4 * g : m);
        if (VPART) { mean[u] = *(LAS const f32x4*)(sm + tl) * (1.f / D_);
            const f32x4 var = *(LAS const f32x4*)(sm + 128 + tl) * (1.f / D_) - mean[u] * mean[u];
#pragma unroll
            for (int r = 0; r < 4; ++r) rstd[u][r] = __builtin_amdgcn_rsqf(fmaxf(var[r], 0.f) + EPS); }
        else { mean[u][0] = sm[tl] * (1.f / D_);
            rstd[u][0] = __builtin_amdgcn_rsqf(fmaxf(sm[128 + tl] * (1.f / D_) - mean[u][0] * mean[u][0], 0.f) + EPS) * (PART == 0 ? QSCALE : 1.f); } }
#pragma unroll
    for (int t = 0; t < NT; ++t) { const int tt = T0 + t;
        if (VPART) { const int n = 16 * tt + m, hl = (n * 3641) >> 16, d = n - 18 * hl, row = d + (d >= 4 ? 5 : 0) - (d >= 12 ? 12 : 0) + (d >= 16 ? 8 : 0);
            const float c1 = (sm[256 + n] + sm[400 + n]) + (sm[544 + n] + sm[688 + n]);
            LAS char* p = stg + hl * 5120 + 2 * mi * 640 + (g >> 1) * 320 + row * 16 + (g & 1) * 8;
#pragma unroll
            for (int u = 0; u < 2; ++u) { const f32x4 a = (acc[t][u] - mean[u] * c1) * rstd[u];
                *(LAS u32x2*)(p + u * 640) = (u32x2){cvtpk(a[0], a[1]), cvtpk(a[2], a[3])}; }
        } else {
            const int n0 = 16 * tt + 4 * g;
            const f32x4 c4 = (*(LAS const f32x4*)(sm + 256 + n0) + *(LAS const f32x4*)(sm + 400 + n0)) + (*(LAS const f32x4*)(sm + 544 + n0) + *(LAS const f32x4*)(sm + 688 + n0));
            int o[2], ts[2];
#pragma unroll
            for (int p = 0; p < 2; ++p) { const int n = n0 + 2 * p, hl = (n * 3641) >> 16, d = n - 18 * hl; const bool tail = d == 16;
                if (PART == 0) { o[p] = tail ? 32768 + hl * 512 : hl * 4096 + d * 2; ts[p] = tail ? 4 : 32; }
                else { o[p] = hl * 4608 + (tail ? 4096 : (d >> 3) * 2048 + (d & 7) * 2); ts[p] = tail ? 4 : 16; } }
#pragma unroll
            for (int u = 0; u < 2; ++u) { const f32x4 a = (acc[t][u] - c4 * mean[u][0]) * rstd[u][0]; const int tl = 32 * mi + 16 * u + m;
                *(LAS unsigned*)(stg + o[0] + tl * ts[0]) = cvtpk(a[0], a[1]); *(LAS unsigned*)(stg + o[1] + tl * ts[1]) = cvtpk(a[2], a[3]); }
        } }
    if (nt == 0 && tid < 128) ((float*)(ws + WS_MUX))[128 * mt + tid] = sm[tid] * (1.f / D_);
    LBAR();
    STAMP(1, 5);
    const int head = 8 * (nt & 3) + wid; const srd_t sws = make_srd(ws, (unsigned)WS_END); const unsigned l16 = lane * 16;
    if (PART == 2) { const unsigned dst = (unsigned)WS_KV + head * KVIMG + (mt >> 1) * KVCHUNK + 9216 + (mt & 1) * 5120;
#pragma unroll
        for (int i = 0; i < 5; ++i) bstore16<QKV_POL>(*(LAS const u32x4*)(stg + wid * 5120 + i * 1024 + l16), sws, l16, dst + i * 1024);
    } else if (PART == 0) { const unsigned dst = (unsigned)WS_QM + (head * S_ + 128 * mt) * 32;
#pragma unroll
        for (int i = 0; i < 4; ++i) bstore16<QKV_POL>(*(LAS const u32x4*)(stg + wid * 4096 + i * 1024 + l16), sws, l16, dst + i * 1024);
        if (lane < 32) bstore16<QKV_POL>(*(LAS const u32x4*)(stg + 32768 + wid * 512 + l16), sws, l16, (unsigned)WS_QT + (head * S_ + 128 * mt) * 4);
    } else { const unsigned dst = (unsigned)WS_KV + head * KVIMG + (mt >> 1) * KVCHUNK; LAS const char* hb = stg + wid * 4608;
#pragma unroll
        for (int i = 0; i < 4; ++i) bstore16<QKV_POL>(*(LAS const u32x4*)(hb + i * 1024 + l16), sws, l16, dst + (i >> 1) * 4096 + (mt & 1) * 2048 + (i & 1) * 1024);
        if (lane < 32) bstore16<QKV_POL>(*(LAS const u32x4*)(hb + 4096 + l16), sws, l16, dst + 8192 + (mt & 1) * 512);
        float mx = 0.f;
#pragma unroll
        for (int i = 0; i < 2; ++i) { const int tl = lane + 64 * i;
            const u32x4 a = *(LAS const u32x4*)(hb + tl * 16), bq = *(LAS const u32x4*)(hb + 2048 + tl * 16); const unsigned c = *(LAS const unsigned*)(hb + 4096 + tl * 4);
            float s2 = dot2(c, c, 0.f);
#pragma unroll
            for (int e = 0; e < 4; ++e) { s2 = dot2(a[e], a[e], s2); s2 = dot2(bq[e], bq[e], s2); }
            mx = fmaxf(mx, s2); }
        mx = fmaxf(mx, xor16(mx)); mx = fmaxf(mx, xor32(mx));
#pragma unroll
        for (int o = 1; o < 16; o <<= 1) mx = fmaxf(mx, __shfl_xor(mx, o));
        if (lane == 0) ((float*)(ws + WS_KMAX))[head * 16 + mt] = mx;
    }
    STAMP(1, 6); RSTAMP(1, 15); STAMP_FLUSH(1);
}
template <int PART> DI void k1_body(const char* x, const char* wqkv, char* ws, char* lds, int mt, int nt, int rot, int shx, int shw) {
    if (threadIdx.x >= 256) k1_wave<PART, 1>(x, wqkv, ws, lds, mt, nt, rot, shx, shw); else k1_wave<PART, 0>(x, wqkv, ws, lds, mt, nt, rot, shx, shw);
}
__global__ __launch_bounds__(512, 2) void k1_qkv(const char* x, const char* wqkv, const char* wo, char* ws) {
    extern __shared__ __attribute__((aligned(16))) char lds[];
    const int b = blockIdx.x;
    if (b >= 192) {
        const int lane = threadIdx.x & 63, wv = rfl(threadIdx.x >> 6), gw = (b - 192) * 8 + wv;
        if (threadIdx.x == 511) ((unsigned*)(ws + WS_CNT))[(b - 192) * 64] = 0u;
        if (threadIdx.x == 510 && b - 192 < 32) ((unsigned*)(ws + WS_CNT))[(64 + b - 192) * 64] = 0u;
        if (gw < 162) { RetileJob J; retile_setup(J, 3888 + gw, lane, wo, nullptr, nullptr, nullptr);
            retile_issue(J, 3888 + gw, lane, (unsigned)(uintptr_t)lds + wv * 4096); WAITV(0); retile_finish(J, lane, (LAS const char*)lds + wv * 4096, ws); }
        for (int c = gw; c < D_; c += 512) {
            const char* row = wo + (size_t)c * (D_ * 2);
            const u32x4 a = *(const u32x4*)(row + lane * 16); float s = 0.f;
#pragma unroll
            for (int e = 0; e < 4; ++e) s = dot2(a[e], 0x3f803f80u, s);
            if (lane < 8) { const u32x4 bq = *(const u32x4*)(row + 1024 + lane * 16);
#pragma unroll
                for (int e = 0; e < 4; ++e) s = dot2(bq[e], 0x3f803f80u, s); }
#pragma unroll
            for (int o = 1; o < 64; o <<= 1) s += __shfl_xor(s, o);
            if (lane == 0) { const float w = s * (1.f / D_); const unsigned hi = cvtpk(w, 0.f) & 0xffffu; const unsigned lo = cvtpk(w - bflo(hi), 0.f) & 0xffffu;
                ((unsigned short*)(ws + WS_WBAR))[c] = (unsigned short)hi; ((unsigned short*)(ws + WS_WBAR))[D_ + c] = (unsigned short)lo; }
        }
        return;
    }
    const int xcd = b & 7, idx = b >> 3, mt = (xcd & 3) * 4 + (idx & 3), nt = (xcd >> 2) * 6 + (idx >> 2);
    const int rot = K1_ROT ? (2 * (idx & 3) + ((idx >> 2) * 3 >> 1)) % 9 : 0;
    const int shx = idx >> 2, shw = idx & 3;
    if (nt >= 8) k1_body<2>(x, wqkv, ws, lds, mt, nt, rot, shx, shw); else if (nt >= 4) k1_body<1>(x, wqkv, ws, lds, mt, nt, rot, shx, shw); else k1_body<0>(x, wqkv, ws, lds, mt, nt, rot, shx, shw);
}

constexpr int K2_LDS = 160 * 1024;
#define EXPS(S, a, b) _Pragma("unroll") for (int r_ = a; r_ < b; ++r_) S[r_] = __builtin_amdgcn_exp2f(S[r_]); SBAR()
#define PLO(S) __builtin_bit_cast(bf16x16, (f32x8)__builtin_shufflevector(S, S, 0, 1, 2, 3, 4, 5, 6, 7))
#define PHI(S) __builtin_bit_cast(bf16x16, (f32x8)__builtin_shufflevector(S, S, 8, 9, 10, 11, 12, 13, 14, 15))
#define KTAIL(p) __builtin_bit_cast(s16x4, (u32x2){*(LAS const unsigned*)(p), kc})
#define QK1(q) __builtin_amdgcn_mfma_f32_32x32x16_bf16(kf, q, (f32x16){}, 0, 0, 0)
#define QK2(q, s) __builtin_amdgcn_mfma_f32_32x32x8bf16_1k(kt, q, s, 0, 0, 0)
#define PV(v, p, o) __builtin_amdgcn_smfmac_f32_32x32x32_bf16(v, p, o, 0xDDDD, 0, 0)
DI void attn_fast(LAS const char* kp, LAS const char* tp, LAS const char* vp, const bf16x8 (&qm)[2], const s16x4 (&qt)[2], f32x16 (&o)[2], unsigned kc) {
    bf16x8 kf = *(LAS const bf16x8*)kp; s16x4 kt = KTAIL(tp); bf16x8 v0, v1;
    f32x16 s0 = QK1(qm[0]), s1; s0 = QK2(qt[0], s0);
    SBAR();
#pragma unroll
    for (int it = 0; it < 8; ++it) {
        if (K2_PRIO) { if (it == 0) __builtin_amdgcn_s_setprio(3); if (it == 2) __builtin_amdgcn_s_setprio(2); if (it == 4) __builtin_amdgcn_s_setprio(1); if (it == 6) __builtin_amdgcn_s_setprio(0); }
        EXPS(s0, 0, 2);
        if (it > 0) { o[1] = PV(v0, PLO(s1), o[1]); SBAR(); }
        EXPS(s0, 2, 6);
        if (it > 0) { o[1] = PV(v1, PHI(s1), o[1]); SBAR(); }
        v0 = *(LAS const bf16x8*)(vp + it * 1280); v1 = *(LAS const bf16x8*)(vp + it * 1280 + 640); SBAR();
        EXPS(s0, 6, 9);
        s1 = QK1(qm[1]); SBAR();
        EXPS(s0, 9, 12);
        s1 = QK2(qt[1], s1); SBAR();
        if (it + 1 < 8) { kf = *(LAS const bf16x8*)(kp + (it + 1) * 512); kt = KTAIL(tp + (it + 1) * 128); SBAR(); }
        EXPS(s0, 12, 16);
        EXPS(s1, 0, 2);
        o[0] = PV(v0, PLO(s0), o[0]); SBAR();
        EXPS(s1, 2, 6);
        o[0] = PV(v1, PHI(s0), o[0]); SBAR();
        EXPS(s1, 6, 9);
        if (it + 1 < 8) { s0 = QK1(qm[0]); SBAR(); }
        EXPS(s1, 9, 12);
        if (it + 1 < 8) { s0 = QK2(qt[0], s0); SBAR(); }
        EXPS(s1, 12, 16);
    }
    o[1] = PV(v0, PLO(s1), o[1]); o[1] = PV(v1, PHI(s1), o[1]);
}
DI void attn_slow(LAS const char* kp, LAS const char* tp, LAS const char* vp, const bf16x8 (&qm)[2], const s16x4 (&qt)[2], f32x16 (&o)[2], float (&mrun)[2], unsigned kc) {
#pragma unroll 1
    for (int it = 0; it < 8; ++it) {
        const bf16x8 kf = *(LAS const bf16x8*)(kp + it * 512); const s16x4 kt = KTAIL(tp + it * 128);
        const bf16x8 v0 = *(LAS const bf16x8*)(vp + it * 1280), v1 = *(LAS const bf16x8*)(vp + it * 1280 + 640);
#pragma unroll
        for (int u = 0; u < 2; ++u) {
            f32x16 s = QK1(qm[u]); s = QK2(qt[u], s);
            float mx = s[0];
#pragma unroll
            for (int r = 1; r < 16; ++r) mx = fmaxf(mx, s[r]);
            mx = fmaxf(mx, xor32(mx));
            const float mn = fmaxf(mrun[u], mx), al = __builtin_amdgcn_exp2f(mrun[u] - mn);
            mrun[u] = mn;
#pragma unroll
            for (int r = 0; r < 16; ++r) { s[r] = __builtin_amdgcn_exp2f(s[r] - mn); o[u][r] *= al; }
            o[u] = PV(v0, PLO(s), o[u]); o[u] = PV(v1, PHI(s), o[u]);
        }
    }
}
__global__ __launch_bounds__(1024, 4) void k2_attn(const char* x, const char* wg, const char* wu, const char* wd, char* ws) {
    extern __shared__ __attribute__((aligned(16))) char lds_generic[];
    LAS char* lds = (LAS char*)lds_generic; const unsigned ldsb = (unsigned)(uintptr_t)lds_generic;
    const int tid = threadIdx.x, lane = tid & 63, wid = rfl(tid >> 6), qg = wid & 3, kq = wid >> 2, r32 = lane & 31, h = lane >> 5;
    const int b = blockIdx.x, xcd = b & 7, idx = b >> 3, head = xcd * 4 + (idx & 3), qb = idx >> 2;
    STAMP_DECL; RSTAMP(2, 14); STAMP(2, 0);
    const srd_t srd = make_srd(ws, (unsigned)WS_END);
    const int c0 = (2 * kq + (qb & 1)) * KVCHUNK, c1 = (2 * kq + ((qb & 1) ^ 1)) * KVCHUNK;
    const unsigned kvb = (unsigned)WS_KV + head * KVIMG, l16 = lane * 16;
    const int q0 = 256 * qb + 64 * qg;
    bf16x8 qm[2]; float qtl[2]; float km;
    { const unsigned so = (unsigned)WS_QM + (head * S_ + q0) * 32, vo = r32 * 32 + h * 16; bload16<0>(qm[0], srd, vo, so); bload16<1024>(qm[1], srd, vo, so);
      const unsigned st = (unsigned)WS_QT + (head * S_ + q0) * 4; bload4<0>(qtl[0], srd, r32 * 4, st); bload4<128>(qtl[1], srd, r32 * 4, st);
      bload4<0>(km, srd, (lane & 15) * 4, (unsigned)WS_KMAX + head * 64); }
    bdma4(srd, l16, kvb + c0 + qg * 5120, ldsb + c0 + qg * 5120); if (qg < 3) bdma1(srd, l16, kvb + c0 + qg * 5120 + 4096, ldsb + c0 + qg * 5120 + 4096);
    const int slot = wid * 32 + idx; const bool rt = slot < (K2_RT ? 486 : 0);
    bf16x8 wb[4]; unsigned rdst[4], rso[4], rvo; const char* rsrc;
    if (slot < 324) { const int gu = slot >= 162, r = slot - 162 * gu, ks = (r * 57) >> 9, cb = 9 * xcd + r - 9 * ks, T0 = 4 * cb, j0 = (T0 * 7282) >> 16, t0 = T0 - 9 * j0;
        rsrc = gu ? wu : wg; rvo = (lane >> 3) * (F_ * 2) + (lane & 7) * 16;
#pragma unroll
        for (int p = 0; p < 4; ++p) { rso[p] = (ks * 32 + p * 8) * (F_ * 2) + cb * 128;
            const int w = t0 + p >= 9, tt = t0 + p - 9 * w, m4 = tt >= 4, m8 = tt >= 8;
            rdst[p] = (unsigned)WS_W1 + ((j0 + w) * 324 + 2 * tt + m4 * 34 * (tt - (tt & 1)) + (ks << (3 - m4 - m8)) + gu) * 1024; }
    } else { const int r2 = slot < 486 ? slot - 324 : 0, q = (r2 * 57) >> 9, cb = r2 - 9 * q, hi = q >= 9, qi = q - 9 * hi, a = 2 * xcd + hi;
        rsrc = wd; rvo = (lane >> 3) * (D_ * 2) + (lane & 7) * 16;
        const int e8 = qi == 8, t0 = 18 * a + 9 * (qi >> 2) + 2 * (qi & 3) - 10 * e8, t1 = t0 + 1 + 8 * e8, T0 = 4 * cb, n0 = (T0 * 7282) >> 16, u0 = T0 - 9 * n0;
#pragma unroll
        for (int p = 0; p < 4; ++p) { rso[p] = (16 * (p >> 1 ? t1 : t0) + 8 * (p & 1)) * (D_ * 2) + cb * 128;
            const int w = u0 + p >= 9, tt = u0 + p - 9 * w;
            rdst[p] = (unsigned)WS_W2 + (((xcd >> 1) * 4 + n0 + w) * 324 + (((q >> 1) * 2 + (xcd & 1)) * 2 + (q & 1)) * 9 + tt) * 1024; }
    }
#pragma unroll
    for (int p = 0; p < 4; ++p) { rso[p] = rfl(rso[p]); rdst[p] = rfl(rdst[p]); }
    asm volatile("s_waitcnt vmcnt(4)" : "+v"(qm[0]), "+v"(qm[1]), "+v"(qtl[0]), "+v"(qtl[1]), "+v"(km) :: "memory");
    km = fmaxf(km, __shfl_xor(km, 1)); km = fmaxf(km, __shfl_xor(km, 2)); km = fmaxf(km, __shfl_xor(km, 4)); km = fmaxf(km, __shfl_xor(km, 8));
    s16x4 qt[2]; bool unsafe = false;
#pragma unroll
    for (int u = 0; u < 2; ++u) { const u32x4 qv = __builtin_bit_cast(u32x4, qm[u]); const unsigned tl = __float_as_uint(qtl[u]);
        float n2 = h ? 0.f : dot2(tl, tl, 0.f);
#pragma unroll
        for (int e = 0; e < 4; ++e) n2 = dot2(qv[e], qv[e], n2);
        n2 += xor32(n2);
        const float bound = __builtin_amdgcn_sqrtf(n2 * km) * 1.0001f + 1e-6f;
        unsafe |= !(bound <= SAFE_B);
        const unsigned cb = (__float_as_uint(bound) + 0xffffu) >> 16;
        const u32x2 w = h ? (u32x2){0u, 0u} : (u32x2){tl, cb | 0x8000u};
        qt[u] = __builtin_bit_cast(s16x4, w); }
    const bool slow = __any(unsafe);
    if (slow) {
#pragma unroll
        for (int u = 0; u < 2; ++u) { u32x2 w = __builtin_bit_cast(u32x2, qt[u]); w[1] = 0u; qt[u] = __builtin_bit_cast(s16x4, w); } }
    f32x16 o[2] = {(f32x16){}, (f32x16){}}; float mrun[2] = {-INFINITY, -INFINITY};
    const int vrow = r32 >= 19 ? 4 : r32 + (r32 >= 4 ? 5 : 0) - (r32 >= 12 ? 12 : 0) + (r32 >= 16 ? 8 : 0);
    const unsigned kc = h ? 0u : 0x00003f80u;
    unsigned kp = ldsb + c0 + h * 4096 + r32 * 16, tp = ldsb + c0 + 8192 + r32 * 4, vp = ldsb + c0 + 9216 + h * 320 + vrow * 16;
#define KP(o) ((LAS const char*)(uintptr_t)kp + (o))
#define TP(o) ((LAS const char*)(uintptr_t)tp + (o))
#define VP(o) ((LAS const char*)(uintptr_t)vp + (o))
    STAMP(2, 1);
    WAIT_BAR(0);
    STAMP(2, 2);
    bdma4(srd, l16, kvb + c1 + qg * 5120, ldsb + c1 + qg * 5120); if (qg < 3) bdma1(srd, l16, kvb + c1 + qg * 5120 + 4096, ldsb + c1 + qg * 5120 + 4096);
    if (rt) { const srd_t sw = make_srd(rsrc, D_ * F_ * 2);
#pragma unroll
        for (int p = 0; p < 4; ++p) bload16<0>(wb[p], sw, rvo, rso[p]); }
    if (slow) attn_slow(KP(0), TP(0), VP(0), qm, qt, o, mrun, kc); else attn_fast(KP(0), TP(0), VP(0), qm, qt, o, kc);
    STAMP(2, 3);
    if (rt) WAIT_BAR(4); else WAIT_BAR(0);
    STAMP(2, 4);
    if (rt) { asm volatile("s_waitcnt vmcnt(0)" : "+v"(wb[0]), "+v"(wb[1]), "+v"(wb[2]), "+v"(wb[3]) :: "memory");
        int ty = threadIdx.x; asm volatile("" : "+v"(ty)); const int ln = ty & 63;
        LAS char* scr = lds + c0 + qg * 4096;
#pragma unroll
        for (int p = 0; p < 4; ++p) *(LAS bf16x8*)(scr + p * 1024 + ln * 16) = wb[p];
        LAS const char* base = scr + (4 * (ln >> 4) + ((ln >> 2) & 3)) * 128 + (ln & 3) * 8;
#pragma unroll
        for (int u = 0; u < 4; ++u) bstore16<RT_POL>(__builtin_bit_cast(u32x4, tr_frag(base + 32 * u, 16 * 128)), srd, ln * 16, rdst[u]); }
    STAMP(2, 5);
    { const int dc = c1 - c0; kp += dc; tp += dc; vp += dc; PINV(kp); PINV(tp); PINV(vp); }
    if (slow) attn_slow(KP(0), TP(0), VP(0), qm, qt, o, mrun, kc); else attn_fast(KP(0), TP(0), VP(0), qm, qt, o, kc);
    STAMP(2, 6);
    int tz = threadIdx.x; asm volatile("" : "+v"(tz));
    const int lane2 = tz & 63, r2 = tz & 31, h2 = (tz >> 5) & 1;
    LAS float* mx = (LAS float*)(lds + KVIMG);
#pragma unroll
    for (int u = 0; u < 2; ++u) { LAS char* pb = lds + c0 + (qg * 2 + u) * 2432;
        *(LAS f32x4*)(pb + lane2 * 16) = (f32x4){o[u][0], o[u][1], o[u][2], o[u][3]};
        *(LAS f32x4*)(pb + 1024 + lane2 * 16) = (f32x4){o[u][4], o[u][5], o[u][6], o[u][7]};
        if (h2 == 0) { *(LAS f32x2*)(pb + 2048 + r2 * 8) = (f32x2){o[u][8], o[u][9]}; *(LAS float*)(pb + 2304 + r2 * 4) = o[u][10];
                      if (slow) mx[wid * 64 + 32 * u + r2] = mrun[u]; } }
    LBAR();
    { const int u = kq & 1, hf = kq >> 1;
      LAS const char* pb = lds + (qb & 1) * KVCHUNK + (qg * 2 + u) * 2432;
      float l = 0.f; f32x4 a = {0, 0, 0, 0}; f32x2 c = {0, 0};
      if (slow) { float mk[4], M = -INFINITY;
#pragma unroll
          for (int k = 0; k < 4; ++k) { mk[k] = mx[(4 * k + qg) * 64 + 32 * u + r2]; M = fmaxf(M, mk[k]); }
#pragma unroll
          for (int k = 0; k < 4; ++k) { LAS const char* p = pb + k * 2 * KVCHUNK; const float f = __builtin_amdgcn_exp2f(mk[k] - M);
              l += f * *(LAS const float*)(p + 2304 + r2 * 4); a += *(LAS const f32x4*)(p + hf * 1024 + lane2 * 16) * f; c += *(LAS const f32x2*)(p + 2048 + r2 * 8) * f; }
      } else {
#pragma unroll
          for (int k = 0; k < 4; ++k) { LAS const char* p = pb + k * 2 * KVCHUNK;
              l += *(LAS const float*)(p + 2304 + r2 * 4); a += *(LAS const f32x4*)(p + hf * 1024 + lane2 * 16); c += *(LAS const f32x2*)(p + 2048 + r2 * 8); } }
      const float rl = __builtin_amdgcn_rcpf(l);
      const int token = q0 + 32 * u + r2; char* ob = ws + WS_OF + (size_t)(token >> 4) * 18 * 1024 + (token & 15) * 16;
      auto put = [&](int d, float x0, float x1) { const int cc = 18 * head + d; *(unsigned*)(ob + (cc >> 5) * 1024 + ((cc & 31) >> 3) * 256 + (cc & 7) * 2) = cvtpk(x0 * rl, x1 * rl); };
      put(8 * hf + 4 * h2, a[0], a[1]); put(8 * hf + 4 * h2 + 2, a[2], a[3]);
      if (hf == 0 && h2 == 0) put(16, c[0], c[1]);
    }
    STAMP(2, 7); RSTAMP(2, 15); STAMP_FLUSH(2);
}

constexpr int K3_X = 108 * 1024, K3_WB = K3_X + 12 * 1024, K3_Z = K3_WB + 3 * 1024, K3_LDS = K3_Z + 2048;
__global__ __launch_bounds__(512, 2) void k3_oproj(const char* x, char* ws) {
    extern __shared__ __attribute__((aligned(16))) char lds_generic[];
    LAS char* lds = (LAS char*)lds_generic; const unsigned ldsb = (unsigned)(uintptr_t)lds_generic;
    const int tid = threadIdx.x, lane = tid & 63, wid = rfl(tid >> 6), mi = wid & 3, kj = wid >> 2, m = lane & 15, g = lane >> 4;
    const int b = blockIdx.x, xcd = b & 7, idx = b >> 3, mt32 = xcd * 4 + (idx & 3), n6 = idx >> 2, mt16 = 4 * mt32 + mi;
    STAMP_DECL; RSTAMP(3, 14); STAMP(3, 0);
    const srd_t srd = make_srd(ws, (unsigned)WS_END), sx = make_srd(x, S_ * D_ * 2);
    const unsigned l16 = lane * 16, wo0 = (unsigned)WS_WO + n6 * (108 * 1024), ob = (unsigned)WS_OF + (mt16 * 18 + 9 * kj) * 1024;
    bf16x8 of[9];
    { const unsigned f0 = (54 * kj + 6 * mi) * 1024;
      bload16<0>(of[0], srd, l16, ob); bload16<1024>(of[1], srd, l16, ob); bload16<2048>(of[2], srd, l16, ob); bload16<3072>(of[3], srd, l16, ob);
      bdma4(srd, l16, wo0 + f0, ldsb + f0); bdma1(srd, l16, wo0 + f0 + 4096, ldsb + f0 + 4096); bdma1(srd, l16, wo0 + f0 + 5120, ldsb + f0 + 5120); }
    if (tid < 72) *(LAS u32x4*)(lds + K3_Z + tid * 16) = (u32x4){0, 0, 0, 0};
    f32x4 acc[6], accw = {0, 0, 0, 0}; float mux;
    asm volatile("s_nop 1" : "+v"(accw));
    LAS const char* ab = lds + kj * (54 * 1024) + lane * 16;
    LAS const char* wbp = lds + (m < 2 ? K3_WB + m * (D_ * 2) + kj * 576 + g * 16 : K3_Z);
    bf16x8 wr[2][6];
    auto rd = [&](int s) {
#pragma unroll
        for (int t = 0; t < 6; ++t) wr[s & 1][t] = lds_frag(ab + (s * 6 + t) * 1024);
        SBAR(); };
    auto step = [&](int s) { PINV(of[s]);
#pragma unroll
        for (int t = 0; t < 6; ++t) { if (s == 0) mfma16_new(acc[t], wr[s & 1][t], of[s]); else mfma16_acc(acc[t], wr[s & 1][t], of[s]); }
        SBAR(); };
    STAMP(3, 1);
    WAIT_BAR(0);
    STAMP(3, 2);
    { const unsigned f1 = (54 * kj + 24 + (mi < 3 ? 8 * mi : 22)) * 1024;
      rd(0); rd(1);
      bload16<0>(of[4], srd, l16, ob + 4096); bload16<1024>(of[5], srd, l16, ob + 4096); bdma4(srd, l16, wo0 + f1, ldsb + f1);
      step(0); rd(2);
      bload16<2048>(of[6], srd, l16, ob + 4096); bload16<3072>(of[7], srd, l16, ob + 4096); bdma4(srd, l16, wo0 + f1 + 4096, ldsb + f1 + 4096);
      step(1); rd(3);
      bload16<0>(of[8], srd, l16, ob + 8192);
#pragma unroll
      for (int i = 0; i < 2; ++i) { int pc = wid + 8 * i; pc = pc > 11 ? 11 : pc; const int q = pc * 64 + lane, row = (q * 5462) >> 16;
          bdma1(sx, row * (D_ * 2) + (q - 12 * row) * 16, (64 * mt32) * (D_ * 2) + n6 * 192, ldsb + K3_X + pc * 1024); }
      { const int pc = wid > 2 ? 2 : wid; bdma1(srd, l16, (unsigned)WS_WBAR + pc * 1024, ldsb + K3_WB + pc * 1024); }
      bload4<0>(mux, srd, m * 4, (unsigned)WS_MUX + mt16 * 64);
      step(2); step(3); }
    STAMP(3, 3);
    WAIT_BAR(0);
    STAMP(3, 4);
    bf16x8 wbf[9];
    rd(4); rd(5);
#pragma unroll
    for (int s = 0; s < 9; ++s) wbf[s] = lds_frag(wbp + s * 64);
    SBAR();
    step(4); rd(6); step(5); rd(7); step(6); rd(8); step(7); step(8);
#pragma unroll
    for (int s = 0; s < 9; ++s) mfma16_acc(accw, wbf[s], of[s]);
    PINV(mux);
    const int token = 16 * mt16 + m;
    u32x2 xr[4];
#pragma unroll
    for (int t = 0; t < 4; ++t) xr[t] = *(LAS const u32x2*)(lds + K3_X + (16 * mi + m) * 192 + (4 * kj + (kj ? (t & 1) : t)) * 32 + g * 8);
    asm volatile("s_nop 7" : "+v"(acc[5]), "+v"(acc[4]), "+v"(accw));
    STAMP(3, 5);
    LBAR();
    LAS char* ex = lds + mi * 8192 + lane * 16;
    if (kj == 0) { *(LAS f32x4*)(ex + 4 * 1024) = acc[4]; *(LAS f32x4*)(ex + 5 * 1024) = acc[5]; *(LAS f32x4*)(ex + 6 * 1024) = accw; }
    else {
#pragma unroll
        for (int t = 0; t < 4; ++t) *(LAS f32x4*)(ex + t * 1024) = acc[t];
        *(LAS f32x4*)(ex + 7 * 1024) = accw; }
    LBAR();
    accw += *(LAS const f32x4*)(ex + (kj ? 6 : 7) * 1024);
    const float mu = mux + __shfl(accw[0] + accw[1], m);
    float ssq = 0.f;
    auto fin = [&](int t, u32x2 xv, unsigned (&pk)[2]) {
        const f32x4 a = acc[t] + *(LAS const f32x4*)(ex + t * 1024);
        const unsigned h0 = cvtpk(a[0] + bflo(xv[0]), a[1] + bfhi(xv[0])), h1 = cvtpk(a[2] + bflo(xv[1]), a[3] + bfhi(xv[1]));
        bstore8<H_POL>((u32x2){h0, h1}, srd, lane * 8, (unsigned)WS_HC + (mt16 * 36 + 6 * n6 + t) * 512);
        pk[0] = cvtpk(bflo(h0) - mu, bfhi(h0) - mu); pk[1] = cvtpk(bflo(h1) - mu, bfhi(h1) - mu);
        ssq = dot2(pk[0], pk[0], ssq); ssq = dot2(pk[1], pk[1], ssq); };
    auto pair = [&](int t, u32x2 xa, u32x2 xb2) { unsigned p0[2], p1[2]; fin(t, xa, p0); fin(t + 1, xb2, p1);
        bstore16<H_POL>((u32x4){p0[0], p0[1], p1[0], p1[1]}, srd, l16, (unsigned)WS_HCF + (mt16 * 18 + 3 * n6 + (t >> 1)) * 1024); };
    if (kj == 0) { pair(0, xr[0], xr[1]); pair(2, xr[2], xr[3]); } else pair(4, xr[0], xr[1]);
    ssq += xor16(ssq); ssq += xor32(ssq);
    if (g == 0) ((float*)(ws + WS_SSQ))[token * 16 + 2 * n6 + kj] = ssq;
    STAMP(3, 6); RSTAMP(3, 15); STAMP_FLUSH(3);
}

constexpr int RING_SLOT = 36 * 1024, G_LDS = 4 * RING_SLOT;
DI void ring_issue(const char* stream, int st, int wid, int lane, unsigned ldsb) {
    const char* sb = stream + (size_t)st * RING_SLOT + wid * 1024; const unsigned db = ldsb + (st & 3) * RING_SLOT + wid * 1024;
#pragma unroll
    for (int i = 0; i < 4; ++i) glds16(sb + i * 8192, lane * 16, db + i * 8192);
    if (wid < 4) glds16(sb + 32768, lane * 16, db + 32768);
}
DI float geglu(float g, float u) {
    const float z = g * __builtin_fmaf(g * g, -0.10294324f, -2.30220818f);
    return g * u * __builtin_amdgcn_rcpf(1.f + __builtin_amdgcn_exp2f(z));
}
struct Ev { int type, arg, n; };
enum { E_SSQ, E_A, E_B, E_WAITB, E_BAR, E_READ, E_MMA, E_GP, E_RS, E_STAMP, E_ST, E_NOP, E_TOUCH, E_A2 };
constexpr int G1_PD = G1PD;
constexpr int g1_grp(int f) { return f < 144 ? 0 : f < 216 ? 1 : f < 288 ? 2 : 3; }
constexpr int g1_ks(int f) { return f < 144 ? f / 8 : f < 288 ? ((f - 144) % 72) / 4 : (f - 288) / 2; }
constexpr int g1_tile(int f) { return f < 288 ? (f >> 1) & 1 : 0; }
constexpr int g1_set(int f) { return f < 144 ? (f >> 2) & 1 : f < 216 ? 0 : f < 288 ? 1 : 0; }
template <bool LATE> struct G1Prog {
    Ev ev[1600]; int n;
    constexpr void add(int t, int a) { ev[n].type = t; ev[n].arg = a; ev[n].n = 0; ++n; }
    static constexpr int vm(const Ev& e) { return e.type == E_SSQ ? 2 : (e.type == E_A || e.type == E_B || e.type == E_ST || e.type == E_TOUCH || e.type == E_A2) ? 1 : 0; }
    static constexpr int NP = LATE ? 4 : 5;
    constexpr void addB(int s, int u) { add(E_B, s * 2 + u); }
    static constexpr int jset(int q) { return q == 0 ? 0 : q == 1 ? 1 : q == 2 ? 0 : q == 3 ? 1 : 0; }
    constexpr G1Prog() : ev{}, n(0) {
        for (int p = 0; p < NP; ++p) add(E_A, p);
        addB(0, 0); addB(0, 1); addB(1, 0); addB(1, 1);
        add(E_BAR, 0); add(E_STAMP, 1);
        for (int f = 0; f < G1_PD; ++f) add(E_READ, f);
        for (int f = 0; f < 324; ++f) {
            const int st = f / 36, i = f % 36, g = g1_grp(f);
            if (g == 0) { const int s = f / 8, q = f % 8;
                if (q == 0) add(E_WAITB, s);
                if (f == 0) { addB(2, 0); addB(2, 1); }
                if (s + 3 < 18 && q == 2) addB(s + 3, 0);
                if (s + 3 < 18 && q == 6) addB(s + 3, 1);
                if (f == 100) add(E_SSQ, 0);
                if (f == 136) add(E_RS, 0); }
            if (st == 0 && i >= 1 && i < 1 + 3 * NP && (i - 1) % 3 == 0) add(E_A, 8 + (i - 1) / 3);
            if (st + 2 < 9) { const int i0 = st == 0 ? 17 : LATE ? 17 : 1, d = 4;
                if (i >= i0 && (i - i0) % d == 0 && (i - i0) / d < NP) add(E_A, (st + 2) * 8 + (i - i0) / d); }
            if (i == 36 - G1_PD && st + 1 < 9) add(E_BAR, st + 1);
            if (f + G1_PD < 324) add(E_READ, f + G1_PD);
            add(E_MMA, f);
            if (f == 143) { add(E_NOP, 0);
                for (int k = 0; k < 32; ++k) { add(E_GP, 0 * 64 + k); if (k == 15 || k == 31) add(E_ST, 0 * 2 + (k == 31)); } }
            if (g > 0) {
                const int r = f - (g == 1 ? 144 : g == 2 ? 216 : 288), step = g < 3 ? 2 : 1, r0 = 3, k = (r - r0) / step;
                if (r >= r0 && (r - r0) % step == 0 && k < 32) { add(E_GP, g * 64 + k);
                    if (k == 15 || k == 31) add(E_ST, g * 2 + (k == 31)); } }
            if (TOUCH && f == 5 * 36 + 8) add(E_TOUCH, 0);
            if (st == 8 && i >= 2 && i % 3 == 2 && (i - 2) / 3 < 2 * NP) add(E_A2, ((i - 2) / 3 / NP) * 8 + ((i - 2) / 3) % NP);
            if (i == 35) add(E_STAMP, 2 + st);
            if (f == 107) add(E_STAMP, 12);
            if (f == 323) add(E_STAMP, 13);
        }
        add(E_NOP, 1);
        for (int k = 0; k < 16; ++k) { add(E_GP, 4 * 64 + k); if (k == 7 || k == 15) add(E_ST, 8 + (k == 15)); }
        for (int i = 0; i < n; ++i) if (ev[i].type == E_WAITB || ev[i].type == E_BAR || ev[i].type == E_RS) {
            int cnt = 0, j = i - 1;
            for (; j >= 0; --j) {
                if (ev[i].type == E_WAITB && ev[j].type == E_B && ev[j].arg == 2 * ev[i].arg + 1) break;
                if (ev[i].type == E_RS && ev[j].type == E_SSQ) break;
                if (ev[i].type == E_BAR && ev[j].type == E_A && ev[j].arg == 8 * ev[i].arg + NP - 1) break;
                cnt += vm(ev[j]); }
            ev[i].n = cnt > 63 ? 63 : cnt; }
    }
};
template <bool LATE> constexpr G1Prog<LATE> g1prog{};
DI float vmul(float a, float b) { float r; asm("v_mul_f32_e32 %0, %1, %2" : "=v"(r) : "v"(a), "v"(b)); return r; }
DI float vfma(float a, float b, float c) { float r; asm("v_fma_f32 %0, %1, %2, %3" : "=v"(r) : "v"(a), "v"(b), "v"(c)); return r; }
struct G1State {
    bf16x8 hf[2][18]; f32x4 acc[2][2][2][2];
    bf16x8 ar[G1_PD + 1]; f32x4 sq[2]; float a0[2], a1[2], kk[2], sink; f32x4 z, w; u32x4 o; unsigned soffT, soffA2;
    unsigned lb0, lb1, lb2; unsigned l16, ldsb, soffA, soffH, soffS, soffAct, vq; int wid, j, g;
};
template <bool LATE, int PC> DI void g1_exec(G1State& c, const srd_t srd) {
    constexpr Ev e = g1prog<LATE>.ev[PC];
    if constexpr (e.type == E_SSQ) { bload16f<0>(c.sq[0], srd, c.vq, c.soffS); bload16f<1024>(c.sq[1], srd, c.vq, c.soffS); }
    else if constexpr (e.type == E_A) { constexpr int st = e.arg >> 3, p = e.arg & 7;
        const unsigned so = c.soffA + st * RING_SLOT, dst = c.ldsb + (st & 3) * RING_SLOT;
        if constexpr (p < 4) bdma_next<p * 1024>(srd, c.l16, so + c.wid * 4096, dst + c.wid * 4096);
        else bdma1(srd, c.l16, so + 32768 + c.wid * 1024, dst + 32768 + c.wid * 1024); }
    else if constexpr (e.type == E_B) { constexpr int s = e.arg >> 1, u = e.arg & 1;
        bload16<((18 * u + s) & 3) * 1024>(c.hf[u][s], srd, c.l16, c.soffH + ((18 * u + s) >> 2) * 4096); }
    else if constexpr (e.type == E_WAITB) { asm volatile("s_waitcnt vmcnt(%2)" : "+v"(c.hf[0][e.arg]), "+v"(c.hf[1][e.arg]) : "n"(e.n)); }
    else if constexpr (e.type == E_BAR) { asm volatile("s_waitcnt vmcnt(%0)\n\ts_barrier" :: "n"(e.n) : "memory"); }
    else if constexpr (e.type == E_RS) { asm volatile("s_waitcnt vmcnt(%2)" : "+v"(c.sq[0]), "+v"(c.sq[1]) : "n"(e.n));
#pragma unroll
        for (int u = 0; u < 2; ++u) { float t = c.g < 3 ? (c.sq[u][0] + c.sq[u][1]) + (c.sq[u][2] + c.sq[u][3]) : 0.f; t += __shfl_xor(t, 16); t += xor32(t);
            const float v = t * (1.f / D_) + EPS, rs = __builtin_amdgcn_rsqf(v);
            c.a0[u] = -2.30220818f * rs; c.a1[u] = -0.10294324f * rs * rs * rs; c.kk[u] = v; }
        SBAR(); }
    else if constexpr (e.type == E_STAMP) {
    }
    else if constexpr (e.type == E_TOUCH) { touch64(c.sink, srd, c.l16 * 8, c.soffT); }
    else if constexpr (e.type == E_A2) { constexpr int st = e.arg >> 3, p = e.arg & 7;
        const unsigned so = c.soffA2 + st * RING_SLOT, dst = c.ldsb + ((st + 1) & 3) * RING_SLOT;
        if constexpr (p < 4) bdma_next<p * 1024>(srd, c.l16, so + c.wid * 4096, dst + c.wid * 4096);
        else bdma1(srd, c.l16, so + 32768 + c.wid * 1024, dst + 32768 + c.wid * 1024); }
    else if constexpr (e.type == E_NOP) {
        asm volatile("s_nop 7" : "+v"(c.acc[0][0][0][0]), "+v"(c.acc[0][0][0][1]), "+v"(c.acc[0][0][1][0]), "+v"(c.acc[0][0][1][1]));
        if constexpr (e.arg == 0) asm volatile("" : "+v"(c.acc[0][1][0][0]), "+v"(c.acc[0][1][0][1]), "+v"(c.acc[0][1][1][0]), "+v"(c.acc[0][1][1][1])); }
    else if constexpr (e.type == E_READ) { constexpr int f = e.arg, off = ((f / 36) & 3) * RING_SLOT + (f % 36) * 1024;
        if constexpr ((off >> 16) == 0) c.ar[f % (G1_PD + 1)] = lds_frag((LAS const char*)(uintptr_t)c.lb0 + (off & 65535));
        else if constexpr ((off >> 16) == 1) c.ar[f % (G1_PD + 1)] = lds_frag((LAS const char*)(uintptr_t)c.lb1 + (off & 65535));
        else c.ar[f % (G1_PD + 1)] = lds_frag((LAS const char*)(uintptr_t)c.lb2 + (off & 65535));
        SBAR(); }
    else if constexpr (e.type == E_MMA) { constexpr int f = e.arg, s = g1_ks(f), t = g1_tile(f), q = f & 1, set = g1_set(f);
        if constexpr (s == 0) { mfma16_new(c.acc[set][t][q][0], c.ar[f % (G1_PD + 1)], c.hf[0][s]); mfma16_new(c.acc[set][t][q][1], c.ar[f % (G1_PD + 1)], c.hf[1][s]); }
        else { mfma16_acc(c.acc[set][t][q][0], c.ar[f % (G1_PD + 1)], c.hf[0][s]); mfma16_acc(c.acc[set][t][q][1], c.ar[f % (G1_PD + 1)], c.hf[1][s]); }
        SBAR(); }
    else if constexpr (e.type == E_GP) {
        constexpr int grp = e.arg >> 6, k = e.arg & 63, quad = k >> 3, ph = k & 7, set = G1Prog<LATE>::jset(grp);
        constexpr int u = grp < 4 ? quad >> 1 : quad, t = grp < 4 ? quad & 1 : 0;
        const f32x4 g = c.acc[set][t][0][u];
#pragma unroll
        for (int r = 0; r < 4; ++r) {
            if constexpr (ph == 0) c.z[r] = vmul(g[r], g[r]);
            if constexpr (ph == 1) c.z[r] = vfma(c.z[r], c.a1[u], c.a0[u]);
            if constexpr (ph == 2) c.z[r] = vmul(c.z[r], g[r]);
            if constexpr (ph == 3) c.z[r] = __builtin_amdgcn_exp2f(c.z[r]);
            if constexpr (ph == 4) { c.z[r] = vfma(c.z[r], c.kk[u], c.kk[u]); }
            if constexpr (ph == 5) { c.z[r] = __builtin_amdgcn_rcpf(c.z[r]); }
            if constexpr (ph == 6) c.w[r] = vmul(vmul(g[r], c.acc[set][t][1][u][r]), c.z[r]); }
        if constexpr (ph == 7) { c.o[2 * t] = cvtpk(c.w[0], c.w[1]); c.o[2 * t + 1] = cvtpk(c.w[2], c.w[3]); }
        SBAR(); }
    else if constexpr (e.type == E_ST) { constexpr int grp = e.arg >> 1, u = e.arg & 1;
        const unsigned so = c.soffAct + u * (144 * 1024);
        if constexpr (grp < 4) bstore16<ACT_POL>(c.o, srd, c.l16, so + (9 * (c.j >> 1) + 4 * (c.j & 1) + grp) * 1024);
        else bstore8<ACT_POL>((u32x2){c.o[0], c.o[1]}, srd, c.l16 + (c.j & 1) * 8, so + (9 * (c.j >> 1) + 8) * 1024);
        SBAR(); }
}
template <bool LATE, int... I> DI void g1_run(G1State& c, const srd_t srd, std::integer_sequence<int, I...>) { (g1_exec<LATE, I>(c, srd), ...); }

constexpr int G2_PD = 4;
template <bool LATE> struct G2Prog {
    Ev ev[700]; int n;
    constexpr void add(int t, int a) { ev[n].type = t; ev[n].arg = a; ev[n].n = 0; ++n; }
    static constexpr int vm(const Ev& e) { return (e.type == E_A || e.type == E_B) ? 1 : 0; }
    static constexpr int NP = LATE ? 4 : 5;
    constexpr G2Prog() : ev{}, n(0) {
        add(E_B, 0); add(E_B, 1); add(E_B, 2); add(E_B, 3);
        add(E_STAMP, 1);
        for (int f = 0; f < G2_PD; ++f) add(E_READ, f);
        for (int f = 0; f < 162; ++f) {
            const int st = f / 18, i = f % 18;
            if (i % 9 == 0) add(E_WAITB, st * 2 + i / 9);
            const int ia = LATE ? 3 : 1, ib = LATE ? 1 : 3;
            if (st == 0) {
                if (i >= 1 && i < 5) add(E_B, 4 + i - 1);
                if (i >= 6 && i < 6 + NP) add(E_A, 16 + i - 6);
                if (i >= 12 && i < 16) add(E_B, 8 + i - 12);
            } else {
                if (st == 1 && i >= 14) add(E_B, 16 + i - 14);
                if (st + 2 < 9 && i >= ia && (i - ia) % 4 == 0 && (i - ia) / 4 < 4) add(E_A, (st + 2) * 8 + (i - ia) / 4);
                if (st + 2 < 9 && NP == 5 && i == 12) add(E_A, (st + 2) * 8 + 4);
                { const int la = st == 1 ? 2 : 3; if (st + la < 9 && (st == 1 || st + la > 4) && i >= ib && (i - ib) % 4 == 0 && (i - ib) / 4 < 4) add(E_B, (st + la) * 4 + (i - ib) / 4); }
            }
            if (i == 18 - G2_PD && st + 1 < 9) add(E_BAR, st + 1);
            if (f + G2_PD < 162) add(E_READ, f + G2_PD);
            add(E_MMA, f);
            if (i == 17) add(E_STAMP, 2 + st);
        }
        for (int i = 0; i < n; ++i) if (ev[i].type == E_WAITB || ev[i].type == E_BAR) {
            int cnt = 0, j = i - 1;
            for (; j >= 0; --j) {
                if (ev[i].type == E_WAITB && ev[j].type == E_B && ev[j].arg == 2 * ev[i].arg + 1) break;
                if (ev[i].type == E_BAR && ev[j].type == E_A && (ev[j].arg >> 3) == ev[i].arg) break;
                cnt += vm(ev[j]); }
            ev[i].n = cnt > 63 ? 63 : cnt; }
    }
};
template <bool LATE> constexpr G2Prog<LATE> g2prog{};
struct G2State {
    bf16x8 af[4][2][2]; f32x4 acc[9][2]; bf16x8 ar[G2_PD + 1];
    unsigned lb0, lb1, lb2; unsigned l16, ldsb, soffA, soffB; int wid;
};
template <bool LATE, int PC> DI void g2_exec(G2State& c, const srd_t srd) {
    constexpr Ev e = g2prog<LATE>.ev[PC];
    if constexpr (e.type == E_A) { constexpr int st = e.arg >> 3, p = e.arg & 7;
        const unsigned so = c.soffA + st * RING_SLOT, dst = c.ldsb + ((st + 1) & 3) * RING_SLOT;
        if constexpr (p < 4) bdma_next<p * 1024>(srd, c.l16, so + c.wid * 4096, dst + c.wid * 4096);
        else bdma1(srd, c.l16, so + 32768 + c.wid * 1024, dst + 32768 + c.wid * 1024); }
    else if constexpr (e.type == E_B) { constexpr int st = e.arg >> 2, s2 = (e.arg >> 1) & 1, u = e.arg & 1;
        bload16_sc1<((2 * st + s2) & 3) * 1024>(c.af[st & 3][s2][u], srd, c.l16, c.soffB + u * (144 * 1024) + ((2 * st + s2) >> 2) * 4096); }
    else if constexpr (e.type == E_WAITB) { asm volatile("s_waitcnt vmcnt(%2)" : "+v"(c.af[(e.arg >> 1) & 3][e.arg & 1][0]), "+v"(c.af[(e.arg >> 1) & 3][e.arg & 1][1]) : "n"(e.n)); }
    else if constexpr (e.type == E_BAR) { asm volatile("s_waitcnt vmcnt(%0)\n\ts_barrier" :: "n"(e.n) : "memory"); }
    else if constexpr (e.type == E_STAMP) {
    }
    else if constexpr (e.type == E_READ) { constexpr int f = e.arg, st = f / 18, off = ((st + 1) & 3) * RING_SLOT + (f % 18) * 1024;
        if constexpr ((off >> 16) == 0) c.ar[f % (G2_PD + 1)] = lds_frag((LAS const char*)(uintptr_t)c.lb0 + (off & 65535));
        else c.ar[f % (G2_PD + 1)] = lds_frag((LAS const char*)(uintptr_t)c.lb1 + (off & 65535));
        SBAR(); }
    else if constexpr (e.type == E_MMA) { constexpr int f = e.arg, st = f / 18, s2 = (f % 18) / 9, t = f % 9;
        if constexpr (f < 9) { mfma16_new(c.acc[t][0], c.ar[f % (G2_PD + 1)], c.af[st & 3][s2][0]); mfma16_new(c.acc[t][1], c.ar[f % (G2_PD + 1)], c.af[st & 3][s2][1]); }
        else { mfma16_acc(c.acc[t][0], c.ar[f % (G2_PD + 1)], c.af[st & 3][s2][0]); mfma16_acc(c.acc[t][1], c.ar[f % (G2_PD + 1)], c.af[st & 3][s2][1]); }
        SBAR(); }
}
template <bool LATE, int... I> DI void g2_run(G2State& c, const srd_t srd, std::integer_sequence<int, I...>) { (g2_exec<LATE, I>(c, srd), ...); }
DI void arrive(char* ws, int counter) { if (threadIdx.x == 0) __hip_atomic_fetch_add((unsigned*)(ws + WS_CNT) + counter * 64, 1u, __ATOMIC_RELAXED, __HIP_MEMORY_SCOPE_AGENT); }
DI bool await(char* ws, int counter, unsigned want, LAS unsigned* flag) {
    if (threadIdx.x == 0) { const unsigned* cnt = (const unsigned*)(ws + WS_CNT) + counter * 64; unsigned bad = 0;
        const unsigned long long t0 = __builtin_amdgcn_s_memrealtime();
        while (__hip_atomic_load(cnt, __ATOMIC_RELAXED, __HIP_MEMORY_SCOPE_AGENT) < want) {
            if (__builtin_amdgcn_s_memrealtime() - t0 > 2000000ull) { bad = 1; break; }
            __builtin_amdgcn_s_sleep(1); }
        *flag = bad; }
    LBAR();
    return *flag != 0;
}
constexpr int FFN_LDS = G_LDS + 1024;
__global__ __launch_bounds__(512, 2) void ffn_kernel(char* ws, char* out) {
    extern __shared__ __attribute__((aligned(16))) char lds_generic[];
    LAS char* lds = (LAS char*)lds_generic;
    LAS unsigned* flag = (LAS unsigned*)(lds + G_LDS);
    const srd_t srd = make_srd(ws, (unsigned)WS_END);
    const int b = blockIdx.x, xcd = b & 7, idx = b >> 3;
    bool bad;
    {
        const int tid = threadIdx.x, lane = tid & 63, wid = rfl(tid >> 6);
        const int j = (xcd >> 1) * 8 + (idx & 7), mt8 = (xcd & 1) * 4 + (idx >> 3), mt16 = 16 * mt8 + 2 * wid;
        G1State c;
        RSTAMP(4, 14); STAMP(4, 0);
        c.l16 = lane * 16; c.ldsb = (unsigned)(uintptr_t)lds_generic; c.wid = wid; c.j = j; c.g = lane >> 4;
        c.lb0 = c.ldsb + lane * 16; c.lb1 = c.lb0 + 65536; c.lb2 = c.lb0 + 131072; PINV(c.lb0); PINV(c.lb1); PINV(c.lb2);
        c.soffA = (unsigned)WS_W1 + j * (324 * 1024); c.soffH = (unsigned)WS_HCF + mt16 * (18 * 1024); c.soffS = (unsigned)WS_SSQ + mt16 * 1024;
        c.soffAct = (unsigned)WS_ACT + mt16 * (144 * 1024); c.vq = (lane & 15) * 64 + (lane >> 4) * 16;
        c.soffA2 = (unsigned)WS_W2 + ((xcd >> 1) * 4 + (idx >> 3)) * (324 * 1024);
        c.sink = 0.f; c.soffT = (unsigned)WS_W2 + (xcd >> 1) * (4 * 324 * 1024) + (idx * 8 + wid) * (41 * 128);
        if (wid >= 4) g1_run<true>(c, srd, std::make_integer_sequence<int, g1prog<true>.n>{});
        else g1_run<false>(c, srd, std::make_integer_sequence<int, g1prog<false>.n>{});
        STAMP(4, 11); RSTAMP(4, 15);
        asm volatile("s_waitcnt vmcnt(0) lgkmcnt(0)\n\ts_barrier" : "+v"(c.sink) :: "memory");
        arrive(ws, 64 + (j >> 3) * 8 + mt8);
        STAMP_FLUSH(4);
    }
    int tz = threadIdx.x; asm volatile("" : "+v"(tz));
    const int lane = tz & 63, wid = rfl(tz >> 6), mi = wid & 3, kj = wid >> 2;
    const int ksl = xcd >> 1, mt = (xcd & 1) * 8 + (idx & 7), n4 = idx >> 3;
    const int mt16 = 8 * mt + 2 * mi;
    G2State c;
    RSTAMP(5, 14); STAMP(5, 0);
    c.l16 = lane * 16; c.ldsb = (unsigned)(uintptr_t)lds_generic; c.wid = wid;
    c.lb0 = c.ldsb + kj * (18 * 1024) + lane * 16; c.lb1 = c.lb0 + 65536; PINV(c.lb0); PINV(c.lb1);
    c.soffA = (unsigned)WS_W2 + (ksl * 4 + n4) * (324 * 1024); c.soffB = (unsigned)WS_ACT + (mt16 * 144 + 36 * ksl + 18 * kj) * 1024;
    bad = await(ws, 64 + ksl * 8 + (mt >> 1), 8u, flag);
    if (wid >= 4) g2_run<true>(c, srd, std::make_integer_sequence<int, g2prog<true>.n>{});
    else g2_run<false>(c, srd, std::make_integer_sequence<int, g2prog<false>.n>{});
    asm volatile("s_nop 7" : "+v"(c.acc[8][0]), "+v"(c.acc[8][1]), "+v"(c.acc[7][0]), "+v"(c.acc[7][1]));
    LBAR();
    LAS char* ex = lds + mi * 18432 + lane * 16;
    if (kj == 0) {
#pragma unroll
        for (int t = 5; t < 9; ++t) { *(LAS f32x4*)(ex + (2 * t) * 1024) = c.acc[t][0]; *(LAS f32x4*)(ex + (2 * t + 1) * 1024) = c.acc[t][1]; }
    } else {
#pragma unroll
        for (int t = 0; t < 5; ++t) { *(LAS f32x4*)(ex + (2 * t) * 1024) = c.acc[t][0]; *(LAS f32x4*)(ex + (2 * t + 1) * 1024) = c.acc[t][1]; }
    }
    LBAR();
    STAMP(5, 11);
    const int tile = mt * 4 + n4; const unsigned xb = (unsigned)WS_PART + tile * (16 * 9 * 1024);
    auto fin = [&](int t) {
        const f32x4 a0 = c.acc[t][0] + *(LAS const f32x4*)(ex + (2 * t) * 1024), a1 = c.acc[t][1] + *(LAS const f32x4*)(ex + (2 * t + 1) * 1024);
        if (mi == ksl) { *(LAS f32x4*)(ex + (2 * t) * 1024) = a0; *(LAS f32x4*)(ex + (2 * t + 1) * 1024) = a1; }
        else bstore16<1>((u32x4){cvtpk(a0[0], a0[1]), cvtpk(a0[2], a0[3]), cvtpk(a1[0], a1[1]), cvtpk(a1[2], a1[3])}, srd, lane * 16, xb + ((mi * 4 + ksl) * 9 + t) * 1024); };
    if (kj == 0) { fin(0); fin(1); fin(2); fin(3); fin(4); } else { fin(5); fin(6); fin(7); fin(8); }
    u32x2 hv[3]; u32x4 pv[3]; u32x2 pw[3];
    const unsigned hb = (unsigned)WS_HC + ((8 * mt + 2 * ksl) * 36 + 9 * n4) * 512;
    bload8<0>(hv[0], srd, lane * 8, hb + wid * 512); bload8<0>(hv[1], srd, lane * 8, hb + (36 + wid) * 512); bload8<0>(hv[2], srd, lane * 8, hb + ((wid & 1) * 36 + 8) * 512);
    STAMP(5, 12);
    asm volatile("s_waitcnt vmcnt(0) lgkmcnt(0)\n\ts_barrier" : "+v"(hv[0]), "+v"(hv[1]), "+v"(hv[2]) :: "memory");
    STAMP(5, 13);
    arrive(ws, tile);
    bad |= await(ws, tile, 4u, flag);
#pragma unroll
    for (int k = 0; k < 3; ++k) { const unsigned sb = xb + (ksl * 4 + (k + (k >= ksl))) * (9 * 1024);
        bload16u_sc1<0>(pv[k], srd, lane * 16, sb + wid * 1024); bload8_sc1<0>(pw[k], srd, lane * 16 + (wid & 1) * 8, sb + 8 * 1024); }
    asm volatile("s_waitcnt vmcnt(0)" : "+v"(pv[0]), "+v"(pv[1]), "+v"(pv[2]), "+v"(pw[0]), "+v"(pw[1]), "+v"(pw[2]) :: "memory");
    const int m = lane & 15, g = lane >> 4;
    auto put = [&](int t, int u, u32x2 h2, u32x2 p0, u32x2 p1, u32x2 p2) {
        f32x4 a = *(LAS const f32x4*)(lds + ksl * 18432 + (2 * t + u) * 1024 + lane * 16);
        a += (f32x4){bflo(h2[0]), bfhi(h2[0]), bflo(h2[1]), bfhi(h2[1])}; a += (f32x4){bflo(p0[0]), bfhi(p0[0]), bflo(p0[1]), bfhi(p0[1])};
        a += (f32x4){bflo(p1[0]), bfhi(p1[0]), bflo(p1[1]), bfhi(p1[1])}; a += (f32x4){bflo(p2[0]), bfhi(p2[0]), bflo(p2[1]), bfhi(p2[1])};
        u32x2 o = {cvtpk(a[0], a[1]), cvtpk(a[2], a[3])};
        if (bad) o = (u32x2){0x7fc07fc0u, 0x7fc07fc0u};
        *(u32x2*)(out + (size_t)(128 * mt + 32 * ksl + 16 * u + m) * (D_ * 2) + (144 * n4 + 16 * t + 4 * g) * 2) = o; };
    put(wid, 0, hv[0], (u32x2){pv[0][0], pv[0][1]}, (u32x2){pv[1][0], pv[1][1]}, (u32x2){pv[2][0], pv[2][1]});
    put(wid, 1, hv[1], (u32x2){pv[0][2], pv[0][3]}, (u32x2){pv[1][2], pv[1][3]}, (u32x2){pv[2][2], pv[2][3]});
    if (wid < 2) put(8, wid, hv[2], pw[0], pw[1], pw[2]);
    RSTAMP(5, 15); STAMP_FLUSH(5);
}

}

extern "C" void kernel_launch(void* const* d_in, const int* in_sizes, int n_in, void* d_out, int out_size, void* d_ws, size_t ws_size, hipStream_t stream) {
    const char* x = (const char*)d_in[0]; const char* wqkv = (const char*)d_in[1]; const char* wo = (const char*)d_in[2];
    const char* wg = (const char*)d_in[4]; const char* wu = (const char*)d_in[5]; const char* wd = (const char*)d_in[6];
    char* ws = (char*)d_ws;
    (void)hipFuncSetAttribute((const void*)k1_qkv, hipFuncAttributeMaxDynamicSharedMemorySize, K1_LDS);
    (void)hipFuncSetAttribute((const void*)k2_attn, hipFuncAttributeMaxDynamicSharedMemorySize, K2_LDS);
    (void)hipFuncSetAttribute((const void*)k3_oproj, hipFuncAttributeMaxDynamicSharedMemorySize, K3_LDS);
    (void)hipFuncSetAttribute((const void*)ffn_kernel, hipFuncAttributeMaxDynamicSharedMemorySize, FFN_LDS);
    k1_qkv<<<256, 512, K1_LDS, stream>>>(x, wqkv, wo, ws);
    k2_attn<<<256, 1024, K2_LDS, stream>>>(x, wg, wu, wd, ws);
    k3_oproj<<<192, 512, K3_LDS, stream>>>(x, ws);
    ffn_kernel<<<256, 512, FFN_LDS, stream>>>(ws, (char*)d_out);
}
```

```cpp
#include <hip/hip_runtime.h>
#include <hip/hip_bf16.h>
#include <stdint.h>
#include <utility>
#define K1_TOUCH 0
#define TOUCH 1
#define K2_RT 1
#define K2_PRIO 1
#define K1_ROT 0
#define G1PD 4
#define ACT_POL 1
#define RT_POL 1
#define PART_POL 1
#define H_POL 1
#define QKV_POL 1
#define G1_PRIO 1

namespace {

typedef __bf16 bf16x2 __attribute__((ext_vector_type(2)));
typedef __bf16 bf16x8 __attribute__((ext_vector_type(8)));
typedef __bf16 bf16x16 __attribute__((ext_vector_type(16)));
typedef short s16x4 __attribute__((ext_vector_type(4)));
typedef float f32x2 __attribute__((ext_vector_type(2)));
typedef float f32x4 __attribute__((ext_vector_type(4)));
typedef float f32x8 __attribute__((ext_vector_type(8)));
typedef float f32x16 __attribute__((ext_vector_type(16)));
typedef unsigned u32x2 __attribute__((ext_vector_type(2)));
typedef unsigned u32x4 __attribute__((ext_vector_type(4)));
#define LAS __attribute__((address_space(3)))
#define DI __device__ __forceinline__

constexpr int S_ = 2048, D_ = 576, H_ = 32, F_ = 4608;
constexpr float EPS = 1e-5f;
constexpr float QSCALE = 0.23570226039551584f * 1.4426950408889634f;
constexpr float SAFE_B = 48.f;

constexpr int KVCHUNK = 19456, KVIMG = 8 * KVCHUNK;
constexpr bool OVERLAY = true;
constexpr size_t WS_QM   = 0;
constexpr size_t WS_QT   = WS_QM + (size_t)32 * 2048 * 32;
constexpr size_t WS_KV   = WS_QT + (size_t)32 * 2048 * 4;
constexpr size_t WS_A_END = WS_KV + (size_t)32 * KVIMG;
constexpr size_t WS_ACT  = OVERLAY ? 0 : WS_A_END;
constexpr size_t WS_OF   = WS_ACT + (size_t)128 * 144 * 1024;
constexpr size_t WS_WO   = WS_OF + (size_t)128 * 18 * 1024;
constexpr size_t WS_KMAX = WS_WO + (size_t)6 * 108 * 1024;
constexpr size_t WS_MUX  = WS_KMAX + 32 * 16 * 4;
constexpr size_t WS_WBAR = WS_MUX + 2048 * 4;
constexpr size_t WS_SSQ  = WS_WBAR + 4096;
constexpr size_t WS_CNT  = WS_SSQ + (size_t)2048 * 64;
constexpr size_t WS_HC   = WS_CNT + 104 * 256;
constexpr size_t WS_HCF  = WS_HC + (size_t)128 * 36 * 512;
constexpr size_t WS_W2   = WS_HCF + (size_t)128 * 18 * 1024;
constexpr size_t WS_W1   = WS_W2 + (size_t)16 * 324 * 1024;
constexpr size_t WS_PART = WS_W1 + (size_t)32 * 324 * 1024;
static_assert(WS_A_END <= (size_t)128 * 144 * 1024, "overlay fits");
constexpr size_t WS_END  = WS_PART + (size_t)64 * 16 * 9 * 1024;

#define STAMP_DECL
#define STAMP(kid, i)
#define RSTAMP(kid, i)
#define STAMP_FLUSH(kid)
DI unsigned cvtpk(float lo, float hi) { f32x2 v = {lo, hi}; bf16x2 b = __builtin_convertvector(v, bf16x2); return __builtin_bit_cast(unsigned, b); }
DI float bflo(unsigned w) { return __uint_as_float(w << 16); }
DI float bfhi(unsigned w) { return __uint_as_float(w & 0xffff0000u); }
DI float dot2(unsigned a, unsigned b, float c) { return __builtin_amdgcn_fdot2_f32_bf16(__builtin_bit_cast(bf16x2, a), __builtin_bit_cast(bf16x2, b), c, false); }
DI int rfl(int x) { return __builtin_amdgcn_readfirstlane(x); }
DI float xor32(float v) { auto r = __builtin_amdgcn_permlane32_swap(__float_as_uint(v), __float_as_uint(v), false, false); return __uint_as_float((threadIdx.x & 32) ? r[0] : r[1]); }
DI float xor16(float v) { auto r = __builtin_amdgcn_permlane16_swap(__float_as_uint(v), __float_as_uint(v), false, false); return __uint_as_float((threadIdx.x & 16) ? r[0] : r[1]); }
DI f32x4 mfma16(bf16x8 a, bf16x8 b, f32x4 c) { return __builtin_amdgcn_mfma_f32_16x16x32_bf16(a, b, c, 0, 0, 0); }
DI void mfma16_acc(f32x4& c, bf16x8 a, bf16x8 b) { asm volatile("v_mfma_f32_16x16x32_bf16 %0, %1, %2, %0" : "+v"(c) : "v"(a), "v"(b)); }
DI void mfma16_new(f32x4& c, bf16x8 a, bf16x8 b) { asm volatile("v_mfma_f32_16x16x32_bf16 %0, %1, %2, 0" : "=&v"(c) : "v"(a), "v"(b)); }
DI void glds16(const void* sbase, unsigned voff, unsigned lds_dst) {
    asm volatile("s_mov_b32 m0, %2\n\ts_nop 0\n\tglobal_load_lds_dwordx4 %0, %1" :: "v"(voff), "s"(sbase), "s"(lds_dst) : "memory");
}
DI void gload16(bf16x8& dst, const void* sbase, unsigned voff) { asm volatile("global_load_dwordx4 %0, %1, %2" : "=v"(dst) : "v"(voff), "s"(sbase)); }
DI void gload16f(f32x4& dst, const void* sbase, unsigned voff) { asm volatile("global_load_dwordx4 %0, %1, %2" : "=v"(dst) : "v"(voff), "s"(sbase)); }
DI void gload8(u32x2& dst, const void* sbase, unsigned voff) { asm volatile("global_load_dwordx2 %0, %1, %2" : "=v"(dst) : "v"(voff), "s"(sbase)); }
DI void gload4(float& dst, const void* sbase, unsigned voff) { asm volatile("global_load_dword %0, %1, %2" : "=v"(dst) : "v"(voff), "s"(sbase)); }
typedef __amdgpu_buffer_rsrc_t srd_t;
DI srd_t make_srd(const void* p, unsigned bytes) { return __builtin_amdgcn_make_buffer_rsrc((void*)p, (short)0, (int)bytes, 0x00020000); }
template <int IMM> DI void bload16(bf16x8& dst, srd_t r, unsigned voff, unsigned soff) {
    asm volatile("buffer_load_dwordx4 %0, %1, %2, %3 offen offset:%4" : "=v"(dst) : "v"(voff), "s"(r), "s"(soff), "n"(IMM)); }
template <int IMM> DI void bload16f(f32x4& dst, srd_t r, unsigned voff, unsigned soff) {
    asm volatile("buffer_load_dwordx4 %0, %1, %2, %3 offen offset:%4" : "=v"(dst) : "v"(voff), "s"(r), "s"(soff), "n"(IMM)); }
template <int IMM> DI void bload4(float& dst, srd_t r, unsigned voff, unsigned soff) {
    asm volatile("buffer_load_dword %0, %1, %2, %3 offen offset:%4" : "=v"(dst) : "v"(voff), "s"(r), "s"(soff), "n"(IMM)); }
template <int IMM> DI void bload16_sc1(bf16x8& dst, srd_t r, unsigned voff, unsigned soff) {
    asm volatile("buffer_load_dwordx4 %0, %1, %2, %3 offen offset:%4 sc1" : "=v"(dst) : "v"(voff), "s"(r), "s"(soff), "n"(IMM)); }
template <int IMM> DI void bload16f_sc1(f32x4& dst, srd_t r, unsigned voff, unsigned soff) {
    asm volatile("buffer_load_dwordx4 %0, %1, %2, %3 offen offset:%4 sc1" : "=v"(dst) : "v"(voff), "s"(r), "s"(soff), "n"(IMM)); }
template <int IMM> DI void bload16u_sc1(u32x4& dst, srd_t r, unsigned voff, unsigned soff) {
    asm volatile("buffer_load_dwordx4 %0, %1, %2, %3 offen offset:%4 sc1" : "=v"(dst) : "v"(voff), "s"(r), "s"(soff), "n"(IMM)); }
template <int IMM> DI void bload8_sc1(u32x2& dst, srd_t r, unsigned voff, unsigned soff) {
    asm volatile("buffer_load_dwordx2 %0, %1, %2, %3 offen offset:%4 sc1" : "=v"(dst) : "v"(voff), "s"(r), "s"(soff), "n"(IMM)); }
template <int IMM> DI void bload8(u32x2& dst, srd_t r, unsigned voff, unsigned soff) {
    asm volatile("buffer_load_dwordx2 %0, %1, %2, %3 offen offset:%4" : "=v"(dst) : "v"(voff), "s"(r), "s"(soff), "n"(IMM)); }
DI void touch64(float& sink, srd_t r, unsigned lane128, unsigned soff) { asm volatile("buffer_load_dword %0, %1, %2, %3 offen" : "+v"(sink) : "v"(lane128), "s"(r), "s"(soff)); }
DI void bdma4(srd_t r, unsigned voff, unsigned soff, unsigned lds_dst) {
    asm volatile("s_mov_b32 m0, %3\n\ts_nop 0\n\tbuffer_load_dwordx4 %0, %1, %2 offen lds\n\tbuffer_load_dwordx4 %0, %1, %2 offen offset:1024 lds\n\t"
                 "buffer_load_dwordx4 %0, %1, %2 offen offset:2048 lds\n\tbuffer_load_dwordx4 %0, %1, %2 offen offset:3072 lds" :: "v"(voff), "s"(r), "s"(soff), "s"(lds_dst) : "memory"); }
DI void bdma1(srd_t r, unsigned voff, unsigned soff, unsigned lds_dst) {
    asm volatile("s_mov_b32 m0, %3\n\ts_nop 0\n\tbuffer_load_dwordx4 %0, %1, %2 offen lds" :: "v"(voff), "s"(r), "s"(soff), "s"(lds_dst) : "memory"); }
template <int IMM> DI void bdma_next(srd_t r, unsigned voff, unsigned soff, unsigned lds_dst) {
    asm volatile("s_mov_b32 m0, %3\n\ts_nop 0\n\tbuffer_load_dwordx4 %0, %1, %2 offen offset:%4 lds" :: "v"(voff), "s"(r), "s"(soff), "s"(lds_dst), "n"(IMM) : "memory"); }
template <int POL> DI void bstore16(u32x4 d, srd_t r, unsigned voff, unsigned soff) {
    if constexpr (POL == 1) asm volatile("buffer_store_dwordx4 %0, %1, %2, %3 offen sc1\n\ts_nop 1" :: "v"(d), "v"(voff), "s"(r), "s"(soff) : "memory");
    else if constexpr (POL == 2) asm volatile("buffer_store_dwordx4 %0, %1, %2, %3 offen nt\n\ts_nop 1" :: "v"(d), "v"(voff), "s"(r), "s"(soff) : "memory");
    else asm volatile("buffer_store_dwordx4 %0, %1, %2, %3 offen\n\ts_nop 1" :: "v"(d), "v"(voff), "s"(r), "s"(soff) : "memory"); }
template <int POL> DI void bstore8(u32x2 d, srd_t r, unsigned voff, unsigned soff) {
    if constexpr (POL == 1) asm volatile("buffer_store_dwordx2 %0, %1, %2, %3 offen sc1\n\ts_nop 1" :: "v"(d), "v"(voff), "s"(r), "s"(soff) : "memory");
    else if constexpr (POL == 2) asm volatile("buffer_store_dwordx2 %0, %1, %2, %3 offen nt\n\ts_nop 1" :: "v"(d), "v"(voff), "s"(r), "s"(soff) : "memory");
    else asm volatile("buffer_store_dwordx2 %0, %1, %2, %3 offen\n\ts_nop 1" :: "v"(d), "v"(voff), "s"(r), "s"(soff) : "memory"); }
#define PINV(x) asm volatile("" : "+v"(x))
#define WAITV(n) asm volatile("s_waitcnt vmcnt(%0)" :: "n"(n) : "memory")
#define WAIT_BAR(n) asm volatile("s_waitcnt vmcnt(%0) lgkmcnt(0)\n\ts_barrier" :: "n"(n) : "memory")
#define SBAR() __builtin_amdgcn_sched_barrier(0)
#define LBAR() asm volatile("s_waitcnt lgkmcnt(0)\n\ts_barrier" ::: "memory")
DI bf16x8 lds_frag(LAS const char* p) { return *(LAS const bf16x8*)p; }
typedef short v4i16_t __attribute__((ext_vector_type(4)));
DI u32x2 lds_tr(LAS const char* p) { return __builtin_bit_cast(u32x2, __builtin_amdgcn_ds_read_tr16_b64_v4i16((LAS v4i16_t*)p)); }
DI bf16x8 tr_frag(LAS const char* p, int off2) { u32x2 a = lds_tr(p), b = lds_tr(p + off2); u32x4 r = {a[0], a[1], b[0], b[1]}; return __builtin_bit_cast(bf16x8, r); }

DI int kstep_to_ftile(int ks, int half) {
    const int a = ks / 9, idx = ks - 9 * a;
    if (idx == 8) return 9 * (2 * a + half) + 8;
    return 9 * (2 * a + (idx >> 2)) + 2 * (idx & 3) + half;
}

struct RetileJob { const char* src; unsigned voff; unsigned dst[4]; bool live; };
DI void retile_setup(RetileJob& J, int b, int lane, const void* wo, const void* wg, const void* wu, const void* wd) {
    J.live = b < 4050;
    const int r8 = lane >> 3, ch = lane & 7;
    if (b < 2592) {
        const int gu = b >= 1296, bb = b - 1296 * gu, s = bb / 72, cb = bb - 72 * s;
        J.src = (const char*)(gu ? wu : wg) + (size_t)s * 32 * (F_ * 2) + cb * 128;
        J.voff = r8 * (F_ * 2) + ch * 16;
#pragma unroll
        for (int u = 0; u < 4; ++u) { const int T = 4 * cb + u, j = T / 9, tt = T - 9 * j;
            J.dst[u] = (unsigned)(WS_W1 + (size_t)(j * 324 + (tt < 8 ? (tt >> 1) * 72 + s * 4 + (tt & 1) * 2 : 288 + s * 2) + gu) * 1024); }
    } else if (b < 3888) {
        const int bb = b - 2592, ks = bb / 9, cb = bb - 9 * ks;
        J.src = (const char*)wd + cb * 128;
        J.voff = (unsigned)ks;
        const int ksl = ks / 36, kap = ks - 36 * ksl, kj = kap / 18, r = kap - 18 * kj, st = r >> 1, s2 = r & 1;
#pragma unroll
        for (int u = 0; u < 4; ++u) { const int T = 4 * cb + u, n4 = T / 9, tt = T - 9 * n4;
            J.dst[u] = (unsigned)(WS_W2 + (size_t)((ksl * 4 + n4) * 324 + ((st * 2 + kj) * 2 + s2) * 9 + tt) * 1024); }
    } else {
        const int bb = (b < 4050 ? b : 4049) - 3888, s = bb / 9, cb = bb - 9 * s;
        J.src = (const char*)wo + (size_t)s * 32 * (D_ * 2) + cb * 128;
        J.voff = ch * 16;
#pragma unroll
        for (int u = 0; u < 4; ++u) { const int T = 4 * cb + u, n6 = T / 6, tt = T - 6 * n6;
            J.dst[u] = (unsigned)(WS_WO + (size_t)(n6 * 108 + s * 6 + tt) * 1024); }
    }
}
DI void retile_issue(const RetileJob& J, int b, int lane, unsigned lds_scratch) {
    const int r8 = lane >> 3, ch = lane & 7;
#pragma unroll
    for (int p = 0; p < 4; ++p) {
        unsigned voff;
        if (b < 2592) voff = J.voff + p * 8 * (F_ * 2);
        else if (b < 3888) { const int rho = 8 * p + r8, T = kstep_to_ftile((int)J.voff, rho >> 4); voff = (16 * T + (rho & 15)) * (D_ * 2) + ch * 16; }
        else { const int rho = 8 * p + r8, k = 8 * ((rho >> 2) & 3) + 4 * (rho >> 4) + (rho & 3); voff = J.voff + k * (D_ * 2); }
        glds16(J.src, voff, lds_scratch + p * 1024);
    }
}
DI void retile_finish(const RetileJob& J, int lane, LAS const char* scratch, char* ws) {
    const int g = lane >> 4, q = (lane >> 2) & 3, p = lane & 3;
    LAS const char* base = scratch + (4 * g + q) * 128 + p * 8;
#pragma unroll
    for (int u = 0; u < 4; ++u) { const bf16x8 f = tr_frag(base + 32 * u, 16 * 128); if (J.live) *(bf16x8*)(ws + J.dst[u] + lane * 16) = f; }
}

constexpr int K1_SLOT = 34 * 1024, K1_STG = 0, K1_SMALL = 4 * K1_SLOT, K1_LDS = K1_SMALL + 4096;
template <int PART, int NJ>
DI void k1_wave(const char* x, const char* wqkv, char* ws, char* lds_generic, int mt, int nt, int rot, int shx, int shw) {
    constexpr bool VPART = PART == 2; constexpr int NT = NJ ? 4 : 5, T0 = NJ ? 5 : 0;
    LAS char* lds = (LAS char*)lds_generic; const unsigned ldsb = (unsigned)(uintptr_t)lds_generic;
    const int tid = threadIdx.x, lane = tid & 63, wid = rfl(tid >> 6), mi = wid & 3, m = lane & 15, g = lane >> 4;
    STAMP_DECL; RSTAMP(1, 14); STAMP(1, 0);
    const srd_t sx = make_srd(x, S_ * D_ * 2), sw = make_srd(wqkv, D_ * 3 * D_ * 2);
    const unsigned xv = (lane >> 3) * (D_ * 2) + (((lane & 7) ^ ((4 * wid + (lane >> 4)) & 7)) << 4);
    unsigned wv[2];
#pragma unroll
    for (int i = 0; i < 2; ++i) { const int idx = (i ? 8 : wid) * 64 + lane, rho = (idx * 3641) >> 16, ch = idx - 18 * rho;
        wv[i] = (8 * ((rho >> 2) & 3) + 4 * (rho >> 4) + (rho & 3)) * 3456 + ch * 16; }
    const unsigned xs = (128 * mt + 8 * wid) * (D_ * 2), wsb = nt * 288;
    auto issue = [&](int st, int p) {
        int k = st + rot; k = k >= 9 ? k - 9 : k; const unsigned dst = ldsb + (st & 3) * K1_SLOT;
        if (p < 2) bdma1(sx, xv, xs + p * 64 * (D_ * 2) + k * 128, dst + (wid + 8 * p) * 1024);
        else if (p < 4) bdma1(sw, wv[0], wsb + (64 * k + 32 * (p - 2)) * 3456, dst + 16384 + ((p - 2) * 9 + wid) * 1024);
        else if (wid < 2) bdma1(sw, wv[1], wsb + (64 * k + 32 * wid) * 3456, dst + 16384 + (wid * 9 + 8) * 1024); };
#pragma unroll
    for (int st = 0; st < 3; ++st)
#pragma unroll
        for (int p = 0; p < 5; ++p) issue(st, p);
    float tch[2] = {0.f, 0.f};
#pragma unroll
    for (int i = 0; i < 2 * K1_TOUCH; ++i) { int t = wid + 8 * i; t = t >= 12 ? t - 4 : t;
        if (t < 3) { const int L = shx * 192 + t * 64 + lane, row = (L * 7282) >> 16; bload4<0>(tch[i], sx, (128 * mt + row) * (D_ * 2) + (L - 9 * row) * 128, 0); }
        else { const int q = (t - 3) * 64 + lane; bload4<0>(tch[i], sw, (shw * 144 + (q >> 2)) * 3456 + ((nt * 288) & ~127) + (q & 3) * 128, 0); } }
    f32x4 acc[NT][2];
    f32x4 accs = {0, 0, 0, 0}, accg = {0, 0, 0, 0}, accc[NT];
#pragma unroll
    for (int t = 0; t < NT; ++t) accc[t] = (f32x4){0, 0, 0, 0};
    bf16x8 ones = __builtin_bit_cast(bf16x8, (u32x4){0x3f803f80u, 0x3f803f80u, 0x3f803f80u, 0x3f803f80u}); PINV(ones);
    asm volatile("s_nop 1" : "+v"(accs), "+v"(accg));
#pragma unroll
    for (int t = 0; t < NT; ++t) PINV(accc[t]);
    const unsigned xa0 = (32 * mi + m) * 128 + ((g ^ ((m >> 1) & 7)) << 4), xa1 = (32 * mi + m) * 128 + (((4 + g) ^ ((m >> 1) & 7)) << 4);
    const unsigned wa = 16384 + (4 * g + ((lane >> 2) & 3)) * 288 + (lane & 3) * 8 + T0 * 32;
    bf16x8 xq[2][2][2], wr[2][2][NT];
    auto rd = [&](int st, int i) {
        LAS const char* sb = lds + (st & 3) * K1_SLOT;
        if (i < 4) xq[st & 1][i >> 1][i & 1] = lds_frag(sb + ((i >> 1) ? xa1 : xa0) + (i & 1) * 2048);
        else { const int s2 = (i - 4) / NT, t = (i - 4) % NT; wr[st & 1][s2][t] = tr_frag(sb + wa + s2 * 9216 + 32 * t, 16 * 288); } };
    STAMP(1, 1);
    WAIT_BAR(8 + K1_TOUCH * 2);
    STAMP(1, 2);
#pragma unroll
    for (int i = 0; i < 4 + 2 * NT; ++i) rd(0, i);
#pragma unroll
    for (int st = 0; st < 9; ++st) {
        if (st < 8) { if (st < 2) WAIT_BAR(4 + K1_TOUCH * 2); else if (st < 6) WAIT_BAR(4); else if (st == 6) WAIT_BAR(4); else WAIT_BAR(0); }
        if (st == 2) { PINV(tch[0]); PINV(tch[1]); }
#pragma unroll
        for (int i = 0; i < 2 * NT; ++i) { const int s2 = i / NT, t = i % NT; const bf16x8 w = wr[st & 1][s2][t], x0 = xq[st & 1][s2][0], x1 = xq[st & 1][s2][1];
            if (st == 0 && s2 == 0) { if (VPART) { mfma16_new(acc[t][0], x0, w); mfma16_new(acc[t][1], x1, w); } else { mfma16_new(acc[t][0], w, x0); mfma16_new(acc[t][1], w, x1); } }
            else { if (VPART) { mfma16_acc(acc[t][0], x0, w); mfma16_acc(acc[t][1], x1, w); } else { mfma16_acc(acc[t][0], w, x0); mfma16_acc(acc[t][1], w, x1); } }
            if (st + 1 < 9) { if (i < 4) { rd(st + 1, i); rd(st + 1, 4 + i); } else if (4 + i < 4 + 2 * NT) rd(st + 1, 4 + i); }
            if (st + 3 < 9 && (i & 1) == 0 && i / 2 < 4) issue(st + 3, i / 2 == 0 ? 0 : i / 2 == 1 ? 2 : i / 2 == 2 ? 1 : 3);
            if (st + 3 < 9 && i == 7) issue(st + 3, 4);
            if (t == 1) mfma16_acc(accs, ones, NJ ? x1 : x0);
            if (t == 2) mfma16_acc(accg, NJ ? x1 : x0, NJ ? x1 : x0);
            if (t == NT - 1 && ((2 * st + s2) & 3) == mi) {
#pragma unroll
                for (int tt = 0; tt < NT; ++tt) { if (VPART) mfma16_acc(accc[tt], ones, wr[st & 1][s2][tt]); else mfma16_acc(accc[tt], wr[st & 1][s2][tt], ones); } }
            SBAR(); }
    }
    STAMP(1, 3);
    LAS float* sm = (LAS float*)(lds + K1_SMALL);
    asm volatile("s_nop 7\n\ts_nop 7" : "+v"(acc[NT - 1][0]), "+v"(acc[NT - 1][1]), "+v"(accs), "+v"(accg));
#pragma unroll
    for (int t = 0; t < NT; ++t) { PINV(accc[t]); PINV(acc[t][0]); PINV(acc[t][1]); }
    if (g == 0) sm[32 * mi + 16 * NJ + m] = accs[0];
    if (g == (m >> 2)) sm[128 + 32 * mi + 16 * NJ + m] = (m & 2) ? ((m & 1) ? accg[3] : accg[2]) : ((m & 1) ? accg[1] : accg[0]);
#pragma unroll
    for (int t = 0; t < NT; ++t) { if (VPART) { if (g == 0) sm[256 + mi * 144 + 16 * (T0 + t) + m] = accc[t][0]; }
        else if (m == 0) *(LAS f32x4*)(sm + 256 + mi * 144 + 16 * (T0 + t) + 4 * g) = accc[t]; }
    LBAR();
    STAMP(1, 4);
    LAS char* stg = lds + K1_STG;
    if (VPART && tid < 256) { const int hs = tid >> 1;
        *(LAS u32x4*)(stg + hs * 320 + ((tid & 1) ? 19 : 4) * 16) = (tid & 1) ? (u32x4){0x3f803f80u, 0x3f803f80u, 0x3f803f80u, 0x3f803f80u} : (u32x4){0, 0, 0, 0}; }
    f32x4 mean[2], rstd[2];
#pragma unroll
    for (int u = 0; u < 2; ++u) { const int tl = 32 * mi + 16 * u + (VPART ? 4 * g : m);
        if (VPART) { mean[u] = *(LAS const f32x4*)(sm + tl) * (1.f / D_);
            const f32x4 var = *(LAS const f32x4*)(sm + 128 + tl) * (1.f / D_) - mean[u] * mean[u];
#pragma unroll
            for (int r = 0; r < 4; ++r) rstd[u][r] = __builtin_amdgcn_rsqf(fmaxf(var[r], 0.f) + EPS); }
        else { mean[u][0] = sm[tl] * (1.f / D_);
            rstd[u][0] = __builtin_amdgcn_rsqf(fmaxf(sm[128 + tl] * (1.f / D_) - mean[u][0] * mean[u][0], 0.f) + EPS) * (PART == 0 ? QSCALE : 1.f); } }
#pragma unroll
    for (int t = 0; t < NT; ++t) { const int tt = T0 + t;
        if (VPART) { const int n = 16 * tt + m, hl = (n * 3641) >> 16, d = n - 18 * hl, row = d + (d >= 4 ? 5 : 0) - (d >= 12 ? 12 : 0) + (d >= 16 ? 8 : 0);
            const float c1 = (sm[256 + n] + sm[400 + n]) + (sm[544 + n] + sm[688 + n]);
            LAS char* p = stg + hl * 5120 + 2 * mi * 640 + (g >> 1) * 320 + row * 16 + (g & 1) * 8;
#pragma unroll
            for (int u = 0; u < 2; ++u) { const f32x4 a = (acc[t][u] - mean[u] * c1) * rstd[u];
                *(LAS u32x2*)(p + u * 640) = (u32x2){cvtpk(a[0], a[1]), cvtpk(a[2], a[3])}; }
        } else {
            const int n0 = 16 * tt + 4 * g;
            const f32x4 c4 = (*(LAS const f32x4*)(sm + 256 + n0) + *(LAS const f32x4*)(sm + 400 + n0)) + (*(LAS const f32x4*)(sm + 544 + n0) + *(LAS const f32x4*)(sm + 688 + n0));
            int o[2], ts[2];
#pragma unroll
            for (int p = 0; p < 2; ++p) { const int n = n0 + 2 * p, hl = (n * 3641) >> 16, d = n - 18 * hl; const bool tail = d == 16;
                if (PART == 0) { o[p] = tail ? 32768 + hl * 512 : hl * 4096 + d * 2; ts[p] = tail ? 4 : 32; }
                else { o[p] = hl * 4608 + (tail ? 4096 : (d >> 3) * 2048 + (d & 7) * 2); ts[p] = tail ? 4 : 16; } }
#pragma unroll
            for (int u = 0; u < 2; ++u) { const f32x4 a = (acc[t][u] - c4 * mean[u][0]) * rstd[u][0]; const int tl = 32 * mi + 16 * u + m;
                *(LAS unsigned*)(stg + o[0] + tl * ts[0]) = cvtpk(a[0], a[1]); *(LAS unsigned*)(stg + o[1] + tl * ts[1]) = cvtpk(a[2], a[3]); }
        } }
    if (nt == 0 && tid < 128) ((float*)(ws + WS_MUX))[128 * mt + tid] = sm[tid] * (1.f / D_);
    LBAR();
    STAMP(1, 5);
    const int head = 8 * (nt & 3) + wid; const srd_t sws = make_srd(ws, (unsigned)WS_END); const unsigned l16 = lane * 16;
    if (PART == 2) { const unsigned dst = (unsigned)WS_KV + head * KVIMG + (mt >> 1) * KVCHUNK + 9216 + (mt & 1) * 5120;
#pragma unroll
        for (int i = 0; i < 5; ++i) bstore16<QKV_POL>(*(LAS const u32x4*)(stg + wid * 5120 + i * 1024 + l16), sws, l16, dst + i * 1024);
    } else if (PART == 0) { const unsigned dst = (unsigned)WS_QM + (head * S_ + 128 * mt) * 32;
#pragma unroll
        for (int i = 0; i < 4; ++i) bstore16<QKV_POL>(*(LAS const u32x4*)(stg + wid * 4096 + i * 1024 + l16), sws, l16, dst + i * 1024);
        if (lane < 32) bstore16<QKV_POL>(*(LAS const u32x4*)(stg + 32768 + wid * 512 + l16), sws, l16, (unsigned)WS_QT + (head * S_ + 128 * mt) * 4);
    } else { const unsigned dst = (unsigned)WS_KV + head * KVIMG + (mt >> 1) * KVCHUNK; LAS const char* hb = stg + wid * 4608;
#pragma unroll
        for (int i = 0; i < 4; ++i) bstore16<QKV_POL>(*(LAS const u32x4*)(hb + i * 1024 + l16), sws, l16, dst + (i >> 1) * 4096 + (mt & 1) * 2048 + (i & 1) * 1024);
        if (lane < 32) bstore16<QKV_POL>(*(LAS const u32x4*)(hb + 4096 + l16), sws, l16, dst + 8192 + (mt & 1) * 512);
        float mx = 0.f;
#pragma unroll
        for (int i = 0; i < 2; ++i) { const int tl = lane + 64 * i;
            const u32x4 a = *(LAS const u32x4*)(hb + tl * 16), bq = *(LAS const u32x4*)(hb + 2048 + tl * 16); const unsigned c = *(LAS const unsigned*)(hb + 4096 + tl * 4);
            float s2 = dot2(c, c, 0.f);
#pragma unroll
            for (int e = 0; e < 4; ++e) { s2 = dot2(a[e], a[e], s2); s2 = dot2(bq[e], bq[e], s2); }
            mx = fmaxf(mx, s2); }
        mx = fmaxf(mx, xor16(mx)); mx = fmaxf(mx, xor32(mx));
#pragma unroll
        for (int o = 1; o < 16; o <<= 1) mx = fmaxf(mx, __shfl_xor(mx, o));
        if (lane == 0) ((float*)(ws + WS_KMAX))[head * 16 + mt] = mx;
    }
    STAMP(1, 6); RSTAMP(1, 15); STAMP_FLUSH(1);
}
template <int PART> DI void k1_body(const char* x, const char* wqkv, char* ws, char* lds, int mt, int nt, int rot, int shx, int shw) {
    if (threadIdx.x >= 256) k1_wave<PART, 1>(x, wqkv, ws, lds, mt, nt, rot, shx, shw); else k1_wave<PART, 0>(x, wqkv, ws, lds, mt, nt, rot, shx, shw);
}
__global__ __launch_bounds__(512, 2) void k1_qkv(const char* x, const char* wqkv, const char* wo, char* ws) {
    extern __shared__ __attribute__((aligned(16))) char lds[];
    const int b = blockIdx.x;
    if (b >= 192) {
        const int lane = threadIdx.x & 63, wv = rfl(threadIdx.x >> 6), gw = (b - 192) * 8 + wv;
        if (threadIdx.x == 511) ((unsigned*)(ws + WS_CNT))[(b - 192) * 64] = 0u;
        if (threadIdx.x == 510 && b - 192 < 40) ((unsigned*)(ws + WS_CNT))[(64 + b - 192) * 64] = 0u;
        if (gw < 162) { RetileJob J; retile_setup(J, 3888 + gw, lane, wo, nullptr, nullptr, nullptr);
            retile_issue(J, 3888 + gw, lane, (unsigned)(uintptr_t)lds + wv * 4096); WAITV(0); retile_finish(J, lane, (LAS const char*)lds + wv * 4096, ws); }
        for (int c = gw; c < D_; c += 512) {
            const char* row = wo + (size_t)c * (D_ * 2);
            const u32x4 a = *(const u32x4*)(row + lane * 16); float s = 0.f;
#pragma unroll
            for (int e = 0; e < 4; ++e) s = dot2(a[e], 0x3f803f80u, s);
            if (lane < 8) { const u32x4 bq = *(const u32x4*)(row + 1024 + lane * 16);
#pragma unroll
                for (int e = 0; e < 4; ++e) s = dot2(bq[e], 0x3f803f80u, s); }
#pragma unroll
            for (int o = 1; o < 64; o <<= 1) s += __shfl_xor(s, o);
            if (lane == 0) { const float w = s * (1.f / D_); const unsigned hi = cvtpk(w, 0.f) & 0xffffu; const unsigned lo = cvtpk(w - bflo(hi), 0.f) & 0xffffu;
                ((unsigned short*)(ws + WS_WBAR))[c] = (unsigned short)hi; ((unsigned short*)(ws + WS_WBAR))[D_ + c] = (unsigned short)lo; }
        }
        return;
    }
    const int xcd = b & 7, idx = b >> 3, mt = (xcd & 3) * 4 + (idx & 3), nt = (xcd >> 2) * 6 + (idx >> 2);
    const int rot = K1_ROT ? (2 * (idx & 3) + ((idx >> 2) * 3 >> 1)) % 9 : 0;
    const int shx = idx >> 2, shw = idx & 3;
    if (nt >= 8) k1_body<2>(x, wqkv, ws, lds, mt, nt, rot, shx, shw); else if (nt >= 4) k1_body<1>(x, wqkv, ws, lds, mt, nt, rot, shx, shw); else k1_body<0>(x, wqkv, ws, lds, mt, nt, rot, shx, shw);
}

constexpr int K2_LDS = 160 * 1024;
#define EXPS(S, a, b) _Pragma("unroll") for (int r_ = a; r_ < b; ++r_) S[r_] = __builtin_amdgcn_exp2f(S[r_]); SBAR()
#define PLO(S) __builtin_bit_cast(bf16x16, (f32x8)__builtin_shufflevector(S, S, 0, 1, 2, 3, 4, 5, 6, 7))
#define PHI(S) __builtin_bit_cast(bf16x16, (f32x8)__builtin_shufflevector(S, S, 8, 9, 10, 11, 12, 13, 14, 15))
#define KTAIL(p) __builtin_bit_cast(s16x4, (u32x2){*(LAS const unsigned*)(p), kc})
#define QK1(q) __builtin_amdgcn_mfma_f32_32x32x16_bf16(kf, q, (f32x16){}, 0, 0, 0)
#define QK2(q, s) __builtin_amdgcn_mfma_f32_32x32x8bf16_1k(kt, q, s, 0, 0, 0)
#define PV(v, p, o) __builtin_amdgcn_smfmac_f32_32x32x32_bf16(v, p, o, 0xDDDD, 0, 0)
DI void attn_fast(LAS const char* kp, LAS const char* tp, LAS const char* vp, const bf16x8 (&qm)[2], const s16x4 (&qt)[2], f32x16 (&o)[2], unsigned kc) {
    bf16x8 kf = *(LAS const bf16x8*)kp; s16x4 kt = KTAIL(tp); bf16x8 v0, v1;
    f32x16 s0 = QK1(qm[0]), s1; s0 = QK2(qt[0], s0);
    SBAR();
#pragma unroll
    for (int it = 0; it < 8; ++it) {
        if (K2_PRIO) { if (it == 0) __builtin_amdgcn_s_setprio(3); if (it == 2) __builtin_amdgcn_s_setprio(2); if (it == 4) __builtin_amdgcn_s_setprio(1); if (it == 6) __builtin_amdgcn_s_setprio(0); }
        EXPS(s0, 0, 2);
        if (it > 0) { o[1] = PV(v0, PLO(s1), o[1]); SBAR(); }
        EXPS(s0, 2, 6);
        if (it > 0) { o[1] = PV(v1, PHI(s1), o[1]); SBAR(); }
        v0 = *(LAS const bf16x8*)(vp + it * 1280); v1 = *(LAS const bf16x8*)(vp + it * 1280 + 640); SBAR();
        EXPS(s0, 6, 9);
        s1 = QK1(qm[1]); SBAR();
        EXPS(s0, 9, 12);
        s1 = QK2(qt[1], s1); SBAR();
        if (it + 1 < 8) { kf = *(LAS const bf16x8*)(kp + (it + 1) * 512); kt = KTAIL(tp + (it + 1) * 128); SBAR(); }
        EXPS(s0, 12, 16);
        EXPS(s1, 0, 2);
        o[0] = PV(v0, PLO(s0), o[0]); SBAR();
        EXPS(s1, 2, 6);
        o[0] = PV(v1, PHI(s0), o[0]); SBAR();
        EXPS(s1, 6, 9);
        if (it + 1 < 8) { s0 = QK1(qm[0]); SBAR(); }
        EXPS(s1, 9, 12);
        if (it + 1 < 8) { s0 = QK2(qt[0], s0); SBAR(); }
        EXPS(s1, 12, 16);
    }
    o[1] = PV(v0, PLO(s1), o[1]); o[1] = PV(v1, PHI(s1), o[1]);
}
DI void attn_slow(LAS const char* kp, LAS const char* tp, LAS const char* vp, const bf16x8 (&qm)[2], const s16x4 (&qt)[2], f32x16 (&o)[2], float (&mrun)[2], unsigned kc) {
#pragma unroll 1
    for (int it = 0; it < 8; ++it) {
        const bf16x8 kf = *(LAS const bf16x8*)(kp + it * 512); const s16x4 kt = KTAIL(tp + it * 128);
        const bf16x8 v0 = *(LAS const bf16x8*)(vp + it * 1280), v1 = *(LAS const bf16x8*)(vp + it * 1280 + 640);
#pragma unroll
        for (int u = 0; u < 2; ++u) {
            f32x16 s = QK1(qm[u]); s = QK2(qt[u], s);
            float mx = s[0];
#pragma unroll
            for (int r = 1; r < 16; ++r) mx = fmaxf(mx, s[r]);
            mx = fmaxf(mx, xor32(mx));
            const float mn = fmaxf(mrun[u], mx), al = __builtin_amdgcn_exp2f(mrun[u] - mn);
            mrun[u] = mn;
#pragma unroll
            for (int r = 0; r < 16; ++r) { s[r] = __builtin_amdgcn_exp2f(s[r] - mn); o[u][r] *= al; }
            o[u] = PV(v0, PLO(s), o[u]); o[u] = PV(v1, PHI(s), o[u]);
        }
    }
}
__global__ __launch_bounds__(1024, 4) void k2_attn(const char* x, const char* wg, const char* wu, const char* wd, char* ws) {
    extern __shared__ __attribute__((aligned(16))) char lds_generic[];
    LAS char* lds = (LAS char*)lds_generic; const unsigned ldsb = (unsigned)(uintptr_t)lds_generic;
    const int tid = threadIdx.x, lane = tid & 63, wid = rfl(tid >> 6), qg = wid & 3, kq = wid >> 2, r32 = lane & 31, h = lane >> 5;
    const int b = blockIdx.x, xcd = b & 7, idx = b >> 3, head = xcd * 4 + (idx & 3), qb = idx >> 2;
    STAMP_DECL; RSTAMP(2, 14); STAMP(2, 0);
    const srd_t srd = make_srd(ws, (unsigned)WS_END);
    const int c0 = (2 * kq + (qb & 1)) * KVCHUNK, c1 = (2 * kq + ((qb & 1) ^ 1)) * KVCHUNK;
    const unsigned kvb = (unsigned)WS_KV + head * KVIMG, l16 = lane * 16;
    const int q0 = 256 * qb + 64 * qg;
    bf16x8 qm[2]; float qtl[2]; float km;
    { const unsigned so = (unsigned)WS_QM + (head * S_ + q0) * 32, vo = r32 * 32 + h * 16; bload16<0>(qm[0], srd, vo, so); bload16<1024>(qm[1], srd, vo, so);
      const unsigned st = (unsigned)WS_QT + (head * S_ + q0) * 4; bload4<0>(qtl[0], srd, r32 * 4, st); bload4<128>(qtl[1], srd, r32 * 4, st);
      bload4<0>(km, srd, (lane & 15) * 4, (unsigned)WS_KMAX + head * 64); }
    bdma4(srd, l16, kvb + c0 + qg * 5120, ldsb + c0 + qg * 5120); if (qg < 3) bdma1(srd, l16, kvb + c0 + qg * 5120 + 4096, ldsb + c0 + qg * 5120 + 4096);
    const int slot = wid * 32 + idx; const bool rt = slot < (K2_RT ? 486 : 0);
    bf16x8 wb[4]; unsigned rdst[4], rso[4], rvo; const char* rsrc;
    if (slot < 324) { const int gu = slot >= 162, r = slot - 162 * gu, ks = (r * 57) >> 9, cb = 9 * xcd + r - 9 * ks, T0 = 4 * cb, j0 = (T0 * 7282) >> 16, t0 = T0 - 9 * j0;
        rsrc = gu ? wu : wg; rvo = (lane >> 3) * (F_ * 2) + (lane & 7) * 16;
#pragma unroll
        for (int p = 0; p < 4; ++p) { rso[p] = (ks * 32 + p * 8) * (F_ * 2) + cb * 128;
            const int w = t0 + p >= 9, tt = t0 + p - 9 * w, m4 = tt >= 4, m8 = tt >= 8;
            rdst[p] = (unsigned)WS_W1 + ((j0 + w) * 324 + 2 * tt + m4 * 34 * (tt - (tt & 1)) + (ks << (3 - m4 - m8)) + gu) * 1024; }
    } else { const int r2 = slot < 486 ? slot - 324 : 0, q = (r2 * 57) >> 9, cb = r2 - 9 * q, hi = q >= 9, qi = q - 9 * hi, a = 2 * xcd + hi;
        rsrc = wd; rvo = (lane >> 3) * (D_ * 2) + (lane & 7) * 16;
        const int e8 = qi == 8, t0 = 18 * a + 9 * (qi >> 2) + 2 * (qi & 3) - 10 * e8, t1 = t0 + 1 + 8 * e8, T0 = 4 * cb, n0 = (T0 * 7282) >> 16, u0 = T0 - 9 * n0;
#pragma unroll
        for (int p = 0; p < 4; ++p) { rso[p] = (16 * (p >> 1 ? t1 : t0) + 8 * (p & 1)) * (D_ * 2) + cb * 128;
            const int w = u0 + p >= 9, tt = u0 + p - 9 * w;
            rdst[p] = (unsigned)WS_W2 + (((xcd >> 1) * 4 + n0 + w) * 324 + (((q >> 1) * 2 + (xcd & 1)) * 2 + (q & 1)) * 9 + tt) * 1024; }
    }
#pragma unroll
    for (int p = 0; p < 4; ++p) { rso[p] = rfl(rso[p]); rdst[p] = rfl(rdst[p]); }
    asm volatile("s_waitcnt vmcnt(4)" : "+v"(qm[0]), "+v"(qm[1]), "+v"(qtl[0]), "+v"(qtl[1]), "+v"(km) :: "memory");
    km = fmaxf(km, __shfl_xor(km, 1)); km = fmaxf(km, __shfl_xor(km, 2)); km = fmaxf(km, __shfl_xor(km, 4)); km = fmaxf(km, __shfl_xor(km, 8));
    s16x4 qt[2]; bool unsafe = false;
#pragma unroll
    for (int u = 0; u < 2; ++u) { const u32x4 qv = __builtin_bit_cast(u32x4, qm[u]); const unsigned tl = __float_as_uint(qtl[u]);
        float n2 = h ? 0.f : dot2(tl, tl, 0.f);
#pragma unroll
        for (int e = 0; e < 4; ++e) n2 = dot2(qv[e], qv[e], n2);
        n2 += xor32(n2);
        const float bound = __builtin_amdgcn_sqrtf(n2 * km) * 1.0001f + 1e-6f;
        unsafe |= !(bound <= SAFE_B);
        const unsigned cb = (__float_as_uint(bound) + 0xffffu) >> 16;
        const u32x2 w = h ? (u32x2){0u, 0u} : (u32x2){tl, cb | 0x8000u};
        qt[u] = __builtin_bit_cast(s16x4, w); }
    const bool slow = __any(unsafe);
    if (slow) {
#pragma unroll
        for (int u = 0; u < 2; ++u) { u32x2 w = __builtin_bit_cast(u32x2, qt[u]); w[1] = 0u; qt[u] = __builtin_bit_cast(s16x4, w); } }
    f32x16 o[2] = {(f32x16){}, (f32x16){}}; float mrun[2] = {-INFINITY, -INFINITY};
    const int vrow = r32 >= 19 ? 4 : r32 + (r32 >= 4 ? 5 : 0) - (r32 >= 12 ? 12 : 0) + (r32 >= 16 ? 8 : 0);
    const unsigned kc = h ? 0u : 0x00003f80u;
    unsigned kp = ldsb + c0 + h * 4096 + r32 * 16, tp = ldsb + c0 + 8192 + r32 * 4, vp = ldsb + c0 + 9216 + h * 320 + vrow * 16;
#define KP(o) ((LAS const char*)(uintptr_t)kp + (o))
#define TP(o) ((LAS const char*)(uintptr_t)tp + (o))
#define VP(o) ((LAS const char*)(uintptr_t)vp + (o))
    STAMP(2, 1);
    WAIT_BAR(0);
    STAMP(2, 2);
    bdma4(srd, l16, kvb + c1 + qg * 5120, ldsb + c1 + qg * 5120); if (qg < 3) bdma1(srd, l16, kvb + c1 + qg * 5120 + 4096, ldsb + c1 + qg * 5120 + 4096);
    if (rt) { const srd_t sw = make_srd(rsrc, D_ * F_ * 2);
#pragma unroll
        for (int p = 0; p < 4; ++p) bload16<0>(wb[p], sw, rvo, rso[p]); }
    if (slow) attn_slow(KP(0), TP(0), VP(0), qm, qt, o, mrun, kc); else attn_fast(KP(0), TP(0), VP(0), qm, qt, o, kc);
    STAMP(2, 3);
    if (rt) WAIT_BAR(4); else WAIT_BAR(0);
    STAMP(2, 4);
    if (rt) { asm volatile("s_waitcnt vmcnt(0)" : "+v"(wb[0]), "+v"(wb[1]), "+v"(wb[2]), "+v"(wb[3]) :: "memory");
        int ty = threadIdx.x; asm volatile("" : "+v"(ty)); const int ln = ty & 63;
        LAS char* scr = lds + c0 + qg * 4096;
#pragma unroll
        for (int p = 0; p < 4; ++p) *(LAS bf16x8*)(scr + p * 1024 + ln * 16) = wb[p];
        LAS const char* base = scr + (4 * (ln >> 4) + ((ln >> 2) & 3)) * 128 + (ln & 3) * 8;
#pragma unroll
        for (int u = 0; u < 4; ++u) bstore16<RT_POL>(__builtin_bit_cast(u32x4, tr_frag(base + 32 * u, 16 * 128)), srd, ln * 16, rdst[u]); }
    STAMP(2, 5);
    { const int dc = c1 - c0; kp += dc; tp += dc; vp += dc; PINV(kp); PINV(tp); PINV(vp); }
    if (slow) attn_slow(KP(0), TP(0), VP(0), qm, qt, o, mrun, kc); else attn_fast(KP(0), TP(0), VP(0), qm, qt, o, kc);
    STAMP(2, 6);
    int tz = threadIdx.x; asm volatile("" : "+v"(tz));
    const int lane2 = tz & 63, r2 = tz & 31, h2 = (tz >> 5) & 1;
    LAS float* mx = (LAS float*)(lds + KVIMG);
#pragma unroll
    for (int u = 0; u < 2; ++u) { LAS char* pb = lds + c0 + (qg * 2 + u) * 2432;
        *(LAS f32x4*)(pb + lane2 * 16) = (f32x4){o[u][0], o[u][1], o[u][2], o[u][3]};
        *(LAS f32x4*)(pb + 1024 + lane2 * 16) = (f32x4){o[u][4], o[u][5], o[u][6], o[u][7]};
        if (h2 == 0) { *(LAS f32x2*)(pb + 2048 + r2 * 8) = (f32x2){o[u][8], o[u][9]}; *(LAS float*)(pb + 2304 + r2 * 4) = o[u][10];
                      if (slow) mx[wid * 64 + 32 * u + r2] = mrun[u]; } }
    LBAR();
    { const int u = kq & 1, hf = kq >> 1;
      LAS const char* pb = lds + (qb & 1) * KVCHUNK + (qg * 2 + u) * 2432;
      float l = 0.f; f32x4 a = {0, 0, 0, 0}; f32x2 c = {0, 0};
      if (slow) { float mk[4], M = -INFINITY;
#pragma unroll
          for (int k = 0; k < 4; ++k) { mk[k] = mx[(4 * k + qg) * 64 + 32 * u + r2]; M = fmaxf(M, mk[k]); }
#pragma unroll
          for (int k = 0; k < 4; ++k) { LAS const char* p = pb + k * 2 * KVCHUNK; const float f = __builtin_amdgcn_exp2f(mk[k] - M);
              l += f * *(LAS const float*)(p + 2304 + r2 * 4); a += *(LAS const f32x4*)(p + hf * 1024 + lane2 * 16) * f; c += *(LAS const f32x2*)(p + 2048 + r2 * 8) * f; }
      } else {
#pragma unroll
          for (int k = 0; k < 4; ++k) { LAS const char* p = pb + k * 2 * KVCHUNK;
              l += *(LAS const float*)(p + 2304 + r2 * 4); a += *(LAS const f32x4*)(p + hf * 1024 + lane2 * 16); c += *(LAS const f32x2*)(p + 2048 + r2 * 8); } }
      const float rl = __builtin_amdgcn_rcpf(l);
      const int token = q0 + 32 * u + r2; char* ob = ws + WS_OF + (size_t)(token >> 4) * 18 * 1024 + (token & 15) * 16;
      auto put = [&](int d, float x0, float x1) { const int cc = 18 * head + d; *(unsigned*)(ob + (cc >> 5) * 1024 + ((cc & 31) >> 3) * 256 + (cc & 7) * 2) = cvtpk(x0 * rl, x1 * rl); };
      put(8 * hf + 4 * h2, a[0], a[1]); put(8 * hf + 4 * h2 + 2, a[2], a[3]);
      if (hf == 0 && h2 == 0) put(16, c[0], c[1]);
    }
    STAMP(2, 7); RSTAMP(2, 15); STAMP_FLUSH(2);
}

constexpr int K3_X = 108 * 1024, K3_WB = K3_X + 12 * 1024, K3_Z = K3_WB + 2304;
DI void k3_phase(const char* x, char* ws, char* lds_generic, int xcd, int idx) {
    LAS char* lds = (LAS char*)lds_generic; const unsigned ldsb = (unsigned)(uintptr_t)lds_generic;
    const int tid = threadIdx.x, lane = tid & 63, wid = rfl(tid >> 6), mi = wid & 3, kj = wid >> 2, m = lane & 15, g = lane >> 4;
    const int mt32 = xcd * 4 + (idx & 3), n6 = idx >> 2, mt16 = 4 * mt32 + mi;
    STAMP_DECL; RSTAMP(3, 14); STAMP(3, 0);
    const srd_t srd = make_srd(ws, (unsigned)WS_END), sx = make_srd(x, S_ * D_ * 2);
    const unsigned l16 = lane * 16, wo0 = (unsigned)WS_WO + n6 * (108 * 1024), ob = (unsigned)WS_OF + (mt16 * 18 + 9 * kj) * 1024;
    bf16x8 of[9];
    { const unsigned f0 = (54 * kj + 6 * mi) * 1024;
      bload16<0>(of[0], srd, l16, ob); bload16<1024>(of[1], srd, l16, ob); bload16<2048>(of[2], srd, l16, ob); bload16<3072>(of[3], srd, l16, ob);
      bdma4(srd, l16, wo0 + f0, ldsb + f0); bdma1(srd, l16, wo0 + f0 + 4096, ldsb + f0 + 4096); bdma1(srd, l16, wo0 + f0 + 5120, ldsb + f0 + 5120); }
    if (tid < 37) *(LAS u32x4*)(lds + K3_Z + tid * 16) = (u32x4){0, 0, 0, 0};
    f32x4 acc[6], accw = {0, 0, 0, 0}; float mux;
    asm volatile("s_nop 1" : "+v"(accw));
    LAS const char* ab = lds + kj * (54 * 1024) + lane * 16;
    LAS const char* wbp = lds + (m < 2 ? K3_WB + m * (D_ * 2) + kj * 576 + g * 16 : K3_Z);
    bf16x8 wr[2][6];
    auto rd = [&](int s) {
#pragma unroll
        for (int t = 0; t < 6; ++t) wr[s & 1][t] = lds_frag(ab + (s * 6 + t) * 1024);
        SBAR(); };
    auto step = [&](int s) { PINV(of[s]);
#pragma unroll
        for (int t = 0; t < 6; ++t) { if (s == 0) mfma16_new(acc[t], wr[s & 1][t], of[s]); else mfma16_acc(acc[t], wr[s & 1][t], of[s]); }
        SBAR(); };
    STAMP(3, 1);
    WAIT_BAR(0);
    STAMP(3, 2);
    { const unsigned f1 = (54 * kj + 24 + (mi < 3 ? 8 * mi : 22)) * 1024;
      rd(0); rd(1);
      bload16<0>(of[4], srd, l16, ob + 4096); bload16<1024>(of[5], srd, l16, ob + 4096); bdma4(srd, l16, wo0 + f1, ldsb + f1);
      step(0); rd(2);
      bload16<2048>(of[6], srd, l16, ob + 4096); bload16<3072>(of[7], srd, l16, ob + 4096); bdma4(srd, l16, wo0 + f1 + 4096, ldsb + f1 + 4096);
      step(1); rd(3);
      bload16<0>(of[8], srd, l16, ob + 8192);
#pragma unroll
      for (int i = 0; i < 2; ++i) { int pc = wid + 8 * i; pc = pc > 11 ? 11 : pc; const int q = pc * 64 + lane, row = (q * 5462) >> 16;
          bdma1(sx, row * (D_ * 2) + (q - 12 * row) * 16, (64 * mt32) * (D_ * 2) + n6 * 192, ldsb + K3_X + pc * 1024); }
      if (wid < 2) bdma1(srd, l16, (unsigned)WS_WBAR + wid * 1024, ldsb + K3_WB + wid * 1024);
      if (wid == 2 && lane < 16) bdma1(srd, l16, (unsigned)WS_WBAR + 2048, ldsb + K3_WB + 2048);
      bload4<0>(mux, srd, m * 4, (unsigned)WS_MUX + mt16 * 64);
      step(2); step(3); }
    STAMP(3, 3);
    WAIT_BAR(0);
    STAMP(3, 4);
    bf16x8 wbf[9];
    rd(4); rd(5);
#pragma unroll
    for (int s = 0; s < 9; ++s) wbf[s] = lds_frag(wbp + s * 64);
    SBAR();
    step(4); rd(6); step(5); rd(7); step(6); rd(8); step(7); step(8);
#pragma unroll
    for (int s = 0; s < 9; ++s) mfma16_acc(accw, wbf[s], of[s]);
    PINV(mux);
    const int token = 16 * mt16 + m;
    u32x2 xr[4];
#pragma unroll
    for (int t = 0; t < 4; ++t) xr[t] = *(LAS const u32x2*)(lds + K3_X + (16 * mi + m) * 192 + (4 * kj + (kj ? (t & 1) : t)) * 32 + g * 8);
    asm volatile("s_nop 7" : "+v"(acc[5]), "+v"(acc[4]), "+v"(accw));
    STAMP(3, 5);
    LBAR();
    LAS char* ex = lds + mi * 8192 + lane * 16;
    if (kj == 0) { *(LAS f32x4*)(ex + 4 * 1024) = acc[4]; *(LAS f32x4*)(ex + 5 * 1024) = acc[5]; *(LAS f32x4*)(ex + 6 * 1024) = accw; }
    else {
#pragma unroll
        for (int t = 0; t < 4; ++t) *(LAS f32x4*)(ex + t * 1024) = acc[t];
        *(LAS f32x4*)(ex + 7 * 1024) = accw; }
    LBAR();
    accw += *(LAS const f32x4*)(ex + (kj ? 6 : 7) * 1024);
    const float mu = mux + __shfl(accw[0] + accw[1], m);
    float ssq = 0.f;
    auto fin = [&](int t, u32x2 xv, unsigned (&pk)[2]) {
        const f32x4 a = acc[t] + *(LAS const f32x4*)(ex + t * 1024);
        const unsigned h0 = cvtpk(a[0] + bflo(xv[0]), a[1] + bfhi(xv[0])), h1 = cvtpk(a[2] + bflo(xv[1]), a[3] + bfhi(xv[1]));
        bstore8<H_POL>((u32x2){h0, h1}, srd, lane * 8, (unsigned)WS_HC + (mt16 * 36 + 6 * n6 + t) * 512);
        pk[0] = cvtpk(bflo(h0) - mu, bfhi(h0) - mu); pk[1] = cvtpk(bflo(h1) - mu, bfhi(h1) - mu);
        ssq = dot2(pk[0], pk[0], ssq); ssq = dot2(pk[1], pk[1], ssq); };
    auto pair = [&](int t, u32x2 xa, u32x2 xb2) { unsigned p0[2], p1[2]; fin(t, xa, p0); fin(t + 1, xb2, p1);
        bstore16<H_POL>((u32x4){p0[0], p0[1], p1[0], p1[1]}, srd, l16, (unsigned)WS_HCF + (mt16 * 18 + 3 * n6 + (t >> 1)) * 1024); };
    if (kj == 0) { pair(0, xr[0], xr[1]); pair(2, xr[2], xr[3]); } else pair(4, xr[0], xr[1]);
    ssq += xor16(ssq); ssq += xor32(ssq);
    if (g == 0) __hip_atomic_store((float*)(ws + WS_SSQ) + token * 16 + 2 * n6 + kj, ssq, __ATOMIC_RELAXED, __HIP_MEMORY_SCOPE_AGENT);
    STAMP(3, 6); RSTAMP(3, 15); STAMP_FLUSH(3);
}

constexpr int RING_SLOT = 36 * 1024, RING0 = 16 * 1024, G_LDS = RING0 + 4 * RING_SLOT;
DI void ring_issue(const char* stream, int st, int wid, int lane, unsigned ldsb) {
    const char* sb = stream + (size_t)st * RING_SLOT + wid * 1024; const unsigned db = ldsb + (st & 3) * RING_SLOT + wid * 1024;
#pragma unroll
    for (int i = 0; i < 4; ++i) glds16(sb + i * 8192, lane * 16, db + i * 8192);
    if (wid < 4) glds16(sb + 32768, lane * 16, db + 32768);
}
DI float geglu(float g, float u) {
    const float z = g * __builtin_fmaf(g * g, -0.10294324f, -2.30220818f);
    return g * u * __builtin_amdgcn_rcpf(1.f + __builtin_amdgcn_exp2f(z));
}
struct Ev { int type, arg, n; };
enum { E_SSQ, E_A, E_B, E_WAITB, E_BAR, E_READ, E_MMA, E_GP, E_RS, E_STAMP, E_ST, E_NOP, E_TOUCH, E_A2 };
constexpr int G1_PD = G1PD;
constexpr int g1_grp(int f) { return f < 144 ? 0 : f < 216 ? 1 : f < 288 ? 2 : 3; }
constexpr int g1_ks(int f) { return f < 144 ? f / 8 : f < 288 ? ((f - 144) % 72) / 4 : (f - 288) / 2; }
constexpr int g1_tile(int f) { return f < 288 ? (f >> 1) & 1 : 0; }
constexpr int g1_set(int f) { return f < 144 ? (f >> 2) & 1 : f < 216 ? 0 : f < 288 ? 1 : 0; }
template <bool LATE> struct G1Prog {
    Ev ev[1600]; int n;
    constexpr void add(int t, int a) { ev[n].type = t; ev[n].arg = a; ev[n].n = 0; ++n; }
    static constexpr int vm(const Ev& e) { return e.type == E_SSQ ? 2 : (e.type == E_A || e.type == E_B || e.type == E_ST || e.type == E_TOUCH || e.type == E_A2) ? 1 : 0; }
    static constexpr int NP = LATE ? 4 : 5;
    constexpr void addB(int s, int u) { add(E_B, s * 2 + u); }
    static constexpr int jset(int q) { return q == 0 ? 0 : q == 1 ? 1 : q == 2 ? 0 : q == 3 ? 1 : 0; }
    constexpr G1Prog() : ev{}, n(0) {
        addB(0, 0); addB(0, 1); addB(1, 0); addB(1, 1);
        add(E_STAMP, 1);
        for (int f = 0; f < G1_PD; ++f) add(E_READ, f);
        for (int f = 0; f < 324; ++f) {
            const int st = f / 36, i = f % 36, g = g1_grp(f);
            if (g == 0) { const int s = f / 8, q = f % 8;
                if (q == 0) add(E_WAITB, s);
                if (f == 0) { addB(2, 0); addB(2, 1); }
                if (s + 3 < 18 && q == 2) addB(s + 3, 0);
                if (s + 3 < 18 && q == 6) addB(s + 3, 1);
                if (f == 100) add(E_SSQ, 0);
                if (f == 136) add(E_RS, 0); }
            if (st + 2 < 9) { const int i0 = LATE ? 17 : 1, d = 4;
                if (i >= i0 && (i - i0) % d == 0 && (i - i0) / d < NP) add(E_A, (st + 2) * 8 + (i - i0) / d); }
            if (i == 36 - G1_PD && st + 1 < 9) add(E_BAR, st + 1);
            if (f + G1_PD < 324) add(E_READ, f + G1_PD);
            add(E_MMA, f);
            if (f == 143) { add(E_NOP, 0);
                for (int k = 0; k < 32; ++k) { add(E_GP, 0 * 64 + k); if (k == 15 || k == 31) add(E_ST, 0 * 2 + (k == 31)); } }
            if (g > 0) {
                const int r = f - (g == 1 ? 144 : g == 2 ? 216 : 288), step = g < 3 ? 2 : 1, r0 = 3, k = (r - r0) / step;
                if (r >= r0 && (r - r0) % step == 0 && k < 32) { add(E_GP, g * 64 + k);
                    if (k == 15 || k == 31) add(E_ST, g * 2 + (k == 31)); } }
            if (TOUCH && f == 5 * 36 + 8) add(E_TOUCH, 0);
            if (st == 8 && i >= 2 && i % 3 == 2 && (i - 2) / 3 < 2 * NP) add(E_A2, ((i - 2) / 3 / NP) * 8 + ((i - 2) / 3) % NP);
            if (i == 35) add(E_STAMP, 2 + st);
            if (f == 107) add(E_STAMP, 12);
            if (f == 323) add(E_STAMP, 13);
        }
        add(E_NOP, 1);
        for (int k = 0; k < 16; ++k) { add(E_GP, 4 * 64 + k); if (k == 7 || k == 15) add(E_ST, 8 + (k == 15)); }
        for (int i = 0; i < n; ++i) if (ev[i].type == E_WAITB || ev[i].type == E_BAR || ev[i].type == E_RS) {
            int cnt = 0, j = i - 1;
            for (; j >= 0; --j) {
                if (ev[i].type == E_WAITB && ev[j].type == E_B && ev[j].arg == 2 * ev[i].arg + 1) break;
                if (ev[i].type == E_RS && ev[j].type == E_SSQ) break;
                if (ev[i].type == E_BAR && ev[j].type == E_A && ev[j].arg == 8 * ev[i].arg + NP - 1) break;
                cnt += vm(ev[j]); }
            ev[i].n = cnt > 63 ? 63 : cnt; }
    }
};
template <bool LATE> constexpr G1Prog<LATE> g1prog{};
DI float vmul(float a, float b) { float r; asm("v_mul_f32_e32 %0, %1, %2" : "=v"(r) : "v"(a), "v"(b)); return r; }
DI float vfma(float a, float b, float c) { float r; asm("v_fma_f32 %0, %1, %2, %3" : "=v"(r) : "v"(a), "v"(b), "v"(c)); return r; }
struct G1State {
    bf16x8 hf[2][18]; f32x4 acc[2][2][2][2];
    bf16x8 ar[G1_PD + 1]; f32x4 sq[2]; float a0[2], a1[2], kk[2], sink; f32x4 z, w; u32x4 o; unsigned soffT, soffA2;
    unsigned lb0, lb1, lb2; unsigned l16, ldsb, soffA, soffH, soffS, soffAct, vq; int wid, j, g;
};
template <bool LATE, int PC> DI void g1_exec(G1State& c, const srd_t srd) {
    constexpr Ev e = g1prog<LATE>.ev[PC];
    if constexpr (e.type == E_SSQ) { bload16f_sc1<0>(c.sq[0], srd, c.vq, c.soffS); bload16f_sc1<1024>(c.sq[1], srd, c.vq, c.soffS); }
    else if constexpr (e.type == E_A) { constexpr int st = e.arg >> 3, p = e.arg & 7;
        const unsigned so = c.soffA + st * RING_SLOT, dst = c.ldsb + RING0 + ((st + 3) & 3) * RING_SLOT;
        if constexpr (p < 4) bdma_next<p * 1024>(srd, c.l16, so + c.wid * 4096, dst + c.wid * 4096);
        else bdma1(srd, c.l16, so + 32768 + c.wid * 1024, dst + 32768 + c.wid * 1024); }
    else if constexpr (e.type == E_B) { constexpr int s = e.arg >> 1, u = e.arg & 1;
        bload16_sc1<((18 * u + s) & 3) * 1024>(c.hf[u][s], srd, c.l16, c.soffH + ((18 * u + s) >> 2) * 4096); }
    else if constexpr (e.type == E_WAITB) { asm volatile("s_waitcnt vmcnt(%2)" : "+v"(c.hf[0][e.arg]), "+v"(c.hf[1][e.arg]) : "n"(e.n)); }
    else if constexpr (e.type == E_BAR) { asm volatile("s_waitcnt vmcnt(%0)\n\ts_barrier" :: "n"(e.n) : "memory"); }
    else if constexpr (e.type == E_RS) { asm volatile("s_waitcnt vmcnt(%2)" : "+v"(c.sq[0]), "+v"(c.sq[1]) : "n"(e.n));
#pragma unroll
        for (int u = 0; u < 2; ++u) { float t = c.g < 3 ? (c.sq[u][0] + c.sq[u][1]) + (c.sq[u][2] + c.sq[u][3]) : 0.f; t += __shfl_xor(t, 16); t += xor32(t);
            const float v = t * (1.f / D_) + EPS, rs = __builtin_amdgcn_rsqf(v);
            c.a0[u] = -2.30220818f * rs; c.a1[u] = -0.10294324f * rs * rs * rs; c.kk[u] = v; }
        SBAR(); }
    else if constexpr (e.type == E_STAMP) {
    }
    else if constexpr (e.type == E_TOUCH) { touch64(c.sink, srd, c.l16 * 8, c.soffT); }
    else if constexpr (e.type == E_A2) { constexpr int st = e.arg >> 3, p = e.arg & 7;
        const unsigned so = c.soffA2 + st * RING_SLOT, dst = c.ldsb + RING0 + (st & 3) * RING_SLOT;
        if constexpr (p < 4) bdma_next<p * 1024>(srd, c.l16, so + c.wid * 4096, dst + c.wid * 4096);
        else bdma1(srd, c.l16, so + 32768 + c.wid * 1024, dst + 32768 + c.wid * 1024); }
    else if constexpr (e.type == E_NOP) {
        asm volatile("s_nop 7" : "+v"(c.acc[0][0][0][0]), "+v"(c.acc[0][0][0][1]), "+v"(c.acc[0][0][1][0]), "+v"(c.acc[0][0][1][1]));
        if constexpr (e.arg == 0) asm volatile("" : "+v"(c.acc[0][1][0][0]), "+v"(c.acc[0][1][0][1]), "+v"(c.acc[0][1][1][0]), "+v"(c.acc[0][1][1][1])); }
    else if constexpr (e.type == E_READ) { constexpr int f = e.arg, off = ((f / 36 + 3) & 3) * RING_SLOT + (f % 36) * 1024;
        if constexpr ((off >> 16) == 0) c.ar[f % (G1_PD + 1)] = lds_frag((LAS const char*)(uintptr_t)c.lb0 + (off & 65535));
        else if constexpr ((off >> 16) == 1) c.ar[f % (G1_PD + 1)] = lds_frag((LAS const char*)(uintptr_t)c.lb1 + (off & 65535));
        else c.ar[f % (G1_PD + 1)] = lds_frag((LAS const char*)(uintptr_t)c.lb2 + (off & 65535));
        SBAR(); }
    else if constexpr (e.type == E_MMA) { constexpr int f = e.arg, s = g1_ks(f), t = g1_tile(f), q = f & 1, set = g1_set(f);
        if constexpr (s == 0) { mfma16_new(c.acc[set][t][q][0], c.ar[f % (G1_PD + 1)], c.hf[0][s]); mfma16_new(c.acc[set][t][q][1], c.ar[f % (G1_PD + 1)], c.hf[1][s]); }
        else { mfma16_acc(c.acc[set][t][q][0], c.ar[f % (G1_PD + 1)], c.hf[0][s]); mfma16_acc(c.acc[set][t][q][1], c.ar[f % (G1_PD + 1)], c.hf[1][s]); }
        SBAR(); }
    else if constexpr (e.type == E_GP) {
        constexpr int grp = e.arg >> 6, k = e.arg & 63, quad = k >> 3, ph = k & 7, set = G1Prog<LATE>::jset(grp);
        constexpr int u = grp < 4 ? quad >> 1 : quad, t = grp < 4 ? quad & 1 : 0;
        const f32x4 g = c.acc[set][t][0][u];
#pragma unroll
        for (int r = 0; r < 4; ++r) {
            if constexpr (ph == 0) c.z[r] = vmul(g[r], g[r]);
            if constexpr (ph == 1) c.z[r] = vfma(c.z[r], c.a1[u], c.a0[u]);
            if constexpr (ph == 2) c.z[r] = vmul(c.z[r], g[r]);
            if constexpr (ph == 3) c.z[r] = __builtin_amdgcn_exp2f(c.z[r]);
            if constexpr (ph == 4) { c.z[r] = vfma(c.z[r], c.kk[u], c.kk[u]); }
            if constexpr (ph == 5) { c.z[r] = __builtin_amdgcn_rcpf(c.z[r]); }
            if constexpr (ph == 6) c.w[r] = vmul(vmul(g[r], c.acc[set][t][1][u][r]), c.z[r]); }
        if constexpr (ph == 7) { c.o[2 * t] = cvtpk(c.w[0], c.w[1]); c.o[2 * t + 1] = cvtpk(c.w[2], c.w[3]); }
        SBAR(); }
    else if constexpr (e.type == E_ST) { constexpr int grp = e.arg >> 1, u = e.arg & 1;
        const unsigned so = c.soffAct + u * (144 * 1024);
        if constexpr (grp < 4) bstore16<ACT_POL>(c.o, srd, c.l16, so + (9 * (c.j >> 1) + 4 * (c.j & 1) + grp) * 1024);
        else bstore8<ACT_POL>((u32x2){c.o[0], c.o[1]}, srd, c.l16 + (c.j & 1) * 8, so + (9 * (c.j >> 1) + 8) * 1024);
        SBAR(); }
}
template <bool LATE, int... I> DI void g1_run(G1State& c, const srd_t srd, std::integer_sequence<int, I...>) { (g1_exec<LATE, I>(c, srd), ...); }

constexpr int G2_PD = 4;
template <bool LATE> struct G2Prog {
    Ev ev[700]; int n;
    constexpr void add(int t, int a) { ev[n].type = t; ev[n].arg = a; ev[n].n = 0; ++n; }
    static constexpr int vm(const Ev& e) { return (e.type == E_A || e.type == E_B) ? 1 : 0; }
    static constexpr int NP = LATE ? 4 : 5;
    constexpr G2Prog() : ev{}, n(0) {
        add(E_B, 0); add(E_B, 1); add(E_B, 2); add(E_B, 3);
        add(E_STAMP, 1);
        for (int f = 0; f < G2_PD; ++f) add(E_READ, f);
        for (int f = 0; f < 162; ++f) {
            const int st = f / 18, i = f % 18;
            if (i % 9 == 0) add(E_WAITB, st * 2 + i / 9);
            const int ia = LATE ? 3 : 1, ib = LATE ? 1 : 3;
            if (st == 0) {
                if (i >= 1 && i < 5) add(E_B, 4 + i - 1);
                if (i >= 6 && i < 6 + NP) add(E_A, 16 + i - 6);
                if (i >= 12 && i < 16) add(E_B, 8 + i - 12);
            } else {
                if (st == 1 && i >= 14) add(E_B, 16 + i - 14);
                if (st + 2 < 9 && i >= ia && (i - ia) % 4 == 0 && (i - ia) / 4 < 4) add(E_A, (st + 2) * 8 + (i - ia) / 4);
                if (st + 2 < 9 && NP == 5 && i == 12) add(E_A, (st + 2) * 8 + 4);
                { const int la = st == 1 ? 2 : 3; if (st + la < 9 && (st == 1 || st + la > 4) && i >= ib && (i - ib) % 4 == 0 && (i - ib) / 4 < 4) add(E_B, (st + la) * 4 + (i - ib) / 4); }
            }
            if (i == 18 - G2_PD && st + 1 < 9) add(E_BAR, st + 1);
            if (f + G2_PD < 162) add(E_READ, f + G2_PD);
            add(E_MMA, f);
            if (i == 17) add(E_STAMP, 2 + st);
        }
        for (int i = 0; i < n; ++i) if (ev[i].type == E_WAITB || ev[i].type == E_BAR) {
            int cnt = 0, j = i - 1;
            for (; j >= 0; --j) {
                if (ev[i].type == E_WAITB && ev[j].type == E_B && ev[j].arg == 2 * ev[i].arg + 1) break;
                if (ev[i].type == E_BAR && ev[j].type == E_A && (ev[j].arg >> 3) == ev[i].arg) break;
                cnt += vm(ev[j]); }
            ev[i].n = cnt > 63 ? 63 : cnt; }
    }
};
template <bool LATE> constexpr G2Prog<LATE> g2prog{};
struct G2State {
    bf16x8 af[4][2][2]; f32x4 acc[9][2]; bf16x8 ar[G2_PD + 1];
    unsigned lb0, lb1, lb2; unsigned l16, ldsb, soffA, soffB; int wid;
};
template <bool LATE, int PC> DI void g2_exec(G2State& c, const srd_t srd) {
    constexpr Ev e = g2prog<LATE>.ev[PC];
    if constexpr (e.type == E_A) { constexpr int st = e.arg >> 3, p = e.arg & 7;
        const unsigned so = c.soffA + st * RING_SLOT, dst = c.ldsb + RING0 + (st & 3) * RING_SLOT;
        if constexpr (p < 4) bdma_next<p * 1024>(srd, c.l16, so + c.wid * 4096, dst + c.wid * 4096);
        else bdma1(srd, c.l16, so + 32768 + c.wid * 1024, dst + 32768 + c.wid * 1024); }
    else if constexpr (e.type == E_B) { constexpr int st = e.arg >> 2, s2 = (e.arg >> 1) & 1, u = e.arg & 1;
        bload16_sc1<((2 * st + s2) & 3) * 1024>(c.af[st & 3][s2][u], srd, c.l16, c.soffB + u * (144 * 1024) + ((2 * st + s2) >> 2) * 4096); }
    else if constexpr (e.type == E_WAITB) { asm volatile("s_waitcnt vmcnt(%2)" : "+v"(c.af[(e.arg >> 1) & 3][e.arg & 1][0]), "+v"(c.af[(e.arg >> 1) & 3][e.arg & 1][1]) : "n"(e.n)); }
    else if constexpr (e.type == E_BAR) { asm volatile("s_waitcnt vmcnt(%0)\n\ts_barrier" :: "n"(e.n) : "memory"); }
    else if constexpr (e.type == E_STAMP) {
    }
    else if constexpr (e.type == E_READ) { constexpr int f = e.arg, st = f / 18, off = (st & 3) * RING_SLOT + (f % 18) * 1024;
        if constexpr ((off >> 16) == 0) c.ar[f % (G2_PD + 1)] = lds_frag((LAS const char*)(uintptr_t)c.lb0 + (off & 65535));
        else c.ar[f % (G2_PD + 1)] = lds_frag((LAS const char*)(uintptr_t)c.lb1 + (off & 65535));
        SBAR(); }
    else if constexpr (e.type == E_MMA) { constexpr int f = e.arg, st = f / 18, s2 = (f % 18) / 9, t = f % 9;
        if constexpr (f < 9) { mfma16_new(c.acc[t][0], c.ar[f % (G2_PD + 1)], c.af[st & 3][s2][0]); mfma16_new(c.acc[t][1], c.ar[f % (G2_PD + 1)], c.af[st & 3][s2][1]); }
        else { mfma16_acc(c.acc[t][0], c.ar[f % (G2_PD + 1)], c.af[st & 3][s2][0]); mfma16_acc(c.acc[t][1], c.ar[f % (G2_PD + 1)], c.af[st & 3][s2][1]); }
        SBAR(); }
}
template <bool LATE, int... I> DI void g2_run(G2State& c, const srd_t srd, std::integer_sequence<int, I...>) { (g2_exec<LATE, I>(c, srd), ...); }
DI void arrive(char* ws, int counter) { if (threadIdx.x == 0) __hip_atomic_fetch_add((unsigned*)(ws + WS_CNT) + counter * 64, 1u, __ATOMIC_RELAXED, __HIP_MEMORY_SCOPE_AGENT); }
DI bool await(char* ws, int counter, unsigned want, LAS unsigned* flag) {
    if (threadIdx.x == 0) { const unsigned* cnt = (const unsigned*)(ws + WS_CNT) + counter * 64; unsigned bad = 0;
        const unsigned long long t0 = __builtin_amdgcn_s_memrealtime();
        while (__hip_atomic_load(cnt, __ATOMIC_RELAXED, __HIP_MEMORY_SCOPE_AGENT) < want) {
            if (__builtin_amdgcn_s_memrealtime() - t0 > 2000000ull) { bad = 1; break; }
            __builtin_amdgcn_s_sleep(1); }
        *flag = bad; }
    LBAR();
    return *flag != 0;
}
constexpr int FFN_LDS = G_LDS;
__global__ __launch_bounds__(512, 2) void ffn_kernel(const char* x, char* ws, char* out) {
    extern __shared__ __attribute__((aligned(16))) char lds_generic[];
    LAS char* lds = (LAS char*)lds_generic;
    const srd_t srd = make_srd(ws, (unsigned)WS_END);
    const int b = blockIdx.x, xcd = b & 7, idx = b >> 3;
    bool bad;
    auto ring_w1 = [&](int st) {
        const int lane = threadIdx.x & 63, wid = rfl(threadIdx.x >> 6);
        const unsigned so = (unsigned)WS_W1 + ((xcd >> 1) * 8 + (idx & 7)) * (324 * 1024) + st * RING_SLOT, dst = (unsigned)(uintptr_t)lds_generic + RING0 + ((st + 3) & 3) * RING_SLOT;
        bdma4(srd, lane * 16, so + wid * 4096, dst + wid * 4096);
        if (wid < 4) bdma1(srd, lane * 16, so + 32768 + wid * 1024, dst + 32768 + wid * 1024); };
    ring_w1(0);
    if (idx < 24) {
        k3_phase(x, ws, lds_generic, xcd, idx);
        asm volatile("s_waitcnt vmcnt(0) lgkmcnt(0)\n\ts_barrier" ::: "memory");
        arrive(ws, 96 + xcd);
    }
    ring_w1(1);
    asm volatile("s_waitcnt vmcnt(0)" ::: "memory");
    LAS unsigned* flag = (LAS unsigned*)(lds + 15 * 1024);
    bad = await(ws, 96 + (xcd & 1) * 4 + (idx >> 3), 24u, flag);
    {
        int tid = threadIdx.x; asm volatile("" : "+v"(tid));
        const int lane = tid & 63, wid = rfl(tid >> 6);
        const int j = (xcd >> 1) * 8 + (idx & 7), mt8 = (xcd & 1) * 4 + (idx >> 3), mt16 = 16 * mt8 + 2 * wid;
        G1State c;
        RSTAMP(4, 14); STAMP(4, 0);
        c.l16 = lane * 16; c.ldsb = (unsigned)(uintptr_t)lds_generic; c.wid = wid; c.j = j; c.g = lane >> 4;
        c.lb0 = c.ldsb + RING0 + lane * 16; c.lb1 = c.lb0 + 65536; c.lb2 = c.lb0 + 131072; PINV(c.lb0); PINV(c.lb1); PINV(c.lb2);
        c.soffA = (unsigned)WS_W1 + j * (324 * 1024); c.soffH = (unsigned)WS_HCF + mt16 * (18 * 1024); c.soffS = (unsigned)WS_SSQ + mt16 * 1024;
        c.soffAct = (unsigned)WS_ACT + mt16 * (144 * 1024); c.vq = (lane & 15) * 64 + (lane >> 4) * 16;
        c.soffA2 = (unsigned)WS_W2 + ((xcd >> 1) * 4 + (idx >> 3)) * (324 * 1024);
        c.sink = 0.f; c.soffT = (unsigned)WS_W2 + (xcd >> 1) * (4 * 324 * 1024) + (idx * 8 + wid) * (41 * 128);
        if (wid >= 4) g1_run<true>(c, srd, std::make_integer_sequence<int, g1prog<true>.n>{});
        else g1_run<false>(c, srd, std::make_integer_sequence<int, g1prog<false>.n>{});
        STAMP(4, 11); RSTAMP(4, 15);
        asm volatile("s_waitcnt vmcnt(0) lgkmcnt(0)\n\ts_barrier" : "+v"(c.sink) :: "memory");
        arrive(ws, 64 + (j >> 3) * 8 + mt8);
        STAMP_FLUSH(4);
    }
    int tz = threadIdx.x; asm volatile("" : "+v"(tz));
    const int lane = tz & 63, wid = rfl(tz >> 6), mi = wid & 3, kj = wid >> 2;
    const int ksl = xcd >> 1, mt = (xcd & 1) * 8 + (idx & 7), n4 = idx >> 3;
    const int mt16 = 8 * mt + 2 * mi;
    G2State c;
    RSTAMP(5, 14); STAMP(5, 0);
    c.l16 = lane * 16; c.ldsb = (unsigned)(uintptr_t)lds_generic; c.wid = wid;
    c.lb0 = c.ldsb + RING0 + kj * (18 * 1024) + lane * 16; c.lb1 = c.lb0 + 65536; PINV(c.lb0); PINV(c.lb1);
    c.soffA = (unsigned)WS_W2 + (ksl * 4 + n4) * (324 * 1024); c.soffB = (unsigned)WS_ACT + (mt16 * 144 + 36 * ksl + 18 * kj) * 1024;
    bad |= await(ws, 64 + ksl * 8 + (mt >> 1), 8u, flag);
    if (wid >= 4) g2_run<true>(c, srd, std::make_integer_sequence<int, g2prog<true>.n>{});
    else g2_run<false>(c, srd, std::make_integer_sequence<int, g2prog<false>.n>{});
    asm volatile("s_nop 7" : "+v"(c.acc[8][0]), "+v"(c.acc[8][1]), "+v"(c.acc[7][0]), "+v"(c.acc[7][1]));
    LBAR();
    LAS char* ex = lds + RING0 + mi * 18432 + lane * 16;
    if (kj == 0) {
#pragma unroll
        for (int t = 5; t < 9; ++t) { *(LAS f32x4*)(ex + (2 * t) * 1024) = c.acc[t][0]; *(LAS f32x4*)(ex + (2 * t + 1) * 1024) = c.acc[t][1]; }
    } else {
#pragma unroll
        for (int t = 0; t < 5; ++t) { *(LAS f32x4*)(ex + (2 * t) * 1024) = c.acc[t][0]; *(LAS f32x4*)(ex + (2 * t + 1) * 1024) = c.acc[t][1]; }
    }
    LBAR();
    STAMP(5, 11);
    const int tile = mt * 4 + n4; const unsigned xb = (unsigned)WS_PART + tile * (16 * 9 * 1024);
    auto fin = [&](int t) {
        const f32x4 a0 = c.acc[t][0] + *(LAS const f32x4*)(ex + (2 * t) * 1024), a1 = c.acc[t][1] + *(LAS const f32x4*)(ex + (2 * t + 1) * 1024);
        if (mi == ksl) { *(LAS f32x4*)(ex + (2 * t) * 1024) = a0; *(LAS f32x4*)(ex + (2 * t + 1) * 1024) = a1; }
        else bstore16<1>((u32x4){cvtpk(a0[0], a0[1]), cvtpk(a0[2], a0[3]), cvtpk(a1[0], a1[1]), cvtpk(a1[2], a1[3])}, srd, lane * 16, xb + ((mi * 4 + ksl) * 9 + t) * 1024); };
    if (kj == 0) { fin(0); fin(1); fin(2); fin(3); fin(4); } else { fin(5); fin(6); fin(7); fin(8); }
    u32x2 hv[3]; u32x4 pv[3]; u32x2 pw[3];
    const unsigned hb = (unsigned)WS_HC + ((8 * mt + 2 * ksl) * 36 + 9 * n4) * 512;
    bload8_sc1<0>(hv[0], srd, lane * 8, hb + wid * 512); bload8_sc1<0>(hv[1], srd, lane * 8, hb + (36 + wid) * 512); bload8_sc1<0>(hv[2], srd, lane * 8, hb + ((wid & 1) * 36 + 8) * 512);
    STAMP(5, 12);
    asm volatile("s_waitcnt vmcnt(0) lgkmcnt(0)\n\ts_barrier" : "+v"(hv[0]), "+v"(hv[1]), "+v"(hv[2]) :: "memory");
    STAMP(5, 13);
    arrive(ws, tile);
    bad |= await(ws, tile, 4u, flag);
#pragma unroll
    for (int k = 0; k < 3; ++k) { const unsigned sb = xb + (ksl * 4 + (k + (k >= ksl))) * (9 * 1024);
        bload16u_sc1<0>(pv[k], srd, lane * 16, sb + wid * 1024); bload8_sc1<0>(pw[k], srd, lane * 16 + (wid & 1) * 8, sb + 8 * 1024); }
    asm volatile("s_waitcnt vmcnt(0)" : "+v"(pv[0]), "+v"(pv[1]), "+v"(pv[2]), "+v"(pw[0]), "+v"(pw[1]), "+v"(pw[2]) :: "memory");
    const int m = lane & 15, g = lane >> 4;
    auto put = [&](int t, int u, u32x2 h2, u32x2 p0, u32x2 p1, u32x2 p2) {
        f32x4 a = *(LAS const f32x4*)(lds + RING0 + ksl * 18432 + (2 * t + u) * 1024 + lane * 16);
        a += (f32x4){bflo(h2[0]), bfhi(h2[0]), bflo(h2[1]), bfhi(h2[1])}; a += (f32x4){bflo(p0[0]), bfhi(p0[0]), bflo(p0[1]), bfhi(p0[1])};
        a += (f32x4){bflo(p1[0]), bfhi(p1[0]), bflo(p1[1]), bfhi(p1[1])}; a += (f32x4){bflo(p2[0]), bfhi(p2[0]), bflo(p2[1]), bfhi(p2[1])};
        u32x2 o = {cvtpk(a[0], a[1]), cvtpk(a[2], a[3])};
        if (bad) o = (u32x2){0x7fc07fc0u, 0x7fc07fc0u};
        *(u32x2*)(out + (size_t)(128 * mt + 32 * ksl + 16 * u + m) * (D_ * 2) + (144 * n4 + 16 * t + 4 * g) * 2) = o; };
    put(wid, 0, hv[0], (u32x2){pv[0][0], pv[0][1]}, (u32x2){pv[1][0], pv[1][1]}, (u32x2){pv[2][0], pv[2][1]});
    put(wid, 1, hv[1], (u32x2){pv[0][2], pv[0][3]}, (u32x2){pv[1][2], pv[1][3]}, (u32x2){pv[2][2], pv[2][3]});
    if (wid < 2) put(8, wid, hv[2], pw[0], pw[1], pw[2]);
    RSTAMP(5, 15); STAMP_FLUSH(5);
}

}

extern "C" void kernel_launch(void* const* d_in, const int* in_sizes, int n_in, void* d_out, int out_size, void* d_ws, size_t ws_size, hipStream_t stream) {
    const char* x = (const char*)d_in[0]; const char* wqkv = (const char*)d_in[1]; const char* wo = (const char*)d_in[2];
    const char* wg = (const char*)d_in[4]; const char* wu = (const char*)d_in[5]; const char* wd = (const char*)d_in[6];
    char* ws = (char*)d_ws;
    (void)hipFuncSetAttribute((const void*)k1_qkv, hipFuncAttributeMaxDynamicSharedMemorySize, K1_LDS);
    (void)hipFuncSetAttribute((const void*)k2_attn, hipFuncAttributeMaxDynamicSharedMemorySize, K2_LDS);
    (void)hipFuncSetAttribute((const void*)ffn_kernel, hipFuncAttributeMaxDynamicSharedMemorySize, FFN_LDS);
    k1_qkv<<<256, 512, K1_LDS, stream>>>(x, wqkv, wo, ws);
    k2_attn<<<256, 1024, K2_LDS, stream>>>(x, wg, wu, wd, ws);
    ffn_kernel<<<256, 512, FFN_LDS, stream>>>(x, ws, (char*)d_out);
}
```

```cpp
#include <hip/hip_runtime.h>
#include <hip/hip_bf16.h>
#include <stdint.h>
#include <utility>
#define K1_TOUCH 0
#define TOUCH 0
#define RT_IT0 2
#define K2_RT 1
#define K2_PRIO 1
#define K1_ROT 0
#define G1PD 4
#define ACT_POL 1
#define RT_POL 1
#define PART_POL 1
#define H_POL 1
#define QKV_POL 1
#define G1_PRIO 1

namespace {

typedef __bf16 bf16x2 __attribute__((ext_vector_type(2)));
typedef __bf16 bf16x8 __attribute__((ext_vector_type(8)));
typedef __bf16 bf16x16 __attribute__((ext_vector_type(16)));
typedef short s16x4 __attribute__((ext_vector_type(4)));
typedef float f32x2 __attribute__((ext_vector_type(2)));
typedef float f32x4 __attribute__((ext_vector_type(4)));
typedef float f32x8 __attribute__((ext_vector_type(8)));
typedef float f32x16 __attribute__((ext_vector_type(16)));
typedef unsigned u32x2 __attribute__((ext_vector_type(2)));
typedef unsigned u32x4 __attribute__((ext_vector_type(4)));
#define LAS __attribute__((address_space(3)))
#define DI __device__ __forceinline__

constexpr int S_ = 2048, D_ = 576, H_ = 32, F_ = 4608;
constexpr float EPS = 1e-5f;
constexpr float QSCALE = 0.23570226039551584f * 1.4426950408889634f;
constexpr float SAFE_B = 48.f;

constexpr int KVCHUNK = 19456, KVIMG = 8 * KVCHUNK;
constexpr bool OVERLAY = true;
constexpr size_t WS_QM   = 0;
constexpr size_t WS_QT   = WS_QM + (size_t)32 * 2048 * 32;
constexpr size_t WS_KV   = WS_QT + (size_t)32 * 2048 * 4;
constexpr size_t WS_A_END = WS_KV + (size_t)32 * KVIMG;
constexpr size_t WS_ACT  = OVERLAY ? 0 : WS_A_END;
constexpr size_t WS_OF   = WS_ACT + (size_t)128 * 144 * 1024;
constexpr size_t WS_WO   = WS_OF + (size_t)128 * 18 * 1024;
constexpr size_t WS_KMAX = WS_WO + (size_t)6 * 108 * 1024;
constexpr size_t WS_MUX  = WS_KMAX + 32 * 16 * 4;
constexpr size_t WS_WBAR = WS_MUX + 2048 * 4;
constexpr size_t WS_SSQ  = WS_WBAR + 4096;
constexpr size_t WS_CNT  = WS_SSQ + (size_t)2048 * 64;
constexpr size_t WS_MUH  = WS_CNT + 104 * 256;
constexpr size_t WS_HCF  = WS_MUH + 2048 * 4;
constexpr size_t WS_W2   = WS_HCF + (size_t)128 * 18 * 1024;
constexpr size_t WS_W1   = WS_W2 + (size_t)16 * 324 * 1024;
constexpr size_t WS_PART = WS_W1 + (size_t)32 * 324 * 1024;
static_assert(WS_A_END <= (size_t)128 * 144 * 1024, "overlay fits");
constexpr size_t WS_END  = WS_PART + (size_t)64 * 16 * 9 * 1024;

#define TLS(i)
#define TL_DECL
#define TL_FLUSH(kid)
#define STAMP_DECL
#define STAMP(kid, i)
#define RSTAMP(kid, i)
#define STAMP_FLUSH(kid)
DI unsigned cvtpk(float lo, float hi) { f32x2 v = {lo, hi}; bf16x2 b = __builtin_convertvector(v, bf16x2); return __builtin_bit_cast(unsigned, b); }
DI float bflo(unsigned w) { return __uint_as_float(w << 16); }
DI float bfhi(unsigned w) { return __uint_as_float(w & 0xffff0000u); }
DI float dot2(unsigned a, unsigned b, float c) { return __builtin_amdgcn_fdot2_f32_bf16(__builtin_bit_cast(bf16x2, a), __builtin_bit_cast(bf16x2, b), c, false); }
DI int rfl(int x) { return __builtin_amdgcn_readfirstlane(x); }
DI float xor32(float v) { auto r = __builtin_amdgcn_permlane32_swap(__float_as_uint(v), __float_as_uint(v), false, false); return __uint_as_float((threadIdx.x & 32) ? r[0] : r[1]); }
DI float xor16(float v) { auto r = __builtin_amdgcn_permlane16_swap(__float_as_uint(v), __float_as_uint(v), false, false); return __uint_as_float((threadIdx.x & 16) ? r[0] : r[1]); }
template <int N> DI float row_ror(float v) { return __uint_as_float(__builtin_amdgcn_update_dpp(0u, __float_as_uint(v), 0x120 + N, 0xf, 0xf, false)); }
DI f32x4 mfma16(bf16x8 a, bf16x8 b, f32x4 c) { return __builtin_amdgcn_mfma_f32_16x16x32_bf16(a, b, c, 0, 0, 0); }
DI void mfma16_acc(f32x4& c, bf16x8 a, bf16x8 b) { asm volatile("v_mfma_f32_16x16x32_bf16 %0, %1, %2, %0" : "+v"(c) : "v"(a), "v"(b)); }
DI void mfma16_new(f32x4& c, bf16x8 a, bf16x8 b) { asm volatile("v_mfma_f32_16x16x32_bf16 %0, %1, %2, 0" : "=&v"(c) : "v"(a), "v"(b)); }
DI void glds16(const void* sbase, unsigned voff, unsigned lds_dst) {
    asm volatile("s_mov_b32 m0, %2\n\ts_nop 0\n\tglobal_load_lds_dwordx4 %0, %1" :: "v"(voff), "s"(sbase), "s"(lds_dst) : "memory");
}
DI void gload16(bf16x8& dst, const void* sbase, unsigned voff) { asm volatile("global_load_dwordx4 %0, %1, %2" : "=v"(dst) : "v"(voff), "s"(sbase)); }
DI void gload16f(f32x4& dst, const void* sbase, unsigned voff) { asm volatile("global_load_dwordx4 %0, %1, %2" : "=v"(dst) : "v"(voff), "s"(sbase)); }
DI void gload8(u32x2& dst, const void* sbase, unsigned voff) { asm volatile("global_load_dwordx2 %0, %1, %2" : "=v"(dst) : "v"(voff), "s"(sbase)); }
DI void gload4(float& dst, const void* sbase, unsigned voff) { asm volatile("global_load_dword %0, %1, %2" : "=v"(dst) : "v"(voff), "s"(sbase)); }
typedef __amdgpu_buffer_rsrc_t srd_t;
DI srd_t make_srd(const void* p, unsigned bytes) { return __builtin_amdgcn_make_buffer_rsrc((void*)p, (short)0, (int)bytes, 0x00020000); }
template <int IMM> DI void bload16(bf16x8& dst, srd_t r, unsigned voff, unsigned soff) {
    asm volatile("buffer_load_dwordx4 %0, %1, %2, %3 offen offset:%4" : "=v"(dst) : "v"(voff), "s"(r), "s"(soff), "n"(IMM)); }
template <int IMM> DI void bload16f(f32x4& dst, srd_t r, unsigned voff, unsigned soff) {
    asm volatile("buffer_load_dwordx4 %0, %1, %2, %3 offen offset:%4" : "=v"(dst) : "v"(voff), "s"(r), "s"(soff), "n"(IMM)); }
template <int IMM> DI void bload4(float& dst, srd_t r, unsigned voff, unsigned soff) {
    asm volatile("buffer_load_dword %0, %1, %2, %3 offen offset:%4" : "=v"(dst) : "v"(voff), "s"(r), "s"(soff), "n"(IMM)); }
template <int IMM> DI void bload16_sc1(bf16x8& dst, srd_t r, unsigned voff, unsigned soff) {
    asm volatile("buffer_load_dwordx4 %0, %1, %2, %3 offen offset:%4 sc1" : "=v"(dst) : "v"(voff), "s"(r), "s"(soff), "n"(IMM)); }
template <int IMM> DI void bload4_sc1(float& dst, srd_t r, unsigned voff, unsigned soff) {
    asm volatile("buffer_load_dword %0, %1, %2, %3 offen offset:%4 sc1" : "=v"(dst) : "v"(voff), "s"(r), "s"(soff), "n"(IMM)); }
template <int IMM> DI void bload16f_sc1(f32x4& dst, srd_t r, unsigned voff, unsigned soff) {
    asm volatile("buffer_load_dwordx4 %0, %1, %2, %3 offen offset:%4 sc1" : "=v"(dst) : "v"(voff), "s"(r), "s"(soff), "n"(IMM)); }
template <int IMM> DI void bload16u_sc1(u32x4& dst, srd_t r, unsigned voff, unsigned soff) {
    asm volatile("buffer_load_dwordx4 %0, %1, %2, %3 offen offset:%4 sc1" : "=v"(dst) : "v"(voff), "s"(r), "s"(soff), "n"(IMM)); }
template <int IMM> DI void bload8_sc1(u32x2& dst, srd_t r, unsigned voff, unsigned soff) {
    asm volatile("buffer_load_dwordx2 %0, %1, %2, %3 offen offset:%4 sc1" : "=v"(dst) : "v"(voff), "s"(r), "s"(soff), "n"(IMM)); }
template <int IMM> DI void bload8(u32x2& dst, srd_t r, unsigned voff, unsigned soff) {
    asm volatile("buffer_load_dwordx2 %0, %1, %2, %3 offen offset:%4" : "=v"(dst) : "v"(voff), "s"(r), "s"(soff), "n"(IMM)); }
DI void touch64(float& sink, srd_t r, unsigned lane128, unsigned soff) { asm volatile("buffer_load_dword %0, %1, %2, %3 offen" : "+v"(sink) : "v"(lane128), "s"(r), "s"(soff)); }
DI void bdma4(srd_t r, unsigned voff, unsigned soff, unsigned lds_dst) {
    asm volatile("s_mov_b32 m0, %3\n\ts_nop 0\n\tbuffer_load_dwordx4 %0, %1, %2 offen lds\n\tbuffer_load_dwordx4 %0, %1, %2 offen offset:1024 lds\n\t"
                 "buffer_load_dwordx4 %0, %1, %2 offen offset:2048 lds\n\tbuffer_load_dwordx4 %0, %1, %2 offen offset:3072 lds" :: "v"(voff), "s"(r), "s"(soff), "s"(lds_dst) : "memory"); }
DI void bdma1(srd_t r, unsigned voff, unsigned soff, unsigned lds_dst) {
    asm volatile("s_mov_b32 m0, %3\n\ts_nop 0\n\tbuffer_load_dwordx4 %0, %1, %2 offen lds" :: "v"(voff), "s"(r), "s"(soff), "s"(lds_dst) : "memory"); }
template <int IMM> DI void bdma_next(srd_t r, unsigned voff, unsigned soff, unsigned lds_dst) {
    asm volatile("s_mov_b32 m0, %3\n\ts_nop 0\n\tbuffer_load_dwordx4 %0, %1, %2 offen offset:%4 lds" :: "v"(voff), "s"(r), "s"(soff), "s"(lds_dst), "n"(IMM) : "memory"); }
template <int POL> DI void bstore16(u32x4 d, srd_t r, unsigned voff, unsigned soff) {
    if constexpr (POL == 1) asm volatile("buffer_store_dwordx4 %0, %1, %2, %3 offen sc1\n\ts_nop 1" :: "v"(d), "v"(voff), "s"(r), "s"(soff) : "memory");
    else if constexpr (POL == 2) asm volatile("buffer_store_dwordx4 %0, %1, %2, %3 offen nt\n\ts_nop 1" :: "v"(d), "v"(voff), "s"(r), "s"(soff) : "memory");
    else asm volatile("buffer_store_dwordx4 %0, %1, %2, %3 offen\n\ts_nop 1" :: "v"(d), "v"(voff), "s"(r), "s"(soff) : "memory"); }
template <int POL> DI void bstore8(u32x2 d, srd_t r, unsigned voff, unsigned soff) {
    if constexpr (POL == 1) asm volatile("buffer_store_dwordx2 %0, %1, %2, %3 offen sc1\n\ts_nop 1" :: "v"(d), "v"(voff), "s"(r), "s"(soff) : "memory");
    else if constexpr (POL == 2) asm volatile("buffer_store_dwordx2 %0, %1, %2, %3 offen nt\n\ts_nop 1" :: "v"(d), "v"(voff), "s"(r), "s"(soff) : "memory");
    else asm volatile("buffer_store_dwordx2 %0, %1, %2, %3 offen\n\ts_nop 1" :: "v"(d), "v"(voff), "s"(r), "s"(soff) : "memory"); }
#define PINV(x) asm volatile("" : "+v"(x))
#define WAITV(n) asm volatile("s_waitcnt vmcnt(%0)" :: "n"(n) : "memory")
#define WAIT_BAR(n) asm volatile("s_waitcnt vmcnt(%0) lgkmcnt(0)\n\ts_barrier" :: "n"(n) : "memory")
#define SBAR() __builtin_amdgcn_sched_barrier(0)
#define LBAR() asm volatile("s_waitcnt lgkmcnt(0)\n\ts_barrier" ::: "memory")
DI bf16x8 lds_frag(LAS const char* p) { return *(LAS const bf16x8*)p; }
typedef short v4i16_t __attribute__((ext_vector_type(4)));
DI u32x2 lds_tr(LAS const char* p) { return __builtin_bit_cast(u32x2, __builtin_amdgcn_ds_read_tr16_b64_v4i16((LAS v4i16_t*)p)); }
DI bf16x8 tr_frag(LAS const char* p, int off2) { u32x2 a = lds_tr(p), b = lds_tr(p + off2); u32x4 r = {a[0], a[1], b[0], b[1]}; return __builtin_bit_cast(bf16x8, r); }

DI int kstep_to_ftile(int ks, int half) {
    const int a = ks / 9, idx = ks - 9 * a;
    if (idx == 8) return 9 * (2 * a + half) + 8;
    return 9 * (2 * a + (idx >> 2)) + 2 * (idx & 3) + half;
}

DI int wo_row(int k) { return k < 512 ? 18 * (k >> 4) + (k & 15) : 18 * ((k - 512) >> 1) + 16 + (k & 1); }
DI void retile_wo(int job, int lane, const char* wo, char* ws, char* scratch_generic) {
    const int s = job / 9, cb = job - 9 * s, r8 = lane >> 3, ch = lane & 7;
#pragma unroll
    for (int p = 0; p < 4; ++p) { const int rho = 8 * p + r8, k = 32 * s + 8 * ((rho >> 2) & 3) + 4 * (rho >> 4) + (rho & 3);
        glds16(wo + cb * 128, wo_row(k) * (D_ * 2) + ch * 16, (unsigned)(uintptr_t)scratch_generic + p * 1024); }
    WAITV(0);
    LAS const char* base = (LAS const char*)scratch_generic + (4 * (lane >> 4) + ((lane >> 2) & 3)) * 128 + (lane & 3) * 8;
#pragma unroll
    for (int u = 0; u < 4; ++u) { const int T = 4 * cb + u, n6 = T / 6, tt = T - 6 * n6;
        *(bf16x8*)(ws + WS_WO + (size_t)(n6 * 108 + s * 6 + tt) * 1024 + lane * 16) = tr_frag(base + 32 * u, 16 * 128); }
}

constexpr int K1_SLOT = 34 * 1024, K1_STG = 0, K1_SMALL = 4 * K1_SLOT, K1_LDS = K1_SMALL + 4096;
template <int PART, int NJ>
DI void k1_wave(const char* x, const char* wqkv, char* ws, char* lds_generic, int mt, int nt, int rot, int shx, int shw) {
    constexpr bool VPART = PART == 2; constexpr int NT = NJ ? 4 : 5, T0 = NJ ? 5 : 0;
    LAS char* lds = (LAS char*)lds_generic; const unsigned ldsb = (unsigned)(uintptr_t)lds_generic;
    const int tid = threadIdx.x, lane = tid & 63, wid = rfl(tid >> 6), mi = wid & 3, m = lane & 15, g = lane >> 4;
    STAMP_DECL; RSTAMP(1, 14); STAMP(1, 0);
    const srd_t sx = make_srd(x, S_ * D_ * 2), sw = make_srd(wqkv, D_ * 3 * D_ * 2);
    const unsigned xv = (lane >> 3) * (D_ * 2) + (((lane & 7) ^ ((4 * wid + (lane >> 4)) & 7)) << 4);
    unsigned wv[2];
#pragma unroll
    for (int i = 0; i < 2; ++i) { const int idx = (i ? 8 : wid) * 64 + lane, rho = (idx * 3641) >> 16, ch = idx - 18 * rho;
        wv[i] = (8 * ((rho >> 2) & 3) + 4 * (rho >> 4) + (rho & 3)) * 3456 + ch * 16; }
    const unsigned xs = (128 * mt + 8 * wid) * (D_ * 2), wsb = nt * 288;
    auto issue = [&](int st, int p) {
        int k = st + rot; k = k >= 9 ? k - 9 : k; const unsigned dst = ldsb + (st & 3) * K1_SLOT;
        if (p < 2) bdma1(sx, xv, xs + p * 64 * (D_ * 2) + k * 128, dst + (wid + 8 * p) * 1024);
        else if (p < 4) bdma1(sw, wv[0], wsb + (64 * k + 32 * (p - 2)) * 3456, dst + 16384 + ((p - 2) * 9 + wid) * 1024);
        else if (wid < 2) bdma1(sw, wv[1], wsb + (64 * k + 32 * wid) * 3456, dst + 16384 + (wid * 9 + 8) * 1024); };
#pragma unroll
    for (int st = 0; st < 2; ++st)
#pragma unroll
        for (int p = 0; p < 5; ++p) issue(st, p);
    float tch[2] = {0.f, 0.f};
#pragma unroll
    for (int i = 0; i < 2 * K1_TOUCH; ++i) { int t = wid + 8 * i; t = t >= 12 ? t - 4 : t;
        if (t < 3) { const int L = shx * 192 + t * 64 + lane, row = (L * 7282) >> 16; bload4<0>(tch[i], sx, (128 * mt + row) * (D_ * 2) + (L - 9 * row) * 128, 0); }
        else { const int q = (t - 3) * 64 + lane; bload4<0>(tch[i], sw, (shw * 144 + (q >> 2)) * 3456 + ((nt * 288) & ~127) + (q & 3) * 128, 0); } }
    f32x4 acc[NT][2];
    f32x4 accs = {0, 0, 0, 0}, accg = {0, 0, 0, 0}, accc[NT];
#pragma unroll
    for (int t = 0; t < NT; ++t) accc[t] = (f32x4){0, 0, 0, 0};
    bf16x8 ones = __builtin_bit_cast(bf16x8, (u32x4){0x3f803f80u, 0x3f803f80u, 0x3f803f80u, 0x3f803f80u}); PINV(ones);
    asm volatile("s_nop 1" : "+v"(accs), "+v"(accg));
#pragma unroll
    for (int t = 0; t < NT; ++t) PINV(accc[t]);
    const unsigned xa0 = (32 * mi + m) * 128 + ((g ^ ((m >> 1) & 7)) << 4), xa1 = (32 * mi + m) * 128 + (((4 + g) ^ ((m >> 1) & 7)) << 4);
    const unsigned wa = 16384 + (4 * g + ((lane >> 2) & 3)) * 288 + (lane & 3) * 8 + T0 * 32;
    bf16x8 xq[2][2][2], wr[2][2][NT];
    auto rd = [&](int st, int i) {
        LAS const char* sb = lds + (st & 3) * K1_SLOT;
        if (i < 4) xq[st & 1][i >> 1][i & 1] = lds_frag(sb + ((i >> 1) ? xa1 : xa0) + (i & 1) * 2048);
        else { const int s2 = (i - 4) / NT, t = (i - 4) % NT; wr[st & 1][s2][t] = tr_frag(sb + wa + s2 * 9216 + 32 * t, 16 * 288); } };
    STAMP(1, 1);
    WAIT_BAR(4 + K1_TOUCH * 2);
    STAMP(1, 2);
#pragma unroll
    for (int i = 0; i < 4 + 2 * NT; ++i) rd(0, i);
#pragma unroll
    for (int p = 0; p < 5; ++p) issue(2, p);
#pragma unroll
    for (int st = 0; st < 9; ++st) {
        if (st < 8) { if (st < 2) WAIT_BAR(4 + K1_TOUCH * 2); else if (st < 6) WAIT_BAR(4); else if (st == 6) WAIT_BAR(4); else WAIT_BAR(0); }
        if (st == 2) { PINV(tch[0]); PINV(tch[1]); }
#pragma unroll
        for (int i = 0; i < 2 * NT; ++i) { const int s2 = i / NT, t = i % NT; const bf16x8 w = wr[st & 1][s2][t], x0 = xq[st & 1][s2][0], x1 = xq[st & 1][s2][1];
            if (st == 0 && s2 == 0) { if (VPART) { mfma16_new(acc[t][0], x0, w); mfma16_new(acc[t][1], x1, w); } else { mfma16_new(acc[t][0], w, x0); mfma16_new(acc[t][1], w, x1); } }
            else { if (VPART) { mfma16_acc(acc[t][0], x0, w); mfma16_acc(acc[t][1], x1, w); } else { mfma16_acc(acc[t][0], w, x0); mfma16_acc(acc[t][1], w, x1); } }
            if (st + 1 < 9) { if (i < 4) { rd(st + 1, i); rd(st + 1, 4 + i); } else if (4 + i < 4 + 2 * NT) rd(st + 1, 4 + i); }
            if (st + 3 < 9 && (i & 1) == 0 && i / 2 < 4) issue(st + 3, i / 2 == 0 ? 0 : i / 2 == 1 ? 2 : i / 2 == 2 ? 1 : 3);
            if (st + 3 < 9 && i == 7) issue(st + 3, 4);
            if (t == 1) mfma16_acc(accs, ones, NJ ? x1 : x0);
            if (t == 2) mfma16_acc(accg, NJ ? x1 : x0, NJ ? x1 : x0);
            if (t == NT - 1 && ((2 * st + s2) & 3) == mi) {
#pragma unroll
                for (int tt = 0; tt < NT; ++tt) { if (VPART) mfma16_acc(accc[tt], ones, wr[st & 1][s2][tt]); else mfma16_acc(accc[tt], wr[st & 1][s2][tt], ones); } }
            SBAR(); }
    }
    STAMP(1, 3);
    LAS float* sm = (LAS float*)(lds + K1_SMALL);
    asm volatile("s_nop 7\n\ts_nop 7" : "+v"(acc[NT - 1][0]), "+v"(acc[NT - 1][1]), "+v"(accs), "+v"(accg));
#pragma unroll
    for (int t = 0; t < NT; ++t) { PINV(accc[t]); PINV(acc[t][0]); PINV(acc[t][1]); }
    if (g == 0) sm[32 * mi + 16 * NJ + m] = accs[0];
    if (g == (m >> 2)) sm[128 + 32 * mi + 16 * NJ + m] = (m & 2) ? ((m & 1) ? accg[3] : accg[2]) : ((m & 1) ? accg[1] : accg[0]);
#pragma unroll
    for (int t = 0; t < NT; ++t) { if (VPART) { if (g == 0) sm[256 + mi * 144 + 16 * (T0 + t) + m] = accc[t][0]; }
        else if (m == 0) *(LAS f32x4*)(sm + 256 + mi * 144 + 16 * (T0 + t) + 4 * g) = accc[t]; }
    LBAR();
    STAMP(1, 4);
    LAS char* stg = lds + K1_STG;
    if (VPART && tid < 256) { const int hs = tid >> 1;
        *(LAS u32x4*)(stg + hs * 320 + ((tid & 1) ? 19 : 4) * 16) = (tid & 1) ? (u32x4){0x3f803f80u, 0x3f803f80u, 0x3f803f80u, 0x3f803f80u} : (u32x4){0, 0, 0, 0}; }
    f32x4 mean[2], rstd[2];
#pragma unroll
    for (int u = 0; u < 2; ++u) { const int tl = 32 * mi + 16 * u + (VPART ? 4 * g : m);
        if (VPART) { mean[u] = *(LAS const f32x4*)(sm + tl) * (1.f / D_);
            const f32x4 var = *(LAS const f32x4*)(sm + 128 + tl) * (1.f / D_) - mean[u] * mean[u];
#pragma unroll
            for (int r = 0; r < 4; ++r) rstd[u][r] = __builtin_amdgcn_rsqf(fmaxf(var[r], 0.f) + EPS); }
        else { mean[u][0] = sm[tl] * (1.f / D_);
            rstd[u][0] = __builtin_amdgcn_rsqf(fmaxf(sm[128 + tl] * (1.f / D_) - mean[u][0] * mean[u][0], 0.f) + EPS) * (PART == 0 ? QSCALE : 1.f); } }
#pragma unroll
    for (int t = 0; t < NT; ++t) { const int tt = T0 + t;
        if (VPART) { const int n = 16 * tt + m, hl = (n * 3641) >> 16, d = n - 18 * hl, row = d + (d >= 4 ? 5 : 0) - (d >= 12 ? 12 : 0) + (d >= 16 ? 8 : 0);
            const float c1 = (sm[256 + n] + sm[400 + n]) + (sm[544 + n] + sm[688 + n]);
            LAS char* p = stg + hl * 5120 + 2 * mi * 640 + (g >> 1) * 320 + row * 16 + (g & 1) * 8;
#pragma unroll
            for (int u = 0; u < 2; ++u) { const f32x4 a = (acc[t][u] - mean[u] * c1) * rstd[u];
                *(LAS u32x2*)(p + u * 640) = (u32x2){cvtpk(a[0], a[1]), cvtpk(a[2], a[3])}; }
        } else {
            const int n0 = 16 * tt + 4 * g;
            const f32x4 c4 = (*(LAS const f32x4*)(sm + 256 + n0) + *(LAS const f32x4*)(sm + 400 + n0)) + (*(LAS const f32x4*)(sm + 544 + n0) + *(LAS const f32x4*)(sm + 688 + n0));
            int o[2], ts[2];
#pragma unroll
            for (int p = 0; p < 2; ++p) { const int n = n0 + 2 * p, hl = (n * 3641) >> 16, d = n - 18 * hl; const bool tail = d == 16;
                if (PART == 0) { o[p] = tail ? 32768 + hl * 512 : hl * 4096 + d * 2; ts[p] = tail ? 4 : 32; }
                else { o[p] = hl * 4608 + (tail ? 4096 : (d >> 3) * 2048 + (d & 7) * 2); ts[p] = tail ? 4 : 16; } }
#pragma unroll
            for (int u = 0; u < 2; ++u) { const f32x4 a = (acc[t][u] - c4 * mean[u][0]) * rstd[u][0]; const int tl = 32 * mi + 16 * u + m;
                *(LAS unsigned*)(stg + o[0] + tl * ts[0]) = cvtpk(a[0], a[1]); *(LAS unsigned*)(stg + o[1] + tl * ts[1]) = cvtpk(a[2], a[3]); }
        } }
    if (nt == 0 && tid < 128) ((float*)(ws + WS_MUX))[128 * mt + tid] = sm[tid] * (1.f / D_);
    LBAR();
    STAMP(1, 5);
    const int head = 8 * (nt & 3) + wid; const srd_t sws = make_srd(ws, (unsigned)WS_END); const unsigned l16 = lane * 16;
    if (PART == 2) { const unsigned dst = (unsigned)WS_KV + head * KVIMG + (mt >> 1) * KVCHUNK + 9216 + (mt & 1) * 5120;
#pragma unroll
        for (int i = 0; i < 5; ++i) bstore16<QKV_POL>(*(LAS const u32x4*)(stg + wid * 5120 + i * 1024 + l16), sws, l16, dst + i * 1024);
    } else if (PART == 0) { const unsigned dst = (unsigned)WS_QM + (head * S_ + 128 * mt) * 32;
#pragma unroll
        for (int i = 0; i < 4; ++i) bstore16<QKV_POL>(*(LAS const u32x4*)(stg + wid * 4096 + i * 1024 + l16), sws, l16, dst + i * 1024);
        if (lane < 32) bstore16<QKV_POL>(*(LAS const u32x4*)(stg + 32768 + wid * 512 + l16), sws, l16, (unsigned)WS_QT + (head * S_ + 128 * mt) * 4);
    } else { const unsigned dst = (unsigned)WS_KV + head * KVIMG + (mt >> 1) * KVCHUNK; LAS const char* hb = stg + wid * 4608;
#pragma unroll
        for (int i = 0; i < 4; ++i) bstore16<QKV_POL>(*(LAS const u32x4*)(hb + i * 1024 + l16), sws, l16, dst + (i >> 1) * 4096 + (mt & 1) * 2048 + (i & 1) * 1024);
        if (lane < 32) bstore16<QKV_POL>(*(LAS const u32x4*)(hb + 4096 + l16), sws, l16, dst + 8192 + (mt & 1) * 512);
        float mx = 0.f;
#pragma unroll
        for (int i = 0; i < 2; ++i) { const int tl = lane + 64 * i;
            const u32x4 a = *(LAS const u32x4*)(hb + tl * 16), bq = *(LAS const u32x4*)(hb + 2048 + tl * 16); const unsigned c = *(LAS const unsigned*)(hb + 4096 + tl * 4);
            float s2 = dot2(c, c, 0.f);
#pragma unroll
            for (int e = 0; e < 4; ++e) { s2 = dot2(a[e], a[e], s2); s2 = dot2(bq[e], bq[e], s2); }
            mx = fmaxf(mx, s2); }
        mx = fmaxf(mx, xor16(mx)); mx = fmaxf(mx, xor32(mx));
#pragma unroll
        for (int o = 1; o < 16; o <<= 1) mx = fmaxf(mx, __shfl_xor(mx, o));
        if (lane == 0) ((float*)(ws + WS_KMAX))[head * 16 + mt] = mx;
    }
    STAMP(1, 6); RSTAMP(1, 15); STAMP_FLUSH(1);
}
template <int PART> DI void k1_body(const char* x, const char* wqkv, char* ws, char* lds, int mt, int nt, int rot, int shx, int shw) {
    if (threadIdx.x >= 256) k1_wave<PART, 1>(x, wqkv, ws, lds, mt, nt, rot, shx, shw); else k1_wave<PART, 0>(x, wqkv, ws, lds, mt, nt, rot, shx, shw);
}
__global__ __launch_bounds__(512, 2) void k1_qkv(const char* x, const char* wqkv, const char* wo, char* ws) {
    extern __shared__ __attribute__((aligned(16))) char lds[];
    const int b = blockIdx.x;
    if (b >= 192) {
        const int lane = threadIdx.x & 63, wv = rfl(threadIdx.x >> 6), gw = (b - 192) * 8 + wv;
        if (threadIdx.x == 511) ((unsigned*)(ws + WS_CNT))[(b - 192) * 64] = 0u;
        if (threadIdx.x == 510 && b - 192 < 40) ((unsigned*)(ws + WS_CNT))[(64 + b - 192) * 64] = 0u;
        if (gw < 162) retile_wo(gw, lane, wo, ws, lds + wv * 4096);
        for (int c = gw; c < D_; c += 512) {
            const char* row = wo + (size_t)wo_row(c) * (D_ * 2);
            const u32x4 a = *(const u32x4*)(row + lane * 16); float s = 0.f;
#pragma unroll
            for (int e = 0; e < 4; ++e) s = dot2(a[e], 0x3f803f80u, s);
            if (lane < 8) { const u32x4 bq = *(const u32x4*)(row + 1024 + lane * 16);
#pragma unroll
                for (int e = 0; e < 4; ++e) s = dot2(bq[e], 0x3f803f80u, s); }
#pragma unroll
            for (int o = 1; o < 64; o <<= 1) s += __shfl_xor(s, o);
            if (lane == 0) { const float w = s * (1.f / D_); const unsigned hi = cvtpk(w, 0.f) & 0xffffu; const unsigned lo = cvtpk(w - bflo(hi), 0.f) & 0xffffu;
                ((unsigned short*)(ws + WS_WBAR))[c] = (unsigned short)hi; ((unsigned short*)(ws + WS_WBAR))[D_ + c] = (unsigned short)lo; }
        }
        return;
    }
    const int xcd = b & 7, idx = b >> 3, mt = (xcd & 3) * 4 + (idx & 3), nt = (xcd >> 2) * 6 + (idx >> 2);
    const int rot = K1_ROT ? (2 * (idx & 3) + ((idx >> 2) * 3 >> 1)) % 9 : 0;
    const int shx = idx >> 2, shw = idx & 3;
    if (nt >= 8) k1_body<2>(x, wqkv, ws, lds, mt, nt, rot, shx, shw); else if (nt >= 4) k1_body<1>(x, wqkv, ws, lds, mt, nt, rot, shx, shw); else k1_body<0>(x, wqkv, ws, lds, mt, nt, rot, shx, shw);
}

constexpr int K2_LDS = 160 * 1024;
struct RtJob { unsigned so[4], dst[4]; };
struct RtTable { RtJob j[8][486];
    constexpr RtTable() : j{} {
        for (int xg = 0; xg < 8; ++xg) for (int slot = 0; slot < 486; ++slot) { RtJob& e = j[xg][slot];
            if (slot < 324) { const int gu = slot >= 162, r = slot - 162 * gu, ks = r / 9, cb = 9 * xg + r % 9;
                for (int p = 0; p < 4; ++p) { const int T = 4 * cb + p, jj = T / 9, tt = T % 9;
                    e.so[p] = (ks * 32 + p * 8) * (F_ * 2) + cb * 128;
                    e.dst[p] = (unsigned)WS_W1 + (jj * 324 + (tt < 4 ? ks * 8 + tt * 2 : tt < 8 ? (tt >> 1) * 72 + ks * 4 + (tt & 1) * 2 : 288 + ks * 2) + gu) * 1024; }
            } else { const int r2 = slot - 324, q = r2 / 9, cb = r2 % 9, ks = 18 * xg + q, a = ks / 9, qi = ks % 9;
                const int t0 = qi == 8 ? 18 * a + 8 : 18 * a + 9 * (qi >> 2) + 2 * (qi & 3), t1 = qi == 8 ? t0 + 9 : t0 + 1;
                for (int p = 0; p < 4; ++p) { const int T = 4 * cb + p, n4 = T / 9, tt = T % 9;
                    e.so[p] = (16 * (p >> 1 ? t1 : t0) + 8 * (p & 1)) * (D_ * 2) + cb * 128;
                    e.dst[p] = (unsigned)WS_W2 + (((xg >> 1) * 4 + n4) * 324 + (((q >> 1) * 2 + (xg & 1)) * 2 + (q & 1)) * 9 + tt) * 1024; } } }
    } };
__device__ const RtTable rt_table{};
#define EXPS(S, a, b) _Pragma("unroll") for (int r_ = a; r_ < b; ++r_) S[r_] = __builtin_amdgcn_exp2f(S[r_]); SBAR()
#define PLO(S) __builtin_bit_cast(bf16x16, (f32x8)__builtin_shufflevector(S, S, 0, 1, 2, 3, 4, 5, 6, 7))
#define PHI(S) __builtin_bit_cast(bf16x16, (f32x8)__builtin_shufflevector(S, S, 8, 9, 10, 11, 12, 13, 14, 15))
#define KTAIL(p) __builtin_bit_cast(s16x4, (u32x2){*(LAS const unsigned*)(p), kc})
#define QK1(q) __builtin_amdgcn_mfma_f32_32x32x16_bf16(kf, q, (f32x16){}, 0, 0, 0)
#define QK2(q, s) __builtin_amdgcn_mfma_f32_32x32x8bf16_1k(kt, q, s, 0, 0, 0)
#define PV(v, p, o) __builtin_amdgcn_smfmac_f32_32x32x32_bf16(v, p, o, 0xDDDD, 0, 0)
template <class Hook>
DI void attn_fast(LAS const char* kp, LAS const char* tp, LAS const char* vp, const bf16x8 (&qm)[2], const s16x4 (&qt)[2], f32x16 (&o)[2], unsigned kc, Hook hook) {
    bf16x8 kf = *(LAS const bf16x8*)kp; s16x4 kt = KTAIL(tp); bf16x8 v0, v1;
    f32x16 s0 = QK1(qm[0]), s1; s0 = QK2(qt[0], s0);
    SBAR();
#pragma unroll
    for (int it = 0; it < 8; ++it) {
        if (K2_PRIO) { if (it == 0) __builtin_amdgcn_s_setprio(3); if (it == 2) __builtin_amdgcn_s_setprio(2); if (it == 4) __builtin_amdgcn_s_setprio(1); if (it == 6) __builtin_amdgcn_s_setprio(0); }
        EXPS(s0, 0, 2);
        if (it > 0) { o[1] = PV(v0, PLO(s1), o[1]); SBAR(); }
        EXPS(s0, 2, 6);
        if (it > 0) { o[1] = PV(v1, PHI(s1), o[1]); SBAR(); }
        v0 = *(LAS const bf16x8*)(vp + it * 1280); v1 = *(LAS const bf16x8*)(vp + it * 1280 + 640); SBAR();
        hook(it);
        EXPS(s0, 6, 9);
        s1 = QK1(qm[1]); SBAR();
        EXPS(s0, 9, 12);
        s1 = QK2(qt[1], s1); SBAR();
        if (it + 1 < 8) { kf = *(LAS const bf16x8*)(kp + (it + 1) * 512); kt = KTAIL(tp + (it + 1) * 128); SBAR(); }
        EXPS(s0, 12, 16);
        EXPS(s1, 0, 2);
        o[0] = PV(v0, PLO(s0), o[0]); SBAR();
        EXPS(s1, 2, 6);
        o[0] = PV(v1, PHI(s0), o[0]); SBAR();
        EXPS(s1, 6, 9);
        if (it + 1 < 8) { s0 = QK1(qm[0]); SBAR(); }
        EXPS(s1, 9, 12);
        if (it + 1 < 8) { s0 = QK2(qt[0], s0); SBAR(); }
        EXPS(s1, 12, 16);
    }
    o[1] = PV(v0, PLO(s1), o[1]); o[1] = PV(v1, PHI(s1), o[1]);
}
DI void attn_slow(LAS const char* kp, LAS const char* tp, LAS const char* vp, const bf16x8 (&qm)[2], const s16x4 (&qt)[2], f32x16 (&o)[2], float (&mrun)[2], unsigned kc) {
#pragma unroll 1
    for (int it = 0; it < 8; ++it) {
        const bf16x8 kf = *(LAS const bf16x8*)(kp + it * 512); const s16x4 kt = KTAIL(tp + it * 128);
        const bf16x8 v0 = *(LAS const bf16x8*)(vp + it * 1280), v1 = *(LAS const bf16x8*)(vp + it * 1280 + 640);
#pragma unroll
        for (int u = 0; u < 2; ++u) {
            f32x16 s = QK1(qm[u]); s = QK2(qt[u], s);
            float mx = s[0];
#pragma unroll
            for (int r = 1; r < 16; ++r) mx = fmaxf(mx, s[r]);
            mx = fmaxf(mx, xor32(mx));
            const float mn = fmaxf(mrun[u], mx), al = __builtin_amdgcn_exp2f(mrun[u] - mn);
            mrun[u] = mn;
#pragma unroll
            for (int r = 0; r < 16; ++r) { s[r] = __builtin_amdgcn_exp2f(s[r] - mn); o[u][r] *= al; }
            o[u] = PV(v0, PLO(s), o[u]); o[u] = PV(v1, PHI(s), o[u]);
        }
    }
}
__global__ __launch_bounds__(1024, 4) void k2_attn(const char* x, const char* wg, const char* wu, const char* wd, char* ws) {
    extern __shared__ __attribute__((aligned(16))) char lds_generic[];
    LAS char* lds = (LAS char*)lds_generic; const unsigned ldsb = (unsigned)(uintptr_t)lds_generic;
    const int tid = threadIdx.x, lane = tid & 63, wid = rfl(tid >> 6), qg = wid & 3, kq = wid >> 2, r32 = lane & 31, h = lane >> 5;
    const int b = blockIdx.x, xcd = b & 7, idx = b >> 3, head = xcd * 4 + (idx & 3), qb = idx >> 2;
    STAMP_DECL; RSTAMP(2, 14); STAMP(2, 0);
    const srd_t srd = make_srd(ws, (unsigned)WS_END);
    const int c0 = (2 * kq + (qb & 1)) * KVCHUNK, c1 = (2 * kq + ((qb & 1) ^ 1)) * KVCHUNK;
    const unsigned kvb = (unsigned)WS_KV + head * KVIMG, l16 = lane * 16;
    const int q0 = 256 * qb + 64 * qg;
    bf16x8 qm[2]; float qtl[2]; float km;
    { const unsigned so = (unsigned)WS_QM + (head * S_ + q0) * 32, vo = r32 * 32 + h * 16; bload16<0>(qm[0], srd, vo, so); bload16<1024>(qm[1], srd, vo, so);
      const unsigned st = (unsigned)WS_QT + (head * S_ + q0) * 4; bload4<0>(qtl[0], srd, r32 * 4, st); bload4<128>(qtl[1], srd, r32 * 4, st);
      bload4<0>(km, srd, (lane & 15) * 4, (unsigned)WS_KMAX + head * 64); }
    bdma4(srd, l16, kvb + c0 + qg * 5120, ldsb + c0 + qg * 5120); if (qg < 3) bdma1(srd, l16, kvb + c0 + qg * 5120 + 4096, ldsb + c0 + qg * 5120 + 4096);
    const int slot = wid * 32 + idx; const bool rt = slot < (K2_RT ? 486 : 0);
    bf16x8 wb[4]; unsigned rdst[4], rso[4];
    { const RtJob& J = rt_table.j[xcd][rt ? slot : 0];
#pragma unroll
      for (int p = 0; p < 4; ++p) { rso[p] = J.so[p]; rdst[p] = J.dst[p]; } }
    const char* rsrc = slot < 162 ? wg : slot < 324 ? wu : wd;
    const unsigned rvo = (lane >> 3) * (slot < 324 ? F_ * 2 : D_ * 2) + (lane & 7) * 16;
    asm volatile("s_waitcnt vmcnt(4)" : "+v"(qm[0]), "+v"(qm[1]), "+v"(qtl[0]), "+v"(qtl[1]), "+v"(km) :: "memory");
    km = fmaxf(km, row_ror<8>(km)); km = fmaxf(km, row_ror<4>(km)); km = fmaxf(km, row_ror<2>(km)); km = fmaxf(km, row_ror<1>(km));
    s16x4 qt[2]; bool unsafe = false;
#pragma unroll
    for (int u = 0; u < 2; ++u) { const u32x4 qv = __builtin_bit_cast(u32x4, qm[u]); const unsigned tl = __float_as_uint(qtl[u]);
        float n2 = h ? 0.f : dot2(tl, tl, 0.f);
#pragma unroll
        for (int e = 0; e < 4; ++e) n2 = dot2(qv[e], qv[e], n2);
        n2 += xor32(n2);
        const float bound = __builtin_amdgcn_sqrtf(n2 * km) * 1.0001f + 1e-6f;
        unsafe |= !(bound <= SAFE_B);
        const unsigned cb = (__float_as_uint(bound) + 0xffffu) >> 16;
        const u32x2 w = h ? (u32x2){0u, 0u} : (u32x2){tl, cb | 0x8000u};
        qt[u] = __builtin_bit_cast(s16x4, w); }
    const bool slow = __any(unsafe);
    if (slow) {
#pragma unroll
        for (int u = 0; u < 2; ++u) { u32x2 w = __builtin_bit_cast(u32x2, qt[u]); w[1] = 0u; qt[u] = __builtin_bit_cast(s16x4, w); } }
    f32x16 o[2] = {(f32x16){}, (f32x16){}}; float mrun[2] = {-INFINITY, -INFINITY};
    const int vrow = r32 >= 19 ? 4 : r32 + (r32 >= 4 ? 5 : 0) - (r32 >= 12 ? 12 : 0) + (r32 >= 16 ? 8 : 0);
    const unsigned kc = h ? 0u : 0x00003f80u;
    unsigned kp = ldsb + c0 + h * 4096 + r32 * 16, tp = ldsb + c0 + 8192 + r32 * 4, vp = ldsb + c0 + 9216 + h * 320 + vrow * 16;
#define KP(o) ((LAS const char*)(uintptr_t)kp + (o))
#define TP(o) ((LAS const char*)(uintptr_t)tp + (o))
#define VP(o) ((LAS const char*)(uintptr_t)vp + (o))
    STAMP(2, 1);
    WAIT_BAR(0);
    STAMP(2, 2);
    const srd_t sw = make_srd(rsrc, D_ * F_ * 2);
    auto chunk1 = [&](int part) { if (part == 0) bdma4(srd, l16, kvb + c1 + qg * 5120, ldsb + c1 + qg * 5120);
                                  else if (qg < 3) bdma1(srd, l16, kvb + c1 + qg * 5120 + 4096, ldsb + c1 + qg * 5120 + 4096); };
    STAMP(2, 8);
    if (slow) { chunk1(0); chunk1(1);
        if (rt) {
#pragma unroll
            for (int p = 0; p < 4; ++p) bload16<0>(wb[p], sw, rvo, rso[p]); }
        attn_slow(KP(0), TP(0), VP(0), qm, qt, o, mrun, kc); }
    else attn_fast(KP(0), TP(0), VP(0), qm, qt, o, kc, [&](int it) {
        if (it < 2) { chunk1(it); SBAR(); }
        if (it >= 2 && it < 6 && rt) { bload16<0>(wb[it - 2], sw, rvo, rso[it - 2]); SBAR(); } });
    STAMP(2, 3);
    if (rt) WAIT_BAR(4); else WAIT_BAR(0);
    STAMP(2, 4);
    if (rt) { asm volatile("s_waitcnt vmcnt(0)" : "+v"(wb[0]), "+v"(wb[1]), "+v"(wb[2]), "+v"(wb[3]) :: "memory");
        int ty = threadIdx.x; asm volatile("" : "+v"(ty)); LAS char* scr = lds + c0 + qg * 4864 + (ty & 63) * 16;
#pragma unroll
        for (int p = 0; p < 4; ++p) *(LAS bf16x8*)(scr + p * 1024) = wb[p]; }
    auto rt_step = [&](int it) {
        if (it < 1 || it > 4 || !rt) return;
        int ty = threadIdx.x; asm volatile("" : "+v"(ty)); const int ln = ty & 63;
        LAS const char* base = lds + c0 + qg * 4864 + (4 * (ln >> 4) + ((ln >> 2) & 3)) * 128 + (ln & 3) * 8;
        bstore16<RT_POL>(__builtin_bit_cast(u32x4, tr_frag(base + 32 * (it - 1), 16 * 128)), srd, ln * 16, rdst[it - 1]);
        SBAR(); };
    STAMP(2, 5);
    { const int dc = c1 - c0; kp += dc; tp += dc; vp += dc; PINV(kp); PINV(tp); PINV(vp); }
    if (slow) {
#pragma unroll
        for (int it = 1; it < 5; ++it) rt_step(it);
        attn_slow(KP(0), TP(0), VP(0), qm, qt, o, mrun, kc); }
    else attn_fast(KP(0), TP(0), VP(0), qm, qt, o, kc, rt_step);
    STAMP(2, 6);
    int tz = threadIdx.x; asm volatile("" : "+v"(tz));
    const int lane2 = tz & 63, r2 = tz & 31, h2 = (tz >> 5) & 1;
    LAS float* mx = (LAS float*)(lds + KVIMG);
#pragma unroll
    for (int u = 0; u < 2; ++u) { LAS char* pb = lds + c0 + (qg * 2 + u) * 2432;
        *(LAS f32x4*)(pb + lane2 * 16) = (f32x4){o[u][0], o[u][1], o[u][2], o[u][3]};
        *(LAS f32x4*)(pb + 1024 + lane2 * 16) = (f32x4){o[u][4], o[u][5], o[u][6], o[u][7]};
        if (h2 == 0) { *(LAS f32x2*)(pb + 2048 + r2 * 8) = (f32x2){o[u][8], o[u][9]}; *(LAS float*)(pb + 2304 + r2 * 4) = o[u][10];
                      if (slow) mx[wid * 64 + 32 * u + r2] = mrun[u]; } }
    STAMP(2, 9);
    LBAR();
    STAMP(2, 10);
    { const int u = kq & 1, hf = kq >> 1;
      LAS const char* pb = lds + (qb & 1) * KVCHUNK + (qg * 2 + u) * 2432;
      float l = 0.f; f32x4 a = {0, 0, 0, 0}; f32x2 c = {0, 0};
      if (slow) { float mk[4], M = -INFINITY;
#pragma unroll
          for (int k = 0; k < 4; ++k) { mk[k] = mx[(4 * k + qg) * 64 + 32 * u + r2]; M = fmaxf(M, mk[k]); }
#pragma unroll
          for (int k = 0; k < 4; ++k) { LAS const char* p = pb + k * 2 * KVCHUNK; const float f = __builtin_amdgcn_exp2f(mk[k] - M);
              l += f * *(LAS const float*)(p + 2304 + r2 * 4); a += *(LAS const f32x4*)(p + hf * 1024 + lane2 * 16) * f; c += *(LAS const f32x2*)(p + 2048 + r2 * 8) * f; }
      } else {
#pragma unroll
          for (int k = 0; k < 4; ++k) { LAS const char* p = pb + k * 2 * KVCHUNK;
              l += *(LAS const float*)(p + 2304 + r2 * 4); a += *(LAS const f32x4*)(p + hf * 1024 + lane2 * 16); c += *(LAS const f32x2*)(p + 2048 + r2 * 8); } }
      const float rl = __builtin_amdgcn_rcpf(l);
      const int token = q0 + 32 * u + r2; char* ob = ws + WS_OF + (size_t)(token >> 4) * 18 * 1024 + (token & 15) * 16;
      *(u32x2*)(ob + (head >> 1) * 1024 + (2 * (head & 1) + hf) * 256 + h2 * 8) = (u32x2){cvtpk(a[0] * rl, a[1] * rl), cvtpk(a[2] * rl, a[3] * rl)};
      if (hf == 0 && h2 == 0) *(unsigned*)(ob + (16 + (head >> 4)) * 1024 + ((head >> 2) & 3) * 256 + (head & 3) * 4) = cvtpk(c[0] * rl, c[1] * rl);
    }
    STAMP(2, 7); RSTAMP(2, 15); STAMP_FLUSH(2);
}

constexpr int K3_X = 108 * 1024, K3_WB = K3_X + 12 * 1024, K3_Z = K3_WB + 2304;
DI void k3_phase(const char* x, char* ws, char* lds_generic, int xcd, int idx) {
    LAS char* lds = (LAS char*)lds_generic; const unsigned ldsb = (unsigned)(uintptr_t)lds_generic;
    const int tid = threadIdx.x, lane = tid & 63, wid = rfl(tid >> 6), mi = wid & 3, kj = wid >> 2, m = lane & 15, g = lane >> 4;
    const int mt32 = xcd * 4 + (idx & 3), n6 = idx >> 2, mt16 = 4 * mt32 + mi;
    STAMP_DECL; RSTAMP(3, 14); STAMP(3, 0);
    const srd_t srd = make_srd(ws, (unsigned)WS_END), sx = make_srd(x, S_ * D_ * 2);
    const unsigned l16 = lane * 16, wo0 = (unsigned)WS_WO + n6 * (108 * 1024), ob = (unsigned)WS_OF + (mt16 * 18 + 9 * kj) * 1024;
    bf16x8 of[9];
    { const unsigned f0 = (54 * kj + 6 * mi) * 1024;
      bload16<0>(of[0], srd, l16, ob); bload16<1024>(of[1], srd, l16, ob); bload16<2048>(of[2], srd, l16, ob); bload16<3072>(of[3], srd, l16, ob);
      bdma4(srd, l16, wo0 + f0, ldsb + f0); bdma1(srd, l16, wo0 + f0 + 4096, ldsb + f0 + 4096); bdma1(srd, l16, wo0 + f0 + 5120, ldsb + f0 + 5120); }
    if (tid < 37) *(LAS u32x4*)(lds + K3_Z + tid * 16) = (u32x4){0, 0, 0, 0};
    f32x4 acc[6], accw = {0, 0, 0, 0}; float mux;
    asm volatile("s_nop 1" : "+v"(accw));
    LAS const char* ab = lds + kj * (54 * 1024) + lane * 16;
    LAS const char* wbp = lds + (m < 2 ? K3_WB + m * (D_ * 2) + kj * 576 + g * 16 : K3_Z);
    bf16x8 wr[2][6];
    auto rd = [&](int s) {
#pragma unroll
        for (int t = 0; t < 6; ++t) wr[s & 1][t] = lds_frag(ab + (s * 6 + t) * 1024);
        SBAR(); };
    auto step = [&](int s) { PINV(of[s]);
#pragma unroll
        for (int t = 0; t < 6; ++t) { if (s == 0) mfma16_new(acc[t], wr[s & 1][t], of[s]); else mfma16_acc(acc[t], wr[s & 1][t], of[s]); }
        SBAR(); };
    STAMP(3, 1);
    WAIT_BAR(0);
    STAMP(3, 2);
    { const unsigned f1 = (54 * kj + 24 + (mi < 3 ? 8 * mi : 22)) * 1024;
      rd(0); rd(1);
      bload16<0>(of[4], srd, l16, ob + 4096); bload16<1024>(of[5], srd, l16, ob + 4096); bdma4(srd, l16, wo0 + f1, ldsb + f1);
      step(0); rd(2);
      bload16<2048>(of[6], srd, l16, ob + 4096); bload16<3072>(of[7], srd, l16, ob + 4096); bdma4(srd, l16, wo0 + f1 + 4096, ldsb + f1 + 4096);
      step(1); rd(3);
      bload16<0>(of[8], srd, l16, ob + 8192);
#pragma unroll
      for (int i = 0; i < 2; ++i) { int pc = wid + 8 * i; pc = pc > 11 ? 11 : pc; const int q = pc * 64 + lane, row = (q * 5462) >> 16;
          bdma1(sx, row * (D_ * 2) + (q - 12 * row) * 16, (64 * mt32) * (D_ * 2) + n6 * 192, ldsb + K3_X + pc * 1024); }
      if (wid < 2) bdma1(srd, l16, (unsigned)WS_WBAR + wid * 1024, ldsb + K3_WB + wid * 1024);
      if (wid == 2 && lane < 16) bdma1(srd, l16, (unsigned)WS_WBAR + 2048, ldsb + K3_WB + 2048);
      bload4<0>(mux, srd, m * 4, (unsigned)WS_MUX + mt16 * 64);
      step(2); step(3); }
    STAMP(3, 3);
    WAIT_BAR(0);
    STAMP(3, 4);
    bf16x8 wbf[9];
    rd(4); rd(5);
#pragma unroll
    for (int s = 0; s < 9; ++s) wbf[s] = lds_frag(wbp + s * 64);
    SBAR();
    step(4); rd(6); step(5); rd(7); step(6); rd(8); step(7); step(8);
#pragma unroll
    for (int s = 0; s < 9; ++s) mfma16_acc(accw, wbf[s], of[s]);
    PINV(mux);
    const int token = 16 * mt16 + m;
    u32x2 xr[4];
#pragma unroll
    for (int t = 0; t < 4; ++t) xr[t] = *(LAS const u32x2*)(lds + K3_X + (16 * mi + m) * 192 + (4 * kj + (kj ? (t & 1) : t)) * 32 + g * 8);
    asm volatile("s_nop 7" : "+v"(acc[5]), "+v"(acc[4]), "+v"(accw));
    STAMP(3, 5);
    LBAR();
    LAS char* ex = lds + mi * 8192 + lane * 16;
    if (kj == 0) { *(LAS f32x4*)(ex + 4 * 1024) = acc[4]; *(LAS f32x4*)(ex + 5 * 1024) = acc[5]; *(LAS f32x4*)(ex + 6 * 1024) = accw; }
    else {
#pragma unroll
        for (int t = 0; t < 4; ++t) *(LAS f32x4*)(ex + t * 1024) = acc[t];
        *(LAS f32x4*)(ex + 7 * 1024) = accw; }
    LBAR();
    accw += *(LAS const f32x4*)(ex + (kj ? 6 : 7) * 1024);
    const float mu = mux + __shfl(accw[0] + accw[1], m);
    float ssq = 0.f;
    auto fin = [&](int t, u32x2 xv, unsigned (&pk)[2]) {
        const f32x4 a = acc[t] + *(LAS const f32x4*)(ex + t * 1024);
        const unsigned h0 = cvtpk(a[0] + bflo(xv[0]), a[1] + bfhi(xv[0])), h1 = cvtpk(a[2] + bflo(xv[1]), a[3] + bfhi(xv[1]));
        pk[0] = cvtpk(bflo(h0) - mu, bfhi(h0) - mu); pk[1] = cvtpk(bflo(h1) - mu, bfhi(h1) - mu);
        ssq = dot2(pk[0], pk[0], ssq); ssq = dot2(pk[1], pk[1], ssq); };
    auto pair = [&](int t, u32x2 xa, u32x2 xb2) { unsigned p0[2], p1[2]; fin(t, xa, p0); fin(t + 1, xb2, p1);
        bstore16<1>((u32x4){p0[0], p0[1], p1[0], p1[1]}, srd, l16, (unsigned)WS_HCF + (mt16 * 18 + 3 * n6 + (t >> 1)) * 1024); };
    if (kj == 0) { pair(0, xr[0], xr[1]); pair(2, xr[2], xr[3]); } else pair(4, xr[0], xr[1]);
    ssq += xor16(ssq); ssq += xor32(ssq);
    if (g == 0) { __hip_atomic_store((float*)(ws + WS_SSQ) + (mt16 * 16 + 2 * n6 + kj) * 16 + m, ssq, __ATOMIC_RELAXED, __HIP_MEMORY_SCOPE_AGENT);
        if (n6 == 0 && kj == 0) __hip_atomic_store((float*)(ws + WS_MUH) + token, mu, __ATOMIC_RELAXED, __HIP_MEMORY_SCOPE_AGENT); }
    STAMP(3, 6); RSTAMP(3, 15); STAMP_FLUSH(3);
}

constexpr int RING_SLOT = 36 * 1024, RING0 = 16 * 1024, G_LDS = RING0 + 4 * RING_SLOT;
DI void ring_issue(const char* stream, int st, int wid, int lane, unsigned ldsb) {
    const char* sb = stream + (size_t)st * RING_SLOT + wid * 1024; const unsigned db = ldsb + (st & 3) * RING_SLOT + wid * 1024;
#pragma unroll
    for (int i = 0; i < 4; ++i) glds16(sb + i * 8192, lane * 16, db + i * 8192);
    if (wid < 4) glds16(sb + 32768, lane * 16, db + 32768);
}
DI float geglu(float g, float u) {
    const float z = g * __builtin_fmaf(g * g, -0.10294324f, -2.30220818f);
    return g * u * __builtin_amdgcn_rcpf(1.f + __builtin_amdgcn_exp2f(z));
}
struct Ev { int type, arg, n; };
enum { E_SSQ, E_A, E_B, E_WAITB, E_BAR, E_READ, E_MMA, E_GP, E_RS, E_STAMP, E_ST, E_NOP, E_TOUCH, E_A2 };
constexpr int G1_PD = G1PD;
constexpr int g1_grp(int f) { return f < 144 ? 0 : f < 216 ? 1 : f < 288 ? 2 : 3; }
constexpr int g1_ks(int f) { return f < 144 ? f / 8 : f < 288 ? ((f - 144) % 72) / 4 : (f - 288) / 2; }
constexpr int g1_tile(int f) { return f < 288 ? (f >> 1) & 1 : 0; }
constexpr int g1_set(int f) { return f < 144 ? (f >> 2) & 1 : f < 216 ? 0 : f < 288 ? 1 : 0; }
template <bool LATE> struct G1Prog {
    Ev ev[1600]; int n;
    constexpr void add(int t, int a) { ev[n].type = t; ev[n].arg = a; ev[n].n = 0; ++n; }
    static constexpr int vm(const Ev& e) { return e.type == E_SSQ ? 6 : (e.type == E_A || e.type == E_B || e.type == E_ST || e.type == E_TOUCH || e.type == E_A2) ? 1 : 0; }
    static constexpr int NP = LATE ? 4 : 5;
    constexpr void addB(int s, int u) { add(E_B, s * 2 + u); }
    static constexpr int jset(int q) { return q == 0 ? 0 : q == 1 ? 1 : q == 2 ? 0 : q == 3 ? 1 : 0; }
    constexpr G1Prog() : ev{}, n(0) {
        addB(0, 0); addB(0, 1); addB(1, 0); addB(1, 1);
        add(E_STAMP, 1);
        for (int f = 0; f < G1_PD; ++f) add(E_READ, f);
        for (int f = 0; f < 324; ++f) {
            const int st = f / 36, i = f % 36, g = g1_grp(f);
            if (g == 0) { const int s = f / 8, q = f % 8;
                if (q == 0) add(E_WAITB, s);
                if (f == 0) { addB(2, 0); addB(2, 1); }
                if (s + 3 < 18 && q == 2) addB(s + 3, 0);
                if (s + 3 < 18 && q == 6) addB(s + 3, 1);
                if (f == 100) add(E_SSQ, 0);
                if (f == 136) add(E_RS, 0); }
            if (st + 2 < 9) { const int i0 = LATE ? 17 : 1, d = 4;
                if (i >= i0 && (i - i0) % d == 0 && (i - i0) / d < NP) add(E_A, (st + 2) * 8 + (i - i0) / d); }
            if (i == 36 - G1_PD && st + 1 < 9) add(E_BAR, st + 1);
            if (f + G1_PD < 324) add(E_READ, f + G1_PD);
            add(E_MMA, f);
            if (f == 143) { add(E_NOP, 0);
                for (int k = 0; k < 32; ++k) { add(E_GP, 0 * 64 + k); if (k == 15 || k == 31) add(E_ST, 0 * 2 + (k == 31)); } }
            if (g > 0) {
                const int r = f - (g == 1 ? 144 : g == 2 ? 216 : 288), step = g < 3 ? 2 : 1, r0 = 3, k = (r - r0) / step;
                if (r >= r0 && (r - r0) % step == 0 && k < 32) { add(E_GP, g * 64 + k);
                    if (k == 15 || k == 31) add(E_ST, g * 2 + (k == 31)); } }
            if (TOUCH && f == 7 * 36 + 1) add(E_TOUCH, 0);
            if (st == 8 && i >= 2 && i % 3 == 2 && (i - 2) / 3 < 2 * NP) add(E_A2, ((i - 2) / 3 / NP) * 8 + ((i - 2) / 3) % NP);
            if (i == 35) add(E_STAMP, 2 + st);
            if (f == 107) add(E_STAMP, 12);
            if (f == 323) add(E_STAMP, 13);
        }
        add(E_NOP, 1);
        for (int k = 0; k < 16; ++k) { add(E_GP, 4 * 64 + k); if (k == 7 || k == 15) add(E_ST, 8 + (k == 15)); }
        for (int i = 0; i < n; ++i) if (ev[i].type == E_WAITB || ev[i].type == E_BAR || ev[i].type == E_RS) {
            int cnt = 0, j = i - 1;
            for (; j >= 0; --j) {
                if (ev[i].type == E_WAITB && ev[j].type == E_B && ev[j].arg == 2 * ev[i].arg + 1) break;
                if (ev[i].type == E_RS && ev[j].type == E_SSQ) break;
                if (ev[i].type == E_BAR && ev[j].type == E_A && ev[j].arg == 8 * ev[i].arg + NP - 1) break;
                cnt += vm(ev[j]); }
            ev[i].n = cnt > 63 ? 63 : cnt; }
    }
};
template <bool LATE> constexpr G1Prog<LATE> g1prog{};
DI float vmul(float a, float b) { float r; asm("v_mul_f32_e32 %0, %1, %2" : "=v"(r) : "v"(a), "v"(b)); return r; }
DI float vfma(float a, float b, float c) { float r; asm("v_fma_f32 %0, %1, %2, %3" : "=v"(r) : "v"(a), "v"(b), "v"(c)); return r; }
struct G1State {
    bf16x8 hf[2][18]; f32x4 acc[2][2][2][2];
    bf16x8 ar[G1_PD + 1]; float sq[2][3]; float a0[2], a1[2], kk[2], sink; f32x4 z, w; u32x4 o; unsigned soffT, soffA2;
    unsigned lb0, lb1, lb2; unsigned l16, ldsb, soffA, soffH, soffS, soffAct, vq; int wid, j, g;
};
template <bool LATE, int PC> DI void g1_exec(G1State& c, const srd_t srd) {
    constexpr Ev e = g1prog<LATE>.ev[PC];
    if constexpr (e.type == E_SSQ) { bload4_sc1<0>(c.sq[0][0], srd, c.vq, c.soffS); bload4_sc1<256>(c.sq[0][1], srd, c.vq, c.soffS); bload4_sc1<512>(c.sq[0][2], srd, c.vq, c.soffS);
                                     bload4_sc1<1024>(c.sq[1][0], srd, c.vq, c.soffS); bload4_sc1<1280>(c.sq[1][1], srd, c.vq, c.soffS); bload4_sc1<1536>(c.sq[1][2], srd, c.vq, c.soffS); }
    else if constexpr (e.type == E_A) { constexpr int st = e.arg >> 3, p = e.arg & 7;
        const unsigned so = c.soffA + st * RING_SLOT, dst = c.ldsb + RING0 + ((st + 3) & 3) * RING_SLOT;
        if constexpr (p < 4) bdma_next<p * 1024>(srd, c.l16, so + c.wid * 4096, dst + c.wid * 4096);
        else bdma1(srd, c.l16, so + 32768 + c.wid * 1024, dst + 32768 + c.wid * 1024); }
    else if constexpr (e.type == E_B) { constexpr int s = e.arg >> 1, u = e.arg & 1;
        bload16_sc1<((18 * u + s) & 3) * 1024>(c.hf[u][s], srd, c.l16, c.soffH + ((18 * u + s) >> 2) * 4096); }
    else if constexpr (e.type == E_WAITB) { asm volatile("s_waitcnt vmcnt(%2)" : "+v"(c.hf[0][e.arg]), "+v"(c.hf[1][e.arg]) : "n"(e.n)); }
    else if constexpr (e.type == E_BAR) { asm volatile("s_waitcnt vmcnt(%0)\n\ts_barrier" :: "n"(e.n) : "memory"); }
    else if constexpr (e.type == E_RS) { asm volatile("s_waitcnt vmcnt(%6)" : "+v"(c.sq[0][0]), "+v"(c.sq[0][1]), "+v"(c.sq[0][2]), "+v"(c.sq[1][0]), "+v"(c.sq[1][1]), "+v"(c.sq[1][2]) : "n"(e.n));
#pragma unroll
        for (int u = 0; u < 2; ++u) { float t = (c.sq[u][0] + c.sq[u][1]) + c.sq[u][2]; t += xor16(t); t += xor32(t);
            const float v = t * (1.f / D_) + EPS, rs = __builtin_amdgcn_rsqf(v);
            c.a0[u] = -2.30220818f * rs; c.a1[u] = -0.10294324f * rs * rs * rs; c.kk[u] = v; }
        SBAR(); }
    else if constexpr (e.type == E_STAMP) {
    }
    else if constexpr (e.type == E_TOUCH) { touch64(c.sink, srd, c.l16 * 8, c.soffT); }
    else if constexpr (e.type == E_A2) { constexpr int st = e.arg >> 3, p = e.arg & 7;
        const unsigned so = c.soffA2 + st * RING_SLOT, dst = c.ldsb + RING0 + (st & 3) * RING_SLOT;
        if constexpr (p < 4) bdma_next<p * 1024>(srd, c.l16, so + c.wid * 4096, dst + c.wid * 4096);
        else bdma1(srd, c.l16, so + 32768 + c.wid * 1024, dst + 32768 + c.wid * 1024); }
    else if constexpr (e.type == E_NOP) {
        asm volatile("s_nop 7" : "+v"(c.acc[0][0][0][0]), "+v"(c.acc[0][0][0][1]), "+v"(c.acc[0][0][1][0]), "+v"(c.acc[0][0][1][1]));
        if constexpr (e.arg == 0) asm volatile("" : "+v"(c.acc[0][1][0][0]), "+v"(c.acc[0][1][0][1]), "+v"(c.acc[0][1][1][0]), "+v"(c.acc[0][1][1][1])); }
    else if constexpr (e.type == E_READ) { constexpr int f = e.arg, off = ((f / 36 + 3) & 3) * RING_SLOT + (f % 36) * 1024;
        if constexpr ((off >> 16) == 0) c.ar[f % (G1_PD + 1)] = lds_frag((LAS const char*)(uintptr_t)c.lb0 + (off & 65535));
        else if constexpr ((off >> 16) == 1) c.ar[f % (G1_PD + 1)] = lds_frag((LAS const char*)(uintptr_t)c.lb1 + (off & 65535));
        else c.ar[f % (G1_PD + 1)] = lds_frag((LAS const char*)(uintptr_t)c.lb2 + (off & 65535));
        SBAR(); }
    else if constexpr (e.type == E_MMA) { constexpr int f = e.arg, s = g1_ks(f), t = g1_tile(f), q = f & 1, set = g1_set(f);
        if constexpr (s == 0) { mfma16_new(c.acc[set][t][q][0], c.ar[f % (G1_PD + 1)], c.hf[0][s]); mfma16_new(c.acc[set][t][q][1], c.ar[f % (G1_PD + 1)], c.hf[1][s]); }
        else { mfma16_acc(c.acc[set][t][q][0], c.ar[f % (G1_PD + 1)], c.hf[0][s]); mfma16_acc(c.acc[set][t][q][1], c.ar[f % (G1_PD + 1)], c.hf[1][s]); }
        SBAR(); }
    else if constexpr (e.type == E_GP) {
        constexpr int grp = e.arg >> 6, k = e.arg & 63, quad = k >> 3, ph = k & 7, set = G1Prog<LATE>::jset(grp);
        constexpr int u = grp < 4 ? quad >> 1 : quad, t = grp < 4 ? quad & 1 : 0;
        const f32x4 g = c.acc[set][t][0][u];
#pragma unroll
        for (int r = 0; r < 4; ++r) {
            if constexpr (ph == 0) c.z[r] = vmul(g[r], g[r]);
            if constexpr (ph == 1) c.z[r] = vfma(c.z[r], c.a1[u], c.a0[u]);
            if constexpr (ph == 2) c.z[r] = vmul(c.z[r], g[r]);
            if constexpr (ph == 3) c.z[r] = __builtin_amdgcn_exp2f(c.z[r]);
            if constexpr (ph == 4) { c.z[r] = vfma(c.z[r], c.kk[u], c.kk[u]); }
            if constexpr (ph == 5) { c.z[r] = __builtin_amdgcn_rcpf(c.z[r]); }
            if constexpr (ph == 6) c.w[r] = vmul(vmul(g[r], c.acc[set][t][1][u][r]), c.z[r]); }
        if constexpr (ph == 7) { c.o[2 * t] = cvtpk(c.w[0], c.w[1]); c.o[2 * t + 1] = cvtpk(c.w[2], c.w[3]); }
        SBAR(); }
    else if constexpr (e.type == E_ST) { constexpr int grp = e.arg >> 1, u = e.arg & 1;
        const unsigned so = c.soffAct + u * (144 * 1024);
        if constexpr (grp < 4) bstore16<ACT_POL>(c.o, srd, c.l16, so + (9 * (c.j >> 1) + 4 * (c.j & 1) + grp) * 1024);
        else bstore8<ACT_POL>((u32x2){c.o[0], c.o[1]}, srd, c.l16 + (c.j & 1) * 8, so + (9 * (c.j >> 1) + 8) * 1024);
        SBAR(); }
}
template <bool LATE, int... I> DI void g1_run(G1State& c, const srd_t srd, std::integer_sequence<int, I...>) { (g1_exec<LATE, I>(c, srd), ...); }

constexpr int G2_PD = 4;
template <bool LATE> struct G2Prog {
    Ev ev[700]; int n;
    constexpr void add(int t, int a) { ev[n].type = t; ev[n].arg = a; ev[n].n = 0; ++n; }
    static constexpr int vm(const Ev& e) { return (e.type == E_A || e.type == E_B) ? 1 : 0; }
    static constexpr int NP = LATE ? 4 : 5;
    constexpr G2Prog() : ev{}, n(0) {
        add(E_B, 0); add(E_B, 1); add(E_B, 2); add(E_B, 3);
        add(E_STAMP, 1);
        for (int f = 0; f < G2_PD; ++f) add(E_READ, f);
        for (int f = 0; f < 162; ++f) {
            const int st = f / 18, i = f % 18;
            if (i % 9 == 0) add(E_WAITB, st * 2 + i / 9);
            const int ia = LATE ? 3 : 1, ib = LATE ? 1 : 3;
            if (st == 0) {
                if (i >= 1 && i < 5) add(E_B, 4 + i - 1);
                if (i >= 6 && i < 6 + NP) add(E_A, 16 + i - 6);
                if (i >= 12 && i < 16) add(E_B, 8 + i - 12);
            } else {
                if (st == 1 && i >= 14) add(E_B, 16 + i - 14);
                if (st + 2 < 9 && i >= ia && (i - ia) % 4 == 0 && (i - ia) / 4 < 4) add(E_A, (st + 2) * 8 + (i - ia) / 4);
                if (st + 2 < 9 && NP == 5 && i == 12) add(E_A, (st + 2) * 8 + 4);
                { const int la = st == 1 ? 2 : 3; if (st + la < 9 && (st == 1 || st + la > 4) && i >= ib && (i - ib) % 4 == 0 && (i - ib) / 4 < 4) add(E_B, (st + la) * 4 + (i - ib) / 4); }
            }
            if (i == 18 - G2_PD && st + 1 < 9) add(E_BAR, st + 1);
            if (f + G2_PD < 162) add(E_READ, f + G2_PD);
            add(E_MMA, f);
            if (i == 17) add(E_STAMP, 2 + st);
        }
        for (int i = 0; i < n; ++i) if (ev[i].type == E_WAITB || ev[i].type == E_BAR) {
            int cnt = 0, j = i - 1;
            for (; j >= 0; --j) {
                if (ev[i].type == E_WAITB && ev[j].type == E_B && ev[j].arg == 2 * ev[i].arg + 1) break;
                if (ev[i].type == E_BAR && ev[j].type == E_A && (ev[j].arg >> 3) == ev[i].arg) break;
                cnt += vm(ev[j]); }
            ev[i].n = cnt > 63 ? 63 : cnt; }
    }
};
template <bool LATE> constexpr G2Prog<LATE> g2prog{};
struct G2State {
    bf16x8 af[4][2][2]; f32x4 acc[9][2]; bf16x8 ar[G2_PD + 1];
    unsigned lb0, lb1, lb2; unsigned l16, ldsb, soffA, soffB; int wid;
};
template <bool LATE, int PC> DI void g2_exec(G2State& c, const srd_t srd) {
    constexpr Ev e = g2prog<LATE>.ev[PC];
    if constexpr (e.type == E_A) { constexpr int st = e.arg >> 3, p = e.arg & 7;
        const unsigned so = c.soffA + st * RING_SLOT, dst = c.ldsb + RING0 + (st & 3) * RING_SLOT;
        if constexpr (p < 4) bdma_next<p * 1024>(srd, c.l16, so + c.wid * 4096, dst + c.wid * 4096);
        else bdma1(srd, c.l16, so + 32768 + c.wid * 1024, dst + 32768 + c.wid * 1024); }
    else if constexpr (e.type == E_B) { constexpr int st = e.arg >> 2, s2 = (e.arg >> 1) & 1, u = e.arg & 1;
        bload16_sc1<((2 * st + s2) & 3) * 1024>(c.af[st & 3][s2][u], srd, c.l16, c.soffB + u * (144 * 1024) + ((2 * st + s2) >> 2) * 4096); }
    else if constexpr (e.type == E_WAITB) { asm volatile("s_waitcnt vmcnt(%2)" : "+v"(c.af[(e.arg >> 1) & 3][e.arg & 1][0]), "+v"(c.af[(e.arg >> 1) & 3][e.arg & 1][1]) : "n"(e.n)); }
    else if constexpr (e.type == E_BAR) { asm volatile("s_waitcnt vmcnt(%0)\n\ts_barrier" :: "n"(e.n) : "memory"); }
    else if constexpr (e.type == E_STAMP) {
    }
    else if constexpr (e.type == E_READ) { constexpr int f = e.arg, st = f / 18, off = (st & 3) * RING_SLOT + (f % 18) * 1024;
        if constexpr ((off >> 16) == 0) c.ar[f % (G2_PD + 1)] = lds_frag((LAS const char*)(uintptr_t)c.lb0 + (off & 65535));
        else c.ar[f % (G2_PD + 1)] = lds_frag((LAS const char*)(uintptr_t)c.lb1 + (off & 65535));
        SBAR(); }
    else if constexpr (e.type == E_MMA) { constexpr int f = e.arg, st = f / 18, s2 = (f % 18) / 9, t = f % 9;
        if constexpr (f < 9) { mfma16_new(c.acc[t][0], c.ar[f % (G2_PD + 1)], c.af[st & 3][s2][0]); mfma16_new(c.acc[t][1], c.ar[f % (G2_PD + 1)], c.af[st & 3][s2][1]); }
        else { mfma16_acc(c.acc[t][0], c.ar[f % (G2_PD + 1)], c.af[st & 3][s2][0]); mfma16_acc(c.acc[t][1], c.ar[f % (G2_PD + 1)], c.af[st & 3][s2][1]); }
        SBAR(); }
}
template <bool LATE, int... I> DI void g2_run(G2State& c, const srd_t srd, std::integer_sequence<int, I...>) { (g2_exec<LATE, I>(c, srd), ...); }
DI void arrive(char* ws, int counter) { if (threadIdx.x == 0) __hip_atomic_fetch_add((unsigned*)(ws + WS_CNT) + counter * 64, 1u, __ATOMIC_RELAXED, __HIP_MEMORY_SCOPE_AGENT); }
DI bool await(char* ws, int counter, unsigned want, LAS unsigned* flag) {
    if (threadIdx.x == 0) { const unsigned* cnt = (const unsigned*)(ws + WS_CNT) + counter * 64; unsigned bad = 0;
        const unsigned long long t0 = __builtin_amdgcn_s_memrealtime();
        while (__hip_atomic_load(cnt, __ATOMIC_RELAXED, __HIP_MEMORY_SCOPE_AGENT) < want) {
            if (__builtin_amdgcn_s_memrealtime() - t0 > 2000000ull) { bad = 1; break; }
            __builtin_amdgcn_s_sleep(1); }
        *flag = bad; }
    LBAR();
    return *flag != 0;
}
constexpr int FFN_LDS = G_LDS;
__global__ __launch_bounds__(512, 2) void ffn_kernel(const char* x, char* ws, char* out) {
    extern __shared__ __attribute__((aligned(16))) char lds_generic[];
    LAS char* lds = (LAS char*)lds_generic;
    const srd_t srd = make_srd(ws, (unsigned)WS_END);
    const int b = blockIdx.x, xcd = b & 7, idx = b >> 3;
    bool bad;
    TL_DECL; TLS(0);
    auto ring_w1 = [&](int st) {
        const int lane = threadIdx.x & 63, wid = rfl(threadIdx.x >> 6);
        const unsigned so = (unsigned)WS_W1 + ((xcd >> 1) * 8 + (idx & 7)) * (324 * 1024) + st * RING_SLOT, dst = (unsigned)(uintptr_t)lds_generic + RING0 + ((st + 3) & 3) * RING_SLOT;
        bdma4(srd, lane * 16, so + wid * 4096, dst + wid * 4096);
        if (wid < 4) bdma1(srd, lane * 16, so + 32768 + wid * 1024, dst + 32768 + wid * 1024); };
    ring_w1(0);
    if (idx < 24) {
        k3_phase(x, ws, lds_generic, xcd, idx);
        TLS(1);
        asm volatile("s_waitcnt vmcnt(0) lgkmcnt(0)\n\ts_barrier" ::: "memory");
        arrive(ws, 96 + xcd);
        TLS(2);
    }
    ring_w1(1);
    asm volatile("s_waitcnt vmcnt(0)" ::: "memory");
    LAS unsigned* flag = (LAS unsigned*)(lds + 15 * 1024);
    bad = await(ws, 96 + (xcd & 1) * 4 + (idx >> 3), 24u, flag);
    TLS(3);
    {
        int tid = threadIdx.x; asm volatile("" : "+v"(tid));
        const int lane = tid & 63, wid = rfl(tid >> 6);
        const int j = (xcd >> 1) * 8 + (idx & 7), mt8 = (xcd & 1) * 4 + (idx >> 3), mt16 = 16 * mt8 + 2 * wid;
        G1State c;
        RSTAMP(4, 14); STAMP(4, 0);
        c.l16 = lane * 16; c.ldsb = (unsigned)(uintptr_t)lds_generic; c.wid = wid; c.j = j; c.g = lane >> 4;
        c.lb0 = c.ldsb + RING0 + lane * 16; c.lb1 = c.lb0 + 65536; c.lb2 = c.lb0 + 131072; PINV(c.lb0); PINV(c.lb1); PINV(c.lb2);
        c.soffA = (unsigned)WS_W1 + j * (324 * 1024); c.soffH = (unsigned)WS_HCF + mt16 * (18 * 1024); c.soffS = (unsigned)WS_SSQ + mt16 * 1024;
        c.soffAct = (unsigned)WS_ACT + mt16 * (144 * 1024); c.vq = (lane >> 4) * 64 + (lane & 15) * 4;
        c.soffA2 = (unsigned)WS_W2 + ((xcd >> 1) * 4 + (idx >> 3)) * (324 * 1024);
        c.sink = 0.f; c.soffT = (unsigned)WS_W2 + (xcd >> 1) * (4 * 324 * 1024) + (idx * 8 + wid) * (41 * 128);
        if (wid >= 4) g1_run<true>(c, srd, std::make_integer_sequence<int, g1prog<true>.n>{});
        else g1_run<false>(c, srd, std::make_integer_sequence<int, g1prog<false>.n>{});
        STAMP(4, 11); RSTAMP(4, 15); TLS(4);
        asm volatile("s_waitcnt vmcnt(0) lgkmcnt(0)\n\ts_barrier" : "+v"(c.sink) :: "memory");
        arrive(ws, 64 + (j >> 3) * 8 + mt8);
        TLS(5);
        STAMP_FLUSH(4);
    }
    int tz = threadIdx.x; asm volatile("" : "+v"(tz));
    const int lane = tz & 63, wid = rfl(tz >> 6), mi = wid & 3, kj = wid >> 2;
    const int ksl = xcd >> 1, mt = (xcd & 1) * 8 + (idx & 7), n4 = idx >> 3;
    const int mt16 = 8 * mt + 2 * mi;
    G2State c;
    RSTAMP(5, 14); STAMP(5, 0);
    c.l16 = lane * 16; c.ldsb = (unsigned)(uintptr_t)lds_generic; c.wid = wid;
    c.lb0 = c.ldsb + RING0 + kj * (18 * 1024) + lane * 16; c.lb1 = c.lb0 + 65536; PINV(c.lb0); PINV(c.lb1);
    c.soffA = (unsigned)WS_W2 + (ksl * 4 + n4) * (324 * 1024); c.soffB = (unsigned)WS_ACT + (mt16 * 144 + 36 * ksl + 18 * kj) * 1024;
    bad |= await(ws, 64 + ksl * 8 + (mt >> 1), 8u, flag);
    TLS(6);
    if (wid >= 4) g2_run<true>(c, srd, std::make_integer_sequence<int, g2prog<true>.n>{});
    else g2_run<false>(c, srd, std::make_integer_sequence<int, g2prog<false>.n>{});
    asm volatile("s_nop 7" : "+v"(c.acc[8][0]), "+v"(c.acc[8][1]), "+v"(c.acc[7][0]), "+v"(c.acc[7][1]));
    TLS(7);
    LBAR();
    LAS char* ex = lds + RING0 + mi * 18432 + lane * 16;
    if (kj == 0) {
#pragma unroll
        for (int t = 5; t < 9; ++t) { *(LAS f32x4*)(ex + (2 * t) * 1024) = c.acc[t][0]; *(LAS f32x4*)(ex + (2 * t + 1) * 1024) = c.acc[t][1]; }
    } else {
#pragma unroll
        for (int t = 0; t < 5; ++t) { *(LAS f32x4*)(ex + (2 * t) * 1024) = c.acc[t][0]; *(LAS f32x4*)(ex + (2 * t + 1) * 1024) = c.acc[t][1]; }
    }
    LBAR();
    STAMP(5, 11);
    const int tile = mt * 4 + n4; const unsigned xb = (unsigned)WS_PART + tile * (16 * 9 * 1024);
    auto fin = [&](int t) {
        const f32x4 a0 = c.acc[t][0] + *(LAS const f32x4*)(ex + (2 * t) * 1024), a1 = c.acc[t][1] + *(LAS const f32x4*)(ex + (2 * t + 1) * 1024);
        if (mi == ksl) { *(LAS f32x4*)(ex + (2 * t) * 1024) = a0; *(LAS f32x4*)(ex + (2 * t + 1) * 1024) = a1; }
        else bstore16<1>((u32x4){cvtpk(a0[0], a0[1]), cvtpk(a0[2], a0[3]), cvtpk(a1[0], a1[1]), cvtpk(a1[2], a1[3])}, srd, lane * 16, xb + ((mi * 4 + ksl) * 9 + t) * 1024); };
    if (kj == 0) { fin(0); fin(1); fin(2); fin(3); fin(4); } else { fin(5); fin(6); fin(7); fin(8); }
    u32x2 hv[3]; u32x4 pv[3]; u32x2 pw[3]; float mu[2];
    { const int T0 = 9 * n4 + wid, T8 = 9 * n4 + 8; const unsigned hb = (unsigned)WS_HCF + (8 * mt + 2 * ksl) * (18 * 1024);
      bload8_sc1<0>(hv[0], srd, lane * 16 + (T0 & 1) * 8, hb + (T0 >> 1) * 1024); bload8_sc1<0>(hv[1], srd, lane * 16 + (T0 & 1) * 8, hb + (18 + (T0 >> 1)) * 1024);
      bload8_sc1<0>(hv[2], srd, lane * 16 + (T8 & 1) * 8, hb + ((wid & 1) * 18 + (T8 >> 1)) * 1024);
      const unsigned mb = (unsigned)WS_MUH + (128 * mt + 32 * ksl) * 4; bload4_sc1<0>(mu[0], srd, (lane & 15) * 4, mb); bload4_sc1<64>(mu[1], srd, (lane & 15) * 4, mb); }
    STAMP(5, 12);
    asm volatile("s_waitcnt vmcnt(0) lgkmcnt(0)\n\ts_barrier" : "+v"(hv[0]), "+v"(hv[1]), "+v"(hv[2]), "+v"(mu[0]), "+v"(mu[1]) :: "memory");
    STAMP(5, 13);
    TLS(8);
    arrive(ws, tile);
    bad |= await(ws, tile, 4u, flag);
    TLS(9);
#pragma unroll
    for (int k = 0; k < 3; ++k) { const unsigned sb = xb + (ksl * 4 + (k + (k >= ksl))) * (9 * 1024);
        bload16u_sc1<0>(pv[k], srd, lane * 16, sb + wid * 1024); bload8_sc1<0>(pw[k], srd, lane * 16 + (wid & 1) * 8, sb + 8 * 1024); }
    asm volatile("s_waitcnt vmcnt(0)" : "+v"(pv[0]), "+v"(pv[1]), "+v"(pv[2]), "+v"(pw[0]), "+v"(pw[1]), "+v"(pw[2]) :: "memory");
    const int m = lane & 15, g = lane >> 4;
    auto put = [&](int t, int u, u32x2 h2, u32x2 p0, u32x2 p1, u32x2 p2) {
        f32x4 a = *(LAS const f32x4*)(lds + RING0 + ksl * 18432 + (2 * t + u) * 1024 + lane * 16);
        a += (f32x4){bflo(h2[0]), bfhi(h2[0]), bflo(h2[1]), bfhi(h2[1])} + (u ? mu[1] : mu[0]); a += (f32x4){bflo(p0[0]), bfhi(p0[0]), bflo(p0[1]), bfhi(p0[1])};
        a += (f32x4){bflo(p1[0]), bfhi(p1[0]), bflo(p1[1]), bfhi(p1[1])}; a += (f32x4){bflo(p2[0]), bfhi(p2[0]), bflo(p2[1]), bfhi(p2[1])};
        u32x2 o = {cvtpk(a[0], a[1]), cvtpk(a[2], a[3])};
        if (bad) o = (u32x2){0x7fc07fc0u, 0x7fc07fc0u};
        *(u32x2*)(out + (size_t)(128 * mt + 32 * ksl + 16 * u + m) * (D_ * 2) + (144 * n4 + 16 * t + 4 * g) * 2) = o; };
    put(wid, 0, hv[0], (u32x2){pv[0][0], pv[0][1]}, (u32x2){pv[1][0], pv[1][1]}, (u32x2){pv[2][0], pv[2][1]});
    put(wid, 1, hv[1], (u32x2){pv[0][2], pv[0][3]}, (u32x2){pv[1][2], pv[1][3]}, (u32x2){pv[2][2], pv[2][3]});
    if (wid < 2) put(8, wid, hv[2], pw[0], pw[1], pw[2]);
    RSTAMP(5, 15); STAMP_FLUSH(5);
    TLS(10); TL_FLUSH(6);
}

}

extern "C" void kernel_launch(void* const* d_in, const int* in_sizes, int n_in, void* d_out, int out_size, void* d_ws, size_t ws_size, hipStream_t stream) {
    const char* x = (const char*)d_in[0]; const char* wqkv = (const char*)d_in[1]; const char* wo = (const char*)d_in[2];
    const char* wg = (const char*)d_in[4]; const char* wu = (const char*)d_in[5]; const char* wd = (const char*)d_in[6];
    char* ws = (char*)d_ws;
    (void)hipFuncSetAttribute((const void*)k1_qkv, hipFuncAttributeMaxDynamicSharedMemorySize, K1_LDS);
    (void)hipFuncSetAttribute((const void*)k2_attn, hipFuncAttributeMaxDynamicSharedMemorySize, K2_LDS);
    (void)hipFuncSetAttribute((const void*)ffn_kernel, hipFuncAttributeMaxDynamicSharedMemorySize, FFN_LDS);
    k1_qkv<<<256, 512, K1_LDS, stream>>>(x, wqkv, wo, ws);
    k2_attn<<<256, 1024, K2_LDS, stream>>>(x, wg, wu, wd, ws);
    ffn_kernel<<<256, 512, FFN_LDS, stream>>>(x, ws, (char*)d_out);
}
```

```cpp
#include <hip/hip_runtime.h>
#include <hip/hip_bf16.h>
#include <stdint.h>
#include <utility>
#define ST_SKEW 0
#define K1_PF 0
#define K1_TOUCH 0
#define TOUCH 0
#define RT_IT0 2
#define K2_RT 1
#define K2_PRIO 1
#define K1_ROT 0
#define G1PD 4
#define ACT_POL 1
#define RT_POL 1
#define PART_POL 1
#define H_POL 1
#define QKV_POL 1
#define G1_PRIO 1

namespace {

typedef __bf16 bf16x2 __attribute__((ext_vector_type(2)));
typedef __bf16 bf16x8 __attribute__((ext_vector_type(8)));
typedef __bf16 bf16x16 __attribute__((ext_vector_type(16)));
typedef short s16x4 __attribute__((ext_vector_type(4)));
typedef float f32x2 __attribute__((ext_vector_type(2)));
typedef float f32x4 __attribute__((ext_vector_type(4)));
typedef float f32x8 __attribute__((ext_vector_type(8)));
typedef float f32x16 __attribute__((ext_vector_type(16)));
typedef unsigned u32x2 __attribute__((ext_vector_type(2)));
typedef unsigned u32x4 __attribute__((ext_vector_type(4)));
#define LAS __attribute__((address_space(3)))
#define DI __device__ __forceinline__

constexpr int S_ = 2048, D_ = 576, H_ = 32, F_ = 4608;
constexpr float EPS = 1e-5f;
constexpr float QSCALE = 0.23570226039551584f * 1.4426950408889634f;
constexpr float SAFE_B = 48.f;

constexpr int KVCHUNK = 19456, KVIMG = 8 * KVCHUNK;
constexpr bool OVERLAY = true;
constexpr size_t WS_QM   = 0;
constexpr size_t WS_QT   = WS_QM + (size_t)32 * 2048 * 32;
constexpr size_t WS_KV   = WS_QT + (size_t)32 * 2048 * 4;
constexpr size_t WS_A_END = WS_KV + (size_t)32 * KVIMG;
constexpr size_t WS_ACT  = OVERLAY ? 0 : WS_A_END;
constexpr size_t WS_OF   = WS_ACT + (size_t)128 * 144 * 1024;
constexpr size_t WS_WO   = WS_OF + (size_t)128 * 18 * 1024;
constexpr size_t WS_KMAX = WS_WO + (size_t)6 * 108 * 1024;
constexpr size_t WS_MUX  = WS_KMAX + 32 * 16 * 4;
constexpr size_t WS_WBAR = WS_MUX + 2048 * 4;
constexpr size_t WS_SSQ  = WS_WBAR + 4096;
constexpr size_t WS_CNT  = WS_SSQ + (size_t)2048 * 64;
constexpr size_t WS_MUH  = WS_CNT + 104 * 256;
constexpr size_t WS_HCF  = WS_MUH + 2048 * 4;
constexpr size_t WS_W2   = WS_HCF + (size_t)128 * 18 * 1024;
constexpr size_t WS_W1   = WS_W2 + (size_t)16 * 324 * 1024;
constexpr size_t WS_PART = WS_W1 + (size_t)32 * 324 * 1024;
static_assert(WS_A_END <= (size_t)128 * 144 * 1024, "overlay fits");
constexpr size_t WS_END  = WS_PART + (size_t)64 * 16 * 9 * 1024;

#define TLS(i)
#define TL_DECL
#define TL_FLUSH(kid)
#define STAMP_DECL
#define STAMP(kid, i)
#define RSTAMP(kid, i)
#define STAMP_FLUSH(kid)
DI unsigned cvtpk(float lo, float hi) { f32x2 v = {lo, hi}; bf16x2 b = __builtin_convertvector(v, bf16x2); return __builtin_bit_cast(unsigned, b); }
DI float bflo(unsigned w) { return __uint_as_float(w << 16); }
DI float bfhi(unsigned w) { return __uint_as_float(w & 0xffff0000u); }
DI float dot2(unsigned a, unsigned b, float c) { return __builtin_amdgcn_fdot2_f32_bf16(__builtin_bit_cast(bf16x2, a), __builtin_bit_cast(bf16x2, b), c, false); }
DI int rfl(int x) { return __builtin_amdgcn_readfirstlane(x); }
DI float xor32(float v) { auto r = __builtin_amdgcn_permlane32_swap(__float_as_uint(v), __float_as_uint(v), false, false); return __uint_as_float((threadIdx.x & 32) ? r[0] : r[1]); }
DI float xor16(float v) { auto r = __builtin_amdgcn_permlane16_swap(__float_as_uint(v), __float_as_uint(v), false, false); return __uint_as_float((threadIdx.x & 16) ? r[0] : r[1]); }
template <int N> DI float row_ror(float v) { return __uint_as_float(__builtin_amdgcn_update_dpp(0u, __float_as_uint(v), 0x120 + N, 0xf, 0xf, false)); }
DI f32x4 mfma16(bf16x8 a, bf16x8 b, f32x4 c) { return __builtin_amdgcn_mfma_f32_16x16x32_bf16(a, b, c, 0, 0, 0); }
DI void mfma16_acc(f32x4& c, bf16x8 a, bf16x8 b) { asm volatile("v_mfma_f32_16x16x32_bf16 %0, %1, %2, %0" : "+v"(c) : "v"(a), "v"(b)); }
DI void mfma16_new(f32x4& c, bf16x8 a, bf16x8 b) { asm volatile("v_mfma_f32_16x16x32_bf16 %0, %1, %2, 0" : "=&v"(c) : "v"(a), "v"(b)); }
DI void glds16(const void* sbase, unsigned voff, unsigned lds_dst) {
    asm volatile("s_mov_b32 m0, %2\n\ts_nop 0\n\tglobal_load_lds_dwordx4 %0, %1" :: "v"(voff), "s"(sbase), "s"(lds_dst) : "memory");
}
DI void gload16(bf16x8& dst, const void* sbase, unsigned voff) { asm volatile("global_load_dwordx4 %0, %1, %2" : "=v"(dst) : "v"(voff), "s"(sbase)); }
DI void gload16f(f32x4& dst, const void* sbase, unsigned voff) { asm volatile("global_load_dwordx4 %0, %1, %2" : "=v"(dst) : "v"(voff), "s"(sbase)); }
DI void gload8(u32x2& dst, const void* sbase, unsigned voff) { asm volatile("global_load_dwordx2 %0, %1, %2" : "=v"(dst) : "v"(voff), "s"(sbase)); }
DI void gload4(float& dst, const void* sbase, unsigned voff) { asm volatile("global_load_dword %0, %1, %2" : "=v"(dst) : "v"(voff), "s"(sbase)); }
typedef __amdgpu_buffer_rsrc_t srd_t;
DI srd_t make_srd(const void* p, unsigned bytes) { return __builtin_amdgcn_make_buffer_rsrc((void*)p, (short)0, (int)bytes, 0x00020000); }
template <int IMM> DI void bload16(bf16x8& dst, srd_t r, unsigned voff, unsigned soff) {
    asm volatile("buffer_load_dwordx4 %0, %1, %2, %3 offen offset:%4" : "=v"(dst) : "v"(voff), "s"(r), "s"(soff), "n"(IMM)); }
template <int IMM> DI void bload16f(f32x4& dst, srd_t r, unsigned voff, unsigned soff) {
    asm volatile("buffer_load_dwordx4 %0, %1, %2, %3 offen offset:%4" : "=v"(dst) : "v"(voff), "s"(r), "s"(soff), "n"(IMM)); }
template <int IMM> DI void bload4(float& dst, srd_t r, unsigned voff, unsigned soff) {
    asm volatile("buffer_load_dword %0, %1, %2, %3 offen offset:%4" : "=v"(dst) : "v"(voff), "s"(r), "s"(soff), "n"(IMM)); }
template <int IMM> DI void bload16_sc1(bf16x8& dst, srd_t r, unsigned voff, unsigned soff) {
    asm volatile("buffer_load_dwordx4 %0, %1, %2, %3 offen offset:%4 sc1" : "=v"(dst) : "v"(voff), "s"(r), "s"(soff), "n"(IMM)); }
template <int IMM> DI void bload4_sc1(float& dst, srd_t r, unsigned voff, unsigned soff) {
    asm volatile("buffer_load_dword %0, %1, %2, %3 offen offset:%4 sc1" : "=v"(dst) : "v"(voff), "s"(r), "s"(soff), "n"(IMM)); }
template <int IMM> DI void bload16f_sc1(f32x4& dst, srd_t r, unsigned voff, unsigned soff) {
    asm volatile("buffer_load_dwordx4 %0, %1, %2, %3 offen offset:%4 sc1" : "=v"(dst) : "v"(voff), "s"(r), "s"(soff), "n"(IMM)); }
template <int IMM> DI void bload16u_sc1(u32x4& dst, srd_t r, unsigned voff, unsigned soff) {
    asm volatile("buffer_load_dwordx4 %0, %1, %2, %3 offen offset:%4 sc1" : "=v"(dst) : "v"(voff), "s"(r), "s"(soff), "n"(IMM)); }
template <int IMM> DI void bload8_sc1(u32x2& dst, srd_t r, unsigned voff, unsigned soff) {
    asm volatile("buffer_load_dwordx2 %0, %1, %2, %3 offen offset:%4 sc1" : "=v"(dst) : "v"(voff), "s"(r), "s"(soff), "n"(IMM)); }
template <int IMM> DI void bload8(u32x2& dst, srd_t r, unsigned voff, unsigned soff) {
    asm volatile("buffer_load_dwordx2 %0, %1, %2, %3 offen offset:%4" : "=v"(dst) : "v"(voff), "s"(r), "s"(soff), "n"(IMM)); }
DI void touch1(float& sink, srd_t r, unsigned voff) { asm volatile("buffer_load_dword %0, %1, %2, 0 offen" : "+v"(sink) : "v"(voff), "s"(r)); }
DI void touch64(float& sink, srd_t r, unsigned lane128, unsigned soff) { asm volatile("buffer_load_dword %0, %1, %2, %3 offen" : "+v"(sink) : "v"(lane128), "s"(r), "s"(soff)); }
DI void bdma4(srd_t r, unsigned voff, unsigned soff, unsigned lds_dst) {
    asm volatile("s_mov_b32 m0, %3\n\ts_nop 0\n\tbuffer_load_dwordx4 %0, %1, %2 offen lds\n\tbuffer_load_dwordx4 %0, %1, %2 offen offset:1024 lds\n\t"
                 "buffer_load_dwordx4 %0, %1, %2 offen offset:2048 lds\n\tbuffer_load_dwordx4 %0, %1, %2 offen offset:3072 lds" :: "v"(voff), "s"(r), "s"(soff), "s"(lds_dst) : "memory"); }
DI void bdma1(srd_t r, unsigned voff, unsigned soff, unsigned lds_dst) {
    asm volatile("s_mov_b32 m0, %3\n\ts_nop 0\n\tbuffer_load_dwordx4 %0, %1, %2 offen lds" :: "v"(voff), "s"(r), "s"(soff), "s"(lds_dst) : "memory"); }
template <int IMM> DI void bdma_next(srd_t r, unsigned voff, unsigned soff, unsigned lds_dst) {
    asm volatile("s_mov_b32 m0, %3\n\ts_nop 0\n\tbuffer_load_dwordx4 %0, %1, %2 offen offset:%4 lds" :: "v"(voff), "s"(r), "s"(soff), "s"(lds_dst), "n"(IMM) : "memory"); }
template <int POL> DI void bstore16(u32x4 d, srd_t r, unsigned voff, unsigned soff) {
    if constexpr (POL == 1) asm volatile("buffer_store_dwordx4 %0, %1, %2, %3 offen sc1\n\ts_nop 1" :: "v"(d), "v"(voff), "s"(r), "s"(soff) : "memory");
    else if constexpr (POL == 2) asm volatile("buffer_store_dwordx4 %0, %1, %2, %3 offen nt\n\ts_nop 1" :: "v"(d), "v"(voff), "s"(r), "s"(soff) : "memory");
    else asm volatile("buffer_store_dwordx4 %0, %1, %2, %3 offen\n\ts_nop 1" :: "v"(d), "v"(voff), "s"(r), "s"(soff) : "memory"); }
template <int POL> DI void bstore8(u32x2 d, srd_t r, unsigned voff, unsigned soff) {
    if constexpr (POL == 1) asm volatile("buffer_store_dwordx2 %0, %1, %2, %3 offen sc1\n\ts_nop 1" :: "v"(d), "v"(voff), "s"(r), "s"(soff) : "memory");
    else if constexpr (POL == 2) asm volatile("buffer_store_dwordx2 %0, %1, %2, %3 offen nt\n\ts_nop 1" :: "v"(d), "v"(voff), "s"(r), "s"(soff) : "memory");
    else asm volatile("buffer_store_dwordx2 %0, %1, %2, %3 offen\n\ts_nop 1" :: "v"(d), "v"(voff), "s"(r), "s"(soff) : "memory"); }
#define PINV(x) asm volatile("" : "+v"(x))
#define WAITV(n) asm volatile("s_waitcnt vmcnt(%0)" :: "n"(n) : "memory")
#define WAIT_BAR(n) asm volatile("s_waitcnt vmcnt(%0) lgkmcnt(0)\n\ts_barrier" :: "n"(n) : "memory")
#define SBAR() __builtin_amdgcn_sched_barrier(0)
#define LBAR() asm volatile("s_waitcnt lgkmcnt(0)\n\ts_barrier" ::: "memory")
DI bf16x8 lds_frag(LAS const char* p) { return *(LAS const bf16x8*)p; }
typedef short v4i16_t __attribute__((ext_vector_type(4)));
DI u32x2 lds_tr(LAS const char* p) { return __builtin_bit_cast(u32x2, __builtin_amdgcn_ds_read_tr16_b64_v4i16((LAS v4i16_t*)p)); }
DI bf16x8 tr_frag(LAS const char* p, int off2) { u32x2 a = lds_tr(p), b = lds_tr(p + off2); u32x4 r = {a[0], a[1], b[0], b[1]}; return __builtin_bit_cast(bf16x8, r); }

DI int kstep_to_ftile(int ks, int half) {
    const int a = ks / 9, idx = ks - 9 * a;
    if (idx == 8) return 9 * (2 * a + half) + 8;
    return 9 * (2 * a + (idx >> 2)) + 2 * (idx & 3) + half;
}

DI int wo_row(int k) { return k < 512 ? 18 * (k >> 4) + (k & 15) : 18 * ((k - 512) >> 1) + 16 + (k & 1); }
DI void retile_wo(int job, int lane, const char* wo, char* ws, char* scratch_generic) {
    const int s = job / 9, cb = job - 9 * s, r8 = lane >> 3, ch = lane & 7;
#pragma unroll
    for (int p = 0; p < 4; ++p) { const int rho = 8 * p + r8, k = 32 * s + 8 * ((rho >> 2) & 3) + 4 * (rho >> 4) + (rho & 3);
        glds16(wo + cb * 128, wo_row(k) * (D_ * 2) + ch * 16, (unsigned)(uintptr_t)scratch_generic + p * 1024); }
    WAITV(0);
    LAS const char* base = (LAS const char*)scratch_generic + (4 * (lane >> 4) + ((lane >> 2) & 3)) * 128 + (lane & 3) * 8;
#pragma unroll
    for (int u = 0; u < 4; ++u) { const int T = 4 * cb + u, n6 = T / 6, tt = T - 6 * n6;
        *(bf16x8*)(ws + WS_WO + (size_t)(n6 * 108 + s * 6 + tt) * 1024 + lane * 16) = tr_frag(base + 32 * u, 16 * 128); }
}

constexpr int K1_SLOT = 34 * 1024, K1_STG = 0, K1_SMALL = 4 * K1_SLOT, K1_LDS = K1_SMALL + 4096;
template <int PART, int NJ>
DI void k1_wave(const char* x, const char* wqkv, char* ws, char* lds_generic, int mt, int nt, int rot, int shx, int shw) {
    constexpr bool VPART = PART == 2; constexpr int NT = NJ ? 4 : 5, T0 = NJ ? 5 : 0;
    LAS char* lds = (LAS char*)lds_generic; const unsigned ldsb = (unsigned)(uintptr_t)lds_generic;
    const int tid = threadIdx.x, lane = tid & 63, wid = rfl(tid >> 6), mi = wid & 3, m = lane & 15, g = lane >> 4;
    STAMP_DECL; RSTAMP(1, 14); STAMP(1, 0);
    const srd_t sx = make_srd(x, S_ * D_ * 2), sw = make_srd(wqkv, D_ * 3 * D_ * 2);
    const unsigned xv = (lane >> 3) * (D_ * 2) + (((lane & 7) ^ ((4 * wid + (lane >> 4)) & 7)) << 4);
    unsigned wv[2];
#pragma unroll
    for (int i = 0; i < 2; ++i) { const int idx = (i ? 8 : wid) * 64 + lane, rho = (idx * 3641) >> 16, ch = idx - 18 * rho;
        wv[i] = (8 * ((rho >> 2) & 3) + 4 * (rho >> 4) + (rho & 3)) * 3456 + ch * 16; }
    const unsigned xs = (128 * mt + 8 * wid) * (D_ * 2), wsb = nt * 288;
    auto issue = [&](int st, int p) {
        int k = st + rot; k = k >= 9 ? k - 9 : k; const unsigned dst = ldsb + (st & 3) * K1_SLOT;
        if (p < 2) bdma1(sx, xv, xs + p * 64 * (D_ * 2) + k * 128, dst + (wid + 8 * p) * 1024);
        else if (p < 4) bdma1(sw, wv[0], wsb + (64 * k + 32 * (p - 2)) * 3456, dst + 16384 + ((p - 2) * 9 + wid) * 1024);
        else if (wid < 2) bdma1(sw, wv[1], wsb + (64 * k + 32 * wid) * 3456, dst + 16384 + (wid * 9 + 8) * 1024); };
#pragma unroll
    for (int st = 0; st < 2; ++st)
#pragma unroll
        for (int p = 0; p < 5; ++p) issue(st, p);
    float tch[2] = {0.f, 0.f};
#pragma unroll
    for (int i = 0; i < 2 * K1_TOUCH; ++i) { int t = wid + 8 * i; t = t >= 12 ? t - 4 : t;
        if (t < 3) { const int L = shx * 192 + t * 64 + lane, row = (L * 7282) >> 16; bload4<0>(tch[i], sx, (128 * mt + row) * (D_ * 2) + (L - 9 * row) * 128, 0); }
        else { const int q = (t - 3) * 64 + lane; bload4<0>(tch[i], sw, (shw * 144 + (q >> 2)) * 3456 + ((nt * 288) & ~127) + (q & 3) * 128, 0); } }
    unsigned sa[NT][2], sd[NT][2];
#pragma unroll
    for (int t = 0; t < NT; ++t) { const int tt = T0 + t;
        if (VPART) { const int n = 16 * tt + m, hl = (n * 3641) >> 16, d = n - 18 * hl, row = d + (d >= 4 ? 5 : 0) - (d >= 12 ? 12 : 0) + (d >= 16 ? 8 : 0);
            sa[t][0] = K1_STG + hl * 5120 + 2 * mi * 640 + (g >> 1) * 320 + row * 16 + (g & 1) * 8; PINV(sa[t][0]); }
        else {
#pragma unroll
            for (int p = 0; p < 2; ++p) { const int n = 16 * tt + 4 * g + 2 * p, hl = (n * 3641) >> 16, d = n - 18 * hl; const bool tail = d == 16;
                const int o = PART == 0 ? (tail ? 32768 + hl * 512 : hl * 4096 + d * 2) : hl * 4608 + (tail ? 4096 : (d >> 3) * 2048 + (d & 7) * 2);
                const int ts = tail ? 4 : PART == 0 ? 32 : 16;
                sa[t][p] = K1_STG + o + (32 * mi + m) * ts; sd[t][p] = 16 * ts; PINV(sa[t][p]); PINV(sd[t][p]); } } }
    f32x4 acc[NT][2];
    f32x4 accs = {0, 0, 0, 0}, accg = {0, 0, 0, 0}, accc[NT];
#pragma unroll
    for (int t = 0; t < NT; ++t) accc[t] = (f32x4){0, 0, 0, 0};
    bf16x8 ones = __builtin_bit_cast(bf16x8, (u32x4){0x3f803f80u, 0x3f803f80u, 0x3f803f80u, 0x3f803f80u}); PINV(ones);
    asm volatile("s_nop 1" : "+v"(accs), "+v"(accg));
#pragma unroll
    for (int t = 0; t < NT; ++t) PINV(accc[t]);
    const unsigned xa0 = (32 * mi + m) * 128 + ((g ^ ((m >> 1) & 7)) << 4), xa1 = (32 * mi + m) * 128 + (((4 + g) ^ ((m >> 1) & 7)) << 4);
    const unsigned wa = 16384 + (4 * g + ((lane >> 2) & 3)) * 288 + (lane & 3) * 8 + T0 * 32;
    bf16x8 xq[2][2][2], wr[2][2][NT];
    auto rd = [&](int st, int i) {
        LAS const char* sb = lds + (st & 3) * K1_SLOT;
        if (i < 4) xq[st & 1][i >> 1][i & 1] = lds_frag(sb + ((i >> 1) ? xa1 : xa0) + (i & 1) * 2048);
        else { const int s2 = (i - 4) / NT, t = (i - 4) % NT; wr[st & 1][s2][t] = tr_frag(sb + wa + s2 * 9216 + 32 * t, 16 * 288); } };
    STAMP(1, 1);
    WAIT_BAR(4 + K1_TOUCH * 2);
    STAMP(1, 2);
#pragma unroll
    for (int i = 0; i < 4 + 2 * NT; ++i) rd(0, i);
#pragma unroll
    for (int p = 0; p < 5; ++p) issue(2, p);
#pragma unroll
    for (int st = 0; st < 9; ++st) {
        if (st < 8) { if (st < 2) WAIT_BAR(4 + K1_TOUCH * 2); else if (st < 6) WAIT_BAR(4); else if (st == 6) WAIT_BAR(4); else WAIT_BAR(0); }
        if (st == 2) { PINV(tch[0]); PINV(tch[1]); }
#pragma unroll
        for (int i = 0; i < 2 * NT; ++i) { const int s2 = i / NT, t = i % NT; const bf16x8 w = wr[st & 1][s2][t], x0 = xq[st & 1][s2][0], x1 = xq[st & 1][s2][1];
            if (st == 0 && s2 == 0) { if (VPART) { mfma16_new(acc[t][0], x0, w); mfma16_new(acc[t][1], x1, w); } else { mfma16_new(acc[t][0], w, x0); mfma16_new(acc[t][1], w, x1); } }
            else { if (VPART) { mfma16_acc(acc[t][0], x0, w); mfma16_acc(acc[t][1], x1, w); } else { mfma16_acc(acc[t][0], w, x0); mfma16_acc(acc[t][1], w, x1); } }
            if (st + 1 < 9) { if (i < 4) { rd(st + 1, i); rd(st + 1, 4 + i); } else if (4 + i < 4 + 2 * NT) rd(st + 1, 4 + i); }
            if (st + 3 < 9 && (i & 1) == 0 && i / 2 < 4) issue(st + 3, i / 2 == 0 ? 0 : i / 2 == 1 ? 2 : i / 2 == 2 ? 1 : 3);
            if (st + 3 < 9 && i == 7) issue(st + 3, 4);
            if (t == 1) mfma16_acc(accs, ones, NJ ? x1 : x0);
            if (t == 2) mfma16_acc(accg, NJ ? x1 : x0, NJ ? x1 : x0);
            if (t == NT - 1 && ((2 * st + s2) & 3) == mi) {
#pragma unroll
                for (int tt = 0; tt < NT; ++tt) { if (VPART) mfma16_acc(accc[tt], ones, wr[st & 1][s2][tt]); else mfma16_acc(accc[tt], wr[st & 1][s2][tt], ones); } }
            SBAR(); }
    }
    STAMP(1, 3);
    LAS float* sm = (LAS float*)(lds + K1_SMALL);
    asm volatile("s_nop 7\n\ts_nop 7" : "+v"(acc[NT - 1][0]), "+v"(acc[NT - 1][1]), "+v"(accs), "+v"(accg));
#pragma unroll
    for (int t = 0; t < NT; ++t) { PINV(accc[t]); PINV(acc[t][0]); PINV(acc[t][1]); }
    if (g == 0) sm[32 * mi + 16 * NJ + m] = accs[0];
    if (g == (m >> 2)) sm[128 + 32 * mi + 16 * NJ + m] = (m & 2) ? ((m & 1) ? accg[3] : accg[2]) : ((m & 1) ? accg[1] : accg[0]);
#pragma unroll
    for (int t = 0; t < NT; ++t) { if (VPART) { if (g == 0) sm[256 + mi * 144 + 16 * (T0 + t) + m] = accc[t][0]; }
        else if (m == 0) *(LAS f32x4*)(sm + 256 + mi * 144 + 16 * (T0 + t) + 4 * g) = accc[t]; }
    LBAR();
    STAMP(1, 4);
    LAS char* stg = lds + K1_STG;
    if (VPART && tid < 256) { const int hs = tid >> 1;
        *(LAS u32x4*)(stg + hs * 320 + ((tid & 1) ? 19 : 4) * 16) = (tid & 1) ? (u32x4){0x3f803f80u, 0x3f803f80u, 0x3f803f80u, 0x3f803f80u} : (u32x4){0, 0, 0, 0}; }
    f32x4 mean[2], rstd[2];
#pragma unroll
    for (int u = 0; u < 2; ++u) { const int tl = 32 * mi + 16 * u + (VPART ? 4 * g : m);
        if (VPART) { mean[u] = *(LAS const f32x4*)(sm + tl) * (1.f / D_);
            const f32x4 var = *(LAS const f32x4*)(sm + 128 + tl) * (1.f / D_) - mean[u] * mean[u];
#pragma unroll
            for (int r = 0; r < 4; ++r) rstd[u][r] = __builtin_amdgcn_rsqf(fmaxf(var[r], 0.f) + EPS); }
        else { mean[u][0] = sm[tl] * (1.f / D_);
            rstd[u][0] = __builtin_amdgcn_rsqf(fmaxf(sm[128 + tl] * (1.f / D_) - mean[u][0] * mean[u][0], 0.f) + EPS) * (PART == 0 ? QSCALE : 1.f); } }
    float c1[NT]; f32x4 c4[NT];
#pragma unroll
    for (int t = 0; t < NT; ++t) { const int tt = T0 + t;
        if (VPART) { const int n = 16 * tt + m; c1[t] = (sm[256 + n] + sm[400 + n]) + (sm[544 + n] + sm[688 + n]); }
        else { const int n0 = 16 * tt + 4 * g;
            c4[t] = (*(LAS const f32x4*)(sm + 256 + n0) + *(LAS const f32x4*)(sm + 400 + n0)) + (*(LAS const f32x4*)(sm + 544 + n0) + *(LAS const f32x4*)(sm + 688 + n0)); } }
#pragma unroll
    for (int t = 0; t < NT; ++t)
#pragma unroll
        for (int u = 0; u < 2; ++u) {
            if (VPART) { const f32x4 a = (acc[t][u] - mean[u] * c1[t]) * rstd[u];
                *(LAS u32x2*)(lds + sa[t][0] + u * 640) = (u32x2){cvtpk(a[0], a[1]), cvtpk(a[2], a[3])}; }
            else { const f32x4 a = (acc[t][u] - c4[t] * mean[u][0]) * rstd[u][0];
                *(LAS unsigned*)(lds + sa[t][0] + (u ? sd[t][0] : 0u)) = cvtpk(a[0], a[1]); *(LAS unsigned*)(lds + sa[t][1] + (u ? sd[t][1] : 0u)) = cvtpk(a[2], a[3]); } }
    if (nt == 0 && tid < 128) ((float*)(ws + WS_MUX))[128 * mt + tid] = sm[tid] * (1.f / D_);
    LBAR();
    STAMP(1, 5);
    const int head = 8 * (nt & 3) + wid; const srd_t sws = make_srd(ws, (unsigned)WS_END); const unsigned l16 = lane * 16;
    if (PART == 2) { const unsigned dst = (unsigned)WS_KV + head * KVIMG + (mt >> 1) * KVCHUNK + 9216 + (mt & 1) * 5120;
#pragma unroll
        for (int i = 0; i < 5; ++i) bstore16<QKV_POL>(*(LAS const u32x4*)(stg + wid * 5120 + i * 1024 + l16), sws, l16, dst + i * 1024);
    } else if (PART == 0) { const unsigned dst = (unsigned)WS_QM + (head * S_ + 128 * mt) * 32;
#pragma unroll
        for (int i = 0; i < 4; ++i) bstore16<QKV_POL>(*(LAS const u32x4*)(stg + wid * 4096 + i * 1024 + l16), sws, l16, dst + i * 1024);
        if (lane < 32) bstore16<QKV_POL>(*(LAS const u32x4*)(stg + 32768 + wid * 512 + l16), sws, l16, (unsigned)WS_QT + (head * S_ + 128 * mt) * 4);
    } else { const unsigned dst = (unsigned)WS_KV + head * KVIMG + (mt >> 1) * KVCHUNK; LAS const char* hb = stg + wid * 4608;
#pragma unroll
        for (int i = 0; i < 4; ++i) bstore16<QKV_POL>(*(LAS const u32x4*)(hb + i * 1024 + l16), sws, l16, dst + (i >> 1) * 4096 + (mt & 1) * 2048 + (i & 1) * 1024);
        if (lane < 32) bstore16<QKV_POL>(*(LAS const u32x4*)(hb + 4096 + l16), sws, l16, dst + 8192 + (mt & 1) * 512);
        float mx = 0.f;
#pragma unroll
        for (int i = 0; i < 2; ++i) { const int tl = lane + 64 * i;
            const u32x4 a = *(LAS const u32x4*)(hb + tl * 16), bq = *(LAS const u32x4*)(hb + 2048 + tl * 16); const unsigned c = *(LAS const unsigned*)(hb + 4096 + tl * 4);
            float s2 = dot2(c, c, 0.f);
#pragma unroll
            for (int e = 0; e < 4; ++e) { s2 = dot2(a[e], a[e], s2); s2 = dot2(bq[e], bq[e], s2); }
            mx = fmaxf(mx, s2); }
        mx = fmaxf(mx, xor16(mx)); mx = fmaxf(mx, xor32(mx));
#pragma unroll
        for (int o = 1; o < 16; o <<= 1) mx = fmaxf(mx, __shfl_xor(mx, o));
        if (lane == 0) ((float*)(ws + WS_KMAX))[head * 16 + mt] = mx;
    }
    STAMP(1, 6); RSTAMP(1, 15); STAMP_FLUSH(1);
}
template <int PART> DI void k1_body(const char* x, const char* wqkv, char* ws, char* lds, int mt, int nt, int rot, int shx, int shw) {
    if (threadIdx.x >= 256) k1_wave<PART, 1>(x, wqkv, ws, lds, mt, nt, rot, shx, shw); else k1_wave<PART, 0>(x, wqkv, ws, lds, mt, nt, rot, shx, shw);
}
__global__ __launch_bounds__(512, 2) void k1_qkv(const char* x, const char* wqkv, const char* wo, char* ws) {
    extern __shared__ __attribute__((aligned(16))) char lds[];
    const int b = blockIdx.x;
    if (b >= 192) {
        const int lane = threadIdx.x & 63, wv = rfl(threadIdx.x >> 6), gw = (b - 192) * 8 + wv;
        float sink[4] = {0.f, 0.f, 0.f, 0.f};
        if (K1_PF) { const int xcd = b & 7, v = ((b - 192) >> 3) * 8 + wv; const srd_t sx = make_srd(x, S_ * D_ * 2), sw = make_srd(wqkv, D_ * 3 * D_ * 2);
#pragma unroll
            for (int i = 0; i < 2; ++i) { const int q = i * 64 + lane, Lw = v * 126 + (q < 126 ? q : 125), rw = (Lw * 9363) >> 17;
                touch1(sink[i], sw, rw * 3456 + ((1728 * (xcd >> 2)) & ~127) + (Lw - 14 * rw) * 128);
                const int Lx = v * 72 + (q < 72 ? q : 71), rx = (Lx * 7282) >> 16;
                touch1(sink[2 + i], sx, (512 * (xcd & 3) + rx) * (D_ * 2) + (Lx - 9 * rx) * 128); } }
        if (threadIdx.x == 511) ((unsigned*)(ws + WS_CNT))[(b - 192) * 64] = 0u;
        if (threadIdx.x == 510 && b - 192 < 40) ((unsigned*)(ws + WS_CNT))[(64 + b - 192) * 64] = 0u;
        if (gw < 162) retile_wo(gw, lane, wo, ws, lds + wv * 4096);
        for (int c = gw; c < D_; c += 512) {
            const char* row = wo + (size_t)wo_row(c) * (D_ * 2);
            const u32x4 a = *(const u32x4*)(row + lane * 16); float s = 0.f;
#pragma unroll
            for (int e = 0; e < 4; ++e) s = dot2(a[e], 0x3f803f80u, s);
            if (lane < 8) { const u32x4 bq = *(const u32x4*)(row + 1024 + lane * 16);
#pragma unroll
                for (int e = 0; e < 4; ++e) s = dot2(bq[e], 0x3f803f80u, s); }
#pragma unroll
            for (int o = 1; o < 64; o <<= 1) s += __shfl_xor(s, o);
            if (lane == 0) { const float w = s * (1.f / D_); const unsigned hi = cvtpk(w, 0.f) & 0xffffu; const unsigned lo = cvtpk(w - bflo(hi), 0.f) & 0xffffu;
                ((unsigned short*)(ws + WS_WBAR))[c] = (unsigned short)hi; ((unsigned short*)(ws + WS_WBAR))[D_ + c] = (unsigned short)lo; }
        }
        asm volatile("s_waitcnt vmcnt(0)" : "+v"(sink[0]), "+v"(sink[1]), "+v"(sink[2]), "+v"(sink[3]));
        return;
    }
    const int xcd = b & 7, idx = b >> 3, mt = (xcd & 3) * 4 + (idx & 3), nt = (xcd >> 2) * 6 + (idx >> 2);
    const int rot = K1_ROT ? (2 * (idx & 3) + ((idx >> 2) * 3 >> 1)) % 9 : 0;
    const int shx = idx >> 2, shw = idx & 3;
    if (nt >= 8) k1_body<2>(x, wqkv, ws, lds, mt, nt, rot, shx, shw); else if (nt >= 4) k1_body<1>(x, wqkv, ws, lds, mt, nt, rot, shx, shw); else k1_body<0>(x, wqkv, ws, lds, mt, nt, rot, shx, shw);
}

constexpr int K2_LDS = 160 * 1024;
struct RtJob { unsigned so[4], dst[4]; };
struct RtTable { RtJob j[8][486];
    constexpr RtTable() : j{} {
        for (int xg = 0; xg < 8; ++xg) for (int slot = 0; slot < 486; ++slot) { RtJob& e = j[xg][slot];
            if (slot < 324) { const int gu = slot >= 162, r = slot - 162 * gu, ks = r / 9, cb = 9 * xg + r % 9;
                for (int p = 0; p < 4; ++p) { const int T = 4 * cb + p, jj = T / 9, tt = T % 9;
                    e.so[p] = (ks * 32 + p * 8) * (F_ * 2) + cb * 128;
                    e.dst[p] = (unsigned)WS_W1 + (jj * 324 + (tt < 4 ? ks * 8 + tt * 2 : tt < 8 ? (tt >> 1) * 72 + ks * 4 + (tt & 1) * 2 : 288 + ks * 2) + gu) * 1024; }
            } else { const int r2 = slot - 324, q = r2 / 9, cb = r2 % 9, ks = 18 * xg + q, a = ks / 9, qi = ks % 9;
                const int t0 = qi == 8 ? 18 * a + 8 : 18 * a + 9 * (qi >> 2) + 2 * (qi & 3), t1 = qi == 8 ? t0 + 9 : t0 + 1;
                for (int p = 0; p < 4; ++p) { const int T = 4 * cb + p, n4 = T / 9, tt = T % 9;
                    e.so[p] = (16 * (p >> 1 ? t1 : t0) + 8 * (p & 1)) * (D_ * 2) + cb * 128;
                    e.dst[p] = (unsigned)WS_W2 + (((xg >> 1) * 4 + n4) * 324 + (((q >> 1) * 2 + (xg & 1)) * 2 + (q & 1)) * 9 + tt) * 1024; } } }
    } };
__device__ const RtTable rt_table{};
#define EXPS(S, a, b) _Pragma("unroll") for (int r_ = a; r_ < b; ++r_) S[r_] = __builtin_amdgcn_exp2f(S[r_]); SBAR()
#define PLO(S) __builtin_bit_cast(bf16x16, (f32x8)__builtin_shufflevector(S, S, 0, 1, 2, 3, 4, 5, 6, 7))
#define PHI(S) __builtin_bit_cast(bf16x16, (f32x8)__builtin_shufflevector(S, S, 8, 9, 10, 11, 12, 13, 14, 15))
#define KTAIL(p) __builtin_bit_cast(s16x4, (u32x2){*(LAS const unsigned*)(p), kc})
#define QK1(q) __builtin_amdgcn_mfma_f32_32x32x16_bf16(kf, q, (f32x16){}, 0, 0, 0)
#define QK2(q, s) __builtin_amdgcn_mfma_f32_32x32x8bf16_1k(kt, q, s, 0, 0, 0)
#define PV(v, p, o) __builtin_amdgcn_smfmac_f32_32x32x32_bf16(v, p, o, 0xDDDD, 0, 0)
template <class Hook>
DI void attn_fast(LAS const char* kp, LAS const char* tp, LAS const char* vp, const bf16x8 (&qm)[2], const s16x4 (&qt)[2], f32x16 (&o)[2], unsigned kc, Hook hook) {
    bf16x8 kf = *(LAS const bf16x8*)kp; s16x4 kt = KTAIL(tp); bf16x8 v0, v1;
    f32x16 s0 = QK1(qm[0]), s1; s0 = QK2(qt[0], s0);
    SBAR();
#pragma unroll
    for (int it = 0; it < 8; ++it) {
        if (K2_PRIO) { if (it == 0) __builtin_amdgcn_s_setprio(3); if (it == 2) __builtin_amdgcn_s_setprio(2); if (it == 4) __builtin_amdgcn_s_setprio(1); if (it == 6) __builtin_amdgcn_s_setprio(0); }
        EXPS(s0, 0, 2);
        if (it > 0) { o[1] = PV(v0, PLO(s1), o[1]); SBAR(); }
        EXPS(s0, 2, 6);
        if (it > 0) { o[1] = PV(v1, PHI(s1), o[1]); SBAR(); }
        v0 = *(LAS const bf16x8*)(vp + it * 1280); v1 = *(LAS const bf16x8*)(vp + it * 1280 + 640); SBAR();
        hook(it);
        EXPS(s0, 6, 9);
        s1 = QK1(qm[1]); SBAR();
        EXPS(s0, 9, 12);
        s1 = QK2(qt[1], s1); SBAR();
        if (it + 1 < 8) { kf = *(LAS const bf16x8*)(kp + (it + 1) * 512); kt = KTAIL(tp + (it + 1) * 128); SBAR(); }
        EXPS(s0, 12, 16);
        EXPS(s1, 0, 2);
        o[0] = PV(v0, PLO(s0), o[0]); SBAR();
        EXPS(s1, 2, 6);
        o[0] = PV(v1, PHI(s0), o[0]); SBAR();
        EXPS(s1, 6, 9);
        if (it + 1 < 8) { s0 = QK1(qm[0]); SBAR(); }
        EXPS(s1, 9, 12);
        if (it + 1 < 8) { s0 = QK2(qt[0], s0); SBAR(); }
        EXPS(s1, 12, 16);
    }
    o[1] = PV(v0, PLO(s1), o[1]); o[1] = PV(v1, PHI(s1), o[1]);
}
DI void attn_slow(LAS const char* kp, LAS const char* tp, LAS const char* vp, const bf16x8 (&qm)[2], const s16x4 (&qt)[2], f32x16 (&o)[2], float (&mrun)[2], unsigned kc) {
#pragma unroll 1
    for (int it = 0; it < 8; ++it) {
        const bf16x8 kf = *(LAS const bf16x8*)(kp + it * 512); const s16x4 kt = KTAIL(tp + it * 128);
        const bf16x8 v0 = *(LAS const bf16x8*)(vp + it * 1280), v1 = *(LAS const bf16x8*)(vp + it * 1280 + 640);
#pragma unroll
        for (int u = 0; u < 2; ++u) {
            f32x16 s = QK1(qm[u]); s = QK2(qt[u], s);
            float mx = s[0];
#pragma unroll
            for (int r = 1; r < 16; ++r) mx = fmaxf(mx, s[r]);
            mx = fmaxf(mx, xor32(mx));
            const float mn = fmaxf(mrun[u], mx), al = __builtin_amdgcn_exp2f(mrun[u] - mn);
            mrun[u] = mn;
#pragma unroll
            for (int r = 0; r < 16; ++r) { s[r] = __builtin_amdgcn_exp2f(s[r] - mn); o[u][r] *= al; }
            o[u] = PV(v0, PLO(s), o[u]); o[u] = PV(v1, PHI(s), o[u]);
        }
    }
}
__global__ __launch_bounds__(1024, 4) void k2_attn(const char* x, const char* wg, const char* wu, const char* wd, char* ws) {
    extern __shared__ __attribute__((aligned(16))) char lds_generic[];
    LAS char* lds = (LAS char*)lds_generic; const unsigned ldsb = (unsigned)(uintptr_t)lds_generic;
    const int tid = threadIdx.x, lane = tid & 63, wid = rfl(tid >> 6), qg = wid & 3, kq = wid >> 2, r32 = lane & 31, h = lane >> 5;
    const int b = blockIdx.x, xcd = b & 7, idx = b >> 3, head = xcd * 4 + (idx & 3), qb = idx >> 2;
    STAMP_DECL; RSTAMP(2, 14); STAMP(2, 0);
    const srd_t srd = make_srd(ws, (unsigned)WS_END);
    const int c0 = (2 * kq + (qb & 1)) * KVCHUNK, c1 = (2 * kq + ((qb & 1) ^ 1)) * KVCHUNK;
    const unsigned kvb = (unsigned)WS_KV + head * KVIMG, l16 = lane * 16;
    const int q0 = 256 * qb + 64 * qg;
    bf16x8 qm[2]; float qtl[2]; float km;
    { const unsigned so = (unsigned)WS_QM + (head * S_ + q0) * 32, vo = r32 * 32 + h * 16; bload16<0>(qm[0], srd, vo, so); bload16<1024>(qm[1], srd, vo, so);
      const unsigned st = (unsigned)WS_QT + (head * S_ + q0) * 4; bload4<0>(qtl[0], srd, r32 * 4, st); bload4<128>(qtl[1], srd, r32 * 4, st);
      bload4<0>(km, srd, (lane & 15) * 4, (unsigned)WS_KMAX + head * 64); }
    bdma4(srd, l16, kvb + c0 + qg * 5120, ldsb + c0 + qg * 5120); if (qg < 3) bdma1(srd, l16, kvb + c0 + qg * 5120 + 4096, ldsb + c0 + qg * 5120 + 4096);
    const int slot = wid * 32 + idx; const bool rt = slot < (K2_RT ? 486 : 0);
    bf16x8 wb[4]; unsigned rdst[4], rso[4];
    { const RtJob& J = rt_table.j[xcd][rt ? slot : 0];
#pragma unroll
      for (int p = 0; p < 4; ++p) { rso[p] = J.so[p]; rdst[p] = J.dst[p]; } }
    const char* rsrc = slot < 162 ? wg : slot < 324 ? wu : wd;
    const unsigned rvo = (lane >> 3) * (slot < 324 ? F_ * 2 : D_ * 2) + (lane & 7) * 16;
    asm volatile("s_waitcnt vmcnt(4)" : "+v"(qm[0]), "+v"(qm[1]), "+v"(qtl[0]), "+v"(qtl[1]), "+v"(km) :: "memory");
    km = fmaxf(km, row_ror<8>(km)); km = fmaxf(km, row_ror<4>(km)); km = fmaxf(km, row_ror<2>(km)); km = fmaxf(km, row_ror<1>(km));
    s16x4 qt[2]; bool unsafe = false;
#pragma unroll
    for (int u = 0; u < 2; ++u) { const u32x4 qv = __builtin_bit_cast(u32x4, qm[u]); const unsigned tl = __float_as_uint(qtl[u]);
        float n2 = h ? 0.f : dot2(tl, tl, 0.f);
#pragma unroll
        for (int e = 0; e < 4; ++e) n2 = dot2(qv[e], qv[e], n2);
        n2 += xor32(n2);
        const float bound = __builtin_amdgcn_sqrtf(n2 * km) * 1.0001f + 1e-6f;
        unsafe |= !(bound <= SAFE_B);
        const unsigned cb = (__float_as_uint(bound) + 0xffffu) >> 16;
        const u32x2 w = h ? (u32x2){0u, 0u} : (u32x2){tl, cb | 0x8000u};
        qt[u] = __builtin_bit_cast(s16x4, w); }
    const bool slow = __any(unsafe);
    if (slow) {
#pragma unroll
        for (int u = 0; u < 2; ++u) { u32x2 w = __builtin_bit_cast(u32x2, qt[u]); w[1] = 0u; qt[u] = __builtin_bit_cast(s16x4, w); } }
    f32x16 o[2] = {(f32x16){}, (f32x16){}}; float mrun[2] = {-INFINITY, -INFINITY};
    const int vrow = r32 >= 19 ? 4 : r32 + (r32 >= 4 ? 5 : 0) - (r32 >= 12 ? 12 : 0) + (r32 >= 16 ? 8 : 0);
    const unsigned kc = h ? 0u : 0x00003f80u;
    unsigned kp = ldsb + c0 + h * 4096 + r32 * 16, tp = ldsb + c0 + 8192 + r32 * 4, vp = ldsb + c0 + 9216 + h * 320 + vrow * 16;
#define KP(o) ((LAS const char*)(uintptr_t)kp + (o))
#define TP(o) ((LAS const char*)(uintptr_t)tp + (o))
#define VP(o) ((LAS const char*)(uintptr_t)vp + (o))
    STAMP(2, 1);
    WAIT_BAR(0);
    STAMP(2, 2);
    const srd_t sw = make_srd(rsrc, D_ * F_ * 2);
    auto chunk1 = [&](int part) { if (part == 0) bdma4(srd, l16, kvb + c1 + qg * 5120, ldsb + c1 + qg * 5120);
                                  else if (qg < 3) bdma1(srd, l16, kvb + c1 + qg * 5120 + 4096, ldsb + c1 + qg * 5120 + 4096); };
    STAMP(2, 8);
    if (slow) { chunk1(0); chunk1(1);
        if (rt) {
#pragma unroll
            for (int p = 0; p < 4; ++p) bload16<0>(wb[p], sw, rvo, rso[p]); }
        attn_slow(KP(0), TP(0), VP(0), qm, qt, o, mrun, kc); }
    else attn_fast(KP(0), TP(0), VP(0), qm, qt, o, kc, [&](int it) {
        if (it < 2) { chunk1(it); SBAR(); }
        if (it >= 2 && it < 6 && rt) { bload16<0>(wb[it - 2], sw, rvo, rso[it - 2]); SBAR(); } });
    STAMP(2, 3);
    if (rt) WAIT_BAR(4); else WAIT_BAR(0);
    STAMP(2, 4);
    if (rt) { asm volatile("s_waitcnt vmcnt(0)" : "+v"(wb[0]), "+v"(wb[1]), "+v"(wb[2]), "+v"(wb[3]) :: "memory");
        int ty = threadIdx.x; asm volatile("" : "+v"(ty)); LAS char* scr = lds + c0 + qg * 4864 + (ty & 63) * 16;
#pragma unroll
        for (int p = 0; p < 4; ++p) *(LAS bf16x8*)(scr + p * 1024) = wb[p]; }
    auto rt_step = [&](int it) {
        if (it < 1 || it > 4 || !rt) return;
        int ty = threadIdx.x; asm volatile("" : "+v"(ty)); const int ln = ty & 63;
        LAS const char* base = lds + c0 + qg * 4864 + (4 * (ln >> 4) + ((ln >> 2) & 3)) * 128 + (ln & 3) * 8;
        bstore16<RT_POL>(__builtin_bit_cast(u32x4, tr_frag(base + 32 * (it - 1), 16 * 128)), srd, ln * 16, rdst[it - 1]);
        SBAR(); };
    STAMP(2, 5);
    { const int dc = c1 - c0; kp += dc; tp += dc; vp += dc; PINV(kp); PINV(tp); PINV(vp); }
    if (slow) {
#pragma unroll
        for (int it = 1; it < 5; ++it) rt_step(it);
        attn_slow(KP(0), TP(0), VP(0), qm, qt, o, mrun, kc); }
    else attn_fast(KP(0), TP(0), VP(0), qm, qt, o, kc, rt_step);
    STAMP(2, 6);
    int tz = threadIdx.x; asm volatile("" : "+v"(tz));
    const int lane2 = tz & 63, r2 = tz & 31, h2 = (tz >> 5) & 1;
    LAS float* mx = (LAS float*)(lds + KVIMG);
#pragma unroll
    for (int u = 0; u < 2; ++u) { LAS char* pb = lds + c0 + (qg * 2 + u) * 2432;
        *(LAS f32x4*)(pb + lane2 * 16) = (f32x4){o[u][0], o[u][1], o[u][2], o[u][3]};
        *(LAS f32x4*)(pb + 1024 + lane2 * 16) = (f32x4){o[u][4], o[u][5], o[u][6], o[u][7]};
        if (h2 == 0) { *(LAS f32x2*)(pb + 2048 + r2 * 8) = (f32x2){o[u][8], o[u][9]}; *(LAS float*)(pb + 2304 + r2 * 4) = o[u][10];
                      if (slow) mx[wid * 64 + 32 * u + r2] = mrun[u]; } }
    STAMP(2, 9);
    LBAR();
    STAMP(2, 10);
    { const int u = kq & 1, hf = kq >> 1;
      LAS const char* pb = lds + (qb & 1) * KVCHUNK + (qg * 2 + u) * 2432;
      float l = 0.f; f32x4 a = {0, 0, 0, 0}; f32x2 c = {0, 0};
      if (slow) { float mk[4], M = -INFINITY;
#pragma unroll
          for (int k = 0; k < 4; ++k) { mk[k] = mx[(4 * k + qg) * 64 + 32 * u + r2]; M = fmaxf(M, mk[k]); }
#pragma unroll
          for (int k = 0; k < 4; ++k) { LAS const char* p = pb + k * 2 * KVCHUNK; const float f = __builtin_amdgcn_exp2f(mk[k] - M);
              l += f * *(LAS const float*)(p + 2304 + r2 * 4); a += *(LAS const f32x4*)(p + hf * 1024 + lane2 * 16) * f; c += *(LAS const f32x2*)(p + 2048 + r2 * 8) * f; }
      } else {
#pragma unroll
          for (int k = 0; k < 4; ++k) { LAS const char* p = pb + k * 2 * KVCHUNK;
              l += *(LAS const float*)(p + 2304 + r2 * 4); a += *(LAS const f32x4*)(p + hf * 1024 + lane2 * 16); c += *(LAS const f32x2*)(p + 2048 + r2 * 8); } }
      const float rl = __builtin_amdgcn_rcpf(l);
      const int token = q0 + 32 * u + r2; char* ob = ws + WS_OF + (size_t)(token >> 4) * 18 * 1024 + (token & 15) * 16;
      *(u32x2*)(ob + (head >> 1) * 1024 + (2 * (head & 1) + hf) * 256 + h2 * 8) = (u32x2){cvtpk(a[0] * rl, a[1] * rl), cvtpk(a[2] * rl, a[3] * rl)};
      if (hf == 0 && h2 == 0) *(unsigned*)(ob + (16 + (head >> 4)) * 1024 + ((head >> 2) & 3) * 256 + (head & 3) * 4) = cvtpk(c[0] * rl, c[1] * rl);
    }
    STAMP(2, 7); RSTAMP(2, 15); STAMP_FLUSH(2);
}

constexpr int RING_SLOT = 36 * 1024, RING0 = 16 * 1024, G_LDS = RING0 + 4 * RING_SLOT;
constexpr int K3_X = 108 * 1024, K3_WB = K3_X + 12 * 1024, K3_Z = K3_WB + 2304;
DI void ring_w1(char* ws, char* lds_generic, int xcd, int idx, int st);
DI void k3_phase(const char* x, char* ws, char* lds_generic, int xcd, int idx) {
    LAS char* lds = (LAS char*)lds_generic; const unsigned ldsb = (unsigned)(uintptr_t)lds_generic;
    const int tid = threadIdx.x, lane = tid & 63, wid = rfl(tid >> 6), mi = wid & 3, kj = wid >> 2, m = lane & 15, g = lane >> 4;
    const int mt32 = xcd * 4 + (idx & 3), n6 = idx >> 2, mt16 = 4 * mt32 + mi;
    STAMP_DECL; RSTAMP(3, 14); STAMP(3, 0);
    const srd_t srd = make_srd(ws, (unsigned)WS_END), sx = make_srd(x, S_ * D_ * 2);
    const unsigned l16 = lane * 16, wo0 = (unsigned)WS_WO + n6 * (108 * 1024), ob = (unsigned)WS_OF + (mt16 * 18 + 9 * kj) * 1024;
    bf16x8 of[9];
    { const unsigned f0 = (54 * kj + 6 * mi) * 1024;
      bload16<0>(of[0], srd, l16, ob); bload16<1024>(of[1], srd, l16, ob); bload16<2048>(of[2], srd, l16, ob); bload16<3072>(of[3], srd, l16, ob);
      bdma4(srd, l16, wo0 + f0, ldsb + f0); bdma1(srd, l16, wo0 + f0 + 4096, ldsb + f0 + 4096); bdma1(srd, l16, wo0 + f0 + 5120, ldsb + f0 + 5120); }
    if (tid < 37) *(LAS u32x4*)(lds + K3_Z + tid * 16) = (u32x4){0, 0, 0, 0};
    f32x4 acc[6], accw = {0, 0, 0, 0}; float mux;
    asm volatile("s_nop 1" : "+v"(accw));
    LAS const char* ab = lds + kj * (54 * 1024) + lane * 16;
    LAS const char* wbp = lds + (m < 2 ? K3_WB + m * (D_ * 2) + kj * 576 + g * 16 : K3_Z);
    bf16x8 wr[2][6];
    auto rd = [&](int s) {
#pragma unroll
        for (int t = 0; t < 6; ++t) wr[s & 1][t] = lds_frag(ab + (s * 6 + t) * 1024);
        SBAR(); };
    auto step = [&](int s) { PINV(of[s]);
#pragma unroll
        for (int t = 0; t < 6; ++t) { if (s == 0) mfma16_new(acc[t], wr[s & 1][t], of[s]); else mfma16_acc(acc[t], wr[s & 1][t], of[s]); }
        SBAR(); };
    STAMP(3, 1);
    WAIT_BAR(0);
    STAMP(3, 2);
    { const unsigned f1 = (54 * kj + 24 + (mi < 3 ? 8 * mi : 22)) * 1024;
      rd(0); rd(1);
      bload16<0>(of[4], srd, l16, ob + 4096); bload16<1024>(of[5], srd, l16, ob + 4096); bdma4(srd, l16, wo0 + f1, ldsb + f1);
      step(0); rd(2);
      bload16<2048>(of[6], srd, l16, ob + 4096); bload16<3072>(of[7], srd, l16, ob + 4096); bdma4(srd, l16, wo0 + f1 + 4096, ldsb + f1 + 4096);
      step(1); rd(3);
      bload16<0>(of[8], srd, l16, ob + 8192);
#pragma unroll
      for (int i = 0; i < 2; ++i) { int pc = wid + 8 * i; pc = pc > 11 ? 11 : pc; const int q = pc * 64 + lane, row = (q * 5462) >> 16;
          bdma1(sx, row * (D_ * 2) + (q - 12 * row) * 16, (64 * mt32) * (D_ * 2) + n6 * 192, ldsb + K3_X + pc * 1024); }
      if (wid < 2) bdma1(srd, l16, (unsigned)WS_WBAR + wid * 1024, ldsb + K3_WB + wid * 1024);
      if (wid == 2 && lane < 16) bdma1(srd, l16, (unsigned)WS_WBAR + 2048, ldsb + K3_WB + 2048);
      bload4<0>(mux, srd, m * 4, (unsigned)WS_MUX + mt16 * 64);
      step(2);
      ring_w1(ws, lds_generic, xcd, idx, 0);
      step(3); }
    STAMP(3, 3);
    WAIT_BAR(0);
    STAMP(3, 4);
    bf16x8 wbf[9];
    rd(4); rd(5);
#pragma unroll
    for (int s = 0; s < 9; ++s) wbf[s] = lds_frag(wbp + s * 64);
    SBAR();
    step(4); rd(6); step(5); rd(7); step(6); rd(8); step(7); step(8);
#pragma unroll
    for (int s = 0; s < 9; ++s) mfma16_acc(accw, wbf[s], of[s]);
    PINV(mux);
    const int token = 16 * mt16 + m;
    u32x2 xr[4];
#pragma unroll
    for (int t = 0; t < 4; ++t) xr[t] = *(LAS const u32x2*)(lds + K3_X + (16 * mi + m) * 192 + (4 * kj + (kj ? (t & 1) : t)) * 32 + g * 8);
    asm volatile("s_nop 7" : "+v"(acc[5]), "+v"(acc[4]), "+v"(accw));
    STAMP(3, 5);
    LBAR();
    LAS char* ex = lds + mi * 8192 + lane * 16;
    if (kj == 0) { *(LAS f32x4*)(ex + 4 * 1024) = acc[4]; *(LAS f32x4*)(ex + 5 * 1024) = acc[5]; *(LAS f32x4*)(ex + 6 * 1024) = accw; }
    else {
#pragma unroll
        for (int t = 0; t < 4; ++t) *(LAS f32x4*)(ex + t * 1024) = acc[t];
        *(LAS f32x4*)(ex + 7 * 1024) = accw; }
    LBAR();
    accw += *(LAS const f32x4*)(ex + (kj ? 6 : 7) * 1024);
    const float mu = mux + __shfl(accw[0] + accw[1], m);
    float ssq = 0.f;
    auto fin = [&](int t, u32x2 xv, unsigned (&pk)[2]) {
        const f32x4 a = acc[t] + *(LAS const f32x4*)(ex + t * 1024);
        const unsigned h0 = cvtpk(a[0] + bflo(xv[0]), a[1] + bfhi(xv[0])), h1 = cvtpk(a[2] + bflo(xv[1]), a[3] + bfhi(xv[1]));
        pk[0] = cvtpk(bflo(h0) - mu, bfhi(h0) - mu); pk[1] = cvtpk(bflo(h1) - mu, bfhi(h1) - mu);
        ssq = dot2(pk[0], pk[0], ssq); ssq = dot2(pk[1], pk[1], ssq); };
    auto pair = [&](int t, u32x2 xa, u32x2 xb2) { unsigned p0[2], p1[2]; fin(t, xa, p0); fin(t + 1, xb2, p1);
        bstore16<1>((u32x4){p0[0], p0[1], p1[0], p1[1]}, srd, l16, rfl((unsigned)WS_HCF + (mt16 * 18 + 3 * n6 + (t >> 1)) * 1024)); };
    if (kj == 0) { pair(0, xr[0], xr[1]); pair(2, xr[2], xr[3]); } else pair(4, xr[0], xr[1]);
    ssq += xor16(ssq); ssq += xor32(ssq);
    if (g == 0) { __hip_atomic_store((float*)(ws + WS_SSQ) + (mt16 * 16 + 2 * n6 + kj) * 16 + m, ssq, __ATOMIC_RELAXED, __HIP_MEMORY_SCOPE_AGENT);
        if (n6 == 0 && kj == 0) __hip_atomic_store((float*)(ws + WS_MUH) + token, mu, __ATOMIC_RELAXED, __HIP_MEMORY_SCOPE_AGENT); }
    STAMP(3, 6); RSTAMP(3, 15); STAMP_FLUSH(3);
}

DI void ring_issue(const char* stream, int st, int wid, int lane, unsigned ldsb) {
    const char* sb = stream + (size_t)st * RING_SLOT + wid * 1024; const unsigned db = ldsb + (st & 3) * RING_SLOT + wid * 1024;
#pragma unroll
    for (int i = 0; i < 4; ++i) glds16(sb + i * 8192, lane * 16, db + i * 8192);
    if (wid < 4) glds16(sb + 32768, lane * 16, db + 32768);
}
DI float geglu(float g, float u) {
    const float z = g * __builtin_fmaf(g * g, -0.10294324f, -2.30220818f);
    return g * u * __builtin_amdgcn_rcpf(1.f + __builtin_amdgcn_exp2f(z));
}
struct Ev { int type, arg, n; };
enum { E_SSQ, E_A, E_B, E_WAITB, E_BAR, E_READ, E_MMA, E_GP, E_RS, E_STAMP, E_ST, E_NOP, E_TOUCH, E_A2 };
constexpr int G1_PD = G1PD;
constexpr int g1_grp(int f) { return f < 144 ? 0 : f < 216 ? 1 : f < 288 ? 2 : 3; }
constexpr int g1_ks(int f) { return f < 144 ? f / 8 : f < 288 ? ((f - 144) % 72) / 4 : (f - 288) / 2; }
constexpr int g1_tile(int f) { return f < 288 ? (f >> 1) & 1 : 0; }
constexpr int g1_set(int f) { return f < 144 ? (f >> 2) & 1 : f < 216 ? 0 : f < 288 ? 1 : 0; }
template <bool LATE> struct G1Prog {
    Ev ev[1600]; int n;
    constexpr void add(int t, int a) { ev[n].type = t; ev[n].arg = a; ev[n].n = 0; ++n; }
    static constexpr int vm(const Ev& e) { return e.type == E_SSQ ? 6 : (e.type == E_A || e.type == E_B || e.type == E_ST || e.type == E_TOUCH || e.type == E_A2) ? 1 : 0; }
    static constexpr int NP = LATE ? 4 : 5;
    constexpr void addB(int s, int u) { add(E_B, s * 2 + u); }
    static constexpr int jset(int q) { return q == 0 ? 0 : q == 1 ? 1 : q == 2 ? 0 : q == 3 ? 1 : 0; }
    constexpr G1Prog() : ev{}, n(0) {
        addB(0, 0); addB(0, 1); addB(1, 0); addB(1, 1);
        add(E_STAMP, 1);
        for (int f = 0; f < G1_PD; ++f) add(E_READ, f);
        for (int f = 0; f < 324; ++f) {
            const int st = f / 36, i = f % 36, g = g1_grp(f);
            if (g == 0) { const int s = f / 8, q = f % 8;
                if (q == 0) add(E_WAITB, s);
                if (f == 0) { addB(2, 0); addB(2, 1); }
                if (s + 3 < 18 && q == 2) addB(s + 3, 0);
                if (s + 3 < 18 && q == 6) addB(s + 3, 1);
                if (f == 100) add(E_SSQ, 0);
                if (f == 136) add(E_RS, 0); }
            if (st + 2 < 9) { const int i0 = LATE ? 17 : 1, d = 4;
                if (i >= i0 && (i - i0) % d == 0 && (i - i0) / d < NP) add(E_A, (st + 2) * 8 + (i - i0) / d); }
            if (i == 36 - G1_PD && st + 1 < 9) add(E_BAR, st + 1);
            if (f + G1_PD < 324) add(E_READ, f + G1_PD);
            add(E_MMA, f);
            if (f == 143) { add(E_NOP, 0);
                for (int k = 0; k < 32; ++k) { add(E_GP, 0 * 64 + k); if (k == 15 || k == 31) add(E_ST, 0 * 2 + (k == 31)); } }
            if (g > 0) {
                const int r = f - (g == 1 ? 144 : g == 2 ? 216 : 288), step = g < 3 ? 2 : 1, r0 = 3, k = (r - r0) / step;
                if (r >= r0 && (r - r0) % step == 0 && k < 32) { add(E_GP, g * 64 + k);
                    if (k == (LATE && ST_SKEW ? 22 : 15)) add(E_ST, g * 2);
                    if (k == 31 && !(LATE && ST_SKEW)) add(E_ST, g * 2 + 1); }
                if (LATE && ST_SKEW && r == (g < 3 ? 71 : 35)) add(E_ST, g * 2 + 1); }
            if (TOUCH && f == 7 * 36 + 1) add(E_TOUCH, 0);
            if (st == 8 && i >= 2 && i % 3 == 2 && (i - 2) / 3 < 2 * NP) add(E_A2, ((i - 2) / 3 / NP) * 8 + ((i - 2) / 3) % NP);
            if (i == 35) add(E_STAMP, 2 + st);
            if (f == 107) add(E_STAMP, 12);
            if (f == 323) add(E_STAMP, 13);
        }
        add(E_NOP, 1);
        for (int k = 0; k < 16; ++k) { add(E_GP, 4 * 64 + k); if (k == 7 || k == 15) add(E_ST, 8 + (k == 15)); }
        for (int i = 0; i < n; ++i) if (ev[i].type == E_WAITB || ev[i].type == E_BAR || ev[i].type == E_RS) {
            int cnt = 0, j = i - 1;
            for (; j >= 0; --j) {
                if (ev[i].type == E_WAITB && ev[j].type == E_B && ev[j].arg == 2 * ev[i].arg + 1) break;
                if (ev[i].type == E_RS && ev[j].type == E_SSQ) break;
                if (ev[i].type == E_BAR && ev[j].type == E_A && ev[j].arg == 8 * ev[i].arg + NP - 1) break;
                cnt += vm(ev[j]); }
            ev[i].n = cnt > 63 ? 63 : cnt; }
    }
};
template <bool LATE> constexpr G1Prog<LATE> g1prog{};
DI float vmul(float a, float b) { float r; asm("v_mul_f32_e32 %0, %1, %2" : "=v"(r) : "v"(a), "v"(b)); return r; }
DI float vfma(float a, float b, float c) { float r; asm("v_fma_f32 %0, %1, %2, %3" : "=v"(r) : "v"(a), "v"(b), "v"(c)); return r; }
struct G1State {
    bf16x8 hf[2][18]; f32x4 acc[2][2][2][2];
    bf16x8 ar[G1_PD + 1]; float sq[2][3]; float a0[2], a1[2], kk[2], sink; f32x4 z, w; u32x4 o; unsigned soffT, soffA2;
    unsigned lb0, lb1, lb2; unsigned l16, ldsb, soffA, soffH, soffS, soffAct, soffAct8, vq; int wid, j, g;
};
template <bool LATE, int PC> DI void g1_exec(G1State& c, const srd_t srd) {
    constexpr Ev e = g1prog<LATE>.ev[PC];
    if constexpr (e.type == E_SSQ) { bload4_sc1<0>(c.sq[0][0], srd, c.vq, c.soffS); bload4_sc1<256>(c.sq[0][1], srd, c.vq, c.soffS); bload4_sc1<512>(c.sq[0][2], srd, c.vq, c.soffS);
                                     bload4_sc1<1024>(c.sq[1][0], srd, c.vq, c.soffS); bload4_sc1<1280>(c.sq[1][1], srd, c.vq, c.soffS); bload4_sc1<1536>(c.sq[1][2], srd, c.vq, c.soffS); }
    else if constexpr (e.type == E_A) { constexpr int st = e.arg >> 3, p = e.arg & 7;
        const unsigned so = c.soffA + st * RING_SLOT, dst = c.ldsb + RING0 + ((st + 3) & 3) * RING_SLOT;
        if constexpr (p < 4) bdma_next<p * 1024>(srd, c.l16, so + c.wid * 4096, dst + c.wid * 4096);
        else bdma1(srd, c.l16, so + 32768 + c.wid * 1024, dst + 32768 + c.wid * 1024); }
    else if constexpr (e.type == E_B) { constexpr int s = e.arg >> 1, u = e.arg & 1;
        bload16_sc1<((18 * u + s) & 3) * 1024>(c.hf[u][s], srd, c.l16, c.soffH + ((18 * u + s) >> 2) * 4096); }
    else if constexpr (e.type == E_WAITB) { asm volatile("s_waitcnt vmcnt(%2)" : "+v"(c.hf[0][e.arg]), "+v"(c.hf[1][e.arg]) : "n"(e.n)); }
    else if constexpr (e.type == E_BAR) { asm volatile("s_waitcnt vmcnt(%0)\n\ts_barrier" :: "n"(e.n) : "memory"); }
    else if constexpr (e.type == E_RS) { asm volatile("s_waitcnt vmcnt(%6)" : "+v"(c.sq[0][0]), "+v"(c.sq[0][1]), "+v"(c.sq[0][2]), "+v"(c.sq[1][0]), "+v"(c.sq[1][1]), "+v"(c.sq[1][2]) : "n"(e.n));
#pragma unroll
        for (int u = 0; u < 2; ++u) { float t = (c.sq[u][0] + c.sq[u][1]) + c.sq[u][2]; t += xor16(t); t += xor32(t);
            const float v = t * (1.f / D_) + EPS, rs = __builtin_amdgcn_rsqf(v);
            c.a0[u] = -2.30220818f * rs; c.a1[u] = -0.10294324f * rs * rs * rs; c.kk[u] = v; }
        SBAR(); }
    else if constexpr (e.type == E_STAMP) {
    }
    else if constexpr (e.type == E_TOUCH) { touch64(c.sink, srd, c.l16 * 8, c.soffT); }
    else if constexpr (e.type == E_A2) { constexpr int st = e.arg >> 3, p = e.arg & 7;
        const unsigned so = c.soffA2 + st * RING_SLOT, dst = c.ldsb + RING0 + (st & 3) * RING_SLOT;
        if constexpr (p < 4) bdma_next<p * 1024>(srd, c.l16, so + c.wid * 4096, dst + c.wid * 4096);
        else bdma1(srd, c.l16, so + 32768 + c.wid * 1024, dst + 32768 + c.wid * 1024); }
    else if constexpr (e.type == E_NOP) {
        asm volatile("s_nop 7" : "+v"(c.acc[0][0][0][0]), "+v"(c.acc[0][0][0][1]), "+v"(c.acc[0][0][1][0]), "+v"(c.acc[0][0][1][1]));
        if constexpr (e.arg == 0) asm volatile("" : "+v"(c.acc[0][1][0][0]), "+v"(c.acc[0][1][0][1]), "+v"(c.acc[0][1][1][0]), "+v"(c.acc[0][1][1][1])); }
    else if constexpr (e.type == E_READ) { constexpr int f = e.arg, off = ((f / 36 + 3) & 3) * RING_SLOT + (f % 36) * 1024;
        if constexpr ((off >> 16) == 0) c.ar[f % (G1_PD + 1)] = lds_frag((LAS const char*)(uintptr_t)c.lb0 + (off & 65535));
        else if constexpr ((off >> 16) == 1) c.ar[f % (G1_PD + 1)] = lds_frag((LAS const char*)(uintptr_t)c.lb1 + (off & 65535));
        else c.ar[f % (G1_PD + 1)] = lds_frag((LAS const char*)(uintptr_t)c.lb2 + (off & 65535));
        SBAR(); }
    else if constexpr (e.type == E_MMA) { constexpr int f = e.arg, s = g1_ks(f), t = g1_tile(f), q = f & 1, set = g1_set(f);
        if constexpr (s == 0) { mfma16_new(c.acc[set][t][q][0], c.ar[f % (G1_PD + 1)], c.hf[0][s]); mfma16_new(c.acc[set][t][q][1], c.ar[f % (G1_PD + 1)], c.hf[1][s]); }
        else { mfma16_acc(c.acc[set][t][q][0], c.ar[f % (G1_PD + 1)], c.hf[0][s]); mfma16_acc(c.acc[set][t][q][1], c.ar[f % (G1_PD + 1)], c.hf[1][s]); }
        SBAR(); }
    else if constexpr (e.type == E_GP) {
        constexpr int grp = e.arg >> 6, k = e.arg & 63, quad = k >> 3, ph = k & 7, set = G1Prog<LATE>::jset(grp);
        constexpr int u = grp < 4 ? quad >> 1 : quad, t = grp < 4 ? quad & 1 : 0;
        const f32x4 g = c.acc[set][t][0][u];
        if constexpr ((grp == 0 || grp == 4) && (ph == 4 || ph == 6)) asm volatile("s_nop 0" : "+v"(c.z));
#pragma unroll
        for (int r = 0; r < 4; ++r) {
            if constexpr (ph == 0) c.z[r] = vmul(g[r], g[r]);
            if constexpr (ph == 1) c.z[r] = vfma(c.z[r], c.a1[u], c.a0[u]);
            if constexpr (ph == 2) c.z[r] = vmul(c.z[r], g[r]);
            if constexpr (ph == 3) c.z[r] = __builtin_amdgcn_exp2f(c.z[r]);
            if constexpr (ph == 4) c.z[r] = vfma(c.z[r], c.kk[u], c.kk[u]);
            if constexpr (ph == 5) c.z[r] = __builtin_amdgcn_rcpf(c.z[r]);
            if constexpr (ph == 6) c.w[r] = vmul(vmul(g[r], c.acc[set][t][1][u][r]), c.z[r]); }
        if constexpr (ph == 7) { c.o[2 * t] = cvtpk(c.w[0], c.w[1]); c.o[2 * t + 1] = cvtpk(c.w[2], c.w[3]); }
        SBAR(); }
    else if constexpr (e.type == E_ST) { constexpr int grp = e.arg >> 1, u = e.arg & 1;
        if constexpr (grp < 4) bstore16<ACT_POL>(c.o, srd, c.l16, c.soffAct + u * (144 * 1024) + grp * 1024);
        else bstore8<ACT_POL>((u32x2){c.o[0], c.o[1]}, srd, c.l16 + (c.j & 1) * 8, c.soffAct8 + u * (144 * 1024));
        SBAR(); }
}
template <bool LATE, int... I> DI void g1_run(G1State& c, const srd_t srd, std::integer_sequence<int, I...>) { (g1_exec<LATE, I>(c, srd), ...); }

constexpr int G2_PD = 4;
template <bool LATE> struct G2Prog {
    Ev ev[700]; int n;
    constexpr void add(int t, int a) { ev[n].type = t; ev[n].arg = a; ev[n].n = 0; ++n; }
    static constexpr int vm(const Ev& e) { return (e.type == E_A || e.type == E_B) ? 1 : 0; }
    static constexpr int NP = LATE ? 4 : 5;
    constexpr G2Prog() : ev{}, n(0) {
        add(E_B, 0); add(E_B, 1); add(E_B, 2); add(E_B, 3);
        add(E_STAMP, 1);
        for (int f = 0; f < G2_PD; ++f) add(E_READ, f);
        for (int f = 0; f < 162; ++f) {
            const int st = f / 18, i = f % 18;
            if (i % 9 == 0) add(E_WAITB, st * 2 + i / 9);
            const int ia = LATE ? 3 : 1, ib = LATE ? 1 : 3;
            if (st == 0) {
                if (i >= 1 && i < 5) add(E_B, 4 + i - 1);
                if (i >= 6 && i < 6 + NP) add(E_A, 16 + i - 6);
                if (i >= 12 && i < 16) add(E_B, 8 + i - 12);
            } else {
                if (st == 1 && i >= 14) add(E_B, 16 + i - 14);
                if (st + 2 < 9 && i >= ia && (i - ia) % 4 == 0 && (i - ia) / 4 < 4) add(E_A, (st + 2) * 8 + (i - ia) / 4);
                if (st + 2 < 9 && NP == 5 && i == 12) add(E_A, (st + 2) * 8 + 4);
                { const int la = st == 1 ? 2 : 3; if (st + la < 9 && (st == 1 || st + la > 4) && i >= ib && (i - ib) % 4 == 0 && (i - ib) / 4 < 4) add(E_B, (st + la) * 4 + (i - ib) / 4); }
            }
            if (i == 18 - G2_PD && st + 1 < 9) add(E_BAR, st + 1);
            if (f + G2_PD < 162) add(E_READ, f + G2_PD);
            add(E_MMA, f);
            if (i == 17) add(E_STAMP, 2 + st);
        }
        for (int i = 0; i < n; ++i) if (ev[i].type == E_WAITB || ev[i].type == E_BAR) {
            int cnt = 0, j = i - 1;
            for (; j >= 0; --j) {
                if (ev[i].type == E_WAITB && ev[j].type == E_B && ev[j].arg == 2 * ev[i].arg + 1) break;
                if (ev[i].type == E_BAR && ev[j].type == E_A && (ev[j].arg >> 3) == ev[i].arg) break;
                cnt += vm(ev[j]); }
            ev[i].n = cnt > 63 ? 63 : cnt; }
    }
};
template <bool LATE> constexpr G2Prog<LATE> g2prog{};
struct G2State {
    bf16x8 af[4][2][2]; f32x4 acc[9][2]; bf16x8 ar[G2_PD + 1];
    unsigned lb0, lb1, lb2; unsigned l16, ldsb, soffA, soffB; int wid;
};
template <bool LATE, int PC> DI void g2_exec(G2State& c, const srd_t srd) {
    constexpr Ev e = g2prog<LATE>.ev[PC];
    if constexpr (e.type == E_A) { constexpr int st = e.arg >> 3, p = e.arg & 7;
        const unsigned so = c.soffA + st * RING_SLOT, dst = c.ldsb + RING0 + (st & 3) * RING_SLOT;
        if constexpr (p < 4) bdma_next<p * 1024>(srd, c.l16, so + c.wid * 4096, dst + c.wid * 4096);
        else bdma1(srd, c.l16, so + 32768 + c.wid * 1024, dst + 32768 + c.wid * 1024); }
    else if constexpr (e.type == E_B) { constexpr int st = e.arg >> 2, s2 = (e.arg >> 1) & 1, u = e.arg & 1;
        bload16_sc1<((2 * st + s2) & 3) * 1024>(c.af[st & 3][s2][u], srd, c.l16, c.soffB + u * (144 * 1024) + ((2 * st + s2) >> 2) * 4096); }
    else if constexpr (e.type == E_WAITB) { asm volatile("s_waitcnt vmcnt(%2)" : "+v"(c.af[(e.arg >> 1) & 3][e.arg & 1][0]), "+v"(c.af[(e.arg >> 1) & 3][e.arg & 1][1]) : "n"(e.n)); }
    else if constexpr (e.type == E_BAR) { asm volatile("s_waitcnt vmcnt(%0)\n\ts_barrier" :: "n"(e.n) : "memory"); }
    else if constexpr (e.type == E_STAMP) {
    }
    else if constexpr (e.type == E_READ) { constexpr int f = e.arg, st = f / 18, off = (st & 3) * RING_SLOT + (f % 18) * 1024;
        if constexpr ((off >> 16) == 0) c.ar[f % (G2_PD + 1)] = lds_frag((LAS const char*)(uintptr_t)c.lb0 + (off & 65535));
        else c.ar[f % (G2_PD + 1)] = lds_frag((LAS const char*)(uintptr_t)c.lb1 + (off & 65535));
        SBAR(); }
    else if constexpr (e.type == E_MMA) { constexpr int f = e.arg, st = f / 18, s2 = (f % 18) / 9, t = f % 9;
        if constexpr (f < 9) { mfma16_new(c.acc[t][0], c.ar[f % (G2_PD + 1)], c.af[st & 3][s2][0]); mfma16_new(c.acc[t][1], c.ar[f % (G2_PD + 1)], c.af[st & 3][s2][1]); }
        else { mfma16_acc(c.acc[t][0], c.ar[f % (G2_PD + 1)], c.af[st & 3][s2][0]); mfma16_acc(c.acc[t][1], c.ar[f % (G2_PD + 1)], c.af[st & 3][s2][1]); }
        SBAR(); }
}
template <bool LATE, int... I> DI void g2_run(G2State& c, const srd_t srd, std::integer_sequence<int, I...>) { (g2_exec<LATE, I>(c, srd), ...); }
DI void arrive(char* ws, int counter) { if (threadIdx.x == 0) __hip_atomic_fetch_add((unsigned*)(ws + WS_CNT) + counter * 64, 1u, __ATOMIC_RELAXED, __HIP_MEMORY_SCOPE_AGENT); }
DI bool await(char* ws, int counter, unsigned want, LAS unsigned* flag) {
    if (threadIdx.x == 0) { const unsigned* cnt = (const unsigned*)(ws + WS_CNT) + counter * 64; unsigned bad = 0;
        const unsigned long long t0 = __builtin_amdgcn_s_memrealtime();
        while (__hip_atomic_load(cnt, __ATOMIC_RELAXED, __HIP_MEMORY_SCOPE_AGENT) < want) {
            if (__builtin_amdgcn_s_memrealtime() - t0 > 2000000ull) { bad = 1; break; }
            __builtin_amdgcn_s_sleep(1); }
        *flag = bad; }
    LBAR();
    return *flag != 0;
}
DI void ring_w1(char* ws, char* lds_generic, int xcd, int idx, int st) {
    const int lane = threadIdx.x & 63, wid = rfl(threadIdx.x >> 6); const srd_t srd = make_srd(ws, (unsigned)WS_END);
    const unsigned so = (unsigned)WS_W1 + ((xcd >> 1) * 8 + (idx & 7)) * (324 * 1024) + st * RING_SLOT, dst = (unsigned)(uintptr_t)lds_generic + RING0 + ((st + 3) & 3) * RING_SLOT;
    bdma4(srd, lane * 16, so + wid * 4096, dst + wid * 4096);
    if (wid < 4) bdma1(srd, lane * 16, so + 32768 + wid * 1024, dst + 32768 + wid * 1024);
}
constexpr int FFN_LDS = G_LDS;
__global__ __launch_bounds__(512, 2) void ffn_kernel(const char* x, char* ws, char* out) {
    extern __shared__ __attribute__((aligned(16))) char lds_generic[];
    LAS char* lds = (LAS char*)lds_generic;
    const srd_t srd = make_srd(ws, (unsigned)WS_END);
    const int b = blockIdx.x, xcd = b & 7, idx = b >> 3;
    bool bad;
    TL_DECL; TLS(0);
    if (idx >= 24) ring_w1(ws, lds_generic, xcd, idx, 0);
    else {
        k3_phase(x, ws, lds_generic, xcd, idx);
        TLS(1);
        asm volatile("s_waitcnt vmcnt(0) lgkmcnt(0)\n\ts_barrier" ::: "memory");
        arrive(ws, 96 + xcd);
        TLS(2);
    }
    ring_w1(ws, lds_generic, xcd, idx, 1);
    asm volatile("s_waitcnt vmcnt(0)" ::: "memory");
    LAS unsigned* flag = (LAS unsigned*)(lds + 15 * 1024);
    bad = await(ws, 96 + (xcd & 1) * 4 + (idx >> 3), 24u, flag);
    TLS(3);
    {
        int tid = threadIdx.x; asm volatile("" : "+v"(tid));
        const int lane = tid & 63, wid = rfl(tid >> 6);
        const int j = (xcd >> 1) * 8 + (idx & 7), mt8 = (xcd & 1) * 4 + (idx >> 3), mt16 = 16 * mt8 + 2 * wid;
        G1State c;
        RSTAMP(4, 14); STAMP(4, 0);
        c.l16 = lane * 16; c.ldsb = (unsigned)(uintptr_t)lds_generic; c.wid = wid; c.j = j; c.g = lane >> 4;
        c.lb0 = c.ldsb + RING0 + lane * 16; c.lb1 = c.lb0 + 65536; c.lb2 = c.lb0 + 131072; PINV(c.lb0); PINV(c.lb1); PINV(c.lb2);
        c.soffA = (unsigned)WS_W1 + j * (324 * 1024); c.soffH = (unsigned)WS_HCF + mt16 * (18 * 1024); c.soffS = (unsigned)WS_SSQ + mt16 * 1024;
        c.soffAct8 = rfl((unsigned)WS_ACT + (mt16 * 144 + 9 * (j >> 1) + 8) * 1024);
        c.soffAct = rfl((unsigned)WS_ACT + (mt16 * 144 + 9 * (j >> 1) + 4 * (j & 1)) * 1024); c.vq = (lane >> 4) * 64 + (lane & 15) * 4;
        c.soffA2 = (unsigned)WS_W2 + ((xcd >> 1) * 4 + (idx >> 3)) * (324 * 1024);
        c.sink = 0.f; c.soffT = (unsigned)WS_W2 + (xcd >> 1) * (4 * 324 * 1024) + (idx * 8 + wid) * (41 * 128);
        if (wid >= 4) g1_run<true>(c, srd, std::make_integer_sequence<int, g1prog<true>.n>{});
        else g1_run<false>(c, srd, std::make_integer_sequence<int, g1prog<false>.n>{});
        STAMP(4, 11); RSTAMP(4, 15); TLS(4);
        asm volatile("s_waitcnt vmcnt(0) lgkmcnt(0)\n\ts_barrier" : "+v"(c.sink) :: "memory");
        arrive(ws, 64 + (j >> 3) * 8 + mt8);
        TLS(5);
        STAMP_FLUSH(4);
    }
    int tz = threadIdx.x; asm volatile("" : "+v"(tz));
    const int lane = tz & 63, wid = rfl(tz >> 6), mi = wid & 3, kj = wid >> 2;
    const int ksl = xcd >> 1, mt = (xcd & 1) * 8 + (idx & 7), n4 = idx >> 3;
    const int mt16 = 8 * mt + 2 * mi;
    G2State c;
    RSTAMP(5, 14); STAMP(5, 0);
    c.l16 = lane * 16; c.ldsb = (unsigned)(uintptr_t)lds_generic; c.wid = wid;
    c.lb0 = c.ldsb + RING0 + kj * (18 * 1024) + lane * 16; c.lb1 = c.lb0 + 65536; PINV(c.lb0); PINV(c.lb1);
    c.soffA = (unsigned)WS_W2 + (ksl * 4 + n4) * (324 * 1024); c.soffB = (unsigned)WS_ACT + (mt16 * 144 + 36 * ksl + 18 * kj) * 1024;
    bad |= await(ws, 64 + ksl * 8 + (mt >> 1), 8u, flag);
    TLS(6);
    if (wid >= 4) g2_run<true>(c, srd, std::make_integer_sequence<int, g2prog<true>.n>{});
    else g2_run<false>(c, srd, std::make_integer_sequence<int, g2prog<false>.n>{});
    asm volatile("s_nop 7" : "+v"(c.acc[8][0]), "+v"(c.acc[8][1]), "+v"(c.acc[7][0]), "+v"(c.acc[7][1]));
    TLS(7);
    u32x2 hv[3]; u32x4 pv[3]; u32x2 pw[3]; float mu[2];
    { const int T0 = 9 * n4 + wid, T8 = 9 * n4 + 8; const unsigned hb = rfl((unsigned)WS_HCF + (8 * mt + 2 * ksl) * (18 * 1024));
      bload8_sc1<0>(hv[0], srd, lane * 16 + (T0 & 1) * 8, hb + (T0 >> 1) * 1024); bload8_sc1<0>(hv[1], srd, lane * 16 + (T0 & 1) * 8, hb + (18 + (T0 >> 1)) * 1024);
      bload8_sc1<0>(hv[2], srd, lane * 16 + (T8 & 1) * 8, hb + ((wid & 1) * 18 + (T8 >> 1)) * 1024);
      const unsigned mb = rfl((unsigned)WS_MUH + (128 * mt + 32 * ksl) * 4); bload4_sc1<0>(mu[0], srd, (lane & 15) * 4, mb); bload4_sc1<64>(mu[1], srd, (lane & 15) * 4, mb); }
    LBAR();
    LAS char* ex = lds + RING0 + mi * 18432 + lane * 16;
    if (kj == 0) {
#pragma unroll
        for (int t = 5; t < 9; ++t) { *(LAS f32x4*)(ex + (2 * t) * 1024) = c.acc[t][0]; *(LAS f32x4*)(ex + (2 * t + 1) * 1024) = c.acc[t][1]; }
    } else {
#pragma unroll
        for (int t = 0; t < 5; ++t) { *(LAS f32x4*)(ex + (2 * t) * 1024) = c.acc[t][0]; *(LAS f32x4*)(ex + (2 * t + 1) * 1024) = c.acc[t][1]; }
    }
    LBAR();
    STAMP(5, 11);
    const int tile = mt * 4 + n4; const unsigned xb = rfl((unsigned)WS_PART + tile * (16 * 9 * 1024));
    const unsigned xs0 = rfl(xb + (mi * 4 + ksl) * (9 * 1024));
    auto fin = [&](int t) {
        const f32x4 a0 = c.acc[t][0] + *(LAS const f32x4*)(ex + (2 * t) * 1024), a1 = c.acc[t][1] + *(LAS const f32x4*)(ex + (2 * t + 1) * 1024);
        if (mi == ksl) { *(LAS f32x4*)(ex + (2 * t) * 1024) = a0; *(LAS f32x4*)(ex + (2 * t + 1) * 1024) = a1; }
        else bstore16<1>((u32x4){cvtpk(a0[0], a0[1]), cvtpk(a0[2], a0[3]), cvtpk(a1[0], a1[1]), cvtpk(a1[2], a1[3])}, srd, lane * 16, xs0 + t * 1024); };
    if (kj == 0) { fin(0); fin(1); fin(2); fin(3); fin(4); } else { fin(5); fin(6); fin(7); fin(8); }
    STAMP(5, 12);
    asm volatile("s_waitcnt vmcnt(0) lgkmcnt(0)\n\ts_barrier" : "+v"(hv[0]), "+v"(hv[1]), "+v"(hv[2]), "+v"(mu[0]), "+v"(mu[1]) :: "memory");
    STAMP(5, 13);
    TLS(8);
    arrive(ws, tile);
    bad |= await(ws, tile, 4u, flag);
    TLS(9);
#pragma unroll
    for (int k = 0; k < 3; ++k) { const unsigned sb = rfl(xb + (ksl * 4 + (k + (k >= ksl))) * (9 * 1024));
        bload16u_sc1<0>(pv[k], srd, lane * 16, sb + wid * 1024); bload8_sc1<0>(pw[k], srd, lane * 16 + (wid & 1) * 8, sb + 8 * 1024); }
    asm volatile("s_waitcnt vmcnt(0)" : "+v"(pv[0]), "+v"(pv[1]), "+v"(pv[2]), "+v"(pw[0]), "+v"(pw[1]), "+v"(pw[2]) :: "memory");
    const int m = lane & 15, g = lane >> 4;
    auto put = [&](int t, int u, u32x2 h2, u32x2 p0, u32x2 p1, u32x2 p2) {
        f32x4 a = *(LAS const f32x4*)(lds + RING0 + ksl * 18432 + (2 * t + u) * 1024 + lane * 16);
        a += (f32x4){bflo(h2[0]), bfhi(h2[0]), bflo(h2[1]), bfhi(h2[1])} + (u ? mu[1] : mu[0]); a += (f32x4){bflo(p0[0]), bfhi(p0[0]), bflo(p0[1]), bfhi(p0[1])};
        a += (f32x4){bflo(p1[0]), bfhi(p1[0]), bflo(p1[1]), bfhi(p1[1])}; a += (f32x4){bflo(p2[0]), bfhi(p2[0]), bflo(p2[1]), bfhi(p2[1])};
        u32x2 o = {cvtpk(a[0], a[1]), cvtpk(a[2], a[3])};
        if (bad) o = (u32x2){0x7fc07fc0u, 0x7fc07fc0u};
        *(u32x2*)(out + (size_t)(128 * mt + 32 * ksl + 16 * u + m) * (D_ * 2) + (144 * n4 + 16 * t + 4 * g) * 2) = o; };
    put(wid, 0, hv[0], (u32x2){pv[0][0], pv[0][1]}, (u32x2){pv[1][0], pv[1][1]}, (u32x2){pv[2][0], pv[2][1]});
    put(wid, 1, hv[1], (u32x2){pv[0][2], pv[0][3]}, (u32x2){pv[1][2], pv[1][3]}, (u32x2){pv[2][2], pv[2][3]});
    if (wid < 2) put(8, wid, hv[2], pw[0], pw[1], pw[2]);
    RSTAMP(5, 15); STAMP_FLUSH(5);
    TLS(10); TL_FLUSH(6);
}

}

extern "C" void kernel_launch(void* const* d_in, const int* in_sizes, int n_in, void* d_out, int out_size, void* d_ws, size_t ws_size, hipStream_t stream) {
    const char* x = (const char*)d_in[0]; const char* wqkv = (const char*)d_in[1]; const char* wo = (const char*)d_in[2];
    const char* wg = (const char*)d_in[4]; const char* wu = (const char*)d_in[5]; const char* wd = (const char*)d_in[6];
    char* ws = (char*)d_ws;
    (void)hipFuncSetAttribute((const void*)k1_qkv, hipFuncAttributeMaxDynamicSharedMemorySize, K1_LDS);
    (void)hipFuncSetAttribute((const void*)k2_attn, hipFuncAttributeMaxDynamicSharedMemorySize, K2_LDS);
    (void)hipFuncSetAttribute((const void*)ffn_kernel, hipFuncAttributeMaxDynamicSharedMemorySize, FFN_LDS);
    k1_qkv<<<256, 512, K1_LDS, stream>>>(x, wqkv, wo, ws);
    k2_attn<<<256, 1024, K2_LDS, stream>>>(x, wg, wu, wd, ws);
    ffn_kernel<<<256, 512, FFN_LDS, stream>>>(x, ws, (char*)d_out);
}
```

```cpp
#include <hip/hip_runtime.h>
#include <hip/hip_bf16.h>
#include <stdint.h>
#include <utility>
#define ST_SKEW 1
#define CODE_PF3 1
#define CODE_PF2 1
#define CODE_PF 1
#define K1_TOUCH 0
#define TOUCH 0
#define RT_IT0 2
#define K2_RT 1
#define K2_PRIO 1
#define K1_ROT 0
#define G1PD 4
#define ACT_POL 1
#define RT_POL 1
#define PART_POL 1
#define H_POL 1
#define QKV_POL 1
#define G1_PRIO 1

namespace {

typedef __bf16 bf16x2 __attribute__((ext_vector_type(2)));
typedef __bf16 bf16x8 __attribute__((ext_vector_type(8)));
typedef __bf16 bf16x16 __attribute__((ext_vector_type(16)));
typedef short s16x4 __attribute__((ext_vector_type(4)));
typedef float f32x2 __attribute__((ext_vector_type(2)));
typedef float f32x4 __attribute__((ext_vector_type(4)));
typedef float f32x8 __attribute__((ext_vector_type(8)));
typedef float f32x16 __attribute__((ext_vector_type(16)));
typedef unsigned u32x2 __attribute__((ext_vector_type(2)));
typedef unsigned u32x4 __attribute__((ext_vector_type(4)));
#define LAS __attribute__((address_space(3)))
#define DI __device__ __forceinline__

constexpr int S_ = 2048, D_ = 576, H_ = 32, F_ = 4608;
constexpr float EPS = 1e-5f;
constexpr float QSCALE = 0.23570226039551584f * 1.4426950408889634f;
constexpr float SAFE_B = 48.f;

constexpr int KVCHUNK = 19456, KVIMG = 8 * KVCHUNK;
constexpr bool OVERLAY = true;
constexpr size_t WS_QM   = 0;
constexpr size_t WS_QT   = WS_QM + (size_t)32 * 2048 * 32;
constexpr size_t WS_KV   = WS_QT + (size_t)32 * 2048 * 4;
constexpr size_t WS_A_END = WS_KV + (size_t)32 * KVIMG;
constexpr size_t WS_ACT  = OVERLAY ? 0 : WS_A_END;
constexpr size_t WS_OF   = WS_ACT + (size_t)128 * 144 * 1024;
constexpr size_t WS_WO   = WS_OF + (size_t)128 * 18 * 1024;
constexpr size_t WS_KMAX = WS_WO + (size_t)6 * 108 * 1024;
constexpr size_t WS_MUX  = WS_KMAX + 32 * 16 * 4;
constexpr size_t WS_WBAR = WS_MUX + 2048 * 4;
constexpr size_t WS_SSQ  = WS_WBAR + 4096;
constexpr size_t WS_CNT  = WS_SSQ + (size_t)2048 * 64;
constexpr size_t WS_MUH  = WS_CNT + 104 * 256;
constexpr size_t WS_HCF  = WS_MUH + 2048 * 4;
constexpr size_t WS_W2   = WS_HCF + (size_t)128 * 18 * 1024;
constexpr size_t WS_W1   = WS_W2 + (size_t)16 * 324 * 1024;
constexpr size_t WS_PART = WS_W1 + (size_t)32 * 324 * 1024;
static_assert(WS_A_END <= (size_t)128 * 144 * 1024, "overlay fits");
constexpr size_t WS_END  = WS_PART + (size_t)64 * 16 * 9 * 1024;

#define TLS(i)
#define TL_DECL
#define TL_FLUSH(kid)
#define STAMP_DECL
#define STAMP(kid, i)
#define RSTAMP(kid, i)
#define STAMP_FLUSH(kid)
DI unsigned cvtpk(float lo, float hi) { f32x2 v = {lo, hi}; bf16x2 b = __builtin_convertvector(v, bf16x2); return __builtin_bit_cast(unsigned, b); }
DI float bflo(unsigned w) { return __uint_as_float(w << 16); }
DI float bfhi(unsigned w) { return __uint_as_float(w & 0xffff0000u); }
DI float dot2(unsigned a, unsigned b, float c) { return __builtin_amdgcn_fdot2_f32_bf16(__builtin_bit_cast(bf16x2, a), __builtin_bit_cast(bf16x2, b), c, false); }
DI int rfl(int x) { return __builtin_amdgcn_readfirstlane(x); }
DI float xor32(float v) { auto r = __builtin_amdgcn_permlane32_swap(__float_as_uint(v), __float_as_uint(v), false, false); return __uint_as_float((threadIdx.x & 32) ? r[0] : r[1]); }
DI float xor16(float v) { auto r = __builtin_amdgcn_permlane16_swap(__float_as_uint(v), __float_as_uint(v), false, false); return __uint_as_float((threadIdx.x & 16) ? r[0] : r[1]); }
template <int N> DI float row_ror(float v) { return __uint_as_float(__builtin_amdgcn_update_dpp(0u, __float_as_uint(v), 0x120 + N, 0xf, 0xf, false)); }
DI f32x4 mfma16(bf16x8 a, bf16x8 b, f32x4 c) { return __builtin_amdgcn_mfma_f32_16x16x32_bf16(a, b, c, 0, 0, 0); }
DI void mfma16_acc(f32x4& c, bf16x8 a, bf16x8 b) { asm volatile("v_mfma_f32_16x16x32_bf16 %0, %1, %2, %0" : "+v"(c) : "v"(a), "v"(b)); }
DI void mfma16_new(f32x4& c, bf16x8 a, bf16x8 b) { asm volatile("v_mfma_f32_16x16x32_bf16 %0, %1, %2, 0" : "=&v"(c) : "v"(a), "v"(b)); }
DI void glds16(const void* sbase, unsigned voff, unsigned lds_dst) {
    asm volatile("s_mov_b32 m0, %2\n\ts_nop 0\n\tglobal_load_lds_dwordx4 %0, %1" :: "v"(voff), "s"(sbase), "s"(lds_dst) : "memory");
}
DI void gload16(bf16x8& dst, const void* sbase, unsigned voff) { asm volatile("global_load_dwordx4 %0, %1, %2" : "=v"(dst) : "v"(voff), "s"(sbase)); }
DI void gload16f(f32x4& dst, const void* sbase, unsigned voff) { asm volatile("global_load_dwordx4 %0, %1, %2" : "=v"(dst) : "v"(voff), "s"(sbase)); }
DI void gload8(u32x2& dst, const void* sbase, unsigned voff) { asm volatile("global_load_dwordx2 %0, %1, %2" : "=v"(dst) : "v"(voff), "s"(sbase)); }
DI void gload4(float& dst, const void* sbase, unsigned voff) { asm volatile("global_load_dword %0, %1, %2" : "=v"(dst) : "v"(voff), "s"(sbase)); }
typedef __amdgpu_buffer_rsrc_t srd_t;
DI srd_t make_srd(const void* p, unsigned bytes) { return __builtin_amdgcn_make_buffer_rsrc((void*)p, (short)0, (int)bytes, 0x00020000); }
template <int IMM> DI void bload16(bf16x8& dst, srd_t r, unsigned voff, unsigned soff) {
    asm volatile("buffer_load_dwordx4 %0, %1, %2, %3 offen offset:%4" : "=v"(dst) : "v"(voff), "s"(r), "s"(soff), "n"(IMM)); }
template <int IMM> DI void bload16f(f32x4& dst, srd_t r, unsigned voff, unsigned soff) {
    asm volatile("buffer_load_dwordx4 %0, %1, %2, %3 offen offset:%4" : "=v"(dst) : "v"(voff), "s"(r), "s"(soff), "n"(IMM)); }
template <int IMM> DI void bload4(float& dst, srd_t r, unsigned voff, unsigned soff) {
    asm volatile("buffer_load_dword %0, %1, %2, %3 offen offset:%4" : "=v"(dst) : "v"(voff), "s"(r), "s"(soff), "n"(IMM)); }
template <int IMM> DI void bload16_sc1(bf16x8& dst, srd_t r, unsigned voff, unsigned soff) {
    asm volatile("buffer_load_dwordx4 %0, %1, %2, %3 offen offset:%4 sc1" : "=v"(dst) : "v"(voff), "s"(r), "s"(soff), "n"(IMM)); }
template <int IMM> DI void bload4_sc1(float& dst, srd_t r, unsigned voff, unsigned soff) {
    asm volatile("buffer_load_dword %0, %1, %2, %3 offen offset:%4 sc1" : "=v"(dst) : "v"(voff), "s"(r), "s"(soff), "n"(IMM)); }
template <int IMM> DI void bload16f_sc1(f32x4& dst, srd_t r, unsigned voff, unsigned soff) {
    asm volatile("buffer_load_dwordx4 %0, %1, %2, %3 offen offset:%4 sc1" : "=v"(dst) : "v"(voff), "s"(r), "s"(soff), "n"(IMM)); }
template <int IMM> DI void bload16u_sc1(u32x4& dst, srd_t r, unsigned voff, unsigned soff) {
    asm volatile("buffer_load_dwordx4 %0, %1, %2, %3 offen offset:%4 sc1" : "=v"(dst) : "v"(voff), "s"(r), "s"(soff), "n"(IMM)); }
template <int IMM> DI void bload8_sc1(u32x2& dst, srd_t r, unsigned voff, unsigned soff) {
    asm volatile("buffer_load_dwordx2 %0, %1, %2, %3 offen offset:%4 sc1" : "=v"(dst) : "v"(voff), "s"(r), "s"(soff), "n"(IMM)); }
template <int IMM> DI void bload8(u32x2& dst, srd_t r, unsigned voff, unsigned soff) {
    asm volatile("buffer_load_dwordx2 %0, %1, %2, %3 offen offset:%4" : "=v"(dst) : "v"(voff), "s"(r), "s"(soff), "n"(IMM)); }
DI void touch1(float& sink, srd_t r, unsigned voff) { asm volatile("buffer_load_dword %0, %1, %2, 0 offen" : "+v"(sink) : "v"(voff), "s"(r)); }
#define CODE_LABEL(name) asm volatile(#name ":")
#define CODE_ADDR(dst, name) do { unsigned long long pc_; int d_; asm volatile("s_getpc_b64 %0\n.Lhere%=:\n\ts_mov_b32 %1, " #name "-.Lhere%=" : "=s"(pc_), "=s"(d_)); dst = (const char*)pc_ + d_; } while (0)
DI void touch_code(float& sink, const char* from, const char* to, int i, int lane) {
    const unsigned bytes = (unsigned)(to - from), off = (unsigned)(i * 64 + lane) * 128u;
    if ((unsigned)i * 8192u < bytes) touch1(sink, make_srd(from, bytes), off < bytes - 4 ? off : bytes - 4);
}
DI void touch64(float& sink, srd_t r, unsigned lane128, unsigned soff) { asm volatile("buffer_load_dword %0, %1, %2, %3 offen" : "+v"(sink) : "v"(lane128), "s"(r), "s"(soff)); }
DI void bdma4(srd_t r, unsigned voff, unsigned soff, unsigned lds_dst) {
    asm volatile("s_mov_b32 m0, %3\n\ts_nop 0\n\tbuffer_load_dwordx4 %0, %1, %2 offen lds\n\tbuffer_load_dwordx4 %0, %1, %2 offen offset:1024 lds\n\t"
                 "buffer_load_dwordx4 %0, %1, %2 offen offset:2048 lds\n\tbuffer_load_dwordx4 %0, %1, %2 offen offset:3072 lds" :: "v"(voff), "s"(r), "s"(soff), "s"(lds_dst) : "memory"); }
DI void bdma1(srd_t r, unsigned voff, unsigned soff, unsigned lds_dst) {
    asm volatile("s_mov_b32 m0, %3\n\ts_nop 0\n\tbuffer_load_dwordx4 %0, %1, %2 offen lds" :: "v"(voff), "s"(r), "s"(soff), "s"(lds_dst) : "memory"); }
template <int IMM> DI void bdma_next(srd_t r, unsigned voff, unsigned soff, unsigned lds_dst) {
    asm volatile("s_mov_b32 m0, %3\n\ts_nop 0\n\tbuffer_load_dwordx4 %0, %1, %2 offen offset:%4 lds" :: "v"(voff), "s"(r), "s"(soff), "s"(lds_dst), "n"(IMM) : "memory"); }
template <int POL> DI void bstore16(u32x4 d, srd_t r, unsigned voff, unsigned soff) {
    if constexpr (POL == 1) asm volatile("buffer_store_dwordx4 %0, %1, %2, %3 offen sc1\n\ts_nop 1" :: "v"(d), "v"(voff), "s"(r), "s"(soff) : "memory");
    else if constexpr (POL == 2) asm volatile("buffer_store_dwordx4 %0, %1, %2, %3 offen nt\n\ts_nop 1" :: "v"(d), "v"(voff), "s"(r), "s"(soff) : "memory");
    else asm volatile("buffer_store_dwordx4 %0, %1, %2, %3 offen\n\ts_nop 1" :: "v"(d), "v"(voff), "s"(r), "s"(soff) : "memory"); }
template <int POL> DI void bstore8(u32x2 d, srd_t r, unsigned voff, unsigned soff) {
    if constexpr (POL == 1) asm volatile("buffer_store_dwordx2 %0, %1, %2, %3 offen sc1\n\ts_nop 1" :: "v"(d), "v"(voff), "s"(r), "s"(soff) : "memory");
    else if constexpr (POL == 2) asm volatile("buffer_store_dwordx2 %0, %1, %2, %3 offen nt\n\ts_nop 1" :: "v"(d), "v"(voff), "s"(r), "s"(soff) : "memory");
    else asm volatile("buffer_store_dwordx2 %0, %1, %2, %3 offen\n\ts_nop 1" :: "v"(d), "v"(voff), "s"(r), "s"(soff) : "memory"); }
#define PINV(x) asm volatile("" : "+v"(x))
#define WAITV(n) asm volatile("s_waitcnt vmcnt(%0)" :: "n"(n) : "memory")
#define WAIT_BAR(n) asm volatile("s_waitcnt vmcnt(%0) lgkmcnt(0)\n\ts_barrier" :: "n"(n) : "memory")
#define SBAR() __builtin_amdgcn_sched_barrier(0)
#define LBAR() asm volatile("s_waitcnt lgkmcnt(0)\n\ts_barrier" ::: "memory")
DI bf16x8 lds_frag(LAS const char* p) { return *(LAS const bf16x8*)p; }
typedef short v4i16_t __attribute__((ext_vector_type(4)));
DI u32x2 lds_tr(LAS const char* p) { return __builtin_bit_cast(u32x2, __builtin_amdgcn_ds_read_tr16_b64_v4i16((LAS v4i16_t*)p)); }
DI bf16x8 tr_frag(LAS const char* p, int off2) { u32x2 a = lds_tr(p), b = lds_tr(p + off2); u32x4 r = {a[0], a[1], b[0], b[1]}; return __builtin_bit_cast(bf16x8, r); }

DI int kstep_to_ftile(int ks, int half) {
    const int a = ks / 9, idx = ks - 9 * a;
    if (idx == 8) return 9 * (2 * a + half) + 8;
    return 9 * (2 * a + (idx >> 2)) + 2 * (idx & 3) + half;
}

DI int wo_row(int k) { return k < 512 ? 18 * (k >> 4) + (k & 15) : 18 * ((k - 512) >> 1) + 16 + (k & 1); }
DI void retile_wo(int job, int lane, const char* wo, char* ws, char* scratch_generic) {
    const int s = job / 9, cb = job - 9 * s, r8 = lane >> 3, ch = lane & 7;
#pragma unroll
    for (int p = 0; p < 4; ++p) { const int rho = 8 * p + r8, k = 32 * s + 8 * ((rho >> 2) & 3) + 4 * (rho >> 4) + (rho & 3);
        glds16(wo + cb * 128, wo_row(k) * (D_ * 2) + ch * 16, (unsigned)(uintptr_t)scratch_generic + p * 1024); }
    WAITV(0);
    LAS const char* base = (LAS const char*)scratch_generic + (4 * (lane >> 4) + ((lane >> 2) & 3)) * 128 + (lane & 3) * 8;
#pragma unroll
    for (int u = 0; u < 4; ++u) { const int T = 4 * cb + u, n6 = T / 6, tt = T - 6 * n6;
        *(bf16x8*)(ws + WS_WO + (size_t)(n6 * 108 + s * 6 + tt) * 1024 + lane * 16) = tr_frag(base + 32 * u, 16 * 128); }
}

constexpr int K1_SLOT = 34 * 1024, K1_STG = 0, K1_SMALL = 4 * K1_SLOT, K1_LDS = K1_SMALL + 4096;
template <int PART, int NJ>
DI void k1_wave(const char* x, const char* wqkv, char* ws, char* lds_generic, int mt, int nt, int rot, int shx, int shw) {
    constexpr bool VPART = PART == 2; constexpr int NT = NJ ? 4 : 5, T0 = NJ ? 5 : 0;
    LAS char* lds = (LAS char*)lds_generic; const unsigned ldsb = (unsigned)(uintptr_t)lds_generic;
    const int tid = threadIdx.x, lane = tid & 63, wid = rfl(tid >> 6), mi = wid & 3, m = lane & 15, g = lane >> 4;
    STAMP_DECL; RSTAMP(1, 14); STAMP(1, 0);
    float csink = 0.f;
    if (CODE_PF3 && mi < 2) { const char* c1; CODE_ADDR(c1, preln_ffn_end); touch_code(csink, (const char*)__builtin_amdgcn_s_getpc(), c1, mi, lane); }
    const srd_t sx = make_srd(x, S_ * D_ * 2), sw = make_srd(wqkv, D_ * 3 * D_ * 2);
    const unsigned xv = (lane >> 3) * (D_ * 2) + (((lane & 7) ^ ((4 * wid + (lane >> 4)) & 7)) << 4);
    unsigned wv[2];
#pragma unroll
    for (int i = 0; i < 2; ++i) { const int idx = (i ? 8 : wid) * 64 + lane, rho = (idx * 3641) >> 16, ch = idx - 18 * rho;
        wv[i] = (8 * ((rho >> 2) & 3) + 4 * (rho >> 4) + (rho & 3)) * 3456 + ch * 16; }
    const unsigned xs = (128 * mt + 8 * wid) * (D_ * 2), wsb = nt * 288;
    auto issue = [&](int st, int p) {
        int k = st + rot; k = k >= 9 ? k - 9 : k; const unsigned dst = ldsb + (st & 3) * K1_SLOT;
        if (p < 2) bdma1(sx, xv, xs + p * 64 * (D_ * 2) + k * 128, dst + (wid + 8 * p) * 1024);
        else if (p < 4) bdma1(sw, wv[0], wsb + (64 * k + 32 * (p - 2)) * 3456, dst + 16384 + ((p - 2) * 9 + wid) * 1024);
        else if (wid < 2) bdma1(sw, wv[1], wsb + (64 * k + 32 * wid) * 3456, dst + 16384 + (wid * 9 + 8) * 1024); };
#pragma unroll
    for (int st = 0; st < 2; ++st)
#pragma unroll
        for (int p = 0; p < 5; ++p) issue(st, p);
    float tch[2] = {0.f, 0.f};
#pragma unroll
    for (int i = 0; i < 2 * K1_TOUCH; ++i) { int t = wid + 8 * i; t = t >= 12 ? t - 4 : t;
        if (t < 3) { const int L = shx * 192 + t * 64 + lane, row = (L * 7282) >> 16; bload4<0>(tch[i], sx, (128 * mt + row) * (D_ * 2) + (L - 9 * row) * 128, 0); }
        else { const int q = (t - 3) * 64 + lane; bload4<0>(tch[i], sw, (shw * 144 + (q >> 2)) * 3456 + ((nt * 288) & ~127) + (q & 3) * 128, 0); } }
    unsigned sa[NT][2], sd[NT][2];
#pragma unroll
    for (int t = 0; t < NT; ++t) { const int tt = T0 + t;
        if (VPART) { const int n = 16 * tt + m, hl = (n * 3641) >> 16, d = n - 18 * hl, row = d + (d >= 4 ? 5 : 0) - (d >= 12 ? 12 : 0) + (d >= 16 ? 8 : 0);
            sa[t][0] = K1_STG + hl * 5120 + 2 * mi * 640 + (g >> 1) * 320 + row * 16 + (g & 1) * 8; PINV(sa[t][0]); }
        else {
#pragma unroll
            for (int p = 0; p < 2; ++p) { const int n = 16 * tt + 4 * g + 2 * p, hl = (n * 3641) >> 16, d = n - 18 * hl; const bool tail = d == 16;
                const int o = PART == 0 ? (tail ? 32768 + hl * 512 : hl * 4096 + d * 2) : hl * 4608 + (tail ? 4096 : (d >> 3) * 2048 + (d & 7) * 2);
                const int ts = tail ? 4 : PART == 0 ? 32 : 16;
                sa[t][p] = K1_STG + o + (32 * mi + m) * ts; sd[t][p] = 16 * ts; PINV(sa[t][p]); PINV(sd[t][p]); } } }
    f32x4 acc[NT][2];
    f32x4 accs = {0, 0, 0, 0}, accg = {0, 0, 0, 0}, accc[NT];
#pragma unroll
    for (int t = 0; t < NT; ++t) accc[t] = (f32x4){0, 0, 0, 0};
    bf16x8 ones = __builtin_bit_cast(bf16x8, (u32x4){0x3f803f80u, 0x3f803f80u, 0x3f803f80u, 0x3f803f80u}); PINV(ones);
    asm volatile("s_nop 1" : "+v"(accs), "+v"(accg));
#pragma unroll
    for (int t = 0; t < NT; ++t) PINV(accc[t]);
    const unsigned xa0 = (32 * mi + m) * 128 + ((g ^ ((m >> 1) & 7)) << 4), xa1 = (32 * mi + m) * 128 + (((4 + g) ^ ((m >> 1) & 7)) << 4);
    const unsigned wa = 16384 + (4 * g + ((lane >> 2) & 3)) * 288 + (lane & 3) * 8 + T0 * 32;
    bf16x8 xq[2][2][2], wr[2][2][NT];
    auto rd = [&](int st, int i) {
        LAS const char* sb = lds + (st & 3) * K1_SLOT;
        if (i < 4) xq[st & 1][i >> 1][i & 1] = lds_frag(sb + ((i >> 1) ? xa1 : xa0) + (i & 1) * 2048);
        else { const int s2 = (i - 4) / NT, t = (i - 4) % NT; wr[st & 1][s2][t] = tr_frag(sb + wa + s2 * 9216 + 32 * t, 16 * 288); } };
    STAMP(1, 1);
    WAIT_BAR(4 + K1_TOUCH * 2);
    PINV(csink);
    STAMP(1, 2);
#pragma unroll
    for (int i = 0; i < 4 + 2 * NT; ++i) rd(0, i);
#pragma unroll
    for (int p = 0; p < 5; ++p) issue(2, p);
#pragma unroll
    for (int st = 0; st < 9; ++st) {
        if (st < 8) { if (st < 2) WAIT_BAR(4 + K1_TOUCH * 2); else if (st < 6) WAIT_BAR(4); else if (st == 6) WAIT_BAR(4); else WAIT_BAR(0); }
        if (st == 2) { PINV(tch[0]); PINV(tch[1]); }
#pragma unroll
        for (int i = 0; i < 2 * NT; ++i) { const int s2 = i / NT, t = i % NT; const bf16x8 w = wr[st & 1][s2][t], x0 = xq[st & 1][s2][0], x1 = xq[st & 1][s2][1];
            if (st == 0 && s2 == 0) { if (VPART) { mfma16_new(acc[t][0], x0, w); mfma16_new(acc[t][1], x1, w); } else { mfma16_new(acc[t][0], w, x0); mfma16_new(acc[t][1], w, x1); } }
            else { if (VPART) { mfma16_acc(acc[t][0], x0, w); mfma16_acc(acc[t][1], x1, w); } else { mfma16_acc(acc[t][0], w, x0); mfma16_acc(acc[t][1], w, x1); } }
            if (st + 1 < 9) { if (i < 4) { rd(st + 1, i); rd(st + 1, 4 + i); } else if (4 + i < 4 + 2 * NT) rd(st + 1, 4 + i); }
            if (st + 3 < 9 && (i & 1) == 0 && i / 2 < 4) issue(st + 3, i / 2 == 0 ? 0 : i / 2 == 1 ? 2 : i / 2 == 2 ? 1 : 3);
            if (st + 3 < 9 && i == 7) issue(st + 3, 4);
            if (t == 1) mfma16_acc(accs, ones, NJ ? x1 : x0);
            if (t == 2) mfma16_acc(accg, NJ ? x1 : x0, NJ ? x1 : x0);
            if (t == NT - 1 && ((2 * st + s2) & 3) == mi) {
#pragma unroll
                for (int tt = 0; tt < NT; ++tt) { if (VPART) mfma16_acc(accc[tt], ones, wr[st & 1][s2][tt]); else mfma16_acc(accc[tt], wr[st & 1][s2][tt], ones); } }
            SBAR(); }
    }
    STAMP(1, 3);
    LAS float* sm = (LAS float*)(lds + K1_SMALL);
    asm volatile("s_nop 7\n\ts_nop 7" : "+v"(acc[NT - 1][0]), "+v"(acc[NT - 1][1]), "+v"(accs), "+v"(accg));
#pragma unroll
    for (int t = 0; t < NT; ++t) { PINV(accc[t]); PINV(acc[t][0]); PINV(acc[t][1]); }
    if (g == 0) sm[32 * mi + 16 * NJ + m] = accs[0];
    if (g == (m >> 2)) sm[128 + 32 * mi + 16 * NJ + m] = (m & 2) ? ((m & 1) ? accg[3] : accg[2]) : ((m & 1) ? accg[1] : accg[0]);
#pragma unroll
    for (int t = 0; t < NT; ++t) { if (VPART) { if (g == 0) sm[256 + mi * 144 + 16 * (T0 + t) + m] = accc[t][0]; }
        else if (m == 0) *(LAS f32x4*)(sm + 256 + mi * 144 + 16 * (T0 + t) + 4 * g) = accc[t]; }
    LBAR();
    STAMP(1, 4);
    LAS char* stg = lds + K1_STG;
    if (VPART && tid < 256) { const int hs = tid >> 1;
        *(LAS u32x4*)(stg + hs * 320 + ((tid & 1) ? 19 : 4) * 16) = (tid & 1) ? (u32x4){0x3f803f80u, 0x3f803f80u, 0x3f803f80u, 0x3f803f80u} : (u32x4){0, 0, 0, 0}; }
    f32x4 mean[2], rstd[2];
#pragma unroll
    for (int u = 0; u < 2; ++u) { const int tl = 32 * mi + 16 * u + (VPART ? 4 * g : m);
        if (VPART) { mean[u] = *(LAS const f32x4*)(sm + tl) * (1.f / D_);
            const f32x4 var = *(LAS const f32x4*)(sm + 128 + tl) * (1.f / D_) - mean[u] * mean[u];
#pragma unroll
            for (int r = 0; r < 4; ++r) rstd[u][r] = __builtin_amdgcn_rsqf(fmaxf(var[r], 0.f) + EPS); }
        else { mean[u][0] = sm[tl] * (1.f / D_);
            rstd[u][0] = __builtin_amdgcn_rsqf(fmaxf(sm[128 + tl] * (1.f / D_) - mean[u][0] * mean[u][0], 0.f) + EPS) * (PART == 0 ? QSCALE : 1.f); } }
    float c1[NT]; f32x4 c4[NT];
#pragma unroll
    for (int t = 0; t < NT; ++t) { const int tt = T0 + t;
        if (VPART) { const int n = 16 * tt + m; c1[t] = (sm[256 + n] + sm[400 + n]) + (sm[544 + n] + sm[688 + n]); }
        else { const int n0 = 16 * tt + 4 * g;
            c4[t] = (*(LAS const f32x4*)(sm + 256 + n0) + *(LAS const f32x4*)(sm + 400 + n0)) + (*(LAS const f32x4*)(sm + 544 + n0) + *(LAS const f32x4*)(sm + 688 + n0)); } }
#pragma unroll
    for (int t = 0; t < NT; ++t)
#pragma unroll
        for (int u = 0; u < 2; ++u) {
            if (VPART) { const f32x4 a = (acc[t][u] - mean[u] * c1[t]) * rstd[u];
                *(LAS u32x2*)(lds + sa[t][0] + u * 640) = (u32x2){cvtpk(a[0], a[1]), cvtpk(a[2], a[3])}; }
            else { const f32x4 a = (acc[t][u] - c4[t] * mean[u][0]) * rstd[u][0];
                *(LAS unsigned*)(lds + sa[t][0] + (u ? sd[t][0] : 0u)) = cvtpk(a[0], a[1]); *(LAS unsigned*)(lds + sa[t][1] + (u ? sd[t][1] : 0u)) = cvtpk(a[2], a[3]); } }
    if (nt == 0 && tid < 128) ((float*)(ws + WS_MUX))[128 * mt + tid] = sm[tid] * (1.f / D_);
    LBAR();
    STAMP(1, 5);
    const int head = 8 * (nt & 3) + wid; const srd_t sws = make_srd(ws, (unsigned)WS_END); const unsigned l16 = lane * 16;
    if (PART == 2) { const unsigned dst = (unsigned)WS_KV + head * KVIMG + (mt >> 1) * KVCHUNK + 9216 + (mt & 1) * 5120;
#pragma unroll
        for (int i = 0; i < 5; ++i) bstore16<QKV_POL>(*(LAS const u32x4*)(stg + wid * 5120 + i * 1024 + l16), sws, l16, dst + i * 1024);
    } else if (PART == 0) { const unsigned dst = (unsigned)WS_QM + (head * S_ + 128 * mt) * 32;
#pragma unroll
        for (int i = 0; i < 4; ++i) bstore16<QKV_POL>(*(LAS const u32x4*)(stg + wid * 4096 + i * 1024 + l16), sws, l16, dst + i * 1024);
        if (lane < 32) bstore16<QKV_POL>(*(LAS const u32x4*)(stg + 32768 + wid * 512 + l16), sws, l16, (unsigned)WS_QT + (head * S_ + 128 * mt) * 4);
    } else { const unsigned dst = (unsigned)WS_KV + head * KVIMG + (mt >> 1) * KVCHUNK; LAS const char* hb = stg + wid * 4608;
#pragma unroll
        for (int i = 0; i < 4; ++i) bstore16<QKV_POL>(*(LAS const u32x4*)(hb + i * 1024 + l16), sws, l16, dst + (i >> 1) * 4096 + (mt & 1) * 2048 + (i & 1) * 1024);
        if (lane < 32) bstore16<QKV_POL>(*(LAS const u32x4*)(hb + 4096 + l16), sws, l16, dst + 8192 + (mt & 1) * 512);
        float mx = 0.f;
#pragma unroll
        for (int i = 0; i < 2; ++i) { const int tl = lane + 64 * i;
            const u32x4 a = *(LAS const u32x4*)(hb + tl * 16), bq = *(LAS const u32x4*)(hb + 2048 + tl * 16); const unsigned c = *(LAS const unsigned*)(hb + 4096 + tl * 4);
            float s2 = dot2(c, c, 0.f);
#pragma unroll
            for (int e = 0; e < 4; ++e) { s2 = dot2(a[e], a[e], s2); s2 = dot2(bq[e], bq[e], s2); }
            mx = fmaxf(mx, s2); }
        mx = fmaxf(mx, xor16(mx)); mx = fmaxf(mx, xor32(mx));
#pragma unroll
        for (int o = 1; o < 16; o <<= 1) mx = fmaxf(mx, __shfl_xor(mx, o));
        if (lane == 0) ((float*)(ws + WS_KMAX))[head * 16 + mt] = mx;
    }
    STAMP(1, 6); RSTAMP(1, 15); STAMP_FLUSH(1);
}
template <int PART> DI void k1_body(const char* x, const char* wqkv, char* ws, char* lds, int mt, int nt, int rot, int shx, int shw) {
    if (threadIdx.x >= 256) k1_wave<PART, 1>(x, wqkv, ws, lds, mt, nt, rot, shx, shw); else k1_wave<PART, 0>(x, wqkv, ws, lds, mt, nt, rot, shx, shw);
}
__global__ __launch_bounds__(512, 2) void k1_qkv(const char* x, const char* wqkv, const char* wo, char* ws) {
    extern __shared__ __attribute__((aligned(16))) char lds[];
    const int b = blockIdx.x;
    if (b >= 192) {
        const int lane = threadIdx.x & 63, wv = rfl(threadIdx.x >> 6), gw = (b - 192) * 8 + wv;
        float sink[4] = {0.f, 0.f, 0.f, 0.f};
        if (CODE_PF && b - 192 < 8) { const char *c0, *c1; CODE_ADDR(c0, preln_k2_begin); CODE_ADDR(c1, preln_ffn_end); touch_code(sink[0], c0, c1, wv + 8 * (b - 192), lane); }
        if (threadIdx.x == 511) ((unsigned*)(ws + WS_CNT))[(b - 192) * 64] = 0u;
        if (threadIdx.x == 510 && b - 192 < 40) ((unsigned*)(ws + WS_CNT))[(64 + b - 192) * 64] = 0u;
        if (gw < 162) retile_wo(gw, lane, wo, ws, lds + wv * 4096);
        for (int c = gw; c < D_; c += 512) {
            const char* row = wo + (size_t)wo_row(c) * (D_ * 2);
            const u32x4 a = *(const u32x4*)(row + lane * 16); float s = 0.f;
#pragma unroll
            for (int e = 0; e < 4; ++e) s = dot2(a[e], 0x3f803f80u, s);
            if (lane < 8) { const u32x4 bq = *(const u32x4*)(row + 1024 + lane * 16);
#pragma unroll
                for (int e = 0; e < 4; ++e) s = dot2(bq[e], 0x3f803f80u, s); }
#pragma unroll
            for (int o = 1; o < 64; o <<= 1) s += __shfl_xor(s, o);
            if (lane == 0) { const float w = s * (1.f / D_); const unsigned hi = cvtpk(w, 0.f) & 0xffffu; const unsigned lo = cvtpk(w - bflo(hi), 0.f) & 0xffffu;
                ((unsigned short*)(ws + WS_WBAR))[c] = (unsigned short)hi; ((unsigned short*)(ws + WS_WBAR))[D_ + c] = (unsigned short)lo; }
        }
        asm volatile("s_waitcnt vmcnt(0)" : "+v"(sink[0]), "+v"(sink[1]), "+v"(sink[2]), "+v"(sink[3]));
        return;
    }
    const int xcd = b & 7, idx = b >> 3, mt = (xcd & 3) * 4 + (idx & 3), nt = (xcd >> 2) * 6 + (idx >> 2);
    const int rot = K1_ROT ? (2 * (idx & 3) + ((idx >> 2) * 3 >> 1)) % 9 : 0;
    const int shx = idx >> 2, shw = idx & 3;
    if (nt >= 8) k1_body<2>(x, wqkv, ws, lds, mt, nt, rot, shx, shw); else if (nt >= 4) k1_body<1>(x, wqkv, ws, lds, mt, nt, rot, shx, shw); else k1_body<0>(x, wqkv, ws, lds, mt, nt, rot, shx, shw);
}

constexpr int K2_LDS = 160 * 1024;
struct RtJob { unsigned so[4], dst[4]; };
struct RtTable { RtJob j[8][486];
    constexpr RtTable() : j{} {
        for (int xg = 0; xg < 8; ++xg) for (int slot = 0; slot < 486; ++slot) { RtJob& e = j[xg][slot];
            if (slot < 324) { const int gu = slot >= 162, r = slot - 162 * gu, ks = r / 9, cb = 9 * xg + r % 9;
                for (int p = 0; p < 4; ++p) { const int T = 4 * cb + p, jj = T / 9, tt = T % 9;
                    e.so[p] = (ks * 32 + p * 8) * (F_ * 2) + cb * 128;
                    e.dst[p] = (unsigned)WS_W1 + (jj * 324 + (tt < 4 ? ks * 8 + tt * 2 : tt < 8 ? (tt >> 1) * 72 + ks * 4 + (tt & 1) * 2 : 288 + ks * 2) + gu) * 1024; }
            } else { const int r2 = slot - 324, q = r2 / 9, cb = r2 % 9, ks = 18 * xg + q, a = ks / 9, qi = ks % 9;
                const int t0 = qi == 8 ? 18 * a + 8 : 18 * a + 9 * (qi >> 2) + 2 * (qi & 3), t1 = qi == 8 ? t0 + 9 : t0 + 1;
                for (int p = 0; p < 4; ++p) { const int T = 4 * cb + p, n4 = T / 9, tt = T % 9;
                    e.so[p] = (16 * (p >> 1 ? t1 : t0) + 8 * (p & 1)) * (D_ * 2) + cb * 128;
                    e.dst[p] = (unsigned)WS_W2 + (((xg >> 1) * 4 + n4) * 324 + (((q >> 1) * 2 + (xg & 1)) * 2 + (q & 1)) * 9 + tt) * 1024; } } }
    } };
__device__ const RtTable rt_table{};
#define EXPS(S, a, b) _Pragma("unroll") for (int r_ = a; r_ < b; ++r_) S[r_] = __builtin_amdgcn_exp2f(S[r_]); SBAR()
#define PLO(S) __builtin_bit_cast(bf16x16, (f32x8)__builtin_shufflevector(S, S, 0, 1, 2, 3, 4, 5, 6, 7))
#define PHI(S) __builtin_bit_cast(bf16x16, (f32x8)__builtin_shufflevector(S, S, 8, 9, 10, 11, 12, 13, 14, 15))
#define KTAIL(p) __builtin_bit_cast(s16x4, (u32x2){*(LAS const unsigned*)(p), kc})
#define QK1(q) __builtin_amdgcn_mfma_f32_32x32x16_bf16(kf, q, (f32x16){}, 0, 0, 0)
#define QK2(q, s) __builtin_amdgcn_mfma_f32_32x32x8bf16_1k(kt, q, s, 0, 0, 0)
#define PV(v, p, o) __builtin_amdgcn_smfmac_f32_32x32x32_bf16(v, p, o, 0xDDDD, 0, 0)
template <class Hook>
DI void attn_fast(LAS const char* kp, LAS const char* tp, LAS const char* vp, const bf16x8 (&qm)[2], const s16x4 (&qt)[2], f32x16 (&o)[2], unsigned kc, Hook hook) {
    bf16x8 kf = *(LAS const bf16x8*)kp; s16x4 kt = KTAIL(tp); bf16x8 v0, v1;
    f32x16 s0 = QK1(qm[0]), s1; s0 = QK2(qt[0], s0);
    SBAR();
#pragma unroll
    for (int it = 0; it < 8; ++it) {
        if (K2_PRIO) { if (it == 0) __builtin_amdgcn_s_setprio(3); if (it == 2) __builtin_amdgcn_s_setprio(2); if (it == 4) __builtin_amdgcn_s_setprio(1); if (it == 6) __builtin_amdgcn_s_setprio(0); }
        EXPS(s0, 0, 2);
        if (it > 0) { o[1] = PV(v0, PLO(s1), o[1]); SBAR(); }
        EXPS(s0, 2, 6);
        if (it > 0) { o[1] = PV(v1, PHI(s1), o[1]); SBAR(); }
        v0 = *(LAS const bf16x8*)(vp + it * 1280); v1 = *(LAS const bf16x8*)(vp + it * 1280 + 640); SBAR();
        hook(it);
        EXPS(s0, 6, 9);
        s1 = QK1(qm[1]); SBAR();
        EXPS(s0, 9, 12);
        s1 = QK2(qt[1], s1); SBAR();
        if (it + 1 < 8) { kf = *(LAS const bf16x8*)(kp + (it + 1) * 512); kt = KTAIL(tp + (it + 1) * 128); SBAR(); }
        EXPS(s0, 12, 16);
        EXPS(s1, 0, 2);
        o[0] = PV(v0, PLO(s0), o[0]); SBAR();
        EXPS(s1, 2, 6);
        o[0] = PV(v1, PHI(s0), o[0]); SBAR();
        EXPS(s1, 6, 9);
        if (it + 1 < 8) { s0 = QK1(qm[0]); SBAR(); }
        EXPS(s1, 9, 12);
        if (it + 1 < 8) { s0 = QK2(qt[0], s0); SBAR(); }
        EXPS(s1, 12, 16);
    }
    o[1] = PV(v0, PLO(s1), o[1]); o[1] = PV(v1, PHI(s1), o[1]);
}
DI void attn_slow(LAS const char* kp, LAS const char* tp, LAS const char* vp, const bf16x8 (&qm)[2], const s16x4 (&qt)[2], f32x16 (&o)[2], float (&mrun)[2], unsigned kc) {
#pragma unroll 1
    for (int it = 0; it < 8; ++it) {
        const bf16x8 kf = *(LAS const bf16x8*)(kp + it * 512); const s16x4 kt = KTAIL(tp + it * 128);
        const bf16x8 v0 = *(LAS const bf16x8*)(vp + it * 1280), v1 = *(LAS const bf16x8*)(vp + it * 1280 + 640);
#pragma unroll
        for (int u = 0; u < 2; ++u) {
            f32x16 s = QK1(qm[u]); s = QK2(qt[u], s);
            float mx = s[0];
#pragma unroll
            for (int r = 1; r < 16; ++r) mx = fmaxf(mx, s[r]);
            mx = fmaxf(mx, xor32(mx));
            const float mn = fmaxf(mrun[u], mx), al = __builtin_amdgcn_exp2f(mrun[u] - mn);
            mrun[u] = mn;
#pragma unroll
            for (int r = 0; r < 16; ++r) { s[r] = __builtin_amdgcn_exp2f(s[r] - mn); o[u][r] *= al; }
            o[u] = PV(v0, PLO(s), o[u]); o[u] = PV(v1, PHI(s), o[u]);
        }
    }
}
__global__ __launch_bounds__(1024, 4) void k2_attn(const char* x, const char* wg, const char* wu, const char* wd, char* ws) {
    extern __shared__ __attribute__((aligned(16))) char lds_generic[];
    LAS char* lds = (LAS char*)lds_generic; const unsigned ldsb = (unsigned)(uintptr_t)lds_generic;
    const int tid = threadIdx.x, lane = tid & 63, wid = rfl(tid >> 6), qg = wid & 3, kq = wid >> 2, r32 = lane & 31, h = lane >> 5;
    CODE_LABEL(preln_k2_begin);
    const int b = blockIdx.x, xcd = b & 7, idx = b >> 3, head = xcd * 4 + (idx & 3), qb = idx >> 2;
    STAMP_DECL; RSTAMP(2, 14); STAMP(2, 0);
    const srd_t srd = make_srd(ws, (unsigned)WS_END);
    const int c0 = (2 * kq + (qb & 1)) * KVCHUNK, c1 = (2 * kq + ((qb & 1) ^ 1)) * KVCHUNK;
    const unsigned kvb = (unsigned)WS_KV + head * KVIMG, l16 = lane * 16;
    const int q0 = 256 * qb + 64 * qg;
    bf16x8 qm[2]; float qtl[2]; float km;
    { const unsigned so = (unsigned)WS_QM + (head * S_ + q0) * 32, vo = r32 * 32 + h * 16; bload16<0>(qm[0], srd, vo, so); bload16<1024>(qm[1], srd, vo, so);
      const unsigned st = (unsigned)WS_QT + (head * S_ + q0) * 4; bload4<0>(qtl[0], srd, r32 * 4, st); bload4<128>(qtl[1], srd, r32 * 4, st);
      bload4<0>(km, srd, (lane & 15) * 4, (unsigned)WS_KMAX + head * 64); }
    bdma4(srd, l16, kvb + c0 + qg * 5120, ldsb + c0 + qg * 5120); if (qg < 3) bdma1(srd, l16, kvb + c0 + qg * 5120 + 4096, ldsb + c0 + qg * 5120 + 4096);
    const int slot = wid * 32 + idx; const bool rt = slot < (K2_RT ? 486 : 0);
    bf16x8 wb[4]; unsigned rdst[4], rso[4];
    { const RtJob& J = rt_table.j[xcd][rt ? slot : 0];
#pragma unroll
      for (int p = 0; p < 4; ++p) { rso[p] = J.so[p]; rdst[p] = J.dst[p]; } }
    const char* rsrc = slot < 162 ? wg : slot < 324 ? wu : wd;
    const unsigned rvo = (lane >> 3) * (slot < 324 ? F_ * 2 : D_ * 2) + (lane & 7) * 16;
    asm volatile("s_waitcnt vmcnt(4)" : "+v"(qm[0]), "+v"(qm[1]), "+v"(qtl[0]), "+v"(qtl[1]), "+v"(km) :: "memory");
    km = fmaxf(km, row_ror<8>(km)); km = fmaxf(km, row_ror<4>(km)); km = fmaxf(km, row_ror<2>(km)); km = fmaxf(km, row_ror<1>(km));
    s16x4 qt[2]; bool unsafe = false;
#pragma unroll
    for (int u = 0; u < 2; ++u) { const u32x4 qv = __builtin_bit_cast(u32x4, qm[u]); const unsigned tl = __float_as_uint(qtl[u]);
        float n2 = h ? 0.f : dot2(tl, tl, 0.f);
#pragma unroll
        for (int e = 0; e < 4; ++e) n2 = dot2(qv[e], qv[e], n2);
        n2 += xor32(n2);
        const float bound = __builtin_amdgcn_sqrtf(n2 * km) * 1.0001f + 1e-6f;
        unsafe |= !(bound <= SAFE_B);
        const unsigned cb = (__float_as_uint(bound) + 0xffffu) >> 16;
        const u32x2 w = h ? (u32x2){0u, 0u} : (u32x2){tl, cb | 0x8000u};
        qt[u] = __builtin_bit_cast(s16x4, w); }
    const bool slow = __any(unsafe);
    if (slow) {
#pragma unroll
        for (int u = 0; u < 2; ++u) { u32x2 w = __builtin_bit_cast(u32x2, qt[u]); w[1] = 0u; qt[u] = __builtin_bit_cast(s16x4, w); } }
    f32x16 o[2] = {(f32x16){}, (f32x16){}}; float mrun[2] = {-INFINITY, -INFINITY};
    const int vrow = r32 >= 19 ? 4 : r32 + (r32 >= 4 ? 5 : 0) - (r32 >= 12 ? 12 : 0) + (r32 >= 16 ? 8 : 0);
    const unsigned kc = h ? 0u : 0x00003f80u;
    unsigned kp = ldsb + c0 + h * 4096 + r32 * 16, tp = ldsb + c0 + 8192 + r32 * 4, vp = ldsb + c0 + 9216 + h * 320 + vrow * 16;
#define KP(o) ((LAS const char*)(uintptr_t)kp + (o))
#define TP(o) ((LAS const char*)(uintptr_t)tp + (o))
#define VP(o) ((LAS const char*)(uintptr_t)vp + (o))
    STAMP(2, 1);
    WAIT_BAR(0);
    STAMP(2, 2);
    const srd_t sw = make_srd(rsrc, D_ * F_ * 2);
    auto chunk1 = [&](int part) { if (part == 0) bdma4(srd, l16, kvb + c1 + qg * 5120, ldsb + c1 + qg * 5120);
                                  else if (qg < 3) bdma1(srd, l16, kvb + c1 + qg * 5120 + 4096, ldsb + c1 + qg * 5120 + 4096); };
    STAMP(2, 8);
    if (slow) { chunk1(0); chunk1(1);
        if (rt) {
#pragma unroll
            for (int p = 0; p < 4; ++p) bload16<0>(wb[p], sw, rvo, rso[p]); }
        attn_slow(KP(0), TP(0), VP(0), qm, qt, o, mrun, kc); }
    else attn_fast(KP(0), TP(0), VP(0), qm, qt, o, kc, [&](int it) {
        if (it < 2) { chunk1(it); SBAR(); }
        if (it >= 2 && it < 6 && rt) { bload16<0>(wb[it - 2], sw, rvo, rso[it - 2]); SBAR(); } });
    STAMP(2, 3);
    if (rt) WAIT_BAR(4); else WAIT_BAR(0);
    STAMP(2, 4);
    if (rt) { asm volatile("s_waitcnt vmcnt(0)" : "+v"(wb[0]), "+v"(wb[1]), "+v"(wb[2]), "+v"(wb[3]) :: "memory");
        int ty = threadIdx.x; asm volatile("" : "+v"(ty)); LAS char* scr = lds + c0 + qg * 4864 + (ty & 63) * 16;
#pragma unroll
        for (int p = 0; p < 4; ++p) *(LAS bf16x8*)(scr + p * 1024) = wb[p]; }
    auto rt_step = [&](int it) {
        if (it < 1 || it > 4 || !rt) return;
        int ty = threadIdx.x; asm volatile("" : "+v"(ty)); const int ln = ty & 63;
        LAS const char* base = lds + c0 + qg * 4864 + (4 * (ln >> 4) + ((ln >> 2) & 3)) * 128 + (ln & 3) * 8;
        bstore16<RT_POL>(__builtin_bit_cast(u32x4, tr_frag(base + 32 * (it - 1), 16 * 128)), srd, ln * 16, rdst[it - 1]);
        SBAR(); };
    STAMP(2, 5);
    { const int dc = c1 - c0; kp += dc; tp += dc; vp += dc; PINV(kp); PINV(tp); PINV(vp); }
    if (slow) {
#pragma unroll
        for (int it = 1; it < 5; ++it) rt_step(it);
        attn_slow(KP(0), TP(0), VP(0), qm, qt, o, mrun, kc); }
    else attn_fast(KP(0), TP(0), VP(0), qm, qt, o, kc, rt_step);
    STAMP(2, 6);
    int tz = threadIdx.x; asm volatile("" : "+v"(tz));
    const int lane2 = tz & 63, r2 = tz & 31, h2 = (tz >> 5) & 1;
    LAS float* mx = (LAS float*)(lds + KVIMG);
#pragma unroll
    for (int u = 0; u < 2; ++u) { LAS char* pb = lds + c0 + (qg * 2 + u) * 2432;
        *(LAS f32x4*)(pb + lane2 * 16) = (f32x4){o[u][0], o[u][1], o[u][2], o[u][3]};
        *(LAS f32x4*)(pb + 1024 + lane2 * 16) = (f32x4){o[u][4], o[u][5], o[u][6], o[u][7]};
        if (h2 == 0) { *(LAS f32x2*)(pb + 2048 + r2 * 8) = (f32x2){o[u][8], o[u][9]}; *(LAS float*)(pb + 2304 + r2 * 4) = o[u][10];
                      if (slow) mx[wid * 64 + 32 * u + r2] = mrun[u]; } }
    STAMP(2, 9);
    LBAR();
    STAMP(2, 10);
    { const int u = kq & 1, hf = kq >> 1;
      LAS const char* pb = lds + (qb & 1) * KVCHUNK + (qg * 2 + u) * 2432;
      float l = 0.f; f32x4 a = {0, 0, 0, 0}; f32x2 c = {0, 0};
      if (slow) { float mk[4], M = -INFINITY;
#pragma unroll
          for (int k = 0; k < 4; ++k) { mk[k] = mx[(4 * k + qg) * 64 + 32 * u + r2]; M = fmaxf(M, mk[k]); }
#pragma unroll
          for (int k = 0; k < 4; ++k) { LAS const char* p = pb + k * 2 * KVCHUNK; const float f = __builtin_amdgcn_exp2f(mk[k] - M);
              l += f * *(LAS const float*)(p + 2304 + r2 * 4); a += *(LAS const f32x4*)(p + hf * 1024 + lane2 * 16) * f; c += *(LAS const f32x2*)(p + 2048 + r2 * 8) * f; }
      } else {
#pragma unroll
          for (int k = 0; k < 4; ++k) { LAS const char* p = pb + k * 2 * KVCHUNK;
              l += *(LAS const float*)(p + 2304 + r2 * 4); a += *(LAS const f32x4*)(p + hf * 1024 + lane2 * 16); c += *(LAS const f32x2*)(p + 2048 + r2 * 8); } }
      const float rl = __builtin_amdgcn_rcpf(l);
      const int token = q0 + 32 * u + r2; char* ob = ws + WS_OF + (size_t)(token >> 4) * 18 * 1024 + (token & 15) * 16;
      *(u32x2*)(ob + (head >> 1) * 1024 + (2 * (head & 1) + hf) * 256 + h2 * 8) = (u32x2){cvtpk(a[0] * rl, a[1] * rl), cvtpk(a[2] * rl, a[3] * rl)};
      if (hf == 0 && h2 == 0) *(unsigned*)(ob + (16 + (head >> 4)) * 1024 + ((head >> 2) & 3) * 256 + (head & 3) * 4) = cvtpk(c[0] * rl, c[1] * rl);
    }
    STAMP(2, 7); RSTAMP(2, 15); STAMP_FLUSH(2);
}

constexpr int RING_SLOT = 36 * 1024, RING0 = 16 * 1024, G_LDS = RING0 + 4 * RING_SLOT;
constexpr int K3_X = 108 * 1024, K3_WB = K3_X + 12 * 1024, K3_Z = K3_WB + 2304;
DI void ring_w1(char* ws, char* lds_generic, int xcd, int idx, int st);
DI void k3_phase(const char* x, char* ws, char* lds_generic, int xcd, int idx) {
    LAS char* lds = (LAS char*)lds_generic; const unsigned ldsb = (unsigned)(uintptr_t)lds_generic;
    const int tid = threadIdx.x, lane = tid & 63, wid = rfl(tid >> 6), mi = wid & 3, kj = wid >> 2, m = lane & 15, g = lane >> 4;
    const int mt32 = xcd * 4 + (idx & 3), n6 = idx >> 2, mt16 = 4 * mt32 + mi;
    STAMP_DECL; RSTAMP(3, 14); STAMP(3, 0);
    const srd_t srd = make_srd(ws, (unsigned)WS_END), sx = make_srd(x, S_ * D_ * 2);
    const unsigned l16 = lane * 16, wo0 = (unsigned)WS_WO + n6 * (108 * 1024), ob = (unsigned)WS_OF + (mt16 * 18 + 9 * kj) * 1024;
    bf16x8 of[9];
    { const unsigned f0 = (54 * kj + 6 * mi) * 1024;
      bload16<0>(of[0], srd, l16, ob); bload16<1024>(of[1], srd, l16, ob); bload16<2048>(of[2], srd, l16, ob); bload16<3072>(of[3], srd, l16, ob);
      bdma4(srd, l16, wo0 + f0, ldsb + f0); bdma1(srd, l16, wo0 + f0 + 4096, ldsb + f0 + 4096); bdma1(srd, l16, wo0 + f0 + 5120, ldsb + f0 + 5120); }
    if (tid < 37) *(LAS u32x4*)(lds + K3_Z + tid * 16) = (u32x4){0, 0, 0, 0};
    f32x4 acc[6], accw = {0, 0, 0, 0}; float mux;
    asm volatile("s_nop 1" : "+v"(accw));
    LAS const char* ab = lds + kj * (54 * 1024) + lane * 16;
    LAS const char* wbp = lds + (m < 2 ? K3_WB + m * (D_ * 2) + kj * 576 + g * 16 : K3_Z);
    bf16x8 wr[2][6];
    auto rd = [&](int s) {
#pragma unroll
        for (int t = 0; t < 6; ++t) wr[s & 1][t] = lds_frag(ab + (s * 6 + t) * 1024);
        SBAR(); };
    auto step = [&](int s) { PINV(of[s]);
#pragma unroll
        for (int t = 0; t < 6; ++t) { if (s == 0) mfma16_new(acc[t], wr[s & 1][t], of[s]); else mfma16_acc(acc[t], wr[s & 1][t], of[s]); }
        SBAR(); };
    STAMP(3, 1);
    WAIT_BAR(0);
    STAMP(3, 2);
    { const unsigned f1 = (54 * kj + 24 + (mi < 3 ? 8 * mi : 22)) * 1024;
      rd(0); rd(1);
      bload16<0>(of[4], srd, l16, ob + 4096); bload16<1024>(of[5], srd, l16, ob + 4096); bdma4(srd, l16, wo0 + f1, ldsb + f1);
      step(0); rd(2);
      bload16<2048>(of[6], srd, l16, ob + 4096); bload16<3072>(of[7], srd, l16, ob + 4096); bdma4(srd, l16, wo0 + f1 + 4096, ldsb + f1 + 4096);
      step(1); rd(3);
      bload16<0>(of[8], srd, l16, ob + 8192);
#pragma unroll
      for (int i = 0; i < 2; ++i) { int pc = wid + 8 * i; pc = pc > 11 ? 11 : pc; const int q = pc * 64 + lane, row = (q * 5462) >> 16;
          bdma1(sx, row * (D_ * 2) + (q - 12 * row) * 16, (64 * mt32) * (D_ * 2) + n6 * 192, ldsb + K3_X + pc * 1024); }
      if (wid < 2) bdma1(srd, l16, (unsigned)WS_WBAR + wid * 1024, ldsb + K3_WB + wid * 1024);
      if (wid == 2 && lane < 16) bdma1(srd, l16, (unsigned)WS_WBAR + 2048, ldsb + K3_WB + 2048);
      bload4<0>(mux, srd, m * 4, (unsigned)WS_MUX + mt16 * 64);
      step(2);
      ring_w1(ws, lds_generic, xcd, idx, 0);
      step(3); }
    STAMP(3, 3);
    WAIT_BAR(0);
    STAMP(3, 4);
    bf16x8 wbf[9];
    rd(4); rd(5);
#pragma unroll
    for (int s = 0; s < 9; ++s) wbf[s] = lds_frag(wbp + s * 64);
    SBAR();
    step(4); rd(6); step(5); rd(7); step(6); rd(8); step(7); step(8);
#pragma unroll
    for (int s = 0; s < 9; ++s) mfma16_acc(accw, wbf[s], of[s]);
    PINV(mux);
    const int token = 16 * mt16 + m;
    u32x2 xr[4];
#pragma unroll
    for (int t = 0; t < 4; ++t) xr[t] = *(LAS const u32x2*)(lds + K3_X + (16 * mi + m) * 192 + (4 * kj + (kj ? (t & 1) : t)) * 32 + g * 8);
    asm volatile("s_nop 7" : "+v"(acc[5]), "+v"(acc[4]), "+v"(accw));
    STAMP(3, 5);
    LBAR();
    LAS char* ex = lds + mi * 8192 + lane * 16;
    if (kj == 0) { *(LAS f32x4*)(ex + 4 * 1024) = acc[4]; *(LAS f32x4*)(ex + 5 * 1024) = acc[5]; *(LAS f32x4*)(ex + 6 * 1024) = accw; }
    else {
#pragma unroll
        for (int t = 0; t < 4; ++t) *(LAS f32x4*)(ex + t * 1024) = acc[t];
        *(LAS f32x4*)(ex + 7 * 1024) = accw; }
    LBAR();
    accw += *(LAS const f32x4*)(ex + (kj ? 6 : 7) * 1024);
    const float mu = mux + __shfl(accw[0] + accw[1], m);
    float ssq = 0.f;
    auto fin = [&](int t, u32x2 xv, unsigned (&pk)[2]) {
        const f32x4 a = acc[t] + *(LAS const f32x4*)(ex + t * 1024);
        const unsigned h0 = cvtpk(a[0] + bflo(xv[0]), a[1] + bfhi(xv[0])), h1 = cvtpk(a[2] + bflo(xv[1]), a[3] + bfhi(xv[1]));
        pk[0] = cvtpk(bflo(h0) - mu, bfhi(h0) - mu); pk[1] = cvtpk(bflo(h1) - mu, bfhi(h1) - mu);
        ssq = dot2(pk[0], pk[0], ssq); ssq = dot2(pk[1], pk[1], ssq); };
    auto pair = [&](int t, u32x2 xa, u32x2 xb2) { unsigned p0[2], p1[2]; fin(t, xa, p0); fin(t + 1, xb2, p1);
        bstore16<1>((u32x4){p0[0], p0[1], p1[0], p1[1]}, srd, l16, rfl((unsigned)WS_HCF + (mt16 * 18 + 3 * n6 + (t >> 1)) * 1024)); };
    if (kj == 0) { pair(0, xr[0], xr[1]); pair(2, xr[2], xr[3]); } else pair(4, xr[0], xr[1]);
    ssq += xor16(ssq); ssq += xor32(ssq);
    if (g == 0) { __hip_atomic_store((float*)(ws + WS_SSQ) + (mt16 * 16 + 2 * n6 + kj) * 16 + m, ssq, __ATOMIC_RELAXED, __HIP_MEMORY_SCOPE_AGENT);
        if (n6 == 0 && kj == 0) __hip_atomic_store((float*)(ws + WS_MUH) + token, mu, __ATOMIC_RELAXED, __HIP_MEMORY_SCOPE_AGENT); }
    STAMP(3, 6); RSTAMP(3, 15); STAMP_FLUSH(3);
}

DI void ring_issue(const char* stream, int st, int wid, int lane, unsigned ldsb) {
    const char* sb = stream + (size_t)st * RING_SLOT + wid * 1024; const unsigned db = ldsb + (st & 3) * RING_SLOT + wid * 1024;
#pragma unroll
    for (int i = 0; i < 4; ++i) glds16(sb + i * 8192, lane * 16, db + i * 8192);
    if (wid < 4) glds16(sb + 32768, lane * 16, db + 32768);
}
DI float geglu(float g, float u) {
    const float z = g * __builtin_fmaf(g * g, -0.10294324f, -2.30220818f);
    return g * u * __builtin_amdgcn_rcpf(1.f + __builtin_amdgcn_exp2f(z));
}
struct Ev { int type, arg, n; };
enum { E_SSQ, E_A, E_B, E_WAITB, E_BAR, E_READ, E_MMA, E_GP, E_RS, E_STAMP, E_ST, E_NOP, E_TOUCH, E_A2 };
constexpr int G1_PD = G1PD;
constexpr int g1_grp(int f) { return f < 144 ? 0 : f < 216 ? 1 : f < 288 ? 2 : 3; }
constexpr int g1_ks(int f) { return f < 144 ? f / 8 : f < 288 ? ((f - 144) % 72) / 4 : (f - 288) / 2; }
constexpr int g1_tile(int f) { return f < 288 ? (f >> 1) & 1 : 0; }
constexpr int g1_set(int f) { return f < 144 ? (f >> 2) & 1 : f < 216 ? 0 : f < 288 ? 1 : 0; }
template <bool LATE> struct G1Prog {
    Ev ev[1600]; int n;
    constexpr void add(int t, int a) { ev[n].type = t; ev[n].arg = a; ev[n].n = 0; ++n; }
    static constexpr int vm(const Ev& e) { return e.type == E_SSQ ? 6 : (e.type == E_A || e.type == E_B || e.type == E_ST || e.type == E_TOUCH || e.type == E_A2) ? 1 : 0; }
    static constexpr int NP = LATE ? 4 : 5;
    constexpr void addB(int s, int u) { add(E_B, s * 2 + u); }
    static constexpr int jset(int q) { return q == 0 ? 0 : q == 1 ? 1 : q == 2 ? 0 : q == 3 ? 1 : 0; }
    constexpr G1Prog() : ev{}, n(0) {
        addB(0, 0); addB(0, 1); addB(1, 0); addB(1, 1);
        add(E_STAMP, 1);
        for (int f = 0; f < G1_PD; ++f) add(E_READ, f);
        for (int f = 0; f < 324; ++f) {
            const int st = f / 36, i = f % 36, g = g1_grp(f);
            if (g == 0) { const int s = f / 8, q = f % 8;
                if (q == 0) add(E_WAITB, s);
                if (f == 0) { addB(2, 0); addB(2, 1); }
                if (s + 3 < 18 && q == 2) addB(s + 3, 0);
                if (s + 3 < 18 && q == 6) addB(s + 3, 1);
                if (f == 100) add(E_SSQ, 0);
                if (f == 136) add(E_RS, 0); }
            if (st + 2 < 9) { const int i0 = LATE ? 17 : 1, d = 4;
                if (i >= i0 && (i - i0) % d == 0 && (i - i0) / d < NP) add(E_A, (st + 2) * 8 + (i - i0) / d); }
            if (i == 36 - G1_PD && st + 1 < 9) add(E_BAR, st + 1);
            if (f + G1_PD < 324) add(E_READ, f + G1_PD);
            add(E_MMA, f);
            if (f == 143) { add(E_NOP, 0);
                for (int k = 0; k < 32; ++k) { add(E_GP, 0 * 64 + k); if (k == 15 || k == 31) add(E_ST, 0 * 2 + (k == 31)); } }
            if (g > 0) {
                const int r = f - (g == 1 ? 144 : g == 2 ? 216 : 288), step = g < 3 ? 2 : 1, r0 = 3, k = (r - r0) / step;
                if (r >= r0 && (r - r0) % step == 0 && k < 32) { add(E_GP, g * 64 + k);
                    if (k == (LATE && ST_SKEW ? 22 : 15)) add(E_ST, g * 2);
                    if (k == 31 && !(LATE && ST_SKEW)) add(E_ST, g * 2 + 1); }
                if (LATE && ST_SKEW && r == (g < 3 ? 71 : 35)) add(E_ST, g * 2 + 1); }
            if (TOUCH && f == 7 * 36 + 1) add(E_TOUCH, 0);
            if (st == 8 && i >= 2 && i % 3 == 2 && (i - 2) / 3 < 2 * NP) add(E_A2, ((i - 2) / 3 / NP) * 8 + ((i - 2) / 3) % NP);
            if (i == 35) add(E_STAMP, 2 + st);
            if (f == 107) add(E_STAMP, 12);
            if (f == 323) add(E_STAMP, 13);
        }
        add(E_NOP, 1);
        for (int k = 0; k < 16; ++k) { add(E_GP, 4 * 64 + k); if (k == 7 || k == 15) add(E_ST, 8 + (k == 15)); }
        for (int i = 0; i < n; ++i) if (ev[i].type == E_WAITB || ev[i].type == E_BAR || ev[i].type == E_RS) {
            int cnt = 0, j = i - 1;
            for (; j >= 0; --j) {
                if (ev[i].type == E_WAITB && ev[j].type == E_B && ev[j].arg == 2 * ev[i].arg + 1) break;
                if (ev[i].type == E_RS && ev[j].type == E_SSQ) break;
                if (ev[i].type == E_BAR && ev[j].type == E_A && ev[j].arg == 8 * ev[i].arg + NP - 1) break;
                cnt += vm(ev[j]); }
            ev[i].n = cnt > 63 ? 63 : cnt; }
    }
};
template <bool LATE> constexpr G1Prog<LATE> g1prog{};
DI float vmul(float a, float b) { float r; asm("v_mul_f32_e32 %0, %1, %2" : "=v"(r) : "v"(a), "v"(b)); return r; }
DI float vfma(float a, float b, float c) { float r; asm("v_fma_f32 %0, %1, %2, %3" : "=v"(r) : "v"(a), "v"(b), "v"(c)); return r; }
struct G1State {
    bf16x8 hf[2][18]; f32x4 acc[2][2][2][2];
    bf16x8 ar[G1_PD + 1]; float sq[2][3]; float a0[2], a1[2], kk[2], sink; f32x4 z, w; u32x4 o; unsigned soffT, soffA2;
    unsigned lb0, lb1, lb2; unsigned l16, ldsb, soffA, soffH, soffS, soffAct, soffAct8, vq; int wid, j, g;
};
template <bool LATE, int PC> DI void g1_exec(G1State& c, const srd_t srd) {
    constexpr Ev e = g1prog<LATE>.ev[PC];
    if constexpr (e.type == E_SSQ) { bload4_sc1<0>(c.sq[0][0], srd, c.vq, c.soffS); bload4_sc1<256>(c.sq[0][1], srd, c.vq, c.soffS); bload4_sc1<512>(c.sq[0][2], srd, c.vq, c.soffS);
                                     bload4_sc1<1024>(c.sq[1][0], srd, c.vq, c.soffS); bload4_sc1<1280>(c.sq[1][1], srd, c.vq, c.soffS); bload4_sc1<1536>(c.sq[1][2], srd, c.vq, c.soffS); }
    else if constexpr (e.type == E_A) { constexpr int st = e.arg >> 3, p = e.arg & 7;
        const unsigned so = c.soffA + st * RING_SLOT, dst = c.ldsb + RING0 + ((st + 3) & 3) * RING_SLOT;
        if constexpr (p < 4) bdma_next<p * 1024>(srd, c.l16, so + c.wid * 4096, dst + c.wid * 4096);
        else bdma1(srd, c.l16, so + 32768 + c.wid * 1024, dst + 32768 + c.wid * 1024); }
    else if constexpr (e.type == E_B) { constexpr int s = e.arg >> 1, u = e.arg & 1;
        bload16_sc1<((18 * u + s) & 3) * 1024>(c.hf[u][s], srd, c.l16, c.soffH + ((18 * u + s) >> 2) * 4096); }
    else if constexpr (e.type == E_WAITB) { asm volatile("s_waitcnt vmcnt(%2)" : "+v"(c.hf[0][e.arg]), "+v"(c.hf[1][e.arg]) : "n"(e.n)); }
    else if constexpr (e.type == E_BAR) { asm volatile("s_waitcnt vmcnt(%0)\n\ts_barrier" :: "n"(e.n) : "memory"); }
    else if constexpr (e.type == E_RS) { asm volatile("s_waitcnt vmcnt(%6)" : "+v"(c.sq[0][0]), "+v"(c.sq[0][1]), "+v"(c.sq[0][2]), "+v"(c.sq[1][0]), "+v"(c.sq[1][1]), "+v"(c.sq[1][2]) : "n"(e.n));
#pragma unroll
        for (int u = 0; u < 2; ++u) { float t = (c.sq[u][0] + c.sq[u][1]) + c.sq[u][2]; t += xor16(t); t += xor32(t);
            const float v = t * (1.f / D_) + EPS, rs = __builtin_amdgcn_rsqf(v);
            c.a0[u] = -2.30220818f * rs; c.a1[u] = -0.10294324f * rs * rs * rs; c.kk[u] = v; }
        SBAR(); }
    else if constexpr (e.type == E_STAMP) {
    }
    else if constexpr (e.type == E_TOUCH) { touch64(c.sink, srd, c.l16 * 8, c.soffT); }
    else if constexpr (e.type == E_A2) { constexpr int st = e.arg >> 3, p = e.arg & 7;
        const unsigned so = c.soffA2 + st * RING_SLOT, dst = c.ldsb + RING0 + (st & 3) * RING_SLOT;
        if constexpr (p < 4) bdma_next<p * 1024>(srd, c.l16, so + c.wid * 4096, dst + c.wid * 4096);
        else bdma1(srd, c.l16, so + 32768 + c.wid * 1024, dst + 32768 + c.wid * 1024); }
    else if constexpr (e.type == E_NOP) {
        asm volatile("s_nop 7" : "+v"(c.acc[0][0][0][0]), "+v"(c.acc[0][0][0][1]), "+v"(c.acc[0][0][1][0]), "+v"(c.acc[0][0][1][1]));
        if constexpr (e.arg == 0) asm volatile("" : "+v"(c.acc[0][1][0][0]), "+v"(c.acc[0][1][0][1]), "+v"(c.acc[0][1][1][0]), "+v"(c.acc[0][1][1][1])); }
    else if constexpr (e.type == E_READ) { constexpr int f = e.arg, off = ((f / 36 + 3) & 3) * RING_SLOT + (f % 36) * 1024;
        if constexpr ((off >> 16) == 0) c.ar[f % (G1_PD + 1)] = lds_frag((LAS const char*)(uintptr_t)c.lb0 + (off & 65535));
        else if constexpr ((off >> 16) == 1) c.ar[f % (G1_PD + 1)] = lds_frag((LAS const char*)(uintptr_t)c.lb1 + (off & 65535));
        else c.ar[f % (G1_PD + 1)] = lds_frag((LAS const char*)(uintptr_t)c.lb2 + (off & 65535));
        SBAR(); }
    else if constexpr (e.type == E_MMA) { constexpr int f = e.arg, s = g1_ks(f), t = g1_tile(f), q = f & 1, set = g1_set(f);
        if constexpr (s == 0) { mfma16_new(c.acc[set][t][q][0], c.ar[f % (G1_PD + 1)], c.hf[0][s]); mfma16_new(c.acc[set][t][q][1], c.ar[f % (G1_PD + 1)], c.hf[1][s]); }
        else { mfma16_acc(c.acc[set][t][q][0], c.ar[f % (G1_PD + 1)], c.hf[0][s]); mfma16_acc(c.acc[set][t][q][1], c.ar[f % (G1_PD + 1)], c.hf[1][s]); }
        SBAR(); }
    else if constexpr (e.type == E_GP) {
        constexpr int grp = e.arg >> 6, k = e.arg & 63, quad = k >> 3, ph = k & 7, set = G1Prog<LATE>::jset(grp);
        constexpr int u = grp < 4 ? quad >> 1 : quad, t = grp < 4 ? quad & 1 : 0;
        const f32x4 g = c.acc[set][t][0][u];
        if constexpr ((grp == 0 || grp == 4) && (ph == 4 || ph == 6)) asm volatile("s_nop 0" : "+v"(c.z));
#pragma unroll
        for (int r = 0; r < 4; ++r) {
            if constexpr (ph == 0) c.z[r] = vmul(g[r], g[r]);
            if constexpr (ph == 1) c.z[r] = vfma(c.z[r], c.a1[u], c.a0[u]);
            if constexpr (ph == 2) c.z[r] = vmul(c.z[r], g[r]);
            if constexpr (ph == 3) c.z[r] = __builtin_amdgcn_exp2f(c.z[r]);
            if constexpr (ph == 4) c.z[r] = vfma(c.z[r], c.kk[u], c.kk[u]);
            if constexpr (ph == 5) c.z[r] = __builtin_amdgcn_rcpf(c.z[r]);
            if constexpr (ph == 6) c.w[r] = vmul(vmul(g[r], c.acc[set][t][1][u][r]), c.z[r]); }
        if constexpr (ph == 7) { c.o[2 * t] = cvtpk(c.w[0], c.w[1]); c.o[2 * t + 1] = cvtpk(c.w[2], c.w[3]); }
        SBAR(); }
    else if constexpr (e.type == E_ST) { constexpr int grp = e.arg >> 1, u = e.arg & 1;
        if constexpr (grp < 4) bstore16<ACT_POL>(c.o, srd, c.l16, c.soffAct + u * (144 * 1024) + grp * 1024);
        else bstore8<ACT_POL>((u32x2){c.o[0], c.o[1]}, srd, c.l16 + (c.j & 1) * 8, c.soffAct8 + u * (144 * 1024));
        SBAR(); }
}
template <bool LATE, int... I> DI void g1_run(G1State& c, const srd_t srd, std::integer_sequence<int, I...>) { (g1_exec<LATE, I>(c, srd), ...); }

constexpr int G2_PD = 4;
template <bool LATE> struct G2Prog {
    Ev ev[700]; int n;
    constexpr void add(int t, int a) { ev[n].type = t; ev[n].arg = a; ev[n].n = 0; ++n; }
    static constexpr int vm(const Ev& e) { return (e.type == E_A || e.type == E_B) ? 1 : 0; }
    static constexpr int NP = LATE ? 4 : 5;
    constexpr G2Prog() : ev{}, n(0) {
        add(E_B, 0); add(E_B, 1); add(E_B, 2); add(E_B, 3);
        add(E_STAMP, 1);
        for (int f = 0; f < G2_PD; ++f) add(E_READ, f);
        for (int f = 0; f < 162; ++f) {
            const int st = f / 18, i = f % 18;
            if (i % 9 == 0) add(E_WAITB, st * 2 + i / 9);
            const int ia = LATE ? 3 : 1, ib = LATE ? 1 : 3;
            if (st == 0) {
                if (i >= 1 && i < 5) add(E_B, 4 + i - 1);
                if (i >= 6 && i < 6 + NP) add(E_A, 16 + i - 6);
                if (i >= 12 && i < 16) add(E_B, 8 + i - 12);
            } else {
                if (st == 1 && i >= 14) add(E_B, 16 + i - 14);
                if (st + 2 < 9 && i >= ia && (i - ia) % 4 == 0 && (i - ia) / 4 < 4) add(E_A, (st + 2) * 8 + (i - ia) / 4);
                if (st + 2 < 9 && NP == 5 && i == 12) add(E_A, (st + 2) * 8 + 4);
                { const int la = st == 1 ? 2 : 3; if (st + la < 9 && (st == 1 || st + la > 4) && i >= ib && (i - ib) % 4 == 0 && (i - ib) / 4 < 4) add(E_B, (st + la) * 4 + (i - ib) / 4); }
            }
            if (i == 18 - G2_PD && st + 1 < 9) add(E_BAR, st + 1);
            if (f + G2_PD < 162) add(E_READ, f + G2_PD);
            add(E_MMA, f);
            if (i == 17) add(E_STAMP, 2 + st);
        }
        for (int i = 0; i < n; ++i) if (ev[i].type == E_WAITB || ev[i].type == E_BAR) {
            int cnt = 0, j = i - 1;
            for (; j >= 0; --j) {
                if (ev[i].type == E_WAITB && ev[j].type == E_B && ev[j].arg == 2 * ev[i].arg + 1) break;
                if (ev[i].type == E_BAR && ev[j].type == E_A && (ev[j].arg >> 3) == ev[i].arg) break;
                cnt += vm(ev[j]); }
            ev[i].n = cnt > 63 ? 63 : cnt; }
    }
};
template <bool LATE> constexpr G2Prog<LATE> g2prog{};
struct G2State {
    bf16x8 af[4][2][2]; f32x4 acc[9][2]; bf16x8 ar[G2_PD + 1];
    unsigned lb0, lb1, lb2; unsigned l16, ldsb, soffA, soffB; int wid;
};
template <bool LATE, int PC> DI void g2_exec(G2State& c, const srd_t srd) {
    constexpr Ev e = g2prog<LATE>.ev[PC];
    if constexpr (e.type == E_A) { constexpr int st = e.arg >> 3, p = e.arg & 7;
        const unsigned so = c.soffA + st * RING_SLOT, dst = c.ldsb + RING0 + (st & 3) * RING_SLOT;
        if constexpr (p < 4) bdma_next<p * 1024>(srd, c.l16, so + c.wid * 4096, dst + c.wid * 4096);
        else bdma1(srd, c.l16, so + 32768 + c.wid * 1024, dst + 32768 + c.wid * 1024); }
    else if constexpr (e.type == E_B) { constexpr int st = e.arg >> 2, s2 = (e.arg >> 1) & 1, u = e.arg & 1;
        bload16_sc1<((2 * st + s2) & 3) * 1024>(c.af[st & 3][s2][u], srd, c.l16, c.soffB + u * (144 * 1024) + ((2 * st + s2) >> 2) * 4096); }
    else if constexpr (e.type == E_WAITB) { asm volatile("s_waitcnt vmcnt(%2)" : "+v"(c.af[(e.arg >> 1) & 3][e.arg & 1][0]), "+v"(c.af[(e.arg >> 1) & 3][e.arg & 1][1]) : "n"(e.n)); }
    else if constexpr (e.type == E_BAR) { asm volatile("s_waitcnt vmcnt(%0)\n\ts_barrier" :: "n"(e.n) : "memory"); }
    else if constexpr (e.type == E_STAMP) {
    }
    else if constexpr (e.type == E_READ) { constexpr int f = e.arg, st = f / 18, off = (st & 3) * RING_SLOT + (f % 18) * 1024;
        if constexpr ((off >> 16) == 0) c.ar[f % (G2_PD + 1)] = lds_frag((LAS const char*)(uintptr_t)c.lb0 + (off & 65535));
        else c.ar[f % (G2_PD + 1)] = lds_frag((LAS const char*)(uintptr_t)c.lb1 + (off & 65535));
        SBAR(); }
    else if constexpr (e.type == E_MMA) { constexpr int f = e.arg, st = f / 18, s2 = (f % 18) / 9, t = f % 9;
        if constexpr (f < 9) { mfma16_new(c.acc[t][0], c.ar[f % (G2_PD + 1)], c.af[st & 3][s2][0]); mfma16_new(c.acc[t][1], c.ar[f % (G2_PD + 1)], c.af[st & 3][s2][1]); }
        else { mfma16_acc(c.acc[t][0], c.ar[f % (G2_PD + 1)], c.af[st & 3][s2][0]); mfma16_acc(c.acc[t][1], c.ar[f % (G2_PD + 1)], c.af[st & 3][s2][1]); }
        SBAR(); }
}
template <bool LATE, int... I> DI void g2_run(G2State& c, const srd_t srd, std::integer_sequence<int, I...>) { (g2_exec<LATE, I>(c, srd), ...); }
DI void arrive(char* ws, int counter) { if (threadIdx.x == 0) __hip_atomic_fetch_add((unsigned*)(ws + WS_CNT) + counter * 64, 1u, __ATOMIC_RELAXED, __HIP_MEMORY_SCOPE_AGENT); }
DI bool await(char* ws, int counter, unsigned want, LAS unsigned* flag) {
    if (threadIdx.x == 0) { const unsigned* cnt = (const unsigned*)(ws + WS_CNT) + counter * 64; unsigned bad = 0;
        const unsigned long long t0 = __builtin_amdgcn_s_memrealtime();
        while (__hip_atomic_load(cnt, __ATOMIC_RELAXED, __HIP_MEMORY_SCOPE_AGENT) < want) {
            if (__builtin_amdgcn_s_memrealtime() - t0 > 2000000ull) { bad = 1; break; }
            __builtin_amdgcn_s_sleep(1); }
        *flag = bad; }
    LBAR();
    return *flag != 0;
}
DI void ring_w1(char* ws, char* lds_generic, int xcd, int idx, int st) {
    const int lane = threadIdx.x & 63, wid = rfl(threadIdx.x >> 6); const srd_t srd = make_srd(ws, (unsigned)WS_END);
    const unsigned so = (unsigned)WS_W1 + ((xcd >> 1) * 8 + (idx & 7)) * (324 * 1024) + st * RING_SLOT, dst = (unsigned)(uintptr_t)lds_generic + RING0 + ((st + 3) & 3) * RING_SLOT;
    bdma4(srd, lane * 16, so + wid * 4096, dst + wid * 4096);
    if (wid < 4) bdma1(srd, lane * 16, so + 32768 + wid * 1024, dst + 32768 + wid * 1024);
}
constexpr int FFN_LDS = G_LDS;
__global__ __launch_bounds__(512, 2) void ffn_kernel(const char* x, char* ws, char* out) {
    extern __shared__ __attribute__((aligned(16))) char lds_generic[];
    LAS char* lds = (LAS char*)lds_generic;
    CODE_LABEL(preln_ffn_begin);
    const srd_t srd = make_srd(ws, (unsigned)WS_END);
    const int b = blockIdx.x, xcd = b & 7, idx = b >> 3;
    bool bad;
    TL_DECL; TLS(0);
    float csink = 0.f;
    if (idx >= 24) { ring_w1(ws, lds_generic, xcd, idx, 0);
        if (CODE_PF2) { const char *c0, *c1; CODE_ADDR(c0, preln_ffn_begin); CODE_ADDR(c1, preln_ffn_end); touch_code(csink, c0, c1, rfl(threadIdx.x >> 6) + 8 * (idx - 24), threadIdx.x & 63); } }
    else {
        k3_phase(x, ws, lds_generic, xcd, idx);
        TLS(1);
        asm volatile("s_waitcnt vmcnt(0) lgkmcnt(0)\n\ts_barrier" ::: "memory");
        arrive(ws, 96 + xcd);
        TLS(2);
    }
    ring_w1(ws, lds_generic, xcd, idx, 1);
    asm volatile("s_waitcnt vmcnt(0)" : "+v"(csink) :: "memory");
    LAS unsigned* flag = (LAS unsigned*)(lds + 15 * 1024);
    bad = await(ws, 96 + (xcd & 1) * 4 + (idx >> 3), 24u, flag);
    TLS(3);
    {
        int tid = threadIdx.x; asm volatile("" : "+v"(tid));
        const int lane = tid & 63, wid = rfl(tid >> 6);
        const int j = (xcd >> 1) * 8 + (idx & 7), mt8 = (xcd & 1) * 4 + (idx >> 3), mt16 = 16 * mt8 + 2 * wid;
        G1State c;
        RSTAMP(4, 14); STAMP(4, 0);
        c.l16 = lane * 16; c.ldsb = (unsigned)(uintptr_t)lds_generic; c.wid = wid; c.j = j; c.g = lane >> 4;
        c.lb0 = c.ldsb + RING0 + lane * 16; c.lb1 = c.lb0 + 65536; c.lb2 = c.lb0 + 131072; PINV(c.lb0); PINV(c.lb1); PINV(c.lb2);
        c.soffA = (unsigned)WS_W1 + j * (324 * 1024); c.soffH = (unsigned)WS_HCF + mt16 * (18 * 1024); c.soffS = (unsigned)WS_SSQ + mt16 * 1024;
        c.soffAct8 = rfl((unsigned)WS_ACT + (mt16 * 144 + 9 * (j >> 1) + 8) * 1024);
        c.soffAct = rfl((unsigned)WS_ACT + (mt16 * 144 + 9 * (j >> 1) + 4 * (j & 1)) * 1024); c.vq = (lane >> 4) * 64 + (lane & 15) * 4;
        c.soffA2 = (unsigned)WS_W2 + ((xcd >> 1) * 4 + (idx >> 3)) * (324 * 1024);
        c.sink = 0.f; c.soffT = (unsigned)WS_W2 + (xcd >> 1) * (4 * 324 * 1024) + (idx * 8 + wid) * (41 * 128);
        if (wid >= 4) g1_run<true>(c, srd, std::make_integer_sequence<int, g1prog<true>.n>{});
        else g1_run<false>(c, srd, std::make_integer_sequence<int, g1prog<false>.n>{});
        STAMP(4, 11); RSTAMP(4, 15); TLS(4);
        asm volatile("s_waitcnt vmcnt(0) lgkmcnt(0)\n\ts_barrier" : "+v"(c.sink) :: "memory");
        arrive(ws, 64 + (j >> 3) * 8 + mt8);
        TLS(5);
        STAMP_FLUSH(4);
    }
    int tz = threadIdx.x; asm volatile("" : "+v"(tz));
    const int lane = tz & 63, wid = rfl(tz >> 6), mi = wid & 3, kj = wid >> 2;
    const int ksl = xcd >> 1, mt = (xcd & 1) * 8 + (idx & 7), n4 = idx >> 3;
    const int mt16 = 8 * mt + 2 * mi;
    G2State c;
    RSTAMP(5, 14); STAMP(5, 0);
    c.l16 = lane * 16; c.ldsb = (unsigned)(uintptr_t)lds_generic; c.wid = wid;
    c.lb0 = c.ldsb + RING0 + kj * (18 * 1024) + lane * 16; c.lb1 = c.lb0 + 65536; PINV(c.lb0); PINV(c.lb1);
    c.soffA = (unsigned)WS_W2 + (ksl * 4 + n4) * (324 * 1024); c.soffB = (unsigned)WS_ACT + (mt16 * 144 + 36 * ksl + 18 * kj) * 1024;
    bad |= await(ws, 64 + ksl * 8 + (mt >> 1), 8u, flag);
    TLS(6);
    if (wid >= 4) g2_run<true>(c, srd, std::make_integer_sequence<int, g2prog<true>.n>{});
    else g2_run<false>(c, srd, std::make_integer_sequence<int, g2prog<false>.n>{});
    asm volatile("s_nop 7" : "+v"(c.acc[8][0]), "+v"(c.acc[8][1]), "+v"(c.acc[7][0]), "+v"(c.acc[7][1]));
    TLS(7);
    u32x2 hv[3]; u32x4 pv[3]; u32x2 pw[3]; float mu[2];
    { const int T0 = 9 * n4 + wid, T8 = 9 * n4 + 8; const unsigned hb = rfl((unsigned)WS_HCF + (8 * mt + 2 * ksl) * (18 * 1024));
      bload8_sc1<0>(hv[0], srd, lane * 16 + (T0 & 1) * 8, hb + (T0 >> 1) * 1024); bload8_sc1<0>(hv[1], srd, lane * 16 + (T0 & 1) * 8, hb + (18 + (T0 >> 1)) * 1024);
      bload8_sc1<0>(hv[2], srd, lane * 16 + (T8 & 1) * 8, hb + ((wid & 1) * 18 + (T8 >> 1)) * 1024);
      const unsigned mb = rfl((unsigned)WS_MUH + (128 * mt + 32 * ksl) * 4); bload4_sc1<0>(mu[0], srd, (lane & 15) * 4, mb); bload4_sc1<64>(mu[1], srd, (lane & 15) * 4, mb); }
    LBAR();
    LAS char* ex = lds + RING0 + mi * 18432 + lane * 16;
    if (kj == 0) {
#pragma unroll
        for (int t = 5; t < 9; ++t) { *(LAS f32x4*)(ex + (2 * t) * 1024) = c.acc[t][0]; *(LAS f32x4*)(ex + (2 * t + 1) * 1024) = c.acc[t][1]; }
    } else {
#pragma unroll
        for (int t = 0; t < 5; ++t) { *(LAS f32x4*)(ex + (2 * t) * 1024) = c.acc[t][0]; *(LAS f32x4*)(ex + (2 * t + 1) * 1024) = c.acc[t][1]; }
    }
    LBAR();
    STAMP(5, 11);
    const int tile = mt * 4 + n4; const unsigned xb = rfl((unsigned)WS_PART + tile * (16 * 9 * 1024));
    const unsigned xs0 = rfl(xb + (mi * 4 + ksl) * (9 * 1024));
    auto fin = [&](int t) {
        const f32x4 a0 = c.acc[t][0] + *(LAS const f32x4*)(ex + (2 * t) * 1024), a1 = c.acc[t][1] + *(LAS const f32x4*)(ex + (2 * t + 1) * 1024);
        if (mi == ksl) { *(LAS f32x4*)(ex + (2 * t) * 1024) = a0; *(LAS f32x4*)(ex + (2 * t + 1) * 1024) = a1; }
        else bstore16<1>((u32x4){cvtpk(a0[0], a0[1]), cvtpk(a0[2], a0[3]), cvtpk(a1[0], a1[1]), cvtpk(a1[2], a1[3])}, srd, lane * 16, xs0 + t * 1024); };
    if (kj == 0) { fin(0); fin(1); fin(2); fin(3); fin(4); } else { fin(5); fin(6); fin(7); fin(8); }
    STAMP(5, 12);
    asm volatile("s_waitcnt vmcnt(0) lgkmcnt(0)\n\ts_barrier" : "+v"(hv[0]), "+v"(hv[1]), "+v"(hv[2]), "+v"(mu[0]), "+v"(mu[1]) :: "memory");
    STAMP(5, 13);
    TLS(8);
    arrive(ws, tile);
    bad |= await(ws, tile, 4u, flag);
    TLS(9);
#pragma unroll
    for (int k = 0; k < 3; ++k) { const unsigned sb = rfl(xb + (ksl * 4 + (k + (k >= ksl))) * (9 * 1024));
        bload16u_sc1<0>(pv[k], srd, lane * 16, sb + wid * 1024); bload8_sc1<0>(pw[k], srd, lane * 16 + (wid & 1) * 8, sb + 8 * 1024); }
    asm volatile("s_waitcnt vmcnt(0)" : "+v"(pv[0]), "+v"(pv[1]), "+v"(pv[2]), "+v"(pw[0]), "+v"(pw[1]), "+v"(pw[2]) :: "memory");
    const int m = lane & 15, g = lane >> 4;
    auto put = [&](int t, int u, u32x2 h2, u32x2 p0, u32x2 p1, u32x2 p2) {
        f32x4 a = *(LAS const f32x4*)(lds + RING0 + ksl * 18432 + (2 * t + u) * 1024 + lane * 16);
        a += (f32x4){bflo(h2[0]), bfhi(h2[0]), bflo(h2[1]), bfhi(h2[1])} + (u ? mu[1] : mu[0]); a += (f32x4){bflo(p0[0]), bfhi(p0[0]), bflo(p0[1]), bfhi(p0[1])};
        a += (f32x4){bflo(p1[0]), bfhi(p1[0]), bflo(p1[1]), bfhi(p1[1])}; a += (f32x4){bflo(p2[0]), bfhi(p2[0]), bflo(p2[1]), bfhi(p2[1])};
        u32x2 o = {cvtpk(a[0], a[1]), cvtpk(a[2], a[3])};
        if (bad) o = (u32x2){0x7fc07fc0u, 0x7fc07fc0u};
        *(u32x2*)(out + (size_t)(128 * mt + 32 * ksl + 16 * u + m) * (D_ * 2) + (144 * n4 + 16 * t + 4 * g) * 2) = o; };
    put(wid, 0, hv[0], (u32x2){pv[0][0], pv[0][1]}, (u32x2){pv[1][0], pv[1][1]}, (u32x2){pv[2][0], pv[2][1]});
    put(wid, 1, hv[1], (u32x2){pv[0][2], pv[0][3]}, (u32x2){pv[1][2], pv[1][3]}, (u32x2){pv[2][2], pv[2][3]});
    if (wid < 2) put(8, wid, hv[2], pw[0], pw[1], pw[2]);
    RSTAMP(5, 15); STAMP_FLUSH(5);
    TLS(10); TL_FLUSH(6);
    CODE_LABEL(preln_ffn_end);
}

}

extern "C" void kernel_launch(void* const* d_in, const int* in_sizes, int n_in, void* d_out, int out_size, void* d_ws, size_t ws_size, hipStream_t stream) {
    const char* x = (const char*)d_in[0]; const char* wqkv = (const char*)d_in[1]; const char* wo = (const char*)d_in[2];
    const char* wg = (const char*)d_in[4]; const char* wu = (const char*)d_in[5]; const char* wd = (const char*)d_in[6];
    char* ws = (char*)d_ws;
    (void)hipFuncSetAttribute((const void*)k1_qkv, hipFuncAttributeMaxDynamicSharedMemorySize, K1_LDS);
    (void)hipFuncSetAttribute((const void*)k2_attn, hipFuncAttributeMaxDynamicSharedMemorySize, K2_LDS);
    (void)hipFuncSetAttribute((const void*)ffn_kernel, hipFuncAttributeMaxDynamicSharedMemorySize, FFN_LDS);
    k1_qkv<<<256, 512, K1_LDS, stream>>>(x, wqkv, wo, ws);
    k2_attn<<<256, 1024, K2_LDS, stream>>>(x, wg, wu, wd, ws);
    ffn_kernel<<<256, 512, FFN_LDS, stream>>>(x, ws, (char*)d_out);
}
```

```cpp
#include <hip/hip_runtime.h>
#include <hip/hip_bf16.h>
#include <stdint.h>
#include <utility>

namespace {

typedef __bf16 bf16x2 __attribute__((ext_vector_type(2)));
typedef __bf16 bf16x8 __attribute__((ext_vector_type(8)));
typedef __bf16 bf16x16 __attribute__((ext_vector_type(16)));
typedef short s16x4 __attribute__((ext_vector_type(4)));
typedef float f32x2 __attribute__((ext_vector_type(2)));
typedef float f32x4 __attribute__((ext_vector_type(4)));
typedef float f32x8 __attribute__((ext_vector_type(8)));
typedef float f32x16 __attribute__((ext_vector_type(16)));
typedef unsigned u32x2 __attribute__((ext_vector_type(2)));
typedef unsigned u32x4 __attribute__((ext_vector_type(4)));
#define LAS __attribute__((address_space(3)))
#define DI __device__ __forceinline__

constexpr int S_ = 2048, D_ = 576, H_ = 32, F_ = 4608;
constexpr float EPS = 1e-5f;
constexpr float QSCALE = 0.23570226039551584f * 1.4426950408889634f;
constexpr float SAFE_B = 48.f;

constexpr int KVCHUNK = 19456, KVIMG = 8 * KVCHUNK;
constexpr bool OVERLAY = true;
constexpr size_t WS_QM   = 0;
constexpr size_t WS_QT   = WS_QM + (size_t)32 * 2048 * 32;
constexpr size_t WS_KV   = WS_QT + (size_t)32 * 2048 * 4;
constexpr size_t WS_A_END = WS_KV + (size_t)32 * KVIMG;
constexpr size_t WS_ACT  = OVERLAY ? 0 : WS_A_END;
constexpr size_t WS_OF   = WS_ACT + (size_t)128 * 144 * 1024;
constexpr size_t WS_WO   = WS_OF + (size_t)128 * 18 * 1024;
constexpr size_t WS_KMAX = WS_WO + (size_t)6 * 108 * 1024;
constexpr size_t WS_MUX  = WS_KMAX + 32 * 16 * 4;
constexpr size_t WS_WBAR = WS_MUX + 2048 * 4;
constexpr size_t WS_SSQ  = WS_WBAR + 4096;
constexpr size_t WS_CNT  = WS_SSQ + (size_t)2048 * 64;
constexpr size_t WS_MUH  = WS_CNT + 104 * 256;
constexpr size_t WS_HCF  = WS_MUH + 2048 * 4;
constexpr size_t WS_W2   = WS_HCF + (size_t)128 * 18 * 1024;
constexpr size_t WS_W1   = WS_W2 + (size_t)16 * 324 * 1024;
constexpr size_t WS_PART = WS_W1 + (size_t)32 * 324 * 1024;
static_assert(WS_A_END <= (size_t)128 * 144 * 1024, "overlay fits");
constexpr size_t WS_END  = WS_PART + (size_t)64 * 16 * 9 * 1024;

DI unsigned cvtpk(float lo, float hi) { f32x2 v = {lo, hi}; bf16x2 b = __builtin_convertvector(v, bf16x2); return __builtin_bit_cast(unsigned, b); }
DI float bflo(unsigned w) { return __uint_as_float(w << 16); }
DI float bfhi(unsigned w) { return __uint_as_float(w & 0xffff0000u); }
DI float dot2(unsigned a, unsigned b, float c) { return __builtin_amdgcn_fdot2_f32_bf16(__builtin_bit_cast(bf16x2, a), __builtin_bit_cast(bf16x2, b), c, false); }
DI int rfl(int x) { return __builtin_amdgcn_readfirstlane(x); }
DI float xor32(float v) { auto r = __builtin_amdgcn_permlane32_swap(__float_as_uint(v), __float_as_uint(v), false, false); return __uint_as_float((threadIdx.x & 32) ? r[0] : r[1]); }
DI float xor16(float v) { auto r = __builtin_amdgcn_permlane16_swap(__float_as_uint(v), __float_as_uint(v), false, false); return __uint_as_float((threadIdx.x & 16) ? r[0] : r[1]); }
template <int N> DI float row_ror(float v) { return __uint_as_float(__builtin_amdgcn_update_dpp(0u, __float_as_uint(v), 0x120 + N, 0xf, 0xf, false)); }
DI void mfma16_acc(f32x4& c, bf16x8 a, bf16x8 b) { asm volatile("v_mfma_f32_16x16x32_bf16 %0, %1, %2, %0" : "+v"(c) : "v"(a), "v"(b)); }
DI void mfma16_new(f32x4& c, bf16x8 a, bf16x8 b) { asm volatile("v_mfma_f32_16x16x32_bf16 %0, %1, %2, 0" : "=&v"(c) : "v"(a), "v"(b)); }
DI void glds16(const void* sbase, unsigned voff, unsigned lds_dst) {
    asm volatile("s_mov_b32 m0, %2\n\ts_nop 0\n\tglobal_load_lds_dwordx4 %0, %1" :: "v"(voff), "s"(sbase), "s"(lds_dst) : "memory");
}
typedef __amdgpu_buffer_rsrc_t srd_t;
DI srd_t make_srd(const void* p, unsigned bytes) { return __builtin_amdgcn_make_buffer_rsrc((void*)p, (short)0, (int)bytes, 0x00020000); }
template <int IMM> DI void bload16(bf16x8& dst, srd_t r, unsigned voff, unsigned soff) {
    asm volatile("buffer_load_dwordx4 %0, %1, %2, %3 offen offset:%4" : "=v"(dst) : "v"(voff), "s"(r), "s"(soff), "n"(IMM)); }
template <int IMM> DI void bload4(float& dst, srd_t r, unsigned voff, unsigned soff) {
    asm volatile("buffer_load_dword %0, %1, %2, %3 offen offset:%4" : "=v"(dst) : "v"(voff), "s"(r), "s"(soff), "n"(IMM)); }
template <int IMM> DI void bload16_sc1(bf16x8& dst, srd_t r, unsigned voff, unsigned soff) {
    asm volatile("buffer_load_dwordx4 %0, %1, %2, %3 offen offset:%4 sc1" : "=v"(dst) : "v"(voff), "s"(r), "s"(soff), "n"(IMM)); }
template <int IMM> DI void bload4_sc1(float& dst, srd_t r, unsigned voff, unsigned soff) {
    asm volatile("buffer_load_dword %0, %1, %2, %3 offen offset:%4 sc1" : "=v"(dst) : "v"(voff), "s"(r), "s"(soff), "n"(IMM)); }
template <int IMM> DI void bload16u_sc1(u32x4& dst, srd_t r, unsigned voff, unsigned soff) {
    asm volatile("buffer_load_dwordx4 %0, %1, %2, %3 offen offset:%4 sc1" : "=v"(dst) : "v"(voff), "s"(r), "s"(soff), "n"(IMM)); }
template <int IMM> DI void bload8_sc1(u32x2& dst, srd_t r, unsigned voff, unsigned soff) {
    asm volatile("buffer_load_dwordx2 %0, %1, %2, %3 offen offset:%4 sc1" : "=v"(dst) : "v"(voff), "s"(r), "s"(soff), "n"(IMM)); }
DI void touch1(float& sink, srd_t r, unsigned voff) { asm volatile("buffer_load_dword %0, %1, %2, 0 offen" : "+v"(sink) : "v"(voff), "s"(r)); }
#define CODE_LABEL(name) asm volatile(#name ":")
#define CODE_ADDR(dst, name) do { unsigned long long pc_; int d_; asm volatile("s_getpc_b64 %0\n.Lhere%=:\n\ts_mov_b32 %1, " #name "-.Lhere%=" : "=s"(pc_), "=s"(d_)); dst = (const char*)pc_ + d_; } while (0)
DI void touch_code(float& sink, const char* from, const char* to, int i, int lane) {
    const unsigned bytes = (unsigned)(to - from), off = (unsigned)(i * 64 + lane) * 128u;
    if ((unsigned)i * 8192u < bytes) touch1(sink, make_srd(from, bytes), off < bytes - 4 ? off : bytes - 4);
}
DI void bdma4(srd_t r, unsigned voff, unsigned soff, unsigned lds_dst) {
    asm volatile("s_mov_b32 m0, %3\n\ts_nop 0\n\tbuffer_load_dwordx4 %0, %1, %2 offen lds\n\tbuffer_load_dwordx4 %0, %1, %2 offen offset:1024 lds\n\t"
                 "buffer_load_dwordx4 %0, %1, %2 offen offset:2048 lds\n\tbuffer_load_dwordx4 %0, %1, %2 offen offset:3072 lds" :: "v"(voff), "s"(r), "s"(soff), "s"(lds_dst) : "memory"); }
DI void bdma1(srd_t r, unsigned voff, unsigned soff, unsigned lds_dst) {
    asm volatile("s_mov_b32 m0, %3\n\ts_nop 0\n\tbuffer_load_dwordx4 %0, %1, %2 offen lds" :: "v"(voff), "s"(r), "s"(soff), "s"(lds_dst) : "memory"); }
template <int IMM> DI void bdma_next(srd_t r, unsigned voff, unsigned soff, unsigned lds_dst) {
    asm volatile("s_mov_b32 m0, %3\n\ts_nop 0\n\tbuffer_load_dwordx4 %0, %1, %2 offen offset:%4 lds" :: "v"(voff), "s"(r), "s"(soff), "s"(lds_dst), "n"(IMM) : "memory"); }
template <int POL> DI void bstore16(u32x4 d, srd_t r, unsigned voff, unsigned soff) {
    if constexpr (POL == 1) asm volatile("buffer_store_dwordx4 %0, %1, %2, %3 offen sc1\n\ts_nop 1" :: "v"(d), "v"(voff), "s"(r), "s"(soff) : "memory");
    else if constexpr (POL == 2) asm volatile("buffer_store_dwordx4 %0, %1, %2, %3 offen nt\n\ts_nop 1" :: "v"(d), "v"(voff), "s"(r), "s"(soff) : "memory");
    else asm volatile("buffer_store_dwordx4 %0, %1, %2, %3 offen\n\ts_nop 1" :: "v"(d), "v"(voff), "s"(r), "s"(soff) : "memory"); }
template <int POL> DI void bstore8(u32x2 d, srd_t r, unsigned voff, unsigned soff) {
    if constexpr (POL == 1) asm volatile("buffer_store_dwordx2 %0, %1, %2, %3 offen sc1\n\ts_nop 1" :: "v"(d), "v"(voff), "s"(r), "s"(soff) : "memory");
    else if constexpr (POL == 2) asm volatile("buffer_store_dwordx2 %0, %1, %2, %3 offen nt\n\ts_nop 1" :: "v"(d), "v"(voff), "s"(r), "s"(soff) : "memory");
    else asm volatile("buffer_store_dwordx2 %0, %1, %2, %3 offen\n\ts_nop 1" :: "v"(d), "v"(voff), "s"(r), "s"(soff) : "memory"); }
#define PINV(x) asm volatile("" : "+v"(x))
#define WAITV(n) asm volatile("s_waitcnt vmcnt(%0)" :: "n"(n) : "memory")
#define WAIT_BAR(n) asm volatile("s_waitcnt vmcnt(%0) lgkmcnt(0)\n\ts_barrier" :: "n"(n) : "memory")
#define SBAR() __builtin_amdgcn_sched_barrier(0)
#define LBAR() asm volatile("s_waitcnt lgkmcnt(0)\n\ts_barrier" ::: "memory")
DI bf16x8 lds_frag(LAS const char* p) { return *(LAS const bf16x8*)p; }
typedef short v4i16_t __attribute__((ext_vector_type(4)));
DI u32x2 lds_tr(LAS const char* p) { return __builtin_bit_cast(u32x2, __builtin_amdgcn_ds_read_tr16_b64_v4i16((LAS v4i16_t*)p)); }
DI bf16x8 tr_frag(LAS const char* p, int off2) { u32x2 a = lds_tr(p), b = lds_tr(p + off2); u32x4 r = {a[0], a[1], b[0], b[1]}; return __builtin_bit_cast(bf16x8, r); }

DI int wo_row(int k) { return k < 512 ? 18 * (k >> 4) + (k & 15) : 18 * ((k - 512) >> 1) + 16 + (k & 1); }
DI void retile_wo(int job, int lane, const char* wo, char* ws, char* scratch_generic) {
    const int s = job / 9, cb = job - 9 * s, r8 = lane >> 3, ch = lane & 7;
#pragma unroll
    for (int p = 0; p < 4; ++p) { const int rho = 8 * p + r8, k = 32 * s + 8 * ((rho >> 2) & 3) + 4 * (rho >> 4) + (rho & 3);
        glds16(wo + cb * 128, wo_row(k) * (D_ * 2) + ch * 16, (unsigned)(uintptr_t)scratch_generic + p * 1024); }
    WAITV(0);
    LAS const char* base = (LAS const char*)scratch_generic + (4 * (lane >> 4) + ((lane >> 2) & 3)) * 128 + (lane & 3) * 8;
#pragma unroll
    for (int u = 0; u < 4; ++u) { const int T = 4 * cb + u, n6 = T / 6, tt = T - 6 * n6;
        *(bf16x8*)(ws + WS_WO + (size_t)(n6 * 108 + s * 6 + tt) * 1024 + lane * 16) = tr_frag(base + 32 * u, 16 * 128); }
}

constexpr int K1_SLOT = 34 * 1024, K1_STG = 0, K1_SMALL = 4 * K1_SLOT, K1_LDS = K1_SMALL + 4096;
template <int PART, int NJ>
DI void k1_wave(const char* x, const char* wqkv, char* ws, char* lds_generic, int mt, int nt) {
    constexpr bool VPART = PART == 2; constexpr int NT = NJ ? 4 : 5, T0 = NJ ? 5 : 0;
    LAS char* lds = (LAS char*)lds_generic; const unsigned ldsb = (unsigned)(uintptr_t)lds_generic;
    const int tid = threadIdx.x, lane = tid & 63, wid = rfl(tid >> 6), mi = wid & 3, m = lane & 15, g = lane >> 4;
    float csink = 0.f;
    if (mi < 2) { const char* c1; CODE_ADDR(c1, preln_ffn_end); touch_code(csink, (const char*)__builtin_amdgcn_s_getpc(), c1, mi, lane); }
    const srd_t sx = make_srd(x, S_ * D_ * 2), sw = make_srd(wqkv, D_ * 3 * D_ * 2);
    const unsigned xv = (lane >> 3) * (D_ * 2) + (((lane & 7) ^ ((4 * wid + (lane >> 4)) & 7)) << 4);
    unsigned wv[2];
#pragma unroll
    for (int i = 0; i < 2; ++i) { const int idx = (i ? 8 : wid) * 64 + lane, rho = (idx * 3641) >> 16, ch = idx - 18 * rho;
        wv[i] = (8 * ((rho >> 2) & 3) + 4 * (rho >> 4) + (rho & 3)) * 3456 + ch * 16; }
    const unsigned xs = (128 * mt + 8 * wid) * (D_ * 2), wsb = nt * 288;
    auto issue = [&](int st, int p) {
        const int k = st; const unsigned dst = ldsb + (st & 3) * K1_SLOT;
        if (p < 2) bdma1(sx, xv, xs + p * 64 * (D_ * 2) + k * 128, dst + (wid + 8 * p) * 1024);
        else if (p < 4) bdma1(sw, wv[0], wsb + (64 * k + 32 * (p - 2)) * 3456, dst + 16384 + ((p - 2) * 9 + wid) * 1024);
        else if (wid < 2) bdma1(sw, wv[1], wsb + (64 * k + 32 * wid) * 3456, dst + 16384 + (wid * 9 + 8) * 1024); };
#pragma unroll
    for (int st = 0; st < 2; ++st)
#pragma unroll
        for (int p = 0; p < 5; ++p) issue(st, p);
    unsigned sa[NT][2], sd[NT][2];
#pragma unroll
    for (int t = 0; t < NT; ++t) { const int tt = T0 + t;
        if (VPART) { const int n = 16 * tt + m, hl = (n * 3641) >> 16, d = n - 18 * hl, row = d + (d >= 4 ? 5 : 0) - (d >= 12 ? 12 : 0) + (d >= 16 ? 8 : 0);
            sa[t][0] = K1_STG + hl * 5120 + 2 * mi * 640 + (g >> 1) * 320 + row * 16 + (g & 1) * 8; PINV(sa[t][0]); }
        else {
#pragma unroll
            for (int p = 0; p < 2; ++p) { const int n = 16 * tt + 4 * g + 2 * p, hl = (n * 3641) >> 16, d = n - 18 * hl; const bool tail = d == 16;
                const int o = PART == 0 ? (tail ? 32768 + hl * 512 : hl * 4096 + d * 2) : hl * 4608 + (tail ? 4096 : (d >> 3) * 2048 + (d & 7) * 2);
                const int ts = tail ? 4 : PART == 0 ? 32 : 16;
                sa[t][p] = K1_STG + o + (32 * mi + m) * ts; sd[t][p] = 16 * ts; PINV(sa[t][p]); PINV(sd[t][p]); } } }
    f32x4 acc[NT][2];
    f32x4 accs = {0, 0, 0, 0}, accg = {0, 0, 0, 0}, accc[NT];
#pragma unroll
    for (int t = 0; t < NT; ++t) accc[t] = (f32x4){0, 0, 0, 0};
    bf16x8 ones = __builtin_bit_cast(bf16x8, (u32x4){0x3f803f80u, 0x3f803f80u, 0x3f803f80u, 0x3f803f80u}); PINV(ones);
    asm volatile("s_nop 1" : "+v"(accs), "+v"(accg));
#pragma unroll
    for (int t = 0; t < NT; ++t) PINV(accc[t]);
    const unsigned xa0 = (32 * mi + m) * 128 + ((g ^ ((m >> 1) & 7)) << 4), xa1 = (32 * mi + m) * 128 + (((4 + g) ^ ((m >> 1) & 7)) << 4);
    const unsigned wa = 16384 + (4 * g + ((lane >> 2) & 3)) * 288 + (lane & 3) * 8 + T0 * 32;
    bf16x8 xq[2][2][2], wr[2][2][NT];
    auto rd = [&](int st, int i) {
        LAS const char* sb = lds + (st & 3) * K1_SLOT;
        if (i < 4) xq[st & 1][i >> 1][i & 1] = lds_frag(sb + ((i >> 1) ? xa1 : xa0) + (i & 1) * 2048);
        else { const int s2 = (i - 4) / NT, t = (i - 4) % NT; wr[st & 1][s2][t] = tr_frag(sb + wa + s2 * 9216 + 32 * t, 16 * 288); } };
    WAIT_BAR(4);
    PINV(csink);

#pragma unroll
    for (int i = 0; i < 4 + 2 * NT; ++i) rd(0, i);
#pragma unroll
    for (int p = 0; p < 5; ++p) issue(2, p);
#pragma unroll
    for (int st = 0; st < 9; ++st) {
        if (st < 7) WAIT_BAR(4); else if (st == 7) WAIT_BAR(0);
#pragma unroll
        for (int i = 0; i < 2 * NT; ++i) { const int s2 = i / NT, t = i % NT; const bf16x8 w = wr[st & 1][s2][t], x0 = xq[st & 1][s2][0], x1 = xq[st & 1][s2][1];
            if (st == 0 && s2 == 0) { if (VPART) { mfma16_new(acc[t][0], x0, w); mfma16_new(acc[t][1], x1, w); } else { mfma16_new(acc[t][0], w, x0); mfma16_new(acc[t][1], w, x1); } }
            else { if (VPART) { mfma16_acc(acc[t][0], x0, w); mfma16_acc(acc[t][1], x1, w); } else { mfma16_acc(acc[t][0], w, x0); mfma16_acc(acc[t][1], w, x1); } }
            if (st + 1 < 9) { if (i < 4) { rd(st + 1, i); rd(st + 1, 4 + i); } else if (4 + i < 4 + 2 * NT) rd(st + 1, 4 + i); }
            if (st + 3 < 9 && (i & 1) == 0 && i / 2 < 4) issue(st + 3, i / 2 == 0 ? 0 : i / 2 == 1 ? 2 : i / 2 == 2 ? 1 : 3);
            if (st + 3 < 9 && i == 7) issue(st + 3, 4);
            if (t == 1) mfma16_acc(accs, ones, NJ ? x1 : x0);
            if (t == 2) mfma16_acc(accg, NJ ? x1 : x0, NJ ? x1 : x0);
            if (t == NT - 1 && ((2 * st + s2) & 3) == mi) {
#pragma unroll
                for (int tt = 0; tt < NT; ++tt) { if (VPART) mfma16_acc(accc[tt], ones, wr[st & 1][s2][tt]); else mfma16_acc(accc[tt], wr[st & 1][s2][tt], ones); } }
            SBAR(); }
    }
    LAS float* sm = (LAS float*)(lds + K1_SMALL);
    asm volatile("s_nop 7\n\ts_nop 7" : "+v"(acc[NT - 1][0]), "+v"(acc[NT - 1][1]), "+v"(accs), "+v"(accg));
#pragma unroll
    for (int t = 0; t < NT; ++t) { PINV(accc[t]); PINV(acc[t][0]); PINV(acc[t][1]); }
    if (g == 0) sm[32 * mi + 16 * NJ + m] = accs[0];
    if (g == (m >> 2)) sm[128 + 32 * mi + 16 * NJ + m] = (m & 2) ? ((m & 1) ? accg[3] : accg[2]) : ((m & 1) ? accg[1] : accg[0]);
#pragma unroll
    for (int t = 0; t < NT; ++t) { if (VPART) { if (g == 0) sm[256 + mi * 144 + 16 * (T0 + t) + m] = accc[t][0]; }
        else if (m == 0) *(LAS f32x4*)(sm + 256 + mi * 144 + 16 * (T0 + t) + 4 * g) = accc[t]; }
    LBAR();
    LAS char* stg = lds + K1_STG;
    if (VPART && tid < 256) { const int hs = tid >> 1;
        *(LAS u32x4*)(stg + hs * 320 + ((tid & 1) ? 19 : 4) * 16) = (tid & 1) ? (u32x4){0x3f803f80u, 0x3f803f80u, 0x3f803f80u, 0x3f803f80u} : (u32x4){0, 0, 0, 0}; }
    f32x4 mean[2], rstd[2];
#pragma unroll
    for (int u = 0; u < 2; ++u) { const int tl = 32 * mi + 16 * u + (VPART ? 4 * g : m);
        if (VPART) { mean[u] = *(LAS const f32x4*)(sm + tl) * (1.f / D_);
            const f32x4 var = *(LAS const f32x4*)(sm + 128 + tl) * (1.f / D_) - mean[u] * mean[u];
#pragma unroll
            for (int r = 0; r < 4; ++r) rstd[u][r] = __builtin_amdgcn_rsqf(fmaxf(var[r], 0.f) + EPS); }
        else { mean[u][0] = sm[tl] * (1.f / D_);
            rstd[u][0] = __builtin_amdgcn_rsqf(fmaxf(sm[128 + tl] * (1.f / D_) - mean[u][0] * mean[u][0], 0.f) + EPS) * (PART == 0 ? QSCALE : 1.f); } }
    float c1[NT]; f32x4 c4[NT];
#pragma unroll
    for (int t = 0; t < NT; ++t) { const int tt = T0 + t;
        if (VPART) { const int n = 16 * tt + m; c1[t] = (sm[256 + n] + sm[400 + n]) + (sm[544 + n] + sm[688 + n]); }
        else { const int n0 = 16 * tt + 4 * g;
            c4[t] = (*(LAS const f32x4*)(sm + 256 + n0) + *(LAS const f32x4*)(sm + 400 + n0)) + (*(LAS const f32x4*)(sm + 544 + n0) + *(LAS const f32x4*)(sm + 688 + n0)); } }
#pragma unroll
    for (int t = 0; t < NT; ++t)
#pragma unroll
        for (int u = 0; u < 2; ++u) {
            if (VPART) { const f32x4 a = (acc[t][u] - mean[u] * c1[t]) * rstd[u];
                *(LAS u32x2*)(lds + sa[t][0] + u * 640) = (u32x2){cvtpk(a[0], a[1]), cvtpk(a[2], a[3])}; }
            else { const f32x4 a = (acc[t][u] - c4[t] * mean[u][0]) * rstd[u][0];
                *(LAS unsigned*)(lds + sa[t][0] + (u ? sd[t][0] : 0u)) = cvtpk(a[0], a[1]); *(LAS unsigned*)(lds + sa[t][1] + (u ? sd[t][1] : 0u)) = cvtpk(a[2], a[3]); } }
    if (nt == 0 && tid < 128) ((float*)(ws + WS_MUX))[128 * mt + tid] = sm[tid] * (1.f / D_);
    LBAR();
    const int head = 8 * (nt & 3) + wid; const srd_t sws = make_srd(ws, (unsigned)WS_END); const unsigned l16 = lane * 16;
    if (PART == 2) { const unsigned dst = (unsigned)WS_KV + head * KVIMG + (mt >> 1) * KVCHUNK + 9216 + (mt & 1) * 5120;
#pragma unroll
        for (int i = 0; i < 5; ++i) bstore16<1>(*(LAS const u32x4*)(stg + wid * 5120 + i * 1024 + l16), sws, l16, dst + i * 1024);
    } else if (PART == 0) { const unsigned dst = (unsigned)WS_QM + (head * S_ + 128 * mt) * 32;
#pragma unroll
        for (int i = 0; i < 4; ++i) bstore16<1>(*(LAS const u32x4*)(stg + wid * 4096 + i * 1024 + l16), sws, l16, dst + i * 1024);
        if (lane < 32) bstore16<1>(*(LAS const u32x4*)(stg + 32768 + wid * 512 + l16), sws, l16, (unsigned)WS_QT + (head * S_ + 128 * mt) * 4);
    } else { const unsigned dst = (unsigned)WS_KV + head * KVIMG + (mt >> 1) * KVCHUNK; LAS const char* hb = stg + wid * 4608;
#pragma unroll
        for (int i = 0; i < 4; ++i) bstore16<1>(*(LAS const u32x4*)(hb + i * 1024 + l16), sws, l16, dst + (i >> 1) * 4096 + (mt & 1) * 2048 + (i & 1) * 1024);
        if (lane < 32) bstore16<1>(*(LAS const u32x4*)(hb + 4096 + l16), sws, l16, dst + 8192 + (mt & 1) * 512);
        float mx = 0.f;
#pragma unroll
        for (int i = 0; i < 2; ++i) { const int tl = lane + 64 * i;
            const u32x4 a = *(LAS const u32x4*)(hb + tl * 16), bq = *(LAS const u32x4*)(hb + 2048 + tl * 16); const unsigned c = *(LAS const unsigned*)(hb + 4096 + tl * 4);
            float s2 = dot2(c, c, 0.f);
#pragma unroll
            for (int e = 0; e < 4; ++e) { s2 = dot2(a[e], a[e], s2); s2 = dot2(bq[e], bq[e], s2); }
            mx = fmaxf(mx, s2); }
        mx = fmaxf(mx, xor16(mx)); mx = fmaxf(mx, xor32(mx));
#pragma unroll
        for (int o = 1; o < 16; o <<= 1) mx = fmaxf(mx, __shfl_xor(mx, o));
        if (lane == 0) ((float*)(ws + WS_KMAX))[head * 16 + mt] = mx;
    }
}
template <int PART> DI void k1_body(const char* x, const char* wqkv, char* ws, char* lds, int mt, int nt) {
    if (rfl(threadIdx.x >> 6) >= 4) k1_wave<PART, 1>(x, wqkv, ws, lds, mt, nt); else k1_wave<PART, 0>(x, wqkv, ws, lds, mt, nt);
}
__global__ __launch_bounds__(512, 2) void k1_qkv(const char* x, const char* wqkv, const char* wo, char* ws) {
    extern __shared__ __attribute__((aligned(16))) char lds[];
    const int b = blockIdx.x;
    if (b >= 192) {
        const int lane = threadIdx.x & 63, wv = rfl(threadIdx.x >> 6), gw = (b - 192) * 8 + wv;
        float sink = 0.f;
        if (b - 192 < 8) { const char *c0, *c1; CODE_ADDR(c0, preln_k2_begin); CODE_ADDR(c1, preln_ffn_end); touch_code(sink, c0, c1, wv + 8 * (b - 192), lane); }
        if (threadIdx.x == 511) ((unsigned*)(ws + WS_CNT))[(b - 192) * 64] = 0u;
        if (threadIdx.x == 510 && b - 192 < 40) ((unsigned*)(ws + WS_CNT))[(64 + b - 192) * 64] = 0u;
        if (gw < 162) retile_wo(gw, lane, wo, ws, lds + wv * 4096);
        for (int c = gw; c < D_; c += 512) {
            const char* row = wo + (size_t)wo_row(c) * (D_ * 2);
            const u32x4 a = *(const u32x4*)(row + lane * 16); float s = 0.f;
#pragma unroll
            for (int e = 0; e < 4; ++e) s = dot2(a[e], 0x3f803f80u, s);
            if (lane < 8) { const u32x4 bq = *(const u32x4*)(row + 1024 + lane * 16);
#pragma unroll
                for (int e = 0; e < 4; ++e) s = dot2(bq[e], 0x3f803f80u, s); }
#pragma unroll
            for (int o = 1; o < 64; o <<= 1) s += __shfl_xor(s, o);
            if (lane == 0) { const float w = s * (1.f / D_); const unsigned hi = cvtpk(w, 0.f) & 0xffffu; const unsigned lo = cvtpk(w - bflo(hi), 0.f) & 0xffffu;
                ((unsigned short*)(ws + WS_WBAR))[c] = (unsigned short)hi; ((unsigned short*)(ws + WS_WBAR))[D_ + c] = (unsigned short)lo; }
        }
        asm volatile("s_waitcnt vmcnt(0)" : "+v"(sink));
        return;
    }
    const int xcd = b & 7, idx = b >> 3, mt = (xcd & 3) * 4 + (idx & 3), nt = (xcd >> 2) * 6 + (idx >> 2);
    if (nt >= 8) k1_body<2>(x, wqkv, ws, lds, mt, nt); else if (nt >= 4) k1_body<1>(x, wqkv, ws, lds, mt, nt); else k1_body<0>(x, wqkv, ws, lds, mt, nt);
}

constexpr int K2_LDS = 160 * 1024;
struct RtJob { unsigned so[4], dst[4]; };
struct RtTable { RtJob j[8][486];
    constexpr RtTable() : j{} {
        for (int xg = 0; xg < 8; ++xg) for (int slot = 0; slot < 486; ++slot) { RtJob& e = j[xg][slot];
            if (slot < 324) { const int gu = slot >= 162, r = slot - 162 * gu, ks = r / 9, cb = 9 * xg + r % 9;
                for (int p = 0; p < 4; ++p) { const int T = 4 * cb + p, jj = T / 9, tt = T % 9;
                    e.so[p] = (ks * 32 + p * 8) * (F_ * 2) + cb * 128;
                    e.dst[p] = (unsigned)WS_W1 + (jj * 324 + (tt < 4 ? ks * 8 + tt * 2 : tt < 8 ? (tt >> 1) * 72 + ks * 4 + (tt & 1) * 2 : 288 + ks * 2) + gu) * 1024; }
            } else { const int r2 = slot - 324, q = r2 / 9, cb = r2 % 9, ks = 18 * xg + q, a = ks / 9, qi = ks % 9;
                const int t0 = qi == 8 ? 18 * a + 8 : 18 * a + 9 * (qi >> 2) + 2 * (qi & 3), t1 = qi == 8 ? t0 + 9 : t0 + 1;
                for (int p = 0; p < 4; ++p) { const int T = 4 * cb + p, n4 = T / 9, tt = T % 9;
                    e.so[p] = (16 * (p >> 1 ? t1 : t0) + 8 * (p & 1)) * (D_ * 2) + cb * 128;
                    e.dst[p] = (unsigned)WS_W2 + (((xg >> 1) * 4 + n4) * 324 + (((q >> 1) * 2 + (xg & 1)) * 2 + (q & 1)) * 9 + tt) * 1024; } } }
    } };
__device__ const RtTable rt_table{};
#define EXPS(S, a, b) _Pragma("unroll") for (int r_ = a; r_ < b; ++r_) S[r_] = __builtin_amdgcn_exp2f(S[r_]); SBAR()
#define PLO(S) __builtin_bit_cast(bf16x16, (f32x8)__builtin_shufflevector(S, S, 0, 1, 2, 3, 4, 5, 6, 7))
#define PHI(S) __builtin_bit_cast(bf16x16, (f32x8)__builtin_shufflevector(S, S, 8, 9, 10, 11, 12, 13, 14, 15))
#define KTAIL(p) __builtin_bit_cast(s16x4, (u32x2){*(LAS const unsigned*)(p), kc})
#define QK1(q) __builtin_amdgcn_mfma_f32_32x32x16_bf16(kf, q, (f32x16){}, 0, 0, 0)
#define QK2(q, s) __builtin_amdgcn_mfma_f32_32x32x8bf16_1k(kt, q, s, 0, 0, 0)
#define PV(v, p, o) __builtin_amdgcn_smfmac_f32_32x32x32_bf16(v, p, o, 0xDDDD, 0, 0)
template <class Hook>
DI void attn_fast(LAS const char* kp, LAS const char* tp, LAS const char* vp, const bf16x8 (&qm)[2], const s16x4 (&qt)[2], f32x16 (&o)[2], unsigned kc, Hook hook) {
    bf16x8 kf = *(LAS const bf16x8*)kp; s16x4 kt = KTAIL(tp); bf16x8 v0, v1;
    f32x16 s0 = QK1(qm[0]), s1; s0 = QK2(qt[0], s0);
    SBAR();
#pragma unroll
    for (int it = 0; it < 8; ++it) {
        { if (it == 0) __builtin_amdgcn_s_setprio(3); if (it == 2) __builtin_amdgcn_s_setprio(2); if (it == 4) __builtin_amdgcn_s_setprio(1); if (it == 6) __builtin_amdgcn_s_setprio(0); }
        EXPS(s0, 0, 2);
        if (it > 0) { o[1] = PV(v0, PLO(s1), o[1]); SBAR(); }
        EXPS(s0, 2, 6);
        if (it > 0) { o[1] = PV(v1, PHI(s1), o[1]); SBAR(); }
        v0 = *(LAS const bf16x8*)(vp + it * 1280); v1 = *(LAS const bf16x8*)(vp + it * 1280 + 640); SBAR();
        hook(it);
        EXPS(s0, 6, 9);
        s1 = QK1(qm[1]); SBAR();
        EXPS(s0, 9, 12);
        s1 = QK2(qt[1], s1); SBAR();
        if (it + 1 < 8) { kf = *(LAS const bf16x8*)(kp + (it + 1) * 512); kt = KTAIL(tp + (it + 1) * 128); SBAR(); }
        EXPS(s0, 12, 16);
        EXPS(s1, 0, 2);
        o[0] = PV(v0, PLO(s0), o[0]); SBAR();
        EXPS(s1, 2, 6);
        o[0] = PV(v1, PHI(s0), o[0]); SBAR();
        EXPS(s1, 6, 9);
        if (it + 1 < 8) { s0 = QK1(qm[0]); SBAR(); }
        EXPS(s1, 9, 12);
        if (it + 1 < 8) { s0 = QK2(qt[0], s0); SBAR(); }
        EXPS(s1, 12, 16);
    }
    o[1] = PV(v0, PLO(s1), o[1]); o[1] = PV(v1, PHI(s1), o[1]);
}
DI void attn_slow(LAS const char* kp, LAS const char* tp, LAS const char* vp, const bf16x8 (&qm)[2], const s16x4 (&qt)[2], f32x16 (&o)[2], float (&mrun)[2], unsigned kc) {
#pragma unroll 1
    for (int it = 0; it < 8; ++it) {
        const bf16x8 kf = *(LAS const bf16x8*)(kp + it * 512); const s16x4 kt = KTAIL(tp + it * 128);
        const bf16x8 v0 = *(LAS const bf16x8*)(vp + it * 1280), v1 = *(LAS const bf16x8*)(vp + it * 1280 + 640);
#pragma unroll
        for (int u = 0; u < 2; ++u) {
            f32x16 s = QK1(qm[u]); s = QK2(qt[u], s);
            float mx = s[0];
#pragma unroll
            for (int r = 1; r < 16; ++r) mx = fmaxf(mx, s[r]);
            mx = fmaxf(mx, xor32(mx));
            const float mn = fmaxf(mrun[u], mx), al = __builtin_amdgcn_exp2f(mrun[u] - mn);
            mrun[u] = mn;
#pragma unroll
            for (int r = 0; r < 16; ++r) { s[r] = __builtin_amdgcn_exp2f(s[r] - mn); o[u][r] *= al; }
            o[u] = PV(v0, PLO(s), o[u]); o[u] = PV(v1, PHI(s), o[u]);
        }
    }
}
__global__ __launch_bounds__(1024, 4) void k2_attn(const char* wg, const char* wu, const char* wd, char* ws) {
    extern __shared__ __attribute__((aligned(16))) char lds_generic[];
    LAS char* lds = (LAS char*)lds_generic; const unsigned ldsb = (unsigned)(uintptr_t)lds_generic;
    const int tid = threadIdx.x, lane = tid & 63, wid = rfl(tid >> 6), qg = wid & 3, kq = wid >> 2, r32 = lane & 31, h = lane >> 5;
    CODE_LABEL(preln_k2_begin);
    const int b = blockIdx.x, xcd = b & 7, idx = b >> 3, head = xcd * 4 + (idx & 3), qb = idx >> 2;
    const srd_t srd = make_srd(ws, (unsigned)WS_END);
    const int c0 = (2 * kq + (qb & 1)) * KVCHUNK, c1 = (2 * kq + ((qb & 1) ^ 1)) * KVCHUNK;
    const unsigned kvb = (unsigned)WS_KV + head * KVIMG, l16 = lane * 16;
    const int q0 = 256 * qb + 64 * qg;
    bf16x8 qm[2]; float qtl[2]; float km;
    { const unsigned so = (unsigned)WS_QM + (head * S_ + q0) * 32, vo = r32 * 32 + h * 16; bload16<0>(qm[0], srd, vo, so); bload16<1024>(qm[1], srd, vo, so);
      const unsigned st = (unsigned)WS_QT + (head * S_ + q0) * 4; bload4<0>(qtl[0], srd, r32 * 4, st); bload4<128>(qtl[1], srd, r32 * 4, st);
      bload4<0>(km, srd, (lane & 15) * 4, (unsigned)WS_KMAX + head * 64); }
    bdma4(srd, l16, kvb + c0 + qg * 5120, ldsb + c0 + qg * 5120); if (qg < 3) bdma1(srd, l16, kvb + c0 + qg * 5120 + 4096, ldsb + c0 + qg * 5120 + 4096);
    const int slot = wid * 32 + idx; const bool rt = slot < 486;
    bf16x8 wb[4]; unsigned rdst[4], rso[4];
    { const RtJob& J = rt_table.j[xcd][rt ? slot : 0];
#pragma unroll
      for (int p = 0; p < 4; ++p) { rso[p] = J.so[p]; rdst[p] = J.dst[p]; } }
    const char* rsrc = slot < 162 ? wg : slot < 324 ? wu : wd;
    const unsigned rvo = (lane >> 3) * (slot < 324 ? F_ * 2 : D_ * 2) + (lane & 7) * 16;
    asm volatile("s_waitcnt vmcnt(4)" : "+v"(qm[0]), "+v"(qm[1]), "+v"(qtl[0]), "+v"(qtl[1]), "+v"(km) :: "memory");
    km = fmaxf(km, row_ror<8>(km)); km = fmaxf(km, row_ror<4>(km)); km = fmaxf(km, row_ror<2>(km)); km = fmaxf(km, row_ror<1>(km));
    s16x4 qt[2]; bool unsafe = false;
#pragma unroll
    for (int u = 0; u < 2; ++u) { const u32x4 qv = __builtin_bit_cast(u32x4, qm[u]); const unsigned tl = __float_as_uint(qtl[u]);
        float n2 = h ? 0.f : dot2(tl, tl, 0.f);
#pragma unroll
        for (int e = 0; e < 4; ++e) n2 = dot2(qv[e], qv[e], n2);
        n2 += xor32(n2);
        const float bound = __builtin_amdgcn_sqrtf(n2 * km) * 1.0001f + 1e-6f;
        unsafe |= !(bound <= SAFE_B);
        const unsigned cb = (__float_as_uint(bound) + 0xffffu) >> 16;
        const u32x2 w = h ? (u32x2){0u, 0u} : (u32x2){tl, cb | 0x8000u};
        qt[u] = __builtin_bit_cast(s16x4, w); }
    const bool slow = __any(unsafe);
    if (slow) {
#pragma unroll
        for (int u = 0; u < 2; ++u) { u32x2 w = __builtin_bit_cast(u32x2, qt[u]); w[1] = 0u; qt[u] = __builtin_bit_cast(s16x4, w); } }
    f32x16 o[2] = {(f32x16){}, (f32x16){}}; float mrun[2] = {-INFINITY, -INFINITY};
    const int vrow = r32 >= 19 ? 4 : r32 + (r32 >= 4 ? 5 : 0) - (r32 >= 12 ? 12 : 0) + (r32 >= 16 ? 8 : 0);
    const unsigned kc = h ? 0u : 0x00003f80u;
    unsigned kp = ldsb + c0 + h * 4096 + r32 * 16, tp = ldsb + c0 + 8192 + r32 * 4, vp = ldsb + c0 + 9216 + h * 320 + vrow * 16;
#define KP(o) ((LAS const char*)(uintptr_t)kp + (o))
#define TP(o) ((LAS const char*)(uintptr_t)tp + (o))
#define VP(o) ((LAS const char*)(uintptr_t)vp + (o))

    WAIT_BAR(0);
    const srd_t sw = make_srd(rsrc, D_ * F_ * 2);
    auto chunk1 = [&](int part) { if (part == 0) bdma4(srd, l16, kvb + c1 + qg * 5120, ldsb + c1 + qg * 5120);
                                  else if (qg < 3) bdma1(srd, l16, kvb + c1 + qg * 5120 + 4096, ldsb + c1 + qg * 5120 + 4096); };
    if (slow) { chunk1(0); chunk1(1);
        if (rt) {
#pragma unroll
            for (int p = 0; p < 4; ++p) bload16<0>(wb[p], sw, rvo, rso[p]); }
        attn_slow(KP(0), TP(0), VP(0), qm, qt, o, mrun, kc); }
    else attn_fast(KP(0), TP(0), VP(0), qm, qt, o, kc, [&](int it) {
        if (it < 2) { chunk1(it); SBAR(); }
        if (it >= 2 && it < 6 && rt) { bload16<0>(wb[it - 2], sw, rvo, rso[it - 2]); SBAR(); } });
    if (rt) WAIT_BAR(4); else WAIT_BAR(0);
    if (rt) { asm volatile("s_waitcnt vmcnt(0)" : "+v"(wb[0]), "+v"(wb[1]), "+v"(wb[2]), "+v"(wb[3]) :: "memory");
        int ty = threadIdx.x; asm volatile("" : "+v"(ty)); LAS char* scr = lds + c0 + qg * 4864 + (ty & 63) * 16;
#pragma unroll
        for (int p = 0; p < 4; ++p) *(LAS bf16x8*)(scr + p * 1024) = wb[p]; }
    auto rt_step = [&](int it) {
        if (it < 1 || it > 4 || !rt) return;
        int ty = threadIdx.x; asm volatile("" : "+v"(ty)); const int ln = ty & 63;
        LAS const char* base = lds + c0 + qg * 4864 + (4 * (ln >> 4) + ((ln >> 2) & 3)) * 128 + (ln & 3) * 8;
        bstore16<1>(__builtin_bit_cast(u32x4, tr_frag(base + 32 * (it - 1), 16 * 128)), srd, ln * 16, rdst[it - 1]);
        SBAR(); };
    { const int dc = c1 - c0; kp += dc; tp += dc; vp += dc; PINV(kp); PINV(tp); PINV(vp); }
    if (slow) {
#pragma unroll
        for (int it = 1; it < 5; ++it) rt_step(it);
        attn_slow(KP(0), TP(0), VP(0), qm, qt, o, mrun, kc); }
    else attn_fast(KP(0), TP(0), VP(0), qm, qt, o, kc, rt_step);
    int tz = threadIdx.x; asm volatile("" : "+v"(tz));
    const int lane2 = tz & 63, r2 = tz & 31, h2 = (tz >> 5) & 1;
    LAS float* mx = (LAS float*)(lds + KVIMG);
#pragma unroll
    for (int u = 0; u < 2; ++u) { LAS char* pb = lds + c0 + (qg * 2 + u) * 2432;
        *(LAS f32x4*)(pb + lane2 * 16) = (f32x4){o[u][0], o[u][1], o[u][2], o[u][3]};
        *(LAS f32x4*)(pb + 1024 + lane2 * 16) = (f32x4){o[u][4], o[u][5], o[u][6], o[u][7]};
        if (h2 == 0) { *(LAS f32x2*)(pb + 2048 + r2 * 8) = (f32x2){o[u][8], o[u][9]}; *(LAS float*)(pb + 2304 + r2 * 4) = o[u][10];
                      if (slow) mx[wid * 64 + 32 * u + r2] = mrun[u]; } }
    LBAR();
    { const int u = kq & 1, hf = kq >> 1;
      LAS const char* pb = lds + (qb & 1) * KVCHUNK + (qg * 2 + u) * 2432;
      float l = 0.f; f32x4 a = {0, 0, 0, 0}; f32x2 c = {0, 0};
      if (slow) { float mk[4], M = -INFINITY;
#pragma unroll
          for (int k = 0; k < 4; ++k) { mk[k] = mx[(4 * k + qg) * 64 + 32 * u + r2]; M = fmaxf(M, mk[k]); }
#pragma unroll
          for (int k = 0; k < 4; ++k) { LAS const char* p = pb + k * 2 * KVCHUNK; const float f = __builtin_amdgcn_exp2f(mk[k] - M);
              l += f * *(LAS const float*)(p + 2304 + r2 * 4); a += *(LAS const f32x4*)(p + hf * 1024 + lane2 * 16) * f; c += *(LAS const f32x2*)(p + 2048 + r2 * 8) * f; }
      } else {
#pragma unroll
          for (int k = 0; k < 4; ++k) { LAS const char* p = pb + k * 2 * KVCHUNK;
              l += *(LAS const float*)(p + 2304 + r2 * 4); a += *(LAS const f32x4*)(p + hf * 1024 + lane2 * 16); c += *(LAS const f32x2*)(p + 2048 + r2 * 8); } }
      const float rl = __builtin_amdgcn_rcpf(l);
      const int token = q0 + 32 * u + r2; char* ob = ws + WS_OF + (size_t)(token >> 4) * 18 * 1024 + (token & 15) * 16;
      *(u32x2*)(ob + (head >> 1) * 1024 + (2 * (head & 1) + hf) * 256 + h2 * 8) = (u32x2){cvtpk(a[0] * rl, a[1] * rl), cvtpk(a[2] * rl, a[3] * rl)};
      if (hf == 0 && h2 == 0) *(unsigned*)(ob + (16 + (head >> 4)) * 1024 + ((head >> 2) & 3) * 256 + (head & 3) * 4) = cvtpk(c[0] * rl, c[1] * rl);
    }
}

constexpr int RING_SLOT = 36 * 1024, RING0 = 16 * 1024, G_LDS = RING0 + 4 * RING_SLOT;
constexpr int K3_X = 108 * 1024, K3_WB = K3_X + 12 * 1024, K3_Z = K3_WB + 2304;
DI void ring_w1(char* ws, char* lds_generic, int xcd, int idx, int st);
DI void k3_phase(const char* x, char* ws, char* lds_generic, int xcd, int idx) {
    LAS char* lds = (LAS char*)lds_generic; const unsigned ldsb = (unsigned)(uintptr_t)lds_generic;
    const int tid = threadIdx.x, lane = tid & 63, wid = rfl(tid >> 6), mi = wid & 3, kj = wid >> 2, m = lane & 15, g = lane >> 4;
    const int mt32 = xcd * 4 + (idx & 3), n6 = idx >> 2, mt16 = 4 * mt32 + mi;
    const srd_t srd = make_srd(ws, (unsigned)WS_END), sx = make_srd(x, S_ * D_ * 2);
    const unsigned l16 = lane * 16, wo0 = (unsigned)WS_WO + n6 * (108 * 1024), ob = (unsigned)WS_OF + (mt16 * 18 + 9 * kj) * 1024;
    bf16x8 of[9];
    float mux;
    { const unsigned f0 = (wid < 7 ? 14 * wid : 94) * 1024;
      bload16<0>(of[0], srd, l16, ob); bload16<1024>(of[1], srd, l16, ob); bload16<2048>(of[2], srd, l16, ob); bload16<3072>(of[3], srd, l16, ob);
      bdma4(srd, l16, wo0 + f0, ldsb + f0); bdma4(srd, l16, wo0 + f0 + 4096, ldsb + f0 + 4096);
      bload16<0>(of[4], srd, l16, ob + 4096); bload16<1024>(of[5], srd, l16, ob + 4096); bload16<2048>(of[6], srd, l16, ob + 4096); bload16<3072>(of[7], srd, l16, ob + 4096);
      bdma4(srd, l16, wo0 + f0 + 8192, ldsb + f0 + 8192); bdma1(srd, l16, wo0 + f0 + 12288, ldsb + f0 + 12288); bdma1(srd, l16, wo0 + f0 + 13312, ldsb + f0 + 13312);
      bload16<0>(of[8], srd, l16, ob + 8192); }
#pragma unroll
    for (int i = 0; i < 2; ++i) { int pc = wid + 8 * i; pc = pc > 11 ? 11 : pc; const int q = pc * 64 + lane, row = (q * 5462) >> 16;
        bdma1(sx, row * (D_ * 2) + (q - 12 * row) * 16, (64 * mt32) * (D_ * 2) + n6 * 192, ldsb + K3_X + pc * 1024); }
    if (wid < 2) bdma1(srd, l16, (unsigned)WS_WBAR + wid * 1024, ldsb + K3_WB + wid * 1024);
    if (wid == 2 && lane < 16) bdma1(srd, l16, (unsigned)WS_WBAR + 2048, ldsb + K3_WB + 2048);
    bload4<0>(mux, srd, m * 4, (unsigned)WS_MUX + mt16 * 64);
    if (tid < 37) *(LAS u32x4*)(lds + K3_Z + tid * 16) = (u32x4){0, 0, 0, 0};
    f32x4 acc[6], accw = {0, 0, 0, 0};
    asm volatile("s_nop 1" : "+v"(accw));
    LAS const char* ab = lds + kj * (54 * 1024) + lane * 16;
    LAS const char* wbp = lds + (m < 2 ? K3_WB + m * (D_ * 2) + kj * 576 + g * 16 : K3_Z);
    bf16x8 wr[3][6];
    auto rd = [&](int s) {
#pragma unroll
        for (int t = 0; t < 6; ++t) wr[s % 3][t] = lds_frag(ab + (s * 6 + t) * 1024);
        SBAR(); };
    auto step = [&](int s) { PINV(of[s]);
#pragma unroll
        for (int t = 0; t < 6; ++t) { if (s == 0) mfma16_new(acc[t], wr[s % 3][t], of[s]); else mfma16_acc(acc[t], wr[s % 3][t], of[s]); }
        SBAR(); };
    WAIT_BAR(0);
    ring_w1(ws, lds_generic, xcd, idx, 0);
    bf16x8 wbf[9];
    rd(0); rd(1);
#pragma unroll
    for (int s = 0; s < 9; ++s) { if (s + 2 < 9) rd(s + 2); wbf[s] = lds_frag(wbp + s * 64); step(s); }

#pragma unroll
    for (int s = 0; s < 9; ++s) mfma16_acc(accw, wbf[s], of[s]);
    PINV(mux);
    const int token = 16 * mt16 + m;
    u32x2 xr[4];
#pragma unroll
    for (int t = 0; t < 4; ++t) xr[t] = *(LAS const u32x2*)(lds + K3_X + (16 * mi + m) * 192 + (4 * kj + (kj ? (t & 1) : t)) * 32 + g * 8);
    asm volatile("s_nop 7" : "+v"(acc[5]), "+v"(acc[4]), "+v"(accw));
    LBAR();
    LAS char* ex = lds + mi * 8192 + lane * 16;
    if (kj == 0) { *(LAS f32x4*)(ex + 4 * 1024) = acc[4]; *(LAS f32x4*)(ex + 5 * 1024) = acc[5]; *(LAS f32x4*)(ex + 6 * 1024) = accw; }
    else {
#pragma unroll
        for (int t = 0; t < 4; ++t) *(LAS f32x4*)(ex + t * 1024) = acc[t];
        *(LAS f32x4*)(ex + 7 * 1024) = accw; }
    LBAR();
    accw += *(LAS const f32x4*)(ex + (kj ? 6 : 7) * 1024);
    const float mu = mux + __shfl(accw[0] + accw[1], m);
    float ssq = 0.f;
    auto fin = [&](int t, u32x2 xv, unsigned (&pk)[2]) {
        const f32x4 a = acc[t] + *(LAS const f32x4*)(ex + t * 1024);
        const unsigned h0 = cvtpk(a[0] + bflo(xv[0]), a[1] + bfhi(xv[0])), h1 = cvtpk(a[2] + bflo(xv[1]), a[3] + bfhi(xv[1]));
        pk[0] = cvtpk(bflo(h0) - mu, bfhi(h0) - mu); pk[1] = cvtpk(bflo(h1) - mu, bfhi(h1) - mu);
        ssq = dot2(pk[0], pk[0], ssq); ssq = dot2(pk[1], pk[1], ssq); };
    auto pair = [&](int t, u32x2 xa, u32x2 xb2) { unsigned p0[2], p1[2]; fin(t, xa, p0); fin(t + 1, xb2, p1);
        bstore16<1>((u32x4){p0[0], p0[1], p1[0], p1[1]}, srd, l16, rfl((unsigned)WS_HCF + (mt16 * 18 + 3 * n6 + (t >> 1)) * 1024)); };
    if (kj == 0) { pair(0, xr[0], xr[1]); pair(2, xr[2], xr[3]); } else pair(4, xr[0], xr[1]);
    ssq += xor16(ssq); ssq += xor32(ssq);
    if (g == 0) { __hip_atomic_store((float*)(ws + WS_SSQ) + (mt16 * 16 + 2 * n6 + kj) * 16 + m, ssq, __ATOMIC_RELAXED, __HIP_MEMORY_SCOPE_AGENT);
        if (n6 == 0 && kj == 0) __hip_atomic_store((float*)(ws + WS_MUH) + token, mu, __ATOMIC_RELAXED, __HIP_MEMORY_SCOPE_AGENT); }
}

struct Ev { int type, arg, n; };
enum { E_SSQ, E_A, E_B, E_WAITB, E_BAR, E_READ, E_MMA, E_GP, E_RS, E_ST, E_NOP, E_A2 };
constexpr int G1_PD = 4;
constexpr int g1_grp(int f) { return f < 144 ? 0 : f < 216 ? 1 : f < 288 ? 2 : 3; }
constexpr int g1_ks(int f) { return f < 144 ? f / 8 : f < 288 ? ((f - 144) % 72) / 4 : (f - 288) / 2; }
constexpr int g1_tile(int f) { return f < 288 ? (f >> 1) & 1 : 0; }
constexpr int g1_set(int f) { return f < 144 ? (f >> 2) & 1 : f < 216 ? 0 : f < 288 ? 1 : 0; }
template <bool LATE> struct G1Prog {
    Ev ev[1600]; int n;
    constexpr void add(int t, int a) { ev[n].type = t; ev[n].arg = a; ev[n].n = 0; ++n; }
    static constexpr int vm(const Ev& e) { return e.type == E_SSQ ? 6 : (e.type == E_A || e.type == E_B || e.type == E_ST || e.type == E_A2) ? 1 : 0; }
    static constexpr int NP = LATE ? 4 : 5;
    constexpr void addB(int s, int u) { add(E_B, s * 2 + u); }
    static constexpr int jset(int q) { return q == 0 ? 0 : q == 1 ? 1 : q == 2 ? 0 : q == 3 ? 1 : 0; }
    constexpr G1Prog() : ev{}, n(0) {
        addB(0, 0); addB(0, 1); addB(1, 0); addB(1, 1);
        for (int f = 0; f < G1_PD; ++f) add(E_READ, f);
        for (int f = 0; f < 324; ++f) {
            const int st = f / 36, i = f % 36, g = g1_grp(f);
            if (g == 0) { const int s = f / 8, q = f % 8;
                if (q == 0) add(E_WAITB, s);
                if (f == 0) { addB(2, 0); addB(2, 1); }
                if (s + 3 < 18 && q == 2) addB(s + 3, 0);
                if (s + 3 < 18 && q == 6) addB(s + 3, 1);
                if (f == 100) add(E_SSQ, 0);
                if (f == 136) add(E_RS, 0); }
            if (st + 2 < 9) { const int i0 = LATE ? 17 : 1, d = 4;
                if (i >= i0 && (i - i0) % d == 0 && (i - i0) / d < NP) add(E_A, (st + 2) * 8 + (i - i0) / d); }
            if (i == 36 - G1_PD && st + 1 < 9) add(E_BAR, st + 1);
            if (f + G1_PD < 324) add(E_READ, f + G1_PD);
            add(E_MMA, f);
            if (f == 143) { add(E_NOP, 0);
                for (int k = 0; k < 32; ++k) { add(E_GP, 0 * 64 + k); if (k == 15 || k == 31) add(E_ST, 0 * 2 + (k == 31)); } }
            if (g > 0) {
                const int r = f - (g == 1 ? 144 : g == 2 ? 216 : 288), step = g < 3 ? 2 : 1, r0 = 3, k = (r - r0) / step;
                if (r >= r0 && (r - r0) % step == 0 && k < 32) { add(E_GP, g * 64 + k);
                    if (k == (LATE ? 22 : 15)) add(E_ST, g * 2);
                    if (k == 31 && !LATE) add(E_ST, g * 2 + 1); }
                if (LATE && r == (g < 3 ? 71 : 35)) add(E_ST, g * 2 + 1); }
            if (st == 8 && i >= 2 && i % 3 == 2 && (i - 2) / 3 < 2 * NP) add(E_A2, ((i - 2) / 3 / NP) * 8 + ((i - 2) / 3) % NP);
        }
        add(E_NOP, 1);
        for (int k = 0; k < 16; ++k) { add(E_GP, 4 * 64 + k); if (k == 7 || k == 15) add(E_ST, 8 + (k == 15)); }
        for (int i = 0; i < n; ++i) if (ev[i].type == E_WAITB || ev[i].type == E_BAR || ev[i].type == E_RS) {
            int cnt = 0, j = i - 1;
            for (; j >= 0; --j) {
                if (ev[i].type == E_WAITB && ev[j].type == E_B && ev[j].arg == 2 * ev[i].arg + 1) break;
                if (ev[i].type == E_RS && ev[j].type == E_SSQ) break;
                if (ev[i].type == E_BAR && ev[j].type == E_A && ev[j].arg == 8 * ev[i].arg + NP - 1) break;
                cnt += vm(ev[j]); }
            ev[i].n = cnt > 63 ? 63 : cnt; }
    }
};
template <bool LATE> constexpr G1Prog<LATE> g1prog{};
DI float vmul(float a, float b) { float r; asm("v_mul_f32_e32 %0, %1, %2" : "=v"(r) : "v"(a), "v"(b)); return r; }
DI float vfma(float a, float b, float c) { float r; asm("v_fma_f32 %0, %1, %2, %3" : "=v"(r) : "v"(a), "v"(b), "v"(c)); return r; }
struct G1State {
    bf16x8 hf[2][18]; f32x4 acc[2][2][2][2];
    bf16x8 ar[G1_PD + 1]; float sq[2][3]; float a0[2], a1[2], kk[2]; f32x4 z, w; u32x4 o; unsigned soffA2;
    unsigned lb0, lb1, lb2; unsigned l16, ldsb, soffA, soffH, soffS, soffAct, soffAct8, vq; int wid, j, g;
};
template <bool LATE, int PC> DI void g1_exec(G1State& c, const srd_t srd) {
    constexpr Ev e = g1prog<LATE>.ev[PC];
    if constexpr (e.type == E_SSQ) { bload4_sc1<0>(c.sq[0][0], srd, c.vq, c.soffS); bload4_sc1<256>(c.sq[0][1], srd, c.vq, c.soffS); bload4_sc1<512>(c.sq[0][2], srd, c.vq, c.soffS);
                                     bload4_sc1<1024>(c.sq[1][0], srd, c.vq, c.soffS); bload4_sc1<1280>(c.sq[1][1], srd, c.vq, c.soffS); bload4_sc1<1536>(c.sq[1][2], srd, c.vq, c.soffS); }
    else if constexpr (e.type == E_A) { constexpr int st = e.arg >> 3, p = e.arg & 7;
        const unsigned so = c.soffA + st * RING_SLOT, dst = c.ldsb + RING0 + ((st + 3) & 3) * RING_SLOT;
        if constexpr (p < 4) bdma_next<p * 1024>(srd, c.l16, so + c.wid * 4096, dst + c.wid * 4096);
        else bdma1(srd, c.l16, so + 32768 + c.wid * 1024, dst + 32768 + c.wid * 1024); }
    else if constexpr (e.type == E_B) { constexpr int s = e.arg >> 1, u = e.arg & 1;
        bload16_sc1<((18 * u + s) & 3) * 1024>(c.hf[u][s], srd, c.l16, c.soffH + ((18 * u + s) >> 2) * 4096); }
    else if constexpr (e.type == E_WAITB) { asm volatile("s_waitcnt vmcnt(%2)" : "+v"(c.hf[0][e.arg]), "+v"(c.hf[1][e.arg]) : "n"(e.n)); }
    else if constexpr (e.type == E_BAR) { asm volatile("s_waitcnt vmcnt(%0)\n\ts_barrier" :: "n"(e.n) : "memory"); }
    else if constexpr (e.type == E_RS) { asm volatile("s_waitcnt vmcnt(%6)" : "+v"(c.sq[0][0]), "+v"(c.sq[0][1]), "+v"(c.sq[0][2]), "+v"(c.sq[1][0]), "+v"(c.sq[1][1]), "+v"(c.sq[1][2]) : "n"(e.n));
#pragma unroll
        for (int u = 0; u < 2; ++u) { float t = (c.sq[u][0] + c.sq[u][1]) + c.sq[u][2]; t += xor16(t); t += xor32(t);
            const float v = t * (1.f / D_) + EPS, rs = __builtin_amdgcn_rsqf(v);
            c.a0[u] = -2.30220818f * rs; c.a1[u] = -0.10294324f * rs * rs * rs; c.kk[u] = v; }
        SBAR(); }
    else if constexpr (e.type == E_A2) { constexpr int st = e.arg >> 3, p = e.arg & 7;
        const unsigned so = c.soffA2 + st * RING_SLOT, dst = c.ldsb + RING0 + (st & 3) * RING_SLOT;
        if constexpr (p < 4) bdma_next<p * 1024>(srd, c.l16, so + c.wid * 4096, dst + c.wid * 4096);
        else bdma1(srd, c.l16, so + 32768 + c.wid * 1024, dst + 32768 + c.wid * 1024); }
    else if constexpr (e.type == E_NOP) {
        asm volatile("s_nop 7" : "+v"(c.acc[0][0][0][0]), "+v"(c.acc[0][0][0][1]), "+v"(c.acc[0][0][1][0]), "+v"(c.acc[0][0][1][1]));
        if constexpr (e.arg == 0) asm volatile("" : "+v"(c.acc[0][1][0][0]), "+v"(c.acc[0][1][0][1]), "+v"(c.acc[0][1][1][0]), "+v"(c.acc[0][1][1][1])); }
    else if constexpr (e.type == E_READ) { constexpr int f = e.arg, off = ((f / 36 + 3) & 3) * RING_SLOT + (f % 36) * 1024;
        if constexpr ((off >> 16) == 0) c.ar[f % (G1_PD + 1)] = lds_frag((LAS const char*)(uintptr_t)c.lb0 + (off & 65535));
        else if constexpr ((off >> 16) == 1) c.ar[f % (G1_PD + 1)] = lds_frag((LAS const char*)(uintptr_t)c.lb1 + (off & 65535));
        else c.ar[f % (G1_PD + 1)] = lds_frag((LAS const char*)(uintptr_t)c.lb2 + (off & 65535));
        SBAR(); }
    else if constexpr (e.type == E_MMA) { constexpr int f = e.arg, s = g1_ks(f), t = g1_tile(f), q = f & 1, set = g1_set(f);
        if constexpr (s == 0) { mfma16_new(c.acc[set][t][q][0], c.ar[f % (G1_PD + 1)], c.hf[0][s]); mfma16_new(c.acc[set][t][q][1], c.ar[f % (G1_PD + 1)], c.hf[1][s]); }
        else { mfma16_acc(c.acc[set][t][q][0], c.ar[f % (G1_PD + 1)], c.hf[0][s]); mfma16_acc(c.acc[set][t][q][1], c.ar[f % (G1_PD + 1)], c.hf[1][s]); }
        SBAR(); }
    else if constexpr (e.type == E_GP) {
        constexpr int grp = e.arg >> 6, k = e.arg & 63, quad = k >> 3, ph = k & 7, set = G1Prog<LATE>::jset(grp);
        constexpr int u = grp < 4 ? quad >> 1 : quad, t = grp < 4 ? quad & 1 : 0;
        const f32x4 g = c.acc[set][t][0][u];
        if constexpr ((grp == 0 || grp == 4) && (ph == 4 || ph == 6)) asm volatile("s_nop 0" : "+v"(c.z));
#pragma unroll
        for (int r = 0; r < 4; ++r) {
            if constexpr (ph == 0) c.z[r] = vmul(g[r], g[r]);
            if constexpr (ph == 1) c.z[r] = vfma(c.z[r], c.a1[u], c.a0[u]);
            if constexpr (ph == 2) c.z[r] = vmul(c.z[r], g[r]);
            if constexpr (ph == 3) c.z[r] = __builtin_amdgcn_exp2f(c.z[r]);
            if constexpr (ph == 4) c.z[r] = vfma(c.z[r], c.kk[u], c.kk[u]);
            if constexpr (ph == 5) c.z[r] = __builtin_amdgcn_rcpf(c.z[r]);
            if constexpr (ph == 6) c.w[r] = vmul(vmul(g[r], c.acc[set][t][1][u][r]), c.z[r]); }
        if constexpr (ph == 7) { c.o[2 * t] = cvtpk(c.w[0], c.w[1]); c.o[2 * t + 1] = cvtpk(c.w[2], c.w[3]); }
        SBAR(); }
    else if constexpr (e.type == E_ST) { constexpr int grp = e.arg >> 1, u = e.arg & 1;
        if constexpr (grp < 4) bstore16<1>(c.o, srd, c.l16, c.soffAct + u * (144 * 1024) + grp * 1024);
        else bstore8<1>((u32x2){c.o[0], c.o[1]}, srd, c.l16 + (c.j & 1) * 8, c.soffAct8 + u * (144 * 1024));
        SBAR(); }
}
template <bool LATE, int... I> DI void g1_run(G1State& c, const srd_t srd, std::integer_sequence<int, I...>) { (g1_exec<LATE, I>(c, srd), ...); }

constexpr int G2_PD = 4;
template <bool LATE> struct G2Prog {
    Ev ev[700]; int n;
    constexpr void add(int t, int a) { ev[n].type = t; ev[n].arg = a; ev[n].n = 0; ++n; }
    static constexpr int vm(const Ev& e) { return (e.type == E_A || e.type == E_B) ? 1 : 0; }
    static constexpr int NP = LATE ? 4 : 5;
    constexpr G2Prog() : ev{}, n(0) {
        add(E_B, 0); add(E_B, 1); add(E_B, 2); add(E_B, 3);
        for (int f = 0; f < G2_PD; ++f) add(E_READ, f);
        for (int f = 0; f < 162; ++f) {
            const int st = f / 18, i = f % 18;
            if (i % 9 == 0) add(E_WAITB, st * 2 + i / 9);
            const int ia = LATE ? 3 : 1, ib = LATE ? 1 : 3;
            if (st == 0) {
                if (i >= 1 && i < 5) add(E_B, 4 + i - 1);
                if (i >= 6 && i < 6 + NP) add(E_A, 16 + i - 6);
                if (i >= 12 && i < 16) add(E_B, 8 + i - 12);
            } else {
                if (st == 1 && i >= 14) add(E_B, 16 + i - 14);
                if (st + 2 < 9 && i >= ia && (i - ia) % 4 == 0 && (i - ia) / 4 < 4) add(E_A, (st + 2) * 8 + (i - ia) / 4);
                if (st + 2 < 9 && NP == 5 && i == 12) add(E_A, (st + 2) * 8 + 4);
                { const int la = st == 1 ? 2 : 3; if (st + la < 9 && (st == 1 || st + la > 4) && i >= ib && (i - ib) % 4 == 0 && (i - ib) / 4 < 4) add(E_B, (st + la) * 4 + (i - ib) / 4); }
            }
            if (i == 18 - G2_PD && st + 1 < 9) add(E_BAR, st + 1);
            if (f + G2_PD < 162) add(E_READ, f + G2_PD);
            add(E_MMA, f);
        }
        for (int i = 0; i < n; ++i) if (ev[i].type == E_WAITB || ev[i].type == E_BAR) {
            int cnt = 0, j = i - 1;
            for (; j >= 0; --j) {
                if (ev[i].type == E_WAITB && ev[j].type == E_B && ev[j].arg == 2 * ev[i].arg + 1) break;
                if (ev[i].type == E_BAR && ev[j].type == E_A && (ev[j].arg >> 3) == ev[i].arg) break;
                cnt += vm(ev[j]); }
            ev[i].n = cnt > 63 ? 63 : cnt; }
    }
};
template <bool LATE> constexpr G2Prog<LATE> g2prog{};
struct G2State {
    bf16x8 af[4][2][2]; f32x4 acc[9][2]; bf16x8 ar[G2_PD + 1];
    unsigned lb0, lb1, lb2; unsigned l16, ldsb, soffA, soffB; int wid;
};
template <bool LATE, int PC> DI void g2_exec(G2State& c, const srd_t srd) {
    constexpr Ev e = g2prog<LATE>.ev[PC];
    if constexpr (e.type == E_A) { constexpr int st = e.arg >> 3, p = e.arg & 7;
        const unsigned so = c.soffA + st * RING_SLOT, dst = c.ldsb + RING0 + (st & 3) * RING_SLOT;
        if constexpr (p < 4) bdma_next<p * 1024>(srd, c.l16, so + c.wid * 4096, dst + c.wid * 4096);
        else bdma1(srd, c.l16, so + 32768 + c.wid * 1024, dst + 32768 + c.wid * 1024); }
    else if constexpr (e.type == E_B) { constexpr int st = e.arg >> 2, s2 = (e.arg >> 1) & 1, u = e.arg & 1;
        bload16_sc1<((2 * st + s2) & 3) * 1024>(c.af[st & 3][s2][u], srd, c.l16, c.soffB + u * (144 * 1024) + ((2 * st + s2) >> 2) * 4096); }
    else if constexpr (e.type == E_WAITB) { asm volatile("s_waitcnt vmcnt(%2)" : "+v"(c.af[(e.arg >> 1) & 3][e.arg & 1][0]), "+v"(c.af[(e.arg >> 1) & 3][e.arg & 1][1]) : "n"(e.n)); }
    else if constexpr (e.type == E_BAR) { asm volatile("s_waitcnt vmcnt(%0)\n\ts_barrier" :: "n"(e.n) : "memory"); }
    else if constexpr (e.type == E_READ) { constexpr int f = e.arg, st = f / 18, off = (st & 3) * RING_SLOT + (f % 18) * 1024;
        if constexpr ((off >> 16) == 0) c.ar[f % (G2_PD + 1)] = lds_frag((LAS const char*)(uintptr_t)c.lb0 + (off & 65535));
        else c.ar[f % (G2_PD + 1)] = lds_frag((LAS const char*)(uintptr_t)c.lb1 + (off & 65535));
        SBAR(); }
    else if constexpr (e.type == E_MMA) { constexpr int f = e.arg, st = f / 18, s2 = (f % 18) / 9, t = f % 9;
        if constexpr (f < 9) { mfma16_new(c.acc[t][0], c.ar[f % (G2_PD + 1)], c.af[st & 3][s2][0]); mfma16_new(c.acc[t][1], c.ar[f % (G2_PD + 1)], c.af[st & 3][s2][1]); }
        else { mfma16_acc(c.acc[t][0], c.ar[f % (G2_PD + 1)], c.af[st & 3][s2][0]); mfma16_acc(c.acc[t][1], c.ar[f % (G2_PD + 1)], c.af[st & 3][s2][1]); }
        SBAR(); }
}
template <bool LATE, int... I> DI void g2_run(G2State& c, const srd_t srd, std::integer_sequence<int, I...>) { (g2_exec<LATE, I>(c, srd), ...); }
DI unsigned* counter_ptr(char* ws, int counter) { return (unsigned*)(ws + WS_CNT) + counter * 64; }
DI void arrive(char* ws, int counter) { if (threadIdx.x == 0) __hip_atomic_fetch_add(counter_ptr(ws, counter), 1u, __ATOMIC_RELAXED, __HIP_MEMORY_SCOPE_AGENT); }
template <bool ADD> DI bool await(char* ws, int counter, unsigned want, LAS unsigned* flag) {
    if (threadIdx.x == 0) { unsigned* cnt = counter_ptr(ws, counter); unsigned bad = 0;
        unsigned seen = ADD ? __hip_atomic_fetch_add(cnt, 1u, __ATOMIC_RELAXED, __HIP_MEMORY_SCOPE_AGENT) + 1u : __hip_atomic_load(cnt, __ATOMIC_RELAXED, __HIP_MEMORY_SCOPE_AGENT);
        const unsigned long long t0 = __builtin_amdgcn_s_memrealtime();
        while (seen < want) {
            if (__builtin_amdgcn_s_memrealtime() - t0 > 2000000ull) { bad = 1; break; }
            seen = __hip_atomic_load(cnt, __ATOMIC_RELAXED, __HIP_MEMORY_SCOPE_AGENT); }
        *flag = bad; }
    LBAR();
    return *flag != 0;
}
DI void ring_w1(char* ws, char* lds_generic, int xcd, int idx, int st) {
    const int lane = threadIdx.x & 63, wid = rfl(threadIdx.x >> 6); const srd_t srd = make_srd(ws, (unsigned)WS_END);
    const unsigned so = (unsigned)WS_W1 + ((xcd >> 1) * 8 + (idx & 7)) * (324 * 1024) + st * RING_SLOT, dst = (unsigned)(uintptr_t)lds_generic + RING0 + ((st + 3) & 3) * RING_SLOT;
    bdma4(srd, lane * 16, so + wid * 4096, dst + wid * 4096);
    if (wid < 4) bdma1(srd, lane * 16, so + 32768 + wid * 1024, dst + 32768 + wid * 1024);
}
constexpr int FFN_LDS = G_LDS;
__global__ __launch_bounds__(512, 2) void ffn_kernel(const char* x, char* ws, char* out) {
    extern __shared__ __attribute__((aligned(16))) char lds_generic[];
    LAS char* lds = (LAS char*)lds_generic;
    CODE_LABEL(preln_ffn_begin);
    const srd_t srd = make_srd(ws, (unsigned)WS_END);
    const int b = blockIdx.x, xcd = b & 7, idx = b >> 3;
    bool bad;
    float csink = 0.f;
    if (idx >= 24) { ring_w1(ws, lds_generic, xcd, idx, 0);
        { const char *c0, *c1; CODE_ADDR(c0, preln_ffn_begin); CODE_ADDR(c1, preln_ffn_end); touch_code(csink, c0, c1, rfl(threadIdx.x >> 6) + 8 * (idx - 24), threadIdx.x & 63); } }
    else {
        k3_phase(x, ws, lds_generic, xcd, idx);
        asm volatile("s_waitcnt vmcnt(0) lgkmcnt(0)\n\ts_barrier" ::: "memory");
        arrive(ws, 96 + xcd);
    }
    ring_w1(ws, lds_generic, xcd, idx, 1);
    asm volatile("s_waitcnt vmcnt(0)" : "+v"(csink) :: "memory");
    LAS unsigned* flag = (LAS unsigned*)(lds + 15 * 1024);
    bad = await<false>(ws, 96 + (xcd & 1) * 4 + (idx >> 3), 24u, flag);
    {
        int tid = threadIdx.x; asm volatile("" : "+v"(tid));
        const int lane = tid & 63, wid = rfl(tid >> 6);
        const int j = (xcd >> 1) * 8 + (idx & 7), mt8 = (xcd & 1) * 4 + (idx >> 3), mt16 = 16 * mt8 + 2 * wid;
        G1State c;
        c.l16 = lane * 16; c.ldsb = (unsigned)(uintptr_t)lds_generic; c.wid = wid; c.j = j; c.g = lane >> 4;
        c.lb0 = c.ldsb + RING0 + lane * 16; c.lb1 = c.lb0 + 65536; c.lb2 = c.lb0 + 131072; PINV(c.lb0); PINV(c.lb1); PINV(c.lb2);
        c.soffA = (unsigned)WS_W1 + j * (324 * 1024); c.soffH = (unsigned)WS_HCF + mt16 * (18 * 1024); c.soffS = (unsigned)WS_SSQ + mt16 * 1024;
        c.soffAct8 = rfl((unsigned)WS_ACT + (mt16 * 144 + 9 * (j >> 1) + 8) * 1024);
        c.soffAct = rfl((unsigned)WS_ACT + (mt16 * 144 + 9 * (j >> 1) + 4 * (j & 1)) * 1024); c.vq = (lane >> 4) * 64 + (lane & 15) * 4;
        c.soffA2 = (unsigned)WS_W2 + ((xcd >> 1) * 4 + (idx >> 3)) * (324 * 1024);
        if (wid >= 4) g1_run<true>(c, srd, std::make_integer_sequence<int, g1prog<true>.n>{});
        else g1_run<false>(c, srd, std::make_integer_sequence<int, g1prog<false>.n>{});
        asm volatile("s_waitcnt vmcnt(0) lgkmcnt(0)\n\ts_barrier" ::: "memory");
        arrive(ws, 64 + (j >> 3) * 8 + mt8);
    }
    int tz = threadIdx.x; asm volatile("" : "+v"(tz));
    const int lane = tz & 63, wid = rfl(tz >> 6), mi = wid & 3, kj = wid >> 2;
    const int ksl = xcd >> 1, mt = (xcd & 1) * 8 + (idx & 7), n4 = idx >> 3;
    const int mt16 = 8 * mt + 2 * mi;
    G2State c;
    c.l16 = lane * 16; c.ldsb = (unsigned)(uintptr_t)lds_generic; c.wid = wid;
    c.lb0 = c.ldsb + RING0 + kj * (18 * 1024) + lane * 16; c.lb1 = c.lb0 + 65536; PINV(c.lb0); PINV(c.lb1);
    c.soffA = (unsigned)WS_W2 + (ksl * 4 + n4) * (324 * 1024); c.soffB = (unsigned)WS_ACT + (mt16 * 144 + 36 * ksl + 18 * kj) * 1024;
    bad |= await<false>(ws, 64 + ksl * 8 + (mt >> 1), 8u, flag);
    if (wid >= 4) g2_run<true>(c, srd, std::make_integer_sequence<int, g2prog<true>.n>{});
    else g2_run<false>(c, srd, std::make_integer_sequence<int, g2prog<false>.n>{});
    asm volatile("s_nop 7" : "+v"(c.acc[8][0]), "+v"(c.acc[8][1]), "+v"(c.acc[7][0]), "+v"(c.acc[7][1]));
    u32x2 hv[3]; u32x4 pv[3]; u32x2 pw[3]; float mu[2];
    { const int T0 = 9 * n4 + wid, T8 = 9 * n4 + 8; const unsigned hb = rfl((unsigned)WS_HCF + (8 * mt + 2 * ksl) * (18 * 1024));
      bload8_sc1<0>(hv[0], srd, lane * 16 + (T0 & 1) * 8, hb + (T0 >> 1) * 1024); bload8_sc1<0>(hv[1], srd, lane * 16 + (T0 & 1) * 8, hb + (18 + (T0 >> 1)) * 1024);
      bload8_sc1<0>(hv[2], srd, lane * 16 + (T8 & 1) * 8, hb + ((wid & 1) * 18 + (T8 >> 1)) * 1024);
      const unsigned mb = rfl((unsigned)WS_MUH + (128 * mt + 32 * ksl) * 4); bload4_sc1<0>(mu[0], srd, (lane & 15) * 4, mb); bload4_sc1<64>(mu[1], srd, (lane & 15) * 4, mb); }
    LBAR();
    LAS char* ex = lds + RING0 + mi * 18432 + lane * 16;
    if (kj == 0) {
#pragma unroll
        for (int t = 5; t < 9; ++t) { *(LAS f32x4*)(ex + (2 * t) * 1024) = c.acc[t][0]; *(LAS f32x4*)(ex + (2 * t + 1) * 1024) = c.acc[t][1]; }
    } else {
#pragma unroll
        for (int t = 0; t < 5; ++t) { *(LAS f32x4*)(ex + (2 * t) * 1024) = c.acc[t][0]; *(LAS f32x4*)(ex + (2 * t + 1) * 1024) = c.acc[t][1]; }
    }
    LBAR();
    const int tile = mt * 4 + n4; const unsigned xb = rfl((unsigned)WS_PART + tile * (16 * 9 * 1024));
    const unsigned xs0 = rfl(xb + (mi * 4 + ksl) * (9 * 1024));
    auto fin = [&](int t) {
        const f32x4 a0 = c.acc[t][0] + *(LAS const f32x4*)(ex + (2 * t) * 1024), a1 = c.acc[t][1] + *(LAS const f32x4*)(ex + (2 * t + 1) * 1024);
        if (mi == ksl) { *(LAS f32x4*)(ex + (2 * t) * 1024) = a0; *(LAS f32x4*)(ex + (2 * t + 1) * 1024) = a1; }
        else bstore16<1>((u32x4){cvtpk(a0[0], a0[1]), cvtpk(a0[2], a0[3]), cvtpk(a1[0], a1[1]), cvtpk(a1[2], a1[3])}, srd, lane * 16, xs0 + t * 1024); };
    if (kj == 0) { fin(0); fin(1); fin(2); fin(3); fin(4); } else { fin(5); fin(6); fin(7); fin(8); }
    asm volatile("s_waitcnt vmcnt(0) lgkmcnt(0)\n\ts_barrier" : "+v"(hv[0]), "+v"(hv[1]), "+v"(hv[2]), "+v"(mu[0]), "+v"(mu[1]) :: "memory");
    bad |= await<true>(ws, tile, 4u, flag);

#pragma unroll
    for (int k = 0; k < 3; ++k) { const unsigned sb = rfl(xb + (ksl * 4 + (k + (k >= ksl))) * (9 * 1024));
        bload16u_sc1<0>(pv[k], srd, lane * 16, sb + wid * 1024); bload8_sc1<0>(pw[k], srd, lane * 16 + (wid & 1) * 8, sb + 8 * 1024); }
    asm volatile("s_waitcnt vmcnt(0)" : "+v"(pv[0]), "+v"(pv[1]), "+v"(pv[2]), "+v"(pw[0]), "+v"(pw[1]), "+v"(pw[2]) :: "memory");
    const int m = lane & 15, g = lane >> 4;
    auto put = [&](int t, int u, u32x2 h2, u32x2 p0, u32x2 p1, u32x2 p2) {
        f32x4 a = *(LAS const f32x4*)(lds + RING0 + ksl * 18432 + (2 * t + u) * 1024 + lane * 16);
        a += (f32x4){bflo(h2[0]), bfhi(h2[0]), bflo(h2[1]), bfhi(h2[1])} + (u ? mu[1] : mu[0]); a += (f32x4){bflo(p0[0]), bfhi(p0[0]), bflo(p0[1]), bfhi(p0[1])};
        a += (f32x4){bflo(p1[0]), bfhi(p1[0]), bflo(p1[1]), bfhi(p1[1])}; a += (f32x4){bflo(p2[0]), bfhi(p2[0]), bflo(p2[1]), bfhi(p2[1])};
        u32x2 o = {cvtpk(a[0], a[1]), cvtpk(a[2], a[3])};
        if (bad) o = (u32x2){0x7fc07fc0u, 0x7fc07fc0u};
        *(u32x2*)(out + (size_t)(128 * mt + 32 * ksl + 16 * u + m) * (D_ * 2) + (144 * n4 + 16 * t + 4 * g) * 2) = o; };
    put(wid, 0, hv[0], (u32x2){pv[0][0], pv[0][1]}, (u32x2){pv[1][0], pv[1][1]}, (u32x2){pv[2][0], pv[2][1]});
    put(wid, 1, hv[1], (u32x2){pv[0][2], pv[0][3]}, (u32x2){pv[1][2], pv[1][3]}, (u32x2){pv[2][2], pv[2][3]});
    if (wid < 2) put(8, wid, hv[2], pw[0], pw[1], pw[2]);
    CODE_LABEL(preln_ffn_end);
}

}

extern "C" void kernel_launch(void* const* d_in, const int* in_sizes, int n_in, void* d_out, int out_size, void* d_ws, size_t ws_size, hipStream_t stream) {
    const char* x = (const char*)d_in[0]; const char* wqkv = (const char*)d_in[1]; const char* wo = (const char*)d_in[2];
    const char* wg = (const char*)d_in[4]; const char* wu = (const char*)d_in[5]; const char* wd = (const char*)d_in[6];
    char* ws = (char*)d_ws;
    (void)hipFuncSetAttribute((const void*)k1_qkv, hipFuncAttributeMaxDynamicSharedMemorySize, K1_LDS);
    (void)hipFuncSetAttribute((const void*)k2_attn, hipFuncAttributeMaxDynamicSharedMemorySize, K2_LDS);
    (void)hipFuncSetAttribute((const void*)ffn_kernel, hipFuncAttributeMaxDynamicSharedMemorySize, FFN_LDS);
    k1_qkv<<<256, 512, K1_LDS, stream>>>(x, wqkv, wo, ws);
    k2_attn<<<256, 1024, K2_LDS, stream>>>(wg, wu, wd, ws);
    ffn_kernel<<<256, 512, FFN_LDS, stream>>>(x, ws, (char*)d_out);
}
```

```cpp
#include <hip/hip_runtime.h>
#include <hip/hip_bf16.h>
#include <stdint.h>
#include <utility>

namespace {

typedef __bf16 bf16x2 __attribute__((ext_vector_type(2)));
typedef __bf16 bf16x8 __attribute__((ext_vector_type(8)));
typedef __bf16 bf16x16 __attribute__((ext_vector_type(16)));
typedef short s16x4 __attribute__((ext_vector_type(4)));
typedef float f32x2 __attribute__((ext_vector_type(2)));
typedef float f32x4 __attribute__((ext_vector_type(4)));
typedef float f32x8 __attribute__((ext_vector_type(8)));
typedef float f32x16 __attribute__((ext_vector_type(16)));
typedef unsigned u32x2 __attribute__((ext_vector_type(2)));
typedef unsigned u32x4 __attribute__((ext_vector_type(4)));
#define LAS __attribute__((address_space(3)))
#define DI __device__ __forceinline__

constexpr int S_ = 2048, D_ = 576, H_ = 32, F_ = 4608;
constexpr float EPS = 1e-5f;
constexpr float QSCALE = 0.23570226039551584f * 1.4426950408889634f;
constexpr float SAFE_B = 48.f;

constexpr int KVCHUNK = 19456, KVIMG = 8 * KVCHUNK;
constexpr bool OVERLAY = true;
constexpr size_t WS_QM   = 0;
constexpr size_t WS_QT   = WS_QM + (size_t)32 * 2048 * 32;
constexpr size_t WS_KV   = WS_QT + (size_t)32 * 2048 * 4;
constexpr size_t WS_A_END = WS_KV + (size_t)32 * KVIMG;
constexpr size_t WS_ACT  = OVERLAY ? 0 : WS_A_END;
constexpr size_t WS_OF   = WS_ACT + (size_t)128 * 144 * 1024;
constexpr size_t WS_WO   = WS_OF + (size_t)128 * 18 * 1024;
constexpr size_t WS_KMAX = WS_WO + (size_t)6 * 108 * 1024;
constexpr size_t WS_MUX  = WS_KMAX + 32 * 16 * 4;
constexpr size_t WS_WBAR = WS_MUX + 2048 * 4;
constexpr size_t WS_SSQ  = WS_WBAR + 4096;
constexpr size_t WS_CNT  = WS_SSQ + (size_t)2048 * 64;
constexpr size_t WS_MUH  = WS_CNT + 104 * 256;
constexpr size_t WS_HCF  = WS_MUH + 2048 * 4;
constexpr size_t WS_W2   = WS_HCF + (size_t)128 * 18 * 1024;
constexpr size_t WS_W1   = WS_W2 + (size_t)16 * 324 * 1024;
constexpr size_t WS_PART = WS_W1 + (size_t)32 * 324 * 1024;
static_assert(WS_A_END <= (size_t)128 * 144 * 1024, "overlay fits");
constexpr size_t WS_END  = WS_PART + (size_t)64 * 16 * 9 * 1024;

DI unsigned cvtpk(float lo, float hi) { f32x2 v = {lo, hi}; bf16x2 b = __builtin_convertvector(v, bf16x2); return __builtin_bit_cast(unsigned, b); }
DI float bflo(unsigned w) { return __uint_as_float(w << 16); }
DI float bfhi(unsigned w) { return __uint_as_float(w & 0xffff0000u); }
DI float dot2(unsigned a, unsigned b, float c) { return __builtin_amdgcn_fdot2_f32_bf16(__builtin_bit_cast(bf16x2, a), __builtin_bit_cast(bf16x2, b), c, false); }
DI int rfl(int x) { return __builtin_amdgcn_readfirstlane(x); }
DI float xor32(float v) { auto r = __builtin_amdgcn_permlane32_swap(__float_as_uint(v), __float_as_uint(v), false, false); return __uint_as_float((threadIdx.x & 32) ? r[0] : r[1]); }
DI float xor16(float v) { auto r = __builtin_amdgcn_permlane16_swap(__float_as_uint(v), __float_as_uint(v), false, false); return __uint_as_float((threadIdx.x & 16) ? r[0] : r[1]); }
template <int N> DI float row_ror(float v) { return __uint_as_float(__builtin_amdgcn_update_dpp(0u, __float_as_uint(v), 0x120 + N, 0xf, 0xf, false)); }
DI void mfma16_acc(f32x4& c, bf16x8 a, bf16x8 b) { asm volatile("v_mfma_f32_16x16x32_bf16 %0, %1, %2, %0" : "+v"(c) : "v"(a), "v"(b)); }
DI void mfma16_new(f32x4& c, bf16x8 a, bf16x8 b) { asm volatile("v_mfma_f32_16x16x32_bf16 %0, %1, %2, 0" : "=&v"(c) : "v"(a), "v"(b)); }
DI void glds16(const void* sbase, unsigned voff, unsigned lds_dst) {
    asm volatile("s_mov_b32 m0, %2\n\ts_nop 0\n\tglobal_load_lds_dwordx4 %0, %1" :: "v"(voff), "s"(sbase), "s"(lds_dst) : "memory");
}
typedef __amdgpu_buffer_rsrc_t srd_t;
DI srd_t make_srd(const void* p, unsigned bytes) { return __builtin_amdgcn_make_buffer_rsrc((void*)p, (short)0, (int)bytes, 0x00020000); }
template <int IMM> DI void bload16(bf16x8& dst, srd_t r, unsigned voff, unsigned soff) {
    asm volatile("buffer_load_dwordx4 %0, %1, %2, %3 offen offset:%4" : "=v"(dst) : "v"(voff), "s"(r), "s"(soff), "n"(IMM)); }
template <int IMM> DI void bload4(float& dst, srd_t r, unsigned voff, unsigned soff) {
    asm volatile("buffer_load_dword %0, %1, %2, %3 offen offset:%4" : "=v"(dst) : "v"(voff), "s"(r), "s"(soff), "n"(IMM)); }
template <int IMM> DI void bload16_sc1(bf16x8& dst, srd_t r, unsigned voff, unsigned soff) {
    asm volatile("buffer_load_dwordx4 %0, %1, %2, %3 offen offset:%4 sc1" : "=v"(dst) : "v"(voff), "s"(r), "s"(soff), "n"(IMM)); }
template <int IMM> DI void bload8(u32x2& dst, srd_t r, unsigned voff, unsigned soff) {
    asm volatile("buffer_load_dwordx2 %0, %1, %2, %3 offen offset:%4" : "=v"(dst) : "v"(voff), "s"(r), "s"(soff), "n"(IMM)); }
template <int IMM> DI void bload16u_sc1(u32x4& dst, srd_t r, unsigned voff, unsigned soff) {
    asm volatile("buffer_load_dwordx4 %0, %1, %2, %3 offen offset:%4 sc1" : "=v"(dst) : "v"(voff), "s"(r), "s"(soff), "n"(IMM)); }
template <int IMM> DI void bload8_sc1(u32x2& dst, srd_t r, unsigned voff, unsigned soff) {
    asm volatile("buffer_load_dwordx2 %0, %1, %2, %3 offen offset:%4 sc1" : "=v"(dst) : "v"(voff), "s"(r), "s"(soff), "n"(IMM)); }
DI void touch1(float& sink, srd_t r, unsigned voff) { asm volatile("buffer_load_dword %0, %1, %2, 0 offen" : "+v"(sink) : "v"(voff), "s"(r)); }
#define CODE_LABEL(name) asm volatile(#name ":")
#define CODE_ADDR(dst, name) do { unsigned long long pc_; int d_; asm volatile("s_getpc_b64 %0\n.Lhere%=:\n\ts_mov_b32 %1, " #name "-.Lhere%=" : "=s"(pc_), "=s"(d_)); dst = (const char*)pc_ + d_; } while (0)
DI void touch_code(float& sink, const char* from, const char* to, int i, int lane) {
    const unsigned bytes = (unsigned)(to - from), off = (unsigned)(i * 64 + lane) * 128u;
    if ((unsigned)i * 8192u < bytes) touch1(sink, make_srd(from, bytes), off < bytes - 4 ? off : bytes - 4);
}
DI void bdma4(srd_t r, unsigned voff, unsigned soff, unsigned lds_dst) {
    asm volatile("s_mov_b32 m0, %3\n\ts_nop 0\n\tbuffer_load_dwordx4 %0, %1, %2 offen lds\n\tbuffer_load_dwordx4 %0, %1, %2 offen offset:1024 lds\n\t"
                 "buffer_load_dwordx4 %0, %1, %2 offen offset:2048 lds\n\tbuffer_load_dwordx4 %0, %1, %2 offen offset:3072 lds" :: "v"(voff), "s"(r), "s"(soff), "s"(lds_dst) : "memory"); }
DI void bdma1(srd_t r, unsigned voff, unsigned soff, unsigned lds_dst) {
    asm volatile("s_mov_b32 m0, %3\n\ts_nop 0\n\tbuffer_load_dwordx4 %0, %1, %2 offen lds" :: "v"(voff), "s"(r), "s"(soff), "s"(lds_dst) : "memory"); }
template <int IMM> DI void bdma_next(srd_t r, unsigned voff, unsigned soff, unsigned lds_dst) {
    asm volatile("s_mov_b32 m0, %3\n\ts_nop 0\n\tbuffer_load_dwordx4 %0, %1, %2 offen offset:%4 lds" :: "v"(voff), "s"(r), "s"(soff), "s"(lds_dst), "n"(IMM) : "memory"); }
template <int POL> DI void bstore16(u32x4 d, srd_t r, unsigned voff, unsigned soff) {
    if constexpr (POL == 1) asm volatile("buffer_store_dwordx4 %0, %1, %2, %3 offen sc1\n\ts_nop 1" :: "v"(d), "v"(voff), "s"(r), "s"(soff) : "memory");
    else if constexpr (POL == 2) asm volatile("buffer_store_dwordx4 %0, %1, %2, %3 offen nt\n\ts_nop 1" :: "v"(d), "v"(voff), "s"(r), "s"(soff) : "memory");
    else asm volatile("buffer_store_dwordx4 %0, %1, %2, %3 offen\n\ts_nop 1" :: "v"(d), "v"(voff), "s"(r), "s"(soff) : "memory"); }
template <int POL> DI void bstore8(u32x2 d, srd_t r, unsigned voff, unsigned soff) {
    if constexpr (POL == 1) asm volatile("buffer_store_dwordx2 %0, %1, %2, %3 offen sc1\n\ts_nop 1" :: "v"(d), "v"(voff), "s"(r), "s"(soff) : "memory");
    else if constexpr (POL == 2) asm volatile("buffer_store_dwordx2 %0, %1, %2, %3 offen nt\n\ts_nop 1" :: "v"(d), "v"(voff), "s"(r), "s"(soff) : "memory");
    else asm volatile("buffer_store_dwordx2 %0, %1, %2, %3 offen\n\ts_nop 1" :: "v"(d), "v"(voff), "s"(r), "s"(soff) : "memory"); }
#define PINV(x) asm volatile("" : "+v"(x))
#define WAITV(n) asm volatile("s_waitcnt vmcnt(%0)" :: "n"(n) : "memory")
#define WAIT_BAR(n) asm volatile("s_waitcnt vmcnt(%0) lgkmcnt(0)\n\ts_barrier" :: "n"(n) : "memory")
#define SBAR() __builtin_amdgcn_sched_barrier(0)
#define LBAR() asm volatile("s_waitcnt lgkmcnt(0)\n\ts_barrier" ::: "memory")
DI bf16x8 lds_frag(LAS const char* p) { return *(LAS const bf16x8*)p; }
typedef short v4i16_t __attribute__((ext_vector_type(4)));
DI u32x2 lds_tr(LAS const char* p) { return __builtin_bit_cast(u32x2, __builtin_amdgcn_ds_read_tr16_b64_v4i16((LAS v4i16_t*)p)); }
DI bf16x8 tr_frag(LAS const char* p, int off2) { u32x2 a = lds_tr(p), b = lds_tr(p + off2); u32x4 r = {a[0], a[1], b[0], b[1]}; return __builtin_bit_cast(bf16x8, r); }

DI int wo_row(int k) { return k < 512 ? 18 * (k >> 4) + (k & 15) : 18 * ((k - 512) >> 1) + 16 + (k & 1); }
DI void retile_wo(int job, int lane, const char* wo, char* ws, char* scratch_generic) {
    const int s = job / 9, cb = job - 9 * s, r8 = lane >> 3, ch = lane & 7;
#pragma unroll
    for (int p = 0; p < 4; ++p) { const int rho = 8 * p + r8, k = 32 * s + 8 * ((rho >> 2) & 3) + 4 * (rho >> 4) + (rho & 3);
        glds16(wo + cb * 128, wo_row(k) * (D_ * 2) + ch * 16, (unsigned)(uintptr_t)scratch_generic + p * 1024); }
    WAITV(0);
    LAS const char* base = (LAS const char*)scratch_generic + (4 * (lane >> 4) + ((lane >> 2) & 3)) * 128 + (lane & 3) * 8;
#pragma unroll
    for (int u = 0; u < 4; ++u) { const int T = 4 * cb + u, n6 = T / 6, tt = T - 6 * n6;
        *(bf16x8*)(ws + WS_WO + (size_t)(n6 * 108 + s * 6 + tt) * 1024 + lane * 16) = tr_frag(base + 32 * u, 16 * 128); }
}

constexpr int K1_SLOT = 34 * 1024, K1_STG = 0, K1_SMALL = 4 * K1_SLOT, K1_LDS = K1_SMALL + 4096;
template <int PART, int NJ>
DI void k1_wave(const char* x, const char* wqkv, char* ws, char* lds_generic, int mt, int nt) {
    constexpr bool VPART = PART == 2; constexpr int NT = NJ ? 4 : 5, T0 = NJ ? 5 : 0;
    LAS char* lds = (LAS char*)lds_generic; const unsigned ldsb = (unsigned)(uintptr_t)lds_generic;
    const int tid = threadIdx.x, lane = tid & 63, wid = rfl(tid >> 6), mi = wid & 3, m = lane & 15, g = lane >> 4;
    float csink = 0.f;
    if (mi < 2) { const char* c1; CODE_ADDR(c1, preln_ffn_end); touch_code(csink, (const char*)__builtin_amdgcn_s_getpc(), c1, mi, lane); }
    const srd_t sx = make_srd(x, S_ * D_ * 2), sw = make_srd(wqkv, D_ * 3 * D_ * 2);
    const unsigned xv = (lane >> 3) * (D_ * 2) + (((lane & 7) ^ ((4 * wid + (lane >> 4)) & 7)) << 4);
    unsigned wv[2];
#pragma unroll
    for (int i = 0; i < 2; ++i) { const int idx = (i ? 8 : wid) * 64 + lane, rho = (idx * 3641) >> 16, ch = idx - 18 * rho;
        wv[i] = (8 * ((rho >> 2) & 3) + 4 * (rho >> 4) + (rho & 3)) * 3456 + ch * 16; }
    const unsigned xs = (128 * mt + 8 * wid) * (D_ * 2), wsb = nt * 288;
    auto issue = [&](int st, int p) {
        const int k = st; const unsigned dst = ldsb + (st & 3) * K1_SLOT;
        if (p < 2) bdma1(sx, xv, xs + p * 64 * (D_ * 2) + k * 128, dst + (wid + 8 * p) * 1024);
        else if (p < 4) bdma1(sw, wv[0], wsb + (64 * k + 32 * (p - 2)) * 3456, dst + 16384 + ((p - 2) * 9 + wid) * 1024);
        else if (wid < 2) bdma1(sw, wv[1], wsb + (64 * k + 32 * wid) * 3456, dst + 16384 + (wid * 9 + 8) * 1024); };
#pragma unroll
    for (int st = 0; st < 2; ++st)
#pragma unroll
        for (int p = 0; p < 5; ++p) issue(st, p);
    unsigned sa[NT][2], sd[NT][2];
#pragma unroll
    for (int t = 0; t < NT; ++t) { const int tt = T0 + t;
        if (VPART) { const int n = 16 * tt + m, hl = (n * 3641) >> 16, d = n - 18 * hl, row = d + (d >= 4 ? 5 : 0) - (d >= 12 ? 12 : 0) + (d >= 16 ? 8 : 0);
            sa[t][0] = K1_STG + hl * 5120 + 2 * mi * 640 + (g >> 1) * 320 + row * 16 + (g & 1) * 8; PINV(sa[t][0]); }
        else {
#pragma unroll
            for (int p = 0; p < 2; ++p) { const int n = 16 * tt + 4 * g + 2 * p, hl = (n * 3641) >> 16, d = n - 18 * hl; const bool tail = d == 16;
                const int o = PART == 0 ? (tail ? 32768 + hl * 512 : hl * 4096 + d * 2) : hl * 4608 + (tail ? 4096 : (d >> 3) * 2048 + (d & 7) * 2);
                const int ts = tail ? 4 : PART == 0 ? 32 : 16;
                sa[t][p] = K1_STG + o + (32 * mi + m) * ts; sd[t][p] = 16 * ts; PINV(sa[t][p]); PINV(sd[t][p]); } } }
    f32x4 acc[NT][2];
    f32x4 accs = {0, 0, 0, 0}, accg = {0, 0, 0, 0}, accc[NT];
#pragma unroll
    for (int t = 0; t < NT; ++t) accc[t] = (f32x4){0, 0, 0, 0};
    bf16x8 ones = __builtin_bit_cast(bf16x8, (u32x4){0x3f803f80u, 0x3f803f80u, 0x3f803f80u, 0x3f803f80u}); PINV(ones);
    asm volatile("s_nop 1" : "+v"(accs), "+v"(accg));
#pragma unroll
    for (int t = 0; t < NT; ++t) PINV(accc[t]);
    const unsigned xa0 = (32 * mi + m) * 128 + ((g ^ ((m >> 1) & 7)) << 4), xa1 = (32 * mi + m) * 128 + (((4 + g) ^ ((m >> 1) & 7)) << 4);
    const unsigned wa = 16384 + (4 * g + ((lane >> 2) & 3)) * 288 + (lane & 3) * 8 + T0 * 32;
    bf16x8 xq[2][2][2], wr[2][2][NT];
    auto rd = [&](int st, int i) {
        LAS const char* sb = lds + (st & 3) * K1_SLOT;
        if (i < 4) xq[st & 1][i >> 1][i & 1] = lds_frag(sb + ((i >> 1) ? xa1 : xa0) + (i & 1) * 2048);
        else { const int s2 = (i - 4) / NT, t = (i - 4) % NT; wr[st & 1][s2][t] = tr_frag(sb + wa + s2 * 9216 + 32 * t, 16 * 288); } };
    WAIT_BAR(4);
    PINV(csink);

#pragma unroll
    for (int i = 0; i < 4 + 2 * NT; ++i) rd(0, i);
#pragma unroll
    for (int p = 0; p < 5; ++p) issue(2, p);
#pragma unroll
    for (int st = 0; st < 9; ++st) {
        if (st < 7) WAIT_BAR(4); else if (st == 7) WAIT_BAR(0);
#pragma unroll
        for (int i = 0; i < 2 * NT; ++i) { const int s2 = i / NT, t = i % NT; const bf16x8 w = wr[st & 1][s2][t], x0 = xq[st & 1][s2][0], x1 = xq[st & 1][s2][1];
            if (st == 0 && s2 == 0) { if (VPART) { mfma16_new(acc[t][0], x0, w); mfma16_new(acc[t][1], x1, w); } else { mfma16_new(acc[t][0], w, x0); mfma16_new(acc[t][1], w, x1); } }
            else { if (VPART) { mfma16_acc(acc[t][0], x0, w); mfma16_acc(acc[t][1], x1, w); } else { mfma16_acc(acc[t][0], w, x0); mfma16_acc(acc[t][1], w, x1); } }
            if (st + 1 < 9) { if (i < 4) { rd(st + 1, i); rd(st + 1, 4 + i); } else if (4 + i < 4 + 2 * NT) rd(st + 1, 4 + i); }
            if (st + 3 < 9 && (i & 1) == 0 && i / 2 < 4) issue(st + 3, i / 2 == 0 ? 0 : i / 2 == 1 ? 2 : i / 2 == 2 ? 1 : 3);
            if (st + 3 < 9 && i == 7) issue(st + 3, 4);
            if (t == 1) mfma16_acc(accs, ones, NJ ? x1 : x0);
            if (t == 2) mfma16_acc(accg, NJ ? x1 : x0, NJ ? x1 : x0);
            if (t == NT - 1 && ((2 * st + s2) & 3) == mi) {
#pragma unroll
                for (int tt = 0; tt < NT; ++tt) { if (VPART) mfma16_acc(accc[tt], ones, wr[st & 1][s2][tt]); else mfma16_acc(accc[tt], wr[st & 1][s2][tt], ones); } }
            SBAR(); }
    }
    LAS float* sm = (LAS float*)(lds + K1_SMALL);
    asm volatile("s_nop 7\n\ts_nop 7" : "+v"(acc[NT - 1][0]), "+v"(acc[NT - 1][1]), "+v"(accs), "+v"(accg));
#pragma unroll
    for (int t = 0; t < NT; ++t) { PINV(accc[t]); PINV(acc[t][0]); PINV(acc[t][1]); }
    if (g == 0) sm[32 * mi + 16 * NJ + m] = accs[0];
    if (g == (m >> 2)) sm[128 + 32 * mi + 16 * NJ + m] = (m & 2) ? ((m & 1) ? accg[3] : accg[2]) : ((m & 1) ? accg[1] : accg[0]);
#pragma unroll
    for (int t = 0; t < NT; ++t) { if (VPART) { if (g == 0) sm[256 + mi * 144 + 16 * (T0 + t) + m] = accc[t][0]; }
        else if (m == 0) *(LAS f32x4*)(sm + 256 + mi * 144 + 16 * (T0 + t) + 4 * g) = accc[t]; }
    LBAR();
    LAS char* stg = lds + K1_STG;
    if (VPART && tid < 256) { const int hs = tid >> 1;
        *(LAS u32x4*)(stg + hs * 320 + ((tid & 1) ? 19 : 4) * 16) = (tid & 1) ? (u32x4){0x3f803f80u, 0x3f803f80u, 0x3f803f80u, 0x3f803f80u} : (u32x4){0, 0, 0, 0}; }
    f32x4 mean[2], rstd[2];
#pragma unroll
    for (int u = 0; u < 2; ++u) { const int tl = 32 * mi + 16 * u + (VPART ? 4 * g : m);
        if (VPART) { mean[u] = *(LAS const f32x4*)(sm + tl) * (1.f / D_);
            const f32x4 var = *(LAS const f32x4*)(sm + 128 + tl) * (1.f / D_) - mean[u] * mean[u];
#pragma unroll
            for (int r = 0; r < 4; ++r) rstd[u][r] = __builtin_amdgcn_rsqf(fmaxf(var[r], 0.f) + EPS); }
        else { mean[u][0] = sm[tl] * (1.f / D_);
            rstd[u][0] = __builtin_amdgcn_rsqf(fmaxf(sm[128 + tl] * (1.f / D_) - mean[u][0] * mean[u][0], 0.f) + EPS) * (PART == 0 ? QSCALE : 1.f); } }
    float c1[NT]; f32x4 c4[NT];
#pragma unroll
    for (int t = 0; t < NT; ++t) { const int tt = T0 + t;
        if (VPART) { const int n = 16 * tt + m; c1[t] = (sm[256 + n] + sm[400 + n]) + (sm[544 + n] + sm[688 + n]); }
        else { const int n0 = 16 * tt + 4 * g;
            c4[t] = (*(LAS const f32x4*)(sm + 256 + n0) + *(LAS const f32x4*)(sm + 400 + n0)) + (*(LAS const f32x4*)(sm + 544 + n0) + *(LAS const f32x4*)(sm + 688 + n0)); } }
#pragma unroll
    for (int t = 0; t < NT; ++t)
#pragma unroll
        for (int u = 0; u < 2; ++u) {
            if (VPART) { const f32x4 a = (acc[t][u] - mean[u] * c1[t]) * rstd[u];
                *(LAS u32x2*)(lds + sa[t][0] + u * 640) = (u32x2){cvtpk(a[0], a[1]), cvtpk(a[2], a[3])}; }
            else { const f32x4 a = (acc[t][u] - c4[t] * mean[u][0]) * rstd[u][0];
                *(LAS unsigned*)(lds + sa[t][0] + (u ? sd[t][0] : 0u)) = cvtpk(a[0], a[1]); *(LAS unsigned*)(lds + sa[t][1] + (u ? sd[t][1] : 0u)) = cvtpk(a[2], a[3]); } }
    if (nt == 0 && tid < 128) ((float*)(ws + WS_MUX))[128 * mt + tid] = sm[tid] * (1.f / D_);
    LBAR();
    const int head = 8 * (nt & 3) + wid; const srd_t sws = make_srd(ws, (unsigned)WS_END); const unsigned l16 = lane * 16;
    if (PART == 2) { const unsigned dst = (unsigned)WS_KV + head * KVIMG + (mt >> 1) * KVCHUNK + 9216 + (mt & 1) * 5120;
#pragma unroll
        for (int i = 0; i < 5; ++i) bstore16<1>(*(LAS const u32x4*)(stg + wid * 5120 + i * 1024 + l16), sws, l16, dst + i * 1024);
    } else if (PART == 0) { const unsigned dst = (unsigned)WS_QM + (head * S_ + 128 * mt) * 32;
#pragma unroll
        for (int i = 0; i < 4; ++i) bstore16<1>(*(LAS const u32x4*)(stg + wid * 4096 + i * 1024 + l16), sws, l16, dst + i * 1024);
        if (lane < 32) bstore16<1>(*(LAS const u32x4*)(stg + 32768 + wid * 512 + l16), sws, l16, (unsigned)WS_QT + (head * S_ + 128 * mt) * 4);
    } else { const unsigned dst = (unsigned)WS_KV + head * KVIMG + (mt >> 1) * KVCHUNK; LAS const char* hb = stg + wid * 4608;
#pragma unroll
        for (int i = 0; i < 4; ++i) bstore16<1>(*(LAS const u32x4*)(hb + i * 1024 + l16), sws, l16, dst + (i >> 1) * 4096 + (mt & 1) * 2048 + (i & 1) * 1024);
        if (lane < 32) bstore16<1>(*(LAS const u32x4*)(hb + 4096 + l16), sws, l16, dst + 8192 + (mt & 1) * 512);
        float mx = 0.f;
#pragma unroll
        for (int i = 0; i < 2; ++i) { const int tl = lane + 64 * i;
            const u32x4 a = *(LAS const u32x4*)(hb + tl * 16), bq = *(LAS const u32x4*)(hb + 2048 + tl * 16); const unsigned c = *(LAS const unsigned*)(hb + 4096 + tl * 4);
            float s2 = dot2(c, c, 0.f);
#pragma unroll
            for (int e = 0; e < 4; ++e) { s2 = dot2(a[e], a[e], s2); s2 = dot2(bq[e], bq[e], s2); }
            mx = fmaxf(mx, s2); }
        mx = fmaxf(mx, xor16(mx)); mx = fmaxf(mx, xor32(mx));
#pragma unroll
        for (int o = 1; o < 16; o <<= 1) mx = fmaxf(mx, __shfl_xor(mx, o));
        if (lane == 0) ((float*)(ws + WS_KMAX))[head * 16 + mt] = mx;
    }
}
template <int PART> DI void k1_body(const char* x, const char* wqkv, char* ws, char* lds, int mt, int nt) {
    if (rfl(threadIdx.x >> 6) >= 4) k1_wave<PART, 1>(x, wqkv, ws, lds, mt, nt); else k1_wave<PART, 0>(x, wqkv, ws, lds, mt, nt);
}
__global__ __launch_bounds__(512, 2) void k1_qkv(const char* x, const char* wqkv, const char* wo, char* ws) {
    extern __shared__ __attribute__((aligned(16))) char lds[];
    const int b = blockIdx.x;
    if (b >= 192) {
        const int lane = threadIdx.x & 63, wv = rfl(threadIdx.x >> 6), gw = (b - 192) * 8 + wv;
        float sink = 0.f;
        if (b - 192 < 8) { const char *c0, *c1; CODE_ADDR(c0, preln_k2_begin); CODE_ADDR(c1, preln_ffn_end); touch_code(sink, c0, c1, wv + 8 * (b - 192), lane); }
        if (threadIdx.x == 511) ((unsigned*)(ws + WS_CNT))[(b - 192) * 64] = 0u;
        if (threadIdx.x == 510 && b - 192 < 40) ((unsigned*)(ws + WS_CNT))[(64 + b - 192) * 64] = 0u;
        if (gw < 162) retile_wo(gw, lane, wo, ws, lds + wv * 4096);
        for (int c = gw; c < D_; c += 512) {
            const char* row = wo + (size_t)wo_row(c) * (D_ * 2);
            const u32x4 a = *(const u32x4*)(row + lane * 16); float s = 0.f;
#pragma unroll
            for (int e = 0; e < 4; ++e) s = dot2(a[e], 0x3f803f80u, s);
            if (lane < 8) { const u32x4 bq = *(const u32x4*)(row + 1024 + lane * 16);
#pragma unroll
                for (int e = 0; e < 4; ++e) s = dot2(bq[e], 0x3f803f80u, s); }
#pragma unroll
            for (int o = 1; o < 64; o <<= 1) s += __shfl_xor(s, o);
            if (lane == 0) { const float w = s * (1.f / D_); const unsigned hi = cvtpk(w, 0.f) & 0xffffu; const unsigned lo = cvtpk(w - bflo(hi), 0.f) & 0xffffu;
                ((unsigned short*)(ws + WS_WBAR))[c] = (unsigned short)hi; ((unsigned short*)(ws + WS_WBAR))[D_ + c] = (unsigned short)lo; }
        }
        asm volatile("s_waitcnt vmcnt(0)" : "+v"(sink));
        return;
    }
    const int xcd = b & 7, idx = b >> 3, mt = (xcd & 3) * 4 + (idx & 3), nt = (xcd >> 2) * 6 + (idx >> 2);
    if (nt >= 8) k1_body<2>(x, wqkv, ws, lds, mt, nt); else if (nt >= 4) k1_body<1>(x, wqkv, ws, lds, mt, nt); else k1_body<0>(x, wqkv, ws, lds, mt, nt);
}

DI unsigned* counter_ptr(char* ws, int counter) { return (unsigned*)(ws + WS_CNT) + counter * 64; }
DI void arrive(char* ws, int counter) { if (threadIdx.x == 0) __hip_atomic_fetch_add(counter_ptr(ws, counter), 1u, __ATOMIC_RELAXED, __HIP_MEMORY_SCOPE_AGENT); }
template <bool ADD, int POLLER = 0, bool DRAIN = false> DI bool await(char* ws, int counter, unsigned want, LAS unsigned* flag) {
    if (threadIdx.x == POLLER) { unsigned* cnt = counter_ptr(ws, counter); unsigned bad = 0;
        unsigned seen = ADD ? __hip_atomic_fetch_add(cnt, 1u, __ATOMIC_RELAXED, __HIP_MEMORY_SCOPE_AGENT) + 1u : __hip_atomic_load(cnt, __ATOMIC_RELAXED, __HIP_MEMORY_SCOPE_AGENT);
        const unsigned long long t0 = __builtin_amdgcn_s_memrealtime();
        while (seen < want) {
            if (__builtin_amdgcn_s_memrealtime() - t0 > 2000000ull) { bad = 1; break; }
            seen = __hip_atomic_load(cnt, __ATOMIC_RELAXED, __HIP_MEMORY_SCOPE_AGENT); }
        *flag = bad; }
    if (DRAIN) WAIT_BAR(0); else LBAR();
    return *flag != 0;
}
constexpr int K2_LDS = 160 * 1024;
struct RtJob { unsigned so[4], dst[4]; };
struct RtTable { RtJob j[8][486];
    constexpr RtTable() : j{} {
        for (int xg = 0; xg < 8; ++xg) for (int slot = 0; slot < 486; ++slot) { RtJob& e = j[xg][slot];
            if (slot < 324) { const int gu = slot >= 162, r = slot - 162 * gu, ks = r / 9, cb = 9 * xg + r % 9;
                for (int p = 0; p < 4; ++p) { const int T = 4 * cb + p, jj = T / 9, tt = T % 9;
                    e.so[p] = (ks * 32 + p * 8) * (F_ * 2) + cb * 128;
                    e.dst[p] = (unsigned)WS_W1 + (jj * 324 + (tt < 4 ? ks * 8 + tt * 2 : tt < 8 ? (tt >> 1) * 72 + ks * 4 + (tt & 1) * 2 : 288 + ks * 2) + gu) * 1024; }
            } else { const int r2 = slot - 324, q = r2 / 9, cb = r2 % 9, ks = 18 * xg + q, a = ks / 9, qi = ks % 9;
                const int t0 = qi == 8 ? 18 * a + 8 : 18 * a + 9 * (qi >> 2) + 2 * (qi & 3), t1 = qi == 8 ? t0 + 9 : t0 + 1;
                for (int p = 0; p < 4; ++p) { const int T = 4 * cb + p, n4 = T / 9, tt = T % 9;
                    e.so[p] = (16 * (p >> 1 ? t1 : t0) + 8 * (p & 1)) * (D_ * 2) + cb * 128;
                    e.dst[p] = (unsigned)WS_W2 + (((xg >> 1) * 4 + n4) * 324 + (((q >> 1) * 2 + (xg & 1)) * 2 + (q & 1)) * 9 + tt) * 1024; } } }
    } };
__device__ const RtTable rt_table{};
#define EXPS(S, a, b) _Pragma("unroll") for (int r_ = a; r_ < b; ++r_) S[r_] = __builtin_amdgcn_exp2f(S[r_]); SBAR()
#define PLO(S) __builtin_bit_cast(bf16x16, (f32x8)__builtin_shufflevector(S, S, 0, 1, 2, 3, 4, 5, 6, 7))
#define PHI(S) __builtin_bit_cast(bf16x16, (f32x8)__builtin_shufflevector(S, S, 8, 9, 10, 11, 12, 13, 14, 15))
#define KTAIL(p) __builtin_bit_cast(s16x4, (u32x2){*(LAS const unsigned*)(p), kc})
#define QK1(q) __builtin_amdgcn_mfma_f32_32x32x16_bf16(kf, q, (f32x16){}, 0, 0, 0)
#define QK2(q, s) __builtin_amdgcn_mfma_f32_32x32x8bf16_1k(kt, q, s, 0, 0, 0)
#define PV(v, p, o) __builtin_amdgcn_smfmac_f32_32x32x32_bf16(v, p, o, 0xDDDD, 0, 0)
template <class Hook>
DI void attn_fast(LAS const char* kp, LAS const char* tp, LAS const char* vp, const bf16x8 (&qm)[2], const s16x4 (&qt)[2], f32x16 (&o)[2], unsigned kc, Hook hook) {
    bf16x8 kf = *(LAS const bf16x8*)kp; s16x4 kt = KTAIL(tp); bf16x8 v0, v1;
    f32x16 s0 = QK1(qm[0]), s1; s0 = QK2(qt[0], s0);
    SBAR();
#pragma unroll
    for (int it = 0; it < 8; ++it) {
        { if (it == 0) __builtin_amdgcn_s_setprio(3); if (it == 2) __builtin_amdgcn_s_setprio(2); if (it == 4) __builtin_amdgcn_s_setprio(1); if (it == 6) __builtin_amdgcn_s_setprio(0); }
        EXPS(s0, 0, 2);
        if (it > 0) { o[1] = PV(v0, PLO(s1), o[1]); SBAR(); }
        EXPS(s0, 2, 6);
        if (it > 0) { o[1] = PV(v1, PHI(s1), o[1]); SBAR(); }
        v0 = *(LAS const bf16x8*)(vp + it * 1280); v1 = *(LAS const bf16x8*)(vp + it * 1280 + 640); SBAR();
        hook(it);
        EXPS(s0, 6, 9);
        s1 = QK1(qm[1]); SBAR();
        EXPS(s0, 9, 12);
        s1 = QK2(qt[1], s1); SBAR();
        if (it + 1 < 8) { kf = *(LAS const bf16x8*)(kp + (it + 1) * 512); kt = KTAIL(tp + (it + 1) * 128); SBAR(); }
        EXPS(s0, 12, 16);
        EXPS(s1, 0, 2);
        o[0] = PV(v0, PLO(s0), o[0]); SBAR();
        EXPS(s1, 2, 6);
        o[0] = PV(v1, PHI(s0), o[0]); SBAR();
        EXPS(s1, 6, 9);
        if (it + 1 < 8) { s0 = QK1(qm[0]); SBAR(); }
        EXPS(s1, 9, 12);
        if (it + 1 < 8) { s0 = QK2(qt[0], s0); SBAR(); }
        EXPS(s1, 12, 16);
    }
    o[1] = PV(v0, PLO(s1), o[1]); o[1] = PV(v1, PHI(s1), o[1]);
}
DI void attn_slow(LAS const char* kp, LAS const char* tp, LAS const char* vp, const bf16x8 (&qm)[2], const s16x4 (&qt)[2], f32x16 (&o)[2], float (&mrun)[2], unsigned kc) {
#pragma unroll 1
    for (int it = 0; it < 8; ++it) {
        const bf16x8 kf = *(LAS const bf16x8*)(kp + it * 512); const s16x4 kt = KTAIL(tp + it * 128);
        const bf16x8 v0 = *(LAS const bf16x8*)(vp + it * 1280), v1 = *(LAS const bf16x8*)(vp + it * 1280 + 640);
#pragma unroll
        for (int u = 0; u < 2; ++u) {
            f32x16 s = QK1(qm[u]); s = QK2(qt[u], s);
            float mx = s[0];
#pragma unroll
            for (int r = 1; r < 16; ++r) mx = fmaxf(mx, s[r]);
            mx = fmaxf(mx, xor32(mx));
            const float mn = fmaxf(mrun[u], mx), al = __builtin_amdgcn_exp2f(mrun[u] - mn);
            mrun[u] = mn;
#pragma unroll
            for (int r = 0; r < 16; ++r) { s[r] = __builtin_amdgcn_exp2f(s[r] - mn); o[u][r] *= al; }
            o[u] = PV(v0, PLO(s), o[u]); o[u] = PV(v1, PHI(s), o[u]);
        }
    }
}
constexpr int K3_X = 108 * 1024, K3_WB = K3_X + 12 * 1024, K3_Z = K3_WB + 2304, K3_FLAG = K3_WB + 4096;
DI void k3_request(const char* x, char* ws, char* lds_generic, int mt32, int n6, float& mux) {
    LAS char* lds = (LAS char*)lds_generic; const unsigned ldsb = (unsigned)(uintptr_t)lds_generic;
    const int tid = threadIdx.x, lane = tid & 63, wid = rfl(tid >> 6);
    const srd_t srd = make_srd(ws, (unsigned)WS_END), sx = make_srd(x, S_ * D_ * 2);
    const unsigned l16 = lane * 16, wo0 = (unsigned)WS_WO + n6 * (108 * 1024), f0 = (wid < 13 ? 8 * wid : 100) * 1024;
    if (wid < 14) { bdma4(srd, l16, wo0 + f0, ldsb + f0); bdma4(srd, l16, wo0 + f0 + 4096, ldsb + f0 + 4096); }
    if (wid < 12) { const int q = wid * 64 + lane, row = (q * 5462) >> 16;
        bdma1(sx, row * (D_ * 2) + (q - 12 * row) * 16, (64 * mt32) * (D_ * 2) + n6 * 192, ldsb + K3_X + wid * 1024); }
    if (wid == 12 || wid == 13) bdma1(srd, l16, (unsigned)WS_WBAR + (wid - 12) * 1024, ldsb + K3_WB + (wid - 12) * 1024);
    if (wid == 14 && lane < 16) bdma1(srd, l16, (unsigned)WS_WBAR + 2048, ldsb + K3_WB + 2048);
    mux = 0.f;
    if (wid < 8) bload4<0>(mux, srd, (lane & 15) * 4, (unsigned)WS_MUX + (4 * mt32 + (wid & 3)) * 64);
    if (tid < 37) *(LAS u32x4*)(lds + K3_Z + tid * 16) = (u32x4){0, 0, 0, 0};
}
DI void k3_compute(char* ws, char* lds_generic, int mt32, int n6, float mux, bool bad) {
    LAS char* lds = (LAS char*)lds_generic;
    int tid = threadIdx.x; asm volatile("" : "+v"(tid));
    const int lane = tid & 63, wid = rfl(tid >> 6), mi = wid & 3, kj = (wid >> 2) & 1, m = lane & 15, g = lane >> 4, mt16 = 4 * mt32 + mi;
    if (wid >= 8) { LBAR(); LBAR(); return; }
    const srd_t srd = make_srd(ws, (unsigned)WS_END);
    const unsigned l16 = lane * 16, ob = (unsigned)WS_OF + (mt16 * 18 + 9 * kj) * 1024;
    bf16x8 of[9];
    bload16_sc1<0>(of[0], srd, l16, ob); bload16_sc1<1024>(of[1], srd, l16, ob); bload16_sc1<2048>(of[2], srd, l16, ob); bload16_sc1<3072>(of[3], srd, l16, ob);
    bload16_sc1<0>(of[4], srd, l16, ob + 4096); bload16_sc1<1024>(of[5], srd, l16, ob + 4096); bload16_sc1<2048>(of[6], srd, l16, ob + 4096); bload16_sc1<3072>(of[7], srd, l16, ob + 4096);
    bload16_sc1<0>(of[8], srd, l16, ob + 8192);
    f32x4 acc[6], accw;
    LAS const char* ab = lds + kj * (54 * 1024) + lane * 16;
    LAS const char* wbp = lds + (m < 2 ? K3_WB + m * (D_ * 2) + kj * 576 + g * 16 : K3_Z);
    bf16x8 wr[7];
    auto rd = [&](int s) {
#pragma unroll
        for (int t = 0; t < 6; ++t) wr[t] = lds_frag(ab + (s * 6 + t) * 1024);
        wr[6] = lds_frag(wbp + s * 64); SBAR(); };
    rd(0);
    u32x2 xr[4];
#pragma unroll
    for (int t = 0; t < 4; ++t) xr[t] = *(LAS const u32x2*)(lds + K3_X + (16 * mi + m) * 192 + (4 * kj + (kj ? (t & 1) : t)) * 32 + g * 8);
#pragma unroll
    for (int s = 0; s < 9; ++s) {
        if (s == 0) asm volatile("s_waitcnt vmcnt(5)" : "+v"(of[0]), "+v"(of[1]), "+v"(of[2]), "+v"(of[3]));
        if (s == 4) asm volatile("s_waitcnt vmcnt(0)" : "+v"(of[4]), "+v"(of[5]), "+v"(of[6]), "+v"(of[7]), "+v"(of[8]));
#pragma unroll
        for (int t = 0; t < 6; ++t) { if (s == 0) mfma16_new(acc[t], wr[t], of[s]); else mfma16_acc(acc[t], wr[t], of[s]); }
        if (s == 0) mfma16_new(accw, wr[6], of[s]); else mfma16_acc(accw, wr[6], of[s]);
        SBAR();
        if (s + 1 < 9) rd(s + 1); }
    asm volatile("s_nop 7" : "+v"(acc[5]), "+v"(acc[4]), "+v"(accw), "+v"(xr[0]), "+v"(xr[1]), "+v"(xr[2]), "+v"(xr[3]));
    LBAR();
    LAS char* ex = lds + mi * 8192 + lane * 16;
    if (kj == 0) { *(LAS f32x4*)(ex + 4 * 1024) = acc[4]; *(LAS f32x4*)(ex + 5 * 1024) = acc[5]; *(LAS f32x4*)(ex + 6 * 1024) = accw; }
    else {
#pragma unroll
        for (int t = 0; t < 4; ++t) *(LAS f32x4*)(ex + t * 1024) = acc[t];
        *(LAS f32x4*)(ex + 7 * 1024) = accw; }
    LBAR();
    accw += *(LAS const f32x4*)(ex + (kj ? 6 : 7) * 1024);
    const float mu = mux + __shfl(accw[0] + accw[1], m);
    float ssq = 0.f;
    auto fin = [&](int t, u32x2 xv, unsigned (&pk)[2]) {
        const f32x4 a = acc[t] + *(LAS const f32x4*)(ex + t * 1024);
        const unsigned h0 = cvtpk(a[0] + bflo(xv[0]), a[1] + bfhi(xv[0])), h1 = cvtpk(a[2] + bflo(xv[1]), a[3] + bfhi(xv[1]));
        pk[0] = cvtpk(bflo(h0) - mu, bfhi(h0) - mu); pk[1] = cvtpk(bflo(h1) - mu, bfhi(h1) - mu);
        ssq = dot2(pk[0], pk[0], ssq); ssq = dot2(pk[1], pk[1], ssq);
        if (bad) { pk[0] = 0x7fc07fc0u; pk[1] = 0x7fc07fc0u; } };
    auto pair = [&](int t, u32x2 xa, u32x2 xb2) { unsigned p0[2], p1[2]; fin(t, xa, p0); fin(t + 1, xb2, p1);
        bstore16<1>((u32x4){p0[0], p0[1], p1[0], p1[1]}, srd, l16, rfl((unsigned)WS_HCF + (mt16 * 18 + 3 * n6 + (t >> 1)) * 1024)); };
    if (kj == 0) { pair(0, xr[0], xr[1]); pair(2, xr[2], xr[3]); } else pair(4, xr[0], xr[1]);
    ssq += xor16(ssq); ssq += xor32(ssq);
    if (g == 0) { ((float*)(ws + WS_SSQ))[(mt16 * 16 + 2 * n6 + kj) * 16 + m] = ssq;
        if (n6 == 0 && kj == 0) ((float*)(ws + WS_MUH))[16 * mt16 + m] = mu; }
}
__global__ __launch_bounds__(1024, 4) void k2_attn(const char* x, const char* wg, const char* wu, const char* wd, char* ws) {
    extern __shared__ __attribute__((aligned(16))) char lds_generic[];
    LAS char* lds = (LAS char*)lds_generic; const unsigned ldsb = (unsigned)(uintptr_t)lds_generic;
    const int tid = threadIdx.x, lane = tid & 63, wid = rfl(tid >> 6), qg = wid & 3, kq = wid >> 2, r32 = lane & 31, h = lane >> 5;
    CODE_LABEL(preln_k2_begin);
    const int b = blockIdx.x, xcd = b & 7, idx = b >> 3, head = xcd * 4 + (idx & 3), qb = idx >> 2;
    const srd_t srd = make_srd(ws, (unsigned)WS_END);
    const int c0 = (2 * kq + (qb & 1)) * KVCHUNK, c1 = (2 * kq + ((qb & 1) ^ 1)) * KVCHUNK;
    const unsigned kvb = (unsigned)WS_KV + head * KVIMG, l16 = lane * 16;
    const int q0 = 256 * qb + 64 * qg;
    bf16x8 qm[2]; float qtl[2]; float km;
    { const unsigned so = (unsigned)WS_QM + (head * S_ + q0) * 32, vo = r32 * 32 + h * 16; bload16<0>(qm[0], srd, vo, so); bload16<1024>(qm[1], srd, vo, so);
      const unsigned st = (unsigned)WS_QT + (head * S_ + q0) * 4; bload4<0>(qtl[0], srd, r32 * 4, st); bload4<128>(qtl[1], srd, r32 * 4, st);
      bload4<0>(km, srd, (lane & 15) * 4, (unsigned)WS_KMAX + head * 64); }
    bdma4(srd, l16, kvb + c0 + qg * 5120, ldsb + c0 + qg * 5120); if (qg < 3) bdma1(srd, l16, kvb + c0 + qg * 5120 + 4096, ldsb + c0 + qg * 5120 + 4096);
    const int slot = wid * 32 + idx; const bool rt = slot < 486;
    bf16x8 wb[4]; unsigned rdst[4], rso[4];
    { const RtJob& J = rt_table.j[xcd][rt ? slot : 0];
#pragma unroll
      for (int p = 0; p < 4; ++p) { rso[p] = J.so[p]; rdst[p] = J.dst[p]; } }
    const char* rsrc = slot < 162 ? wg : slot < 324 ? wu : wd;
    const unsigned rvo = (lane >> 3) * (slot < 324 ? F_ * 2 : D_ * 2) + (lane & 7) * 16;
    asm volatile("s_waitcnt vmcnt(4)" : "+v"(qm[0]), "+v"(qm[1]), "+v"(qtl[0]), "+v"(qtl[1]), "+v"(km) :: "memory");
    km = fmaxf(km, row_ror<8>(km)); km = fmaxf(km, row_ror<4>(km)); km = fmaxf(km, row_ror<2>(km)); km = fmaxf(km, row_ror<1>(km));
    s16x4 qt[2]; bool unsafe = false;
#pragma unroll
    for (int u = 0; u < 2; ++u) { const u32x4 qv = __builtin_bit_cast(u32x4, qm[u]); const unsigned tl = __float_as_uint(qtl[u]);
        float n2 = h ? 0.f : dot2(tl, tl, 0.f);
#pragma unroll
        for (int e = 0; e < 4; ++e) n2 = dot2(qv[e], qv[e], n2);
        n2 += xor32(n2);
        const float bound = __builtin_amdgcn_sqrtf(n2 * km) * 1.0001f + 1e-6f;
        unsafe |= !(bound <= SAFE_B);
        const unsigned cb = (__float_as_uint(bound) + 0xffffu) >> 16;
        const u32x2 w = h ? (u32x2){0u, 0u} : (u32x2){tl, cb | 0x8000u};
        qt[u] = __builtin_bit_cast(s16x4, w); }
    const bool slow = __any(unsafe);
    if (slow) {
#pragma unroll
        for (int u = 0; u < 2; ++u) { u32x2 w = __builtin_bit_cast(u32x2, qt[u]); w[1] = 0u; qt[u] = __builtin_bit_cast(s16x4, w); } }
    f32x16 o[2] = {(f32x16){}, (f32x16){}}; float mrun[2] = {-INFINITY, -INFINITY};
    const int vrow = r32 >= 19 ? 4 : r32 + (r32 >= 4 ? 5 : 0) - (r32 >= 12 ? 12 : 0) + (r32 >= 16 ? 8 : 0);
    const unsigned kc = h ? 0u : 0x00003f80u;
    unsigned kp = ldsb + c0 + h * 4096 + r32 * 16, tp = ldsb + c0 + 8192 + r32 * 4, vp = ldsb + c0 + 9216 + h * 320 + vrow * 16;
#define KP(o) ((LAS const char*)(uintptr_t)kp + (o))
#define TP(o) ((LAS const char*)(uintptr_t)tp + (o))
#define VP(o) ((LAS const char*)(uintptr_t)vp + (o))

    WAIT_BAR(0);
    const srd_t sw = make_srd(rsrc, D_ * F_ * 2);
    auto chunk1 = [&](int part) { if (part == 0) bdma4(srd, l16, kvb + c1 + qg * 5120, ldsb + c1 + qg * 5120);
                                  else if (qg < 3) bdma1(srd, l16, kvb + c1 + qg * 5120 + 4096, ldsb + c1 + qg * 5120 + 4096); };
    if (slow) { chunk1(0); chunk1(1);
        if (rt) {
#pragma unroll
            for (int p = 0; p < 4; ++p) bload16<0>(wb[p], sw, rvo, rso[p]); }
        attn_slow(KP(0), TP(0), VP(0), qm, qt, o, mrun, kc); }
    else attn_fast(KP(0), TP(0), VP(0), qm, qt, o, kc, [&](int it) {
        if (it < 2) { chunk1(it); SBAR(); }
        if (it >= 2 && it < 6 && rt) { bload16<0>(wb[it - 2], sw, rvo, rso[it - 2]); SBAR(); } });
    if (rt) WAIT_BAR(4); else WAIT_BAR(0);
    if (rt) { asm volatile("s_waitcnt vmcnt(0)" : "+v"(wb[0]), "+v"(wb[1]), "+v"(wb[2]), "+v"(wb[3]) :: "memory");
        int ty = threadIdx.x; asm volatile("" : "+v"(ty)); LAS char* scr = lds + c0 + qg * 4864 + (ty & 63) * 16;
#pragma unroll
        for (int p = 0; p < 4; ++p) *(LAS bf16x8*)(scr + p * 1024) = wb[p]; }
    auto rt_step = [&](int it) {
        if (it < 1 || it > 4 || !rt) return;
        int ty = threadIdx.x; asm volatile("" : "+v"(ty)); const int ln = ty & 63;
        LAS const char* base = lds + c0 + qg * 4864 + (4 * (ln >> 4) + ((ln >> 2) & 3)) * 128 + (ln & 3) * 8;
        bstore16<1>(__builtin_bit_cast(u32x4, tr_frag(base + 32 * (it - 1), 16 * 128)), srd, ln * 16, rdst[it - 1]);
        SBAR(); };
    { const int dc = c1 - c0; kp += dc; tp += dc; vp += dc; PINV(kp); PINV(tp); PINV(vp); }
    if (slow) {
#pragma unroll
        for (int it = 1; it < 5; ++it) rt_step(it);
        attn_slow(KP(0), TP(0), VP(0), qm, qt, o, mrun, kc); }
    else attn_fast(KP(0), TP(0), VP(0), qm, qt, o, kc, rt_step);
    int tz = threadIdx.x; asm volatile("" : "+v"(tz));
    const int lane2 = tz & 63, r2 = tz & 31, h2 = (tz >> 5) & 1;
    LAS float* mx = (LAS float*)(lds + KVIMG);
#pragma unroll
    for (int u = 0; u < 2; ++u) { LAS char* pb = lds + c0 + (qg * 2 + u) * 2432;
        *(LAS f32x4*)(pb + lane2 * 16) = (f32x4){o[u][0], o[u][1], o[u][2], o[u][3]};
        *(LAS f32x4*)(pb + 1024 + lane2 * 16) = (f32x4){o[u][4], o[u][5], o[u][6], o[u][7]};
        if (h2 == 0) { *(LAS f32x2*)(pb + 2048 + r2 * 8) = (f32x2){o[u][8], o[u][9]}; *(LAS float*)(pb + 2304 + r2 * 4) = o[u][10];
                      if (slow) mx[wid * 64 + 32 * u + r2] = mrun[u]; } }
    LBAR();
    { const int u = kq & 1, hf = kq >> 1;
      LAS const char* pb = lds + (qb & 1) * KVCHUNK + (qg * 2 + u) * 2432;
      float l = 0.f; f32x4 a = {0, 0, 0, 0}; f32x2 c = {0, 0};
      if (slow) { float mk[4], M = -INFINITY;
#pragma unroll
          for (int k = 0; k < 4; ++k) { mk[k] = mx[(4 * k + qg) * 64 + 32 * u + r2]; M = fmaxf(M, mk[k]); }
#pragma unroll
          for (int k = 0; k < 4; ++k) { LAS const char* p = pb + k * 2 * KVCHUNK; const float f = __builtin_amdgcn_exp2f(mk[k] - M);
              l += f * *(LAS const float*)(p + 2304 + r2 * 4); a += *(LAS const f32x4*)(p + hf * 1024 + lane2 * 16) * f; c += *(LAS const f32x2*)(p + 2048 + r2 * 8) * f; }
      } else {
#pragma unroll
          for (int k = 0; k < 4; ++k) { LAS const char* p = pb + k * 2 * KVCHUNK;
              l += *(LAS const float*)(p + 2304 + r2 * 4); a += *(LAS const f32x4*)(p + hf * 1024 + lane2 * 16); c += *(LAS const f32x2*)(p + 2048 + r2 * 8); } }
      const float rl = __builtin_amdgcn_rcpf(l);
      const int token = q0 + 32 * u + r2; const unsigned ov = (token >> 4) * (18 * 1024) + (token & 15) * 16;
      bstore8<1>((u32x2){cvtpk(a[0] * rl, a[1] * rl), cvtpk(a[2] * rl, a[3] * rl)}, srd, ov + h2 * 8, rfl((unsigned)WS_OF + (head >> 1) * 1024 + (2 * (head & 1) + hf) * 256));
      if (hf == 0 && h2 == 0) __hip_atomic_store((unsigned*)(ws + WS_OF + ov + (16 + (head >> 4)) * 1024 + ((head >> 2) & 3) * 256 + (head & 3) * 4), cvtpk(c[0] * rl, c[1] * rl), __ATOMIC_RELAXED, __HIP_MEMORY_SCOPE_AGENT);
    }
    asm volatile("s_waitcnt vmcnt(0) lgkmcnt(0)\n\ts_barrier" ::: "memory");
    arrive(ws, 96 + qb);
    if (idx < 24) { const int mt32 = 4 * xcd + (idx & 3), n6 = idx >> 2; float mux;
        k3_request(x, ws, lds_generic, mt32, n6, mux);
        const bool bad = await<false, 960, true>(ws, 96 + xcd, 32u, (LAS unsigned*)(lds + K3_FLAG));
        PINV(mux);
        k3_compute(ws, lds_generic, mt32, n6, mux, bad); }
}

constexpr int RING_SLOT = 36 * 1024, G_LDS = 4 * RING_SLOT;

struct Ev { int type, arg, n; };
enum { E_SSQ, E_A, E_B, E_WAITB, E_BAR, E_READ, E_MMA, E_GP, E_RS, E_ST, E_NOP, E_A2 };
constexpr int G1_PD = 4;
constexpr int g1_grp(int f) { return f < 144 ? 0 : f < 216 ? 1 : f < 288 ? 2 : 3; }
constexpr int g1_ks(int f) { return f < 144 ? f / 8 : f < 288 ? ((f - 144) % 72) / 4 : (f - 288) / 2; }
constexpr int g1_tile(int f) { return f < 288 ? (f >> 1) & 1 : 0; }
constexpr int g1_set(int f) { return f < 144 ? (f >> 2) & 1 : f < 216 ? 0 : f < 288 ? 1 : 0; }
template <bool LATE> struct G1Prog {
    Ev ev[1600]; int n;
    constexpr void add(int t, int a) { ev[n].type = t; ev[n].arg = a; ev[n].n = 0; ++n; }
    static constexpr int vm(const Ev& e) { return e.type == E_SSQ ? 6 : (e.type == E_A || e.type == E_B || e.type == E_ST || e.type == E_A2) ? 1 : 0; }
    static constexpr int NP = LATE ? 4 : 5;
    constexpr void addB(int s, int u) { add(E_B, s * 2 + u); }
    static constexpr int jset(int q) { return q == 0 ? 0 : q == 1 ? 1 : q == 2 ? 0 : q == 3 ? 1 : 0; }
    constexpr G1Prog() : ev{}, n(0) {
        for (int p = 0; p < NP; ++p) add(E_A, p);
        addB(0, 0); addB(0, 1); addB(1, 0); addB(1, 1);
        add(E_BAR, 0);
        for (int f = 0; f < G1_PD; ++f) add(E_READ, f);
        for (int f = 0; f < 324; ++f) {
            const int st = f / 36, i = f % 36, g = g1_grp(f);
            if (g == 0) { const int s = f / 8, q = f % 8;
                if (q == 0) add(E_WAITB, s);
                if (f == 0) { addB(2, 0); addB(2, 1); }
                if (s + 3 < 18 && q == 2) addB(s + 3, 0);
                if (s + 3 < 18 && q == 6) addB(s + 3, 1);
                if (f == 100) add(E_SSQ, 0);
                if (f == 136) add(E_RS, 0); }
            if (st == 0 && i >= 1 && i < 1 + 3 * NP && (i - 1) % 3 == 0) add(E_A, 8 + (i - 1) / 3);
            if (st + 2 < 9) { const int i0 = st == 0 ? 17 : LATE ? 17 : 1, d = 4;
                if (i >= i0 && (i - i0) % d == 0 && (i - i0) / d < NP) add(E_A, (st + 2) * 8 + (i - i0) / d); }
            if (i == 36 - G1_PD && st + 1 < 9) add(E_BAR, st + 1);
            if (f + G1_PD < 324) add(E_READ, f + G1_PD);
            add(E_MMA, f);
            if (f == 143) { add(E_NOP, 0);
                for (int k = 0; k < 32; ++k) { add(E_GP, 0 * 64 + k); if (k == 15 || k == 31) add(E_ST, 0 * 2 + (k == 31)); } }
            if (g > 0) {
                const int r = f - (g == 1 ? 144 : g == 2 ? 216 : 288), step = g < 3 ? 2 : 1, r0 = 3, k = (r - r0) / step;
                if (r >= r0 && (r - r0) % step == 0 && k < 32) { add(E_GP, g * 64 + k);
                    if (k == (LATE ? 22 : 15)) add(E_ST, g * 2);
                    if (k == 31 && !LATE) add(E_ST, g * 2 + 1); }
                if (LATE && r == (g < 3 ? 71 : 35)) add(E_ST, g * 2 + 1); }
            if (st == 8 && i >= 2 && i % 3 == 2 && (i - 2) / 3 < 2 * NP) add(E_A2, ((i - 2) / 3 / NP) * 8 + ((i - 2) / 3) % NP);
        }
        add(E_NOP, 1);
        for (int k = 0; k < 16; ++k) { add(E_GP, 4 * 64 + k); if (k == 7 || k == 15) add(E_ST, 8 + (k == 15)); }
        for (int i = 0; i < n; ++i) if (ev[i].type == E_WAITB || ev[i].type == E_BAR || ev[i].type == E_RS) {
            int cnt = 0, j = i - 1;
            for (; j >= 0; --j) {
                if (ev[i].type == E_WAITB && ev[j].type == E_B && ev[j].arg == 2 * ev[i].arg + 1) break;
                if (ev[i].type == E_RS && ev[j].type == E_SSQ) break;
                if (ev[i].type == E_BAR && ev[j].type == E_A && ev[j].arg == 8 * ev[i].arg + NP - 1) break;
                cnt += vm(ev[j]); }
            ev[i].n = cnt > 63 ? 63 : cnt; }
    }
};
template <bool LATE> constexpr G1Prog<LATE> g1prog{};
DI float vmul(float a, float b) { float r; asm("v_mul_f32_e32 %0, %1, %2" : "=v"(r) : "v"(a), "v"(b)); return r; }
DI float vfma(float a, float b, float c) { float r; asm("v_fma_f32 %0, %1, %2, %3" : "=v"(r) : "v"(a), "v"(b), "v"(c)); return r; }
struct G1State {
    bf16x8 hf[2][18]; f32x4 acc[2][2][2][2];
    bf16x8 ar[G1_PD + 1]; float sq[2][3]; float a0[2], a1[2], kk[2]; f32x4 z, w; u32x4 o; unsigned soffA2;
    unsigned lb0, lb1, lb2; unsigned l16, ldsb, soffA, soffH, soffS, soffAct, soffAct8, vq; int wid, j, g;
};
template <bool LATE, int PC> DI void g1_exec(G1State& c, const srd_t srd) {
    constexpr Ev e = g1prog<LATE>.ev[PC];
    if constexpr (e.type == E_SSQ) {
        bload4<0>(c.sq[0][0], srd, c.vq, c.soffS); bload4<256>(c.sq[0][1], srd, c.vq, c.soffS); bload4<512>(c.sq[0][2], srd, c.vq, c.soffS);
        bload4<1024>(c.sq[1][0], srd, c.vq, c.soffS); bload4<1280>(c.sq[1][1], srd, c.vq, c.soffS); bload4<1536>(c.sq[1][2], srd, c.vq, c.soffS); }
    else if constexpr (e.type == E_A) { constexpr int st = e.arg >> 3, p = e.arg & 7;
        const unsigned so = c.soffA + st * RING_SLOT, dst = c.ldsb + (st & 3) * RING_SLOT;
        if constexpr (p < 4) bdma_next<p * 1024>(srd, c.l16, so + c.wid * 4096, dst + c.wid * 4096);
        else bdma1(srd, c.l16, so + 32768 + c.wid * 1024, dst + 32768 + c.wid * 1024); }
    else if constexpr (e.type == E_B) { constexpr int s = e.arg >> 1, u = e.arg & 1;
        bload16<((18 * u + s) & 3) * 1024>(c.hf[u][s], srd, c.l16, c.soffH + ((18 * u + s) >> 2) * 4096); }
    else if constexpr (e.type == E_WAITB) { asm volatile("s_waitcnt vmcnt(%2)" : "+v"(c.hf[0][e.arg]), "+v"(c.hf[1][e.arg]) : "n"(e.n)); }
    else if constexpr (e.type == E_BAR) { asm volatile("s_waitcnt vmcnt(%0)\n\ts_barrier" :: "n"(e.n) : "memory"); }
    else if constexpr (e.type == E_RS) { asm volatile("s_waitcnt vmcnt(%6)" : "+v"(c.sq[0][0]), "+v"(c.sq[0][1]), "+v"(c.sq[0][2]), "+v"(c.sq[1][0]), "+v"(c.sq[1][1]), "+v"(c.sq[1][2]) : "n"(e.n));
#pragma unroll
        for (int u = 0; u < 2; ++u) { float t = (c.sq[u][0] + c.sq[u][1]) + c.sq[u][2]; t += xor16(t); t += xor32(t);
            const float v = t * (1.f / D_) + EPS, rs = __builtin_amdgcn_rsqf(v);
            c.a0[u] = -2.30220818f * rs; c.a1[u] = -0.10294324f * rs * rs * rs; c.kk[u] = v; }
        SBAR(); }
    else if constexpr (e.type == E_A2) { constexpr int st = e.arg >> 3, p = e.arg & 7;
        const unsigned so = c.soffA2 + st * RING_SLOT, dst = c.ldsb + ((st + 1) & 3) * RING_SLOT;
        if constexpr (p < 4) bdma_next<p * 1024>(srd, c.l16, so + c.wid * 4096, dst + c.wid * 4096);
        else bdma1(srd, c.l16, so + 32768 + c.wid * 1024, dst + 32768 + c.wid * 1024); }
    else if constexpr (e.type == E_NOP) {
        asm volatile("s_nop 7" : "+v"(c.acc[0][0][0][0]), "+v"(c.acc[0][0][0][1]), "+v"(c.acc[0][0][1][0]), "+v"(c.acc[0][0][1][1]));
        if constexpr (e.arg == 0) asm volatile("" : "+v"(c.acc[0][1][0][0]), "+v"(c.acc[0][1][0][1]), "+v"(c.acc[0][1][1][0]), "+v"(c.acc[0][1][1][1])); }
    else if constexpr (e.type == E_READ) { constexpr int f = e.arg, off = ((f / 36) & 3) * RING_SLOT + (f % 36) * 1024;
        if constexpr ((off >> 16) == 0) c.ar[f % (G1_PD + 1)] = lds_frag((LAS const char*)(uintptr_t)c.lb0 + (off & 65535));
        else if constexpr ((off >> 16) == 1) c.ar[f % (G1_PD + 1)] = lds_frag((LAS const char*)(uintptr_t)c.lb1 + (off & 65535));
        else c.ar[f % (G1_PD + 1)] = lds_frag((LAS const char*)(uintptr_t)c.lb2 + (off & 65535));
        SBAR(); }
    else if constexpr (e.type == E_MMA) { constexpr int f = e.arg, s = g1_ks(f), t = g1_tile(f), q = f & 1, set = g1_set(f);
        if constexpr (s == 0) { mfma16_new(c.acc[set][t][q][0], c.ar[f % (G1_PD + 1)], c.hf[0][s]); mfma16_new(c.acc[set][t][q][1], c.ar[f % (G1_PD + 1)], c.hf[1][s]); }
        else { mfma16_acc(c.acc[set][t][q][0], c.ar[f % (G1_PD + 1)], c.hf[0][s]); mfma16_acc(c.acc[set][t][q][1], c.ar[f % (G1_PD + 1)], c.hf[1][s]); }
        SBAR(); }
    else if constexpr (e.type == E_GP) {
        constexpr int grp = e.arg >> 6, k = e.arg & 63, quad = k >> 3, ph = k & 7, set = G1Prog<LATE>::jset(grp);
        constexpr int u = grp < 4 ? quad >> 1 : quad, t = grp < 4 ? quad & 1 : 0;
        const f32x4 g = c.acc[set][t][0][u];
        if constexpr ((grp == 0 || grp == 4) && (ph == 4 || ph == 6)) asm volatile("s_nop 0" : "+v"(c.z));
#pragma unroll
        for (int r = 0; r < 4; ++r) {
            if constexpr (ph == 0) c.z[r] = vmul(g[r], g[r]);
            if constexpr (ph == 1) c.z[r] = vfma(c.z[r], c.a1[u], c.a0[u]);
            if constexpr (ph == 2) c.z[r] = vmul(c.z[r], g[r]);
            if constexpr (ph == 3) c.z[r] = __builtin_amdgcn_exp2f(c.z[r]);
            if constexpr (ph == 4) c.z[r] = vfma(c.z[r], c.kk[u], c.kk[u]);
            if constexpr (ph == 5) c.z[r] = __builtin_amdgcn_rcpf(c.z[r]);
            if constexpr (ph == 6) c.w[r] = vmul(vmul(g[r], c.acc[set][t][1][u][r]), c.z[r]); }
        if constexpr (ph == 7) { c.o[2 * t] = cvtpk(c.w[0], c.w[1]); c.o[2 * t + 1] = cvtpk(c.w[2], c.w[3]); }
        SBAR(); }
    else if constexpr (e.type == E_ST) { constexpr int grp = e.arg >> 1, u = e.arg & 1;
        if constexpr (grp < 4) bstore16<1>(c.o, srd, c.l16, c.soffAct + u * (144 * 1024) + grp * 1024);
        else bstore8<1>((u32x2){c.o[0], c.o[1]}, srd, c.l16 + (c.j & 1) * 8, c.soffAct8 + u * (144 * 1024));
        SBAR(); }
}
template <bool LATE, int... I> DI void g1_run(G1State& c, const srd_t srd, std::integer_sequence<int, I...>) { (g1_exec<LATE, I>(c, srd), ...); }

constexpr int G2_PD = 4;
template <bool LATE> struct G2Prog {
    Ev ev[700]; int n;
    constexpr void add(int t, int a) { ev[n].type = t; ev[n].arg = a; ev[n].n = 0; ++n; }
    static constexpr int vm(const Ev& e) { return (e.type == E_A || e.type == E_B) ? 1 : 0; }
    static constexpr int NP = LATE ? 4 : 5;
    constexpr G2Prog() : ev{}, n(0) {
        add(E_B, 0); add(E_B, 1); add(E_B, 2); add(E_B, 3);
        for (int f = 0; f < G2_PD; ++f) add(E_READ, f);
        for (int f = 0; f < 162; ++f) {
            const int st = f / 18, i = f % 18;
            if (i % 9 == 0) add(E_WAITB, st * 2 + i / 9);
            const int ia = LATE ? 3 : 1, ib = LATE ? 1 : 3;
            if (st == 0) {
                if (i >= 1 && i < 5) add(E_B, 4 + i - 1);
                if (i >= 6 && i < 6 + NP) add(E_A, 16 + i - 6);
                if (i >= 12 && i < 16) add(E_B, 8 + i - 12);
            } else {
                if (st == 1 && i >= 14) add(E_B, 16 + i - 14);
                if (st + 2 < 9 && i >= ia && (i - ia) % 4 == 0 && (i - ia) / 4 < 4) add(E_A, (st + 2) * 8 + (i - ia) / 4);
                if (st + 2 < 9 && NP == 5 && i == 12) add(E_A, (st + 2) * 8 + 4);
                { const int la = st == 1 ? 2 : 3; if (st + la < 9 && (st == 1 || st + la > 4) && i >= ib && (i - ib) % 4 == 0 && (i - ib) / 4 < 4) add(E_B, (st + la) * 4 + (i - ib) / 4); }
            }
            if (i == 18 - G2_PD && st + 1 < 9) add(E_BAR, st + 1);
            if (f + G2_PD < 162) add(E_READ, f + G2_PD);
            add(E_MMA, f);
        }
        for (int i = 0; i < n; ++i) if (ev[i].type == E_WAITB || ev[i].type == E_BAR) {
            int cnt = 0, j = i - 1;
            for (; j >= 0; --j) {
                if (ev[i].type == E_WAITB && ev[j].type == E_B && ev[j].arg == 2 * ev[i].arg + 1) break;
                if (ev[i].type == E_BAR && ev[j].type == E_A && (ev[j].arg >> 3) == ev[i].arg) break;
                cnt += vm(ev[j]); }
            ev[i].n = cnt > 63 ? 63 : cnt; }
    }
};
template <bool LATE> constexpr G2Prog<LATE> g2prog{};
struct G2State {
    bf16x8 af[4][2][2]; f32x4 acc[9][2]; bf16x8 ar[G2_PD + 1];
    unsigned lb0, lb1, lb2; unsigned l16, ldsb, soffA, soffB; int wid;
};
template <bool LATE, int PC> DI void g2_exec(G2State& c, const srd_t srd) {
    constexpr Ev e = g2prog<LATE>.ev[PC];
    if constexpr (e.type == E_A) { constexpr int st = e.arg >> 3, p = e.arg & 7;
        const unsigned so = c.soffA + st * RING_SLOT, dst = c.ldsb + ((st + 1) & 3) * RING_SLOT;
        if constexpr (p < 4) bdma_next<p * 1024>(srd, c.l16, so + c.wid * 4096, dst + c.wid * 4096);
        else bdma1(srd, c.l16, so + 32768 + c.wid * 1024, dst + 32768 + c.wid * 1024); }
    else if constexpr (e.type == E_B) { constexpr int st = e.arg >> 2, s2 = (e.arg >> 1) & 1, u = e.arg & 1;
        bload16_sc1<((2 * st + s2) & 3) * 1024>(c.af[st & 3][s2][u], srd, c.l16, c.soffB + u * (144 * 1024) + ((2 * st + s2) >> 2) * 4096); }
    else if constexpr (e.type == E_WAITB) { asm volatile("s_waitcnt vmcnt(%2)" : "+v"(c.af[(e.arg >> 1) & 3][e.arg & 1][0]), "+v"(c.af[(e.arg >> 1) & 3][e.arg & 1][1]) : "n"(e.n)); }
    else if constexpr (e.type == E_BAR) { asm volatile("s_waitcnt vmcnt(%0)\n\ts_barrier" :: "n"(e.n) : "memory"); }
    else if constexpr (e.type == E_READ) { constexpr int f = e.arg, st = f / 18, off = ((st + 1) & 3) * RING_SLOT + (f % 18) * 1024;
        if constexpr ((off >> 16) == 0) c.ar[f % (G2_PD + 1)] = lds_frag((LAS const char*)(uintptr_t)c.lb0 + (off & 65535));
        else c.ar[f % (G2_PD + 1)] = lds_frag((LAS const char*)(uintptr_t)c.lb1 + (off & 65535));
        SBAR(); }
    else if constexpr (e.type == E_MMA) { constexpr int f = e.arg, st = f / 18, s2 = (f % 18) / 9, t = f % 9;
        if constexpr (f < 9) { mfma16_new(c.acc[t][0], c.ar[f % (G2_PD + 1)], c.af[st & 3][s2][0]); mfma16_new(c.acc[t][1], c.ar[f % (G2_PD + 1)], c.af[st & 3][s2][1]); }
        else { mfma16_acc(c.acc[t][0], c.ar[f % (G2_PD + 1)], c.af[st & 3][s2][0]); mfma16_acc(c.acc[t][1], c.ar[f % (G2_PD + 1)], c.af[st & 3][s2][1]); }
        SBAR(); }
}
template <bool LATE, int... I> DI void g2_run(G2State& c, const srd_t srd, std::integer_sequence<int, I...>) { (g2_exec<LATE, I>(c, srd), ...); }
constexpr int FFN_LDS = G_LDS + 1024;
__global__ __launch_bounds__(512, 2) void ffn_kernel(char* ws, char* out) {
    extern __shared__ __attribute__((aligned(16))) char lds_generic[];
    LAS char* lds = (LAS char*)lds_generic;
    CODE_LABEL(preln_ffn_begin);
    const srd_t srd = make_srd(ws, (unsigned)WS_END);
    const int b = blockIdx.x, xcd = b & 7, idx = b >> 3;
    LAS unsigned* flag = (LAS unsigned*)(lds + G_LDS);
    float csink = 0.f;
    if (idx == 0) { const char *c0, *c1; CODE_ADDR(c0, preln_ffn_begin); CODE_ADDR(c1, preln_ffn_end); touch_code(csink, c0, c1, rfl(threadIdx.x >> 6), threadIdx.x & 63); }
    {
        int tid = threadIdx.x; asm volatile("" : "+v"(tid));
        const int lane = tid & 63, wid = rfl(tid >> 6);
        const int j = (xcd >> 1) * 8 + (idx & 7), mt8 = (xcd & 1) * 4 + (idx >> 3), mt16 = 16 * mt8 + 2 * wid;
        G1State c;
        c.l16 = lane * 16; c.ldsb = (unsigned)(uintptr_t)lds_generic; c.wid = wid; c.j = j; c.g = lane >> 4;
        c.lb0 = c.ldsb + lane * 16; c.lb1 = c.lb0 + 65536; c.lb2 = c.lb0 + 131072; PINV(c.lb0); PINV(c.lb1); PINV(c.lb2);
        c.soffA = (unsigned)WS_W1 + j * (324 * 1024); c.soffH = (unsigned)WS_HCF + mt16 * (18 * 1024); c.soffS = (unsigned)WS_SSQ + mt16 * 1024;
        c.soffAct8 = rfl((unsigned)WS_ACT + (mt16 * 144 + 9 * (j >> 1) + 8) * 1024);
        c.soffAct = rfl((unsigned)WS_ACT + (mt16 * 144 + 9 * (j >> 1) + 4 * (j & 1)) * 1024); c.vq = (lane >> 4) * 64 + (lane & 15) * 4;
        c.soffA2 = (unsigned)WS_W2 + ((xcd >> 1) * 4 + (idx >> 3)) * (324 * 1024);
        if (wid >= 4) g1_run<true>(c, srd, std::make_integer_sequence<int, g1prog<true>.n>{});
        else g1_run<false>(c, srd, std::make_integer_sequence<int, g1prog<false>.n>{});
        asm volatile("s_waitcnt vmcnt(0) lgkmcnt(0)\n\ts_barrier" : "+v"(csink) :: "memory");
        arrive(ws, 64 + (j >> 3) * 8 + mt8);
    }
    int tz = threadIdx.x; asm volatile("" : "+v"(tz));
    const int lane = tz & 63, wid = rfl(tz >> 6), mi = wid & 3, kj = wid >> 2;
    const int ksl = xcd >> 1, mt = (xcd & 1) * 8 + (idx & 7), n4 = idx >> 3;
    const int mt16 = 8 * mt + 2 * mi;
    G2State c;
    c.l16 = lane * 16; c.ldsb = (unsigned)(uintptr_t)lds_generic; c.wid = wid;
    c.lb0 = c.ldsb + kj * (18 * 1024) + lane * 16; c.lb1 = c.lb0 + 65536; PINV(c.lb0); PINV(c.lb1);
    c.soffA = (unsigned)WS_W2 + (ksl * 4 + n4) * (324 * 1024); c.soffB = (unsigned)WS_ACT + (mt16 * 144 + 36 * ksl + 18 * kj) * 1024;
    bool bad = await<false>(ws, 64 + ksl * 8 + (mt >> 1), 8u, flag);
    if (wid >= 4) g2_run<true>(c, srd, std::make_integer_sequence<int, g2prog<true>.n>{});
    else g2_run<false>(c, srd, std::make_integer_sequence<int, g2prog<false>.n>{});
    asm volatile("s_nop 7" : "+v"(c.acc[8][0]), "+v"(c.acc[8][1]), "+v"(c.acc[7][0]), "+v"(c.acc[7][1]));
    u32x2 hv[3]; u32x4 pv[3]; u32x2 pw[3]; float mu[2];
    { const int T0 = 9 * n4 + wid, T8 = 9 * n4 + 8; const unsigned hb = rfl((unsigned)WS_HCF + (8 * mt + 2 * ksl) * (18 * 1024));
      bload8<0>(hv[0], srd, lane * 16 + (T0 & 1) * 8, hb + (T0 >> 1) * 1024); bload8<0>(hv[1], srd, lane * 16 + (T0 & 1) * 8, hb + (18 + (T0 >> 1)) * 1024);
      bload8<0>(hv[2], srd, lane * 16 + (T8 & 1) * 8, hb + ((wid & 1) * 18 + (T8 >> 1)) * 1024);
      const unsigned mb = rfl((unsigned)WS_MUH + (128 * mt + 32 * ksl) * 4); bload4<0>(mu[0], srd, (lane & 15) * 4, mb); bload4<64>(mu[1], srd, (lane & 15) * 4, mb); }
    LBAR();
    LAS char* ex = lds + mi * 18432 + lane * 16;
    if (kj == 0) {
#pragma unroll
        for (int t = 5; t < 9; ++t) { *(LAS f32x4*)(ex + (2 * t) * 1024) = c.acc[t][0]; *(LAS f32x4*)(ex + (2 * t + 1) * 1024) = c.acc[t][1]; }
    } else {
#pragma unroll
        for (int t = 0; t < 5; ++t) { *(LAS f32x4*)(ex + (2 * t) * 1024) = c.acc[t][0]; *(LAS f32x4*)(ex + (2 * t + 1) * 1024) = c.acc[t][1]; }
    }
    LBAR();
    const int tile = mt * 4 + n4; const unsigned xb = rfl((unsigned)WS_PART + tile * (16 * 9 * 1024));
    const unsigned xs0 = rfl(xb + (mi * 4 + ksl) * (9 * 1024));
    auto fin = [&](int t) {
        const f32x4 a0 = c.acc[t][0] + *(LAS const f32x4*)(ex + (2 * t) * 1024), a1 = c.acc[t][1] + *(LAS const f32x4*)(ex + (2 * t + 1) * 1024);
        if (mi == ksl) { *(LAS f32x4*)(ex + (2 * t) * 1024) = a0; *(LAS f32x4*)(ex + (2 * t + 1) * 1024) = a1; }
        else bstore16<1>((u32x4){cvtpk(a0[0], a0[1]), cvtpk(a0[2], a0[3]), cvtpk(a1[0], a1[1]), cvtpk(a1[2], a1[3])}, srd, lane * 16, xs0 + t * 1024); };
    if (kj == 0) { fin(0); fin(1); fin(2); fin(3); fin(4); } else { fin(5); fin(6); fin(7); fin(8); }
    asm volatile("s_waitcnt vmcnt(0) lgkmcnt(0)\n\ts_barrier" : "+v"(hv[0]), "+v"(hv[1]), "+v"(hv[2]), "+v"(mu[0]), "+v"(mu[1]) :: "memory");
    bad |= await<true>(ws, tile, 4u, flag);

#pragma unroll
    for (int k = 0; k < 3; ++k) { const unsigned sb = rfl(xb + (ksl * 4 + (k + (k >= ksl))) * (9 * 1024));
        bload16u_sc1<0>(pv[k], srd, lane * 16, sb + wid * 1024); bload8_sc1<0>(pw[k], srd, lane * 16 + (wid & 1) * 8, sb + 8 * 1024); }
    asm volatile("s_waitcnt vmcnt(0)" : "+v"(pv[0]), "+v"(pv[1]), "+v"(pv[2]), "+v"(pw[0]), "+v"(pw[1]), "+v"(pw[2]) :: "memory");
    const int m = lane & 15, g = lane >> 4;
    auto put = [&](int t, int u, u32x2 h2, u32x2 p0, u32x2 p1, u32x2 p2) {
        f32x4 a = *(LAS const f32x4*)(lds + ksl * 18432 + (2 * t + u) * 1024 + lane * 16);
        a += (f32x4){bflo(h2[0]), bfhi(h2[0]), bflo(h2[1]), bfhi(h2[1])} + (u ? mu[1] : mu[0]); a += (f32x4){bflo(p0[0]), bfhi(p0[0]), bflo(p0[1]), bfhi(p0[1])};
        a += (f32x4){bflo(p1[0]), bfhi(p1[0]), bflo(p1[1]), bfhi(p1[1])}; a += (f32x4){bflo(p2[0]), bfhi(p2[0]), bflo(p2[1]), bfhi(p2[1])};
        u32x2 o = {cvtpk(a[0], a[1]), cvtpk(a[2], a[3])};
        if (bad) o = (u32x2){0x7fc07fc0u, 0x7fc07fc0u};
        *(u32x2*)(out + (size_t)(128 * mt + 32 * ksl + 16 * u + m) * (D_ * 2) + (144 * n4 + 16 * t + 4 * g) * 2) = o; };
    put(wid, 0, hv[0], (u32x2){pv[0][0], pv[0][1]}, (u32x2){pv[1][0], pv[1][1]}, (u32x2){pv[2][0], pv[2][1]});
    put(wid, 1, hv[1], (u32x2){pv[0][2], pv[0][3]}, (u32x2){pv[1][2], pv[1][3]}, (u32x2){pv[2][2], pv[2][3]});
    if (wid < 2) put(8, wid, hv[2], pw[0], pw[1], pw[2]);
    CODE_LABEL(preln_ffn_end);
}

}

extern "C" void kernel_launch(void* const* d_in, const int* in_sizes, int n_in, void* d_out, int out_size, void* d_ws, size_t ws_size, hipStream_t stream) {
    const char* x = (const char*)d_in[0]; const char* wqkv = (const char*)d_in[1]; const char* wo = (const char*)d_in[2];
    const char* wg = (const char*)d_in[4]; const char* wu = (const char*)d_in[5]; const char* wd = (const char*)d_in[6];
    char* ws = (char*)d_ws;
    (void)hipFuncSetAttribute((const void*)k1_qkv, hipFuncAttributeMaxDynamicSharedMemorySize, K1_LDS);
    (void)hipFuncSetAttribute((const void*)k2_attn, hipFuncAttributeMaxDynamicSharedMemorySize, K2_LDS);
    (void)hipFuncSetAttribute((const void*)ffn_kernel, hipFuncAttributeMaxDynamicSharedMemorySize, FFN_LDS);
    k1_qkv<<<256, 512, K1_LDS, stream>>>(x, wqkv, wo, ws);
    k2_attn<<<256, 1024, K2_LDS, stream>>>(x, wg, wu, wd, ws);
    ffn_kernel<<<256, 512, FFN_LDS, stream>>>(ws, (char*)d_out);
}
```

```cpp
#include <hip/hip_runtime.h>
#include <hip/hip_bf16.h>
#include <stdint.h>
#include <utility>

namespace {

typedef __bf16 bf16x2 __attribute__((ext_vector_type(2)));
typedef __bf16 bf16x8 __attribute__((ext_vector_type(8)));
typedef __bf16 bf16x16 __attribute__((ext_vector_type(16)));
typedef short s16x4 __attribute__((ext_vector_type(4)));
typedef float f32x2 __attribute__((ext_vector_type(2)));
typedef float f32x4 __attribute__((ext_vector_type(4)));
typedef float f32x8 __attribute__((ext_vector_type(8)));
typedef float f32x16 __attribute__((ext_vector_type(16)));
typedef unsigned u32x2 __attribute__((ext_vector_type(2)));
typedef unsigned u32x4 __attribute__((ext_vector_type(4)));
#define LAS __attribute__((address_space(3)))
#define DI __device__ __forceinline__

constexpr int S_ = 2048, D_ = 576, H_ = 32, F_ = 4608;
constexpr float EPS = 1e-5f;
constexpr float QSCALE = 0.23570226039551584f * 1.4426950408889634f;
constexpr float SAFE_B = 48.f;

constexpr int KVCHUNK = 19456, KVIMG = 8 * KVCHUNK;
constexpr size_t WS_QM   = 0;
constexpr size_t WS_QT   = WS_QM + (size_t)32 * 2048 * 32;
constexpr size_t WS_KV   = WS_QT + (size_t)32 * 2048 * 4;
constexpr size_t WS_ACT  = 0;
constexpr size_t WS_OF   = WS_ACT + (size_t)128 * 144 * 1024;
constexpr size_t WS_WO   = WS_OF + (size_t)128 * 18 * 1024;
constexpr size_t WS_KMAX = WS_WO + (size_t)6 * 108 * 1024;
constexpr size_t WS_MUX  = WS_KMAX + 32 * 16 * 4;
constexpr size_t WS_WBAR = WS_MUX + 2048 * 4;
constexpr size_t WS_SSQ  = WS_WBAR + 4096;
constexpr size_t WS_CNT  = WS_SSQ + (size_t)2048 * 64;
constexpr size_t WS_MUH  = WS_CNT + 104 * 256;
constexpr size_t WS_HCF  = WS_MUH + 2048 * 4;
constexpr size_t WS_W2   = WS_HCF + (size_t)128 * 18 * 1024;
constexpr size_t WS_W1   = WS_W2 + (size_t)16 * 324 * 1024;
constexpr size_t WS_PART = WS_W1 + (size_t)32 * 324 * 1024;
static_assert(WS_KV + (size_t)32 * KVIMG <= (size_t)128 * 144 * 1024, "Q / K / V fit under act");
constexpr size_t WS_END  = WS_PART + (size_t)64 * 16 * 9 * 1024;

DI unsigned cvtpk(float lo, float hi) { f32x2 v = {lo, hi}; bf16x2 b = __builtin_convertvector(v, bf16x2); return __builtin_bit_cast(unsigned, b); }
DI float bflo(unsigned w) { return __uint_as_float(w << 16); }
DI float bfhi(unsigned w) { return __uint_as_float(w & 0xffff0000u); }
DI float dot2(unsigned a, unsigned b, float c) { return __builtin_amdgcn_fdot2_f32_bf16(__builtin_bit_cast(bf16x2, a), __builtin_bit_cast(bf16x2, b), c, false); }
DI int rfl(int x) { return __builtin_amdgcn_readfirstlane(x); }
DI float xor32(float v) { auto r = __builtin_amdgcn_permlane32_swap(__float_as_uint(v), __float_as_uint(v), false, false); return __uint_as_float((threadIdx.x & 32) ? r[0] : r[1]); }
DI float xor16(float v) { auto r = __builtin_amdgcn_permlane16_swap(__float_as_uint(v), __float_as_uint(v), false, false); return __uint_as_float((threadIdx.x & 16) ? r[0] : r[1]); }
template <int N> DI float row_ror(float v) { return __uint_as_float(__builtin_amdgcn_update_dpp(0u, __float_as_uint(v), 0x120 + N, 0xf, 0xf, false)); }
DI void mfma16_acc(f32x4& c, bf16x8 a, bf16x8 b) { asm volatile("v_mfma_f32_16x16x32_bf16 %0, %1, %2, %0" : "+v"(c) : "v"(a), "v"(b)); }
DI void mfma16_new(f32x4& c, bf16x8 a, bf16x8 b) { asm volatile("v_mfma_f32_16x16x32_bf16 %0, %1, %2, 0" : "=&v"(c) : "v"(a), "v"(b)); }
DI void glds16(const void* sbase, unsigned voff, unsigned lds_dst) {
    asm volatile("s_mov_b32 m0, %2\n\ts_nop 0\n\tglobal_load_lds_dwordx4 %0, %1" :: "v"(voff), "s"(sbase), "s"(lds_dst) : "memory");
}
typedef __amdgpu_buffer_rsrc_t srd_t;
DI srd_t make_srd(const void* p, unsigned bytes) { return __builtin_amdgcn_make_buffer_rsrc((void*)p, (short)0, (int)bytes, 0x00020000); }
template <int IMM> DI void bload16(bf16x8& dst, srd_t r, unsigned voff, unsigned soff) {
    asm volatile("buffer_load_dwordx4 %0, %1, %2, %3 offen offset:%4" : "=v"(dst) : "v"(voff), "s"(r), "s"(soff), "n"(IMM)); }
template <int IMM> DI void bload4(float& dst, srd_t r, unsigned voff, unsigned soff) {
    asm volatile("buffer_load_dword %0, %1, %2, %3 offen offset:%4" : "=v"(dst) : "v"(voff), "s"(r), "s"(soff), "n"(IMM)); }
template <int IMM> DI void bload16_sc1(bf16x8& dst, srd_t r, unsigned voff, unsigned soff) {
    asm volatile("buffer_load_dwordx4 %0, %1, %2, %3 offen offset:%4 sc1" : "=v"(dst) : "v"(voff), "s"(r), "s"(soff), "n"(IMM)); }
template <int IMM> DI void bload8(u32x2& dst, srd_t r, unsigned voff, unsigned soff) {
    asm volatile("buffer_load_dwordx2 %0, %1, %2, %3 offen offset:%4" : "=v"(dst) : "v"(voff), "s"(r), "s"(soff), "n"(IMM)); }
template <int IMM> DI void bload16u_sc1(u32x4& dst, srd_t r, unsigned voff, unsigned soff) {
    asm volatile("buffer_load_dwordx4 %0, %1, %2, %3 offen offset:%4 sc1" : "=v"(dst) : "v"(voff), "s"(r), "s"(soff), "n"(IMM)); }
template <int IMM> DI void bload8_sc1(u32x2& dst, srd_t r, unsigned voff, unsigned soff) {
    asm volatile("buffer_load_dwordx2 %0, %1, %2, %3 offen offset:%4 sc1" : "=v"(dst) : "v"(voff), "s"(r), "s"(soff), "n"(IMM)); }
DI void touch1(float& sink, srd_t r, unsigned voff) { asm volatile("buffer_load_dword %0, %1, %2, 0 offen" : "+v"(sink) : "v"(voff), "s"(r)); }
#define CODE_LABEL(name) asm volatile(#name ":")
#define CODE_ADDR(dst, name) do { unsigned long long pc_; int d_; asm volatile("s_getpc_b64 %0\n.Lhere%=:\n\ts_mov_b32 %1, " #name "-.Lhere%=" : "=s"(pc_), "=s"(d_)); dst = (const char*)pc_ + d_; } while (0)
DI void touch_code(float& sink, const char* from, const char* to, int i, int lane) {
    const unsigned bytes = (unsigned)(to - from), off = (unsigned)(i * 64 + lane) * 128u;
    if ((unsigned)i * 8192u < bytes) touch1(sink, make_srd(from, bytes), off < bytes - 4 ? off : bytes - 4);
}
DI void bdma4(srd_t r, unsigned voff, unsigned soff, unsigned lds_dst) {
    asm volatile("s_mov_b32 m0, %3\n\ts_nop 0\n\tbuffer_load_dwordx4 %0, %1, %2 offen lds\n\tbuffer_load_dwordx4 %0, %1, %2 offen offset:1024 lds\n\t"
                 "buffer_load_dwordx4 %0, %1, %2 offen offset:2048 lds\n\tbuffer_load_dwordx4 %0, %1, %2 offen offset:3072 lds" :: "v"(voff), "s"(r), "s"(soff), "s"(lds_dst) : "memory"); }
DI void bdma1(srd_t r, unsigned voff, unsigned soff, unsigned lds_dst) {
    asm volatile("s_mov_b32 m0, %3\n\ts_nop 0\n\tbuffer_load_dwordx4 %0, %1, %2 offen lds" :: "v"(voff), "s"(r), "s"(soff), "s"(lds_dst) : "memory"); }
template <int IMM> DI void bdma_next(srd_t r, unsigned voff, unsigned soff, unsigned lds_dst) {
    asm volatile("s_mov_b32 m0, %3\n\ts_nop 0\n\tbuffer_load_dwordx4 %0, %1, %2 offen offset:%4 lds" :: "v"(voff), "s"(r), "s"(soff), "s"(lds_dst), "n"(IMM) : "memory"); }
template <int POL> DI void bstore16(u32x4 d, srd_t r, unsigned voff, unsigned soff) {
    if constexpr (POL == 1) asm volatile("buffer_store_dwordx4 %0, %1, %2, %3 offen sc1\n\ts_nop 1" :: "v"(d), "v"(voff), "s"(r), "s"(soff) : "memory");
    else if constexpr (POL == 2) asm volatile("buffer_store_dwordx4 %0, %1, %2, %3 offen nt\n\ts_nop 1" :: "v"(d), "v"(voff), "s"(r), "s"(soff) : "memory");
    else asm volatile("buffer_store_dwordx4 %0, %1, %2, %3 offen\n\ts_nop 1" :: "v"(d), "v"(voff), "s"(r), "s"(soff) : "memory"); }
template <int POL> DI void bstore8(u32x2 d, srd_t r, unsigned voff, unsigned soff) {
    if constexpr (POL == 1) asm volatile("buffer_store_dwordx2 %0, %1, %2, %3 offen sc1\n\ts_nop 1" :: "v"(d), "v"(voff), "s"(r), "s"(soff) : "memory");
    else if constexpr (POL == 2) asm volatile("buffer_store_dwordx2 %0, %1, %2, %3 offen nt\n\ts_nop 1" :: "v"(d), "v"(voff), "s"(r), "s"(soff) : "memory");
    else asm volatile("buffer_store_dwordx2 %0, %1, %2, %3 offen\n\ts_nop 1" :: "v"(d), "v"(voff), "s"(r), "s"(soff) : "memory"); }
#define PINV(x) asm volatile("" : "+v"(x))
#define WAITV(n) asm volatile("s_waitcnt vmcnt(%0)" :: "n"(n) : "memory")
#define WAIT_BAR(n) asm volatile("s_waitcnt vmcnt(%0) lgkmcnt(0)\n\ts_barrier" :: "n"(n) : "memory")
#define SBAR() __builtin_amdgcn_sched_barrier(0)
#define LBAR() asm volatile("s_waitcnt lgkmcnt(0)\n\ts_barrier" ::: "memory")
DI bf16x8 lds_frag(LAS const char* p) { return *(LAS const bf16x8*)p; }
typedef short v4i16_t __attribute__((ext_vector_type(4)));
DI u32x2 lds_tr(LAS const char* p) { return __builtin_bit_cast(u32x2, __builtin_amdgcn_ds_read_tr16_b64_v4i16((LAS v4i16_t*)p)); }
DI bf16x8 tr_frag(LAS const char* p, int off2) { u32x2 a = lds_tr(p), b = lds_tr(p + off2); u32x4 r = {a[0], a[1], b[0], b[1]}; return __builtin_bit_cast(bf16x8, r); }

DI int wo_row(int k) { return k < 512 ? 18 * (k >> 4) + (k & 15) : 18 * ((k - 512) >> 1) + 16 + (k & 1); }
DI void retile_wo(int job, int lane, const char* wo, char* ws, char* scratch_generic) {
    const int s = job / 9, cb = job - 9 * s, r8 = lane >> 3, ch = lane & 7;
#pragma unroll
    for (int p = 0; p < 4; ++p) { const int rho = 8 * p + r8, k = 32 * s + 8 * ((rho >> 2) & 3) + 4 * (rho >> 4) + (rho & 3);
        glds16(wo + cb * 128, wo_row(k) * (D_ * 2) + ch * 16, (unsigned)(uintptr_t)scratch_generic + p * 1024); }
    WAITV(0);
    LAS const char* base = (LAS const char*)scratch_generic + (4 * (lane >> 4) + ((lane >> 2) & 3)) * 128 + (lane & 3) * 8;
#pragma unroll
    for (int u = 0; u < 4; ++u) { const int T = 4 * cb + u, n6 = T / 6, tt = T - 6 * n6;
        *(bf16x8*)(ws + WS_WO + (size_t)(n6 * 108 + s * 6 + tt) * 1024 + lane * 16) = tr_frag(base + 32 * u, 16 * 128); }
}

constexpr int K1_SLOT = 34 * 1024, K1_STG = 0, K1_SMALL = 4 * K1_SLOT, K1_LDS = K1_SMALL + 4096;
template <int PART, int NJ>
DI void k1_wave(const char* x, const char* wqkv, char* ws, char* lds_generic, int mt, int nt) {
    constexpr bool VPART = PART == 2; constexpr int NT = NJ ? 4 : 5, T0 = NJ ? 5 : 0;
    LAS char* lds = (LAS char*)lds_generic; const unsigned ldsb = (unsigned)(uintptr_t)lds_generic;
    const int tid = threadIdx.x, lane = tid & 63, wid = rfl(tid >> 6), mi = wid & 3, m = lane & 15, g = lane >> 4;
    float csink = 0.f;
    if (mi < 2) { const char* c1; CODE_ADDR(c1, preln_ffn_end); touch_code(csink, (const char*)__builtin_amdgcn_s_getpc(), c1, mi, lane); }
    const srd_t sx = make_srd(x, S_ * D_ * 2), sw = make_srd(wqkv, D_ * 3 * D_ * 2);
    const unsigned xv = (lane >> 3) * (D_ * 2) + (((lane & 7) ^ ((4 * wid + (lane >> 4)) & 7)) << 4);
    unsigned wv[2];
#pragma unroll
    for (int i = 0; i < 2; ++i) { const int idx = (i ? 8 : wid) * 64 + lane, rho = (idx * 3641) >> 16, ch = idx - 18 * rho;
        wv[i] = (8 * ((rho >> 2) & 3) + 4 * (rho >> 4) + (rho & 3)) * 3456 + ch * 16; }
    const unsigned xs = (128 * mt + 8 * wid) * (D_ * 2), wsb = nt * 288;
    auto issue = [&](int st, int p) {
        const int k = st; const unsigned dst = ldsb + (st & 3) * K1_SLOT;
        if (p < 2) bdma1(sx, xv, xs + p * 64 * (D_ * 2) + k * 128, dst + (wid + 8 * p) * 1024);
        else if (p < 4) bdma1(sw, wv[0], wsb + (64 * k + 32 * (p - 2)) * 3456, dst + 16384 + ((p - 2) * 9 + wid) * 1024);
        else if (wid < 2) bdma1(sw, wv[1], wsb + (64 * k + 32 * wid) * 3456, dst + 16384 + (wid * 9 + 8) * 1024); };
#pragma unroll
    for (int st = 0; st < 2; ++st)
#pragma unroll
        for (int p = 0; p < 5; ++p) issue(st, p);
    unsigned sa[NT][2], sd[NT][2];
#pragma unroll
    for (int t = 0; t < NT; ++t) { const int tt = T0 + t;
        if (VPART) { const int n = 16 * tt + m, hl = (n * 3641) >> 16, d = n - 18 * hl, row = d + (d >= 4 ? 5 : 0) - (d >= 12 ? 12 : 0) + (d >= 16 ? 8 : 0);
            sa[t][0] = K1_STG + hl * 5120 + 2 * mi * 640 + (g >> 1) * 320 + row * 16 + (g & 1) * 8; PINV(sa[t][0]); }
        else {
#pragma unroll
            for (int p = 0; p < 2; ++p) { const int n = 16 * tt + 4 * g + 2 * p, hl = (n * 3641) >> 16, d = n - 18 * hl; const bool tail = d == 16;
                const int o = PART == 0 ? (tail ? 32768 + hl * 512 : hl * 4096 + d * 2) : hl * 4608 + (tail ? 4096 : (d >> 3) * 2048 + (d & 7) * 2);
                const int ts = tail ? 4 : PART == 0 ? 32 : 16;
                sa[t][p] = K1_STG + o + (32 * mi + m) * ts; sd[t][p] = 16 * ts; PINV(sa[t][p]); PINV(sd[t][p]); } } }
    f32x4 acc[NT][2];
    f32x4 accs = {0, 0, 0, 0}, accg = {0, 0, 0, 0}, accc[NT];
#pragma unroll
    for (int t = 0; t < NT; ++t) accc[t] = (f32x4){0, 0, 0, 0};
    bf16x8 ones = __builtin_bit_cast(bf16x8, (u32x4){0x3f803f80u, 0x3f803f80u, 0x3f803f80u, 0x3f803f80u}); PINV(ones);
    asm volatile("s_nop 1" : "+v"(accs), "+v"(accg));
#pragma unroll
    for (int t = 0; t < NT; ++t) PINV(accc[t]);
    const unsigned xa0 = (32 * mi + m) * 128 + ((g ^ ((m >> 1) & 7)) << 4), xa1 = (32 * mi + m) * 128 + (((4 + g) ^ ((m >> 1) & 7)) << 4);
    const unsigned wa = 16384 + (4 * g + ((lane >> 2) & 3)) * 288 + (lane & 3) * 8 + T0 * 32;
    bf16x8 xq[2][2][2], wr[2][2][NT];
    auto rd = [&](int st, int i) {
        LAS const char* sb = lds + (st & 3) * K1_SLOT;
        if (i < 4) xq[st & 1][i >> 1][i & 1] = lds_frag(sb + ((i >> 1) ? xa1 : xa0) + (i & 1) * 2048);
        else { const int s2 = (i - 4) / NT, t = (i - 4) % NT; wr[st & 1][s2][t] = tr_frag(sb + wa + s2 * 9216 + 32 * t, 16 * 288); } };
    WAIT_BAR(4);
    PINV(csink);

#pragma unroll
    for (int i = 0; i < 4 + 2 * NT; ++i) rd(0, i);
#pragma unroll
    for (int p = 0; p < 5; ++p) issue(2, p);
#pragma unroll
    for (int st = 0; st < 9; ++st) {
        if (st < 7) WAIT_BAR(4); else if (st == 7) WAIT_BAR(0);
#pragma unroll
        for (int i = 0; i < 2 * NT; ++i) { const int s2 = i / NT, t = i % NT; const bf16x8 w = wr[st & 1][s2][t], x0 = xq[st & 1][s2][0], x1 = xq[st & 1][s2][1];
            if (st == 0 && s2 == 0) { if (VPART) { mfma16_new(acc[t][0], x0, w); mfma16_new(acc[t][1], x1, w); } else { mfma16_new(acc[t][0], w, x0); mfma16_new(acc[t][1], w, x1); } }
            else { if (VPART) { mfma16_acc(acc[t][0], x0, w); mfma16_acc(acc[t][1], x1, w); } else { mfma16_acc(acc[t][0], w, x0); mfma16_acc(acc[t][1], w, x1); } }
            if (st + 1 < 9) { if (i < 4) { rd(st + 1, i); rd(st + 1, 4 + i); } else if (4 + i < 4 + 2 * NT) rd(st + 1, 4 + i); }
            if (st + 3 < 9 && (i & 1) == 0 && i / 2 < 4) issue(st + 3, i / 2 == 0 ? 0 : i / 2 == 1 ? 2 : i / 2 == 2 ? 1 : 3);
            if (st + 3 < 9 && i == 7) issue(st + 3, 4);
            if (t == 1) mfma16_acc(accs, ones, NJ ? x1 : x0);
            if (t == 2) mfma16_acc(accg, NJ ? x1 : x0, NJ ? x1 : x0);
            if (t == NT - 1 && ((2 * st + s2) & 3) == mi) {
#pragma unroll
                for (int tt = 0; tt < NT; ++tt) { if (VPART) mfma16_acc(accc[tt], ones, wr[st & 1][s2][tt]); else mfma16_acc(accc[tt], wr[st & 1][s2][tt], ones); } }
            SBAR(); }
    }
    LAS float* sm = (LAS float*)(lds + K1_SMALL);
    asm volatile("s_nop 7\n\ts_nop 7" : "+v"(acc[NT - 1][0]), "+v"(acc[NT - 1][1]), "+v"(accs), "+v"(accg));
#pragma unroll
    for (int t = 0; t < NT; ++t) { PINV(accc[t]); PINV(acc[t][0]); PINV(acc[t][1]); }
    if (g == 0) sm[32 * mi + 16 * NJ + m] = accs[0];
    if (g == (m >> 2)) sm[128 + 32 * mi + 16 * NJ + m] = (m & 2) ? ((m & 1) ? accg[3] : accg[2]) : ((m & 1) ? accg[1] : accg[0]);
#pragma unroll
    for (int t = 0; t < NT; ++t) { if (VPART) { if (g == 0) sm[256 + mi * 144 + 16 * (T0 + t) + m] = accc[t][0]; }
        else if (m == 0) *(LAS f32x4*)(sm + 256 + mi * 144 + 16 * (T0 + t) + 4 * g) = accc[t]; }
    LBAR();
    LAS char* stg = lds + K1_STG;
    if (VPART && tid < 256) { const int hs = tid >> 1;
        *(LAS u32x4*)(stg + hs * 320 + ((tid & 1) ? 19 : 4) * 16) = (tid & 1) ? (u32x4){0x3f803f80u, 0x3f803f80u, 0x3f803f80u, 0x3f803f80u} : (u32x4){0, 0, 0, 0}; }
    f32x4 mean[2], rstd[2];
#pragma unroll
    for (int u = 0; u < 2; ++u) { const int tl = 32 * mi + 16 * u + (VPART ? 4 * g : m);
        if (VPART) { mean[u] = *(LAS const f32x4*)(sm + tl) * (1.f / D_);
            const f32x4 var = *(LAS const f32x4*)(sm + 128 + tl) * (1.f / D_) - mean[u] * mean[u];
#pragma unroll
            for (int r = 0; r < 4; ++r) rstd[u][r] = __builtin_amdgcn_rsqf(fmaxf(var[r], 0.f) + EPS); }
        else { mean[u][0] = sm[tl] * (1.f / D_);
            rstd[u][0] = __builtin_amdgcn_rsqf(fmaxf(sm[128 + tl] * (1.f / D_) - mean[u][0] * mean[u][0], 0.f) + EPS) * (PART == 0 ? QSCALE : 1.f); } }
    float c1[NT]; f32x4 c4[NT];
#pragma unroll
    for (int t = 0; t < NT; ++t) { const int tt = T0 + t;
        if (VPART) { const int n = 16 * tt + m; c1[t] = (sm[256 + n] + sm[400 + n]) + (sm[544 + n] + sm[688 + n]); }
        else { const int n0 = 16 * tt + 4 * g;
            c4[t] = (*(LAS const f32x4*)(sm + 256 + n0) + *(LAS const f32x4*)(sm + 400 + n0)) + (*(LAS const f32x4*)(sm + 544 + n0) + *(LAS const f32x4*)(sm + 688 + n0)); } }
#pragma unroll
    for (int t = 0; t < NT; ++t)
#pragma unroll
        for (int u = 0; u < 2; ++u) {
            if (VPART) { const f32x4 a = (acc[t][u] - mean[u] * c1[t]) * rstd[u];
                *(LAS u32x2*)(lds + sa[t][0] + u * 640) = (u32x2){cvtpk(a[0], a[1]), cvtpk(a[2], a[3])}; }
            else { const f32x4 a = (acc[t][u] - c4[t] * mean[u][0]) * rstd[u][0];
                *(LAS unsigned*)(lds + sa[t][0] + (u ? sd[t][0] : 0u)) = cvtpk(a[0], a[1]); *(LAS unsigned*)(lds + sa[t][1] + (u ? sd[t][1] : 0u)) = cvtpk(a[2], a[3]); } }
    if (nt == 0 && tid < 128) ((float*)(ws + WS_MUX))[128 * mt + tid] = sm[tid] * (1.f / D_);
    LBAR();
    const int head = 8 * (nt & 3) + wid; const srd_t sws = make_srd(ws, (unsigned)WS_END); const unsigned l16 = lane * 16;
    if (PART == 2) { const unsigned dst = (unsigned)WS_KV + head * KVIMG + (mt >> 1) * KVCHUNK + 9216 + (mt & 1) * 5120;
#pragma unroll
        for (int i = 0; i < 5; ++i) bstore16<1>(*(LAS const u32x4*)(stg + wid * 5120 + i * 1024 + l16), sws, l16, dst + i * 1024);
    } else if (PART == 0) { const unsigned dst = (unsigned)WS_QM + (head * S_ + 128 * mt) * 32;
#pragma unroll
        for (int i = 0; i < 4; ++i) bstore16<1>(*(LAS const u32x4*)(stg + wid * 4096 + i * 1024 + l16), sws, l16, dst + i * 1024);
        if (lane < 32) bstore16<1>(*(LAS const u32x4*)(stg + 32768 + wid * 512 + l16), sws, l16, (unsigned)WS_QT + (head * S_ + 128 * mt) * 4);
    } else { const unsigned dst = (unsigned)WS_KV + head * KVIMG + (mt >> 1) * KVCHUNK; LAS const char* hb = stg + wid * 4608;
#pragma unroll
        for (int i = 0; i < 4; ++i) bstore16<1>(*(LAS const u32x4*)(hb + i * 1024 + l16), sws, l16, dst + (i >> 1) * 4096 + (mt & 1) * 2048 + (i & 1) * 1024);
        if (lane < 32) bstore16<1>(*(LAS const u32x4*)(hb + 4096 + l16), sws, l16, dst + 8192 + (mt & 1) * 512);
        float mx = 0.f;
#pragma unroll
        for (int i = 0; i < 2; ++i) { const int tl = lane + 64 * i;
            const u32x4 a = *(LAS const u32x4*)(hb + tl * 16), bq = *(LAS const u32x4*)(hb + 2048 + tl * 16); const unsigned c = *(LAS const unsigned*)(hb + 4096 + tl * 4);
            float s2 = dot2(c, c, 0.f);
#pragma unroll
            for (int e = 0; e < 4; ++e) { s2 = dot2(a[e], a[e], s2); s2 = dot2(bq[e], bq[e], s2); }
            mx = fmaxf(mx, s2); }
        mx = fmaxf(mx, xor16(mx)); mx = fmaxf(mx, xor32(mx));
#pragma unroll
        for (int o = 1; o < 16; o <<= 1) mx = fmaxf(mx, __shfl_xor(mx, o));
        if (lane == 0) ((float*)(ws + WS_KMAX))[head * 16 + mt] = mx;
    }
}
template <int PART> DI void k1_body(const char* x, const char* wqkv, char* ws, char* lds, int mt, int nt) {
    if (rfl(threadIdx.x >> 6) >= 4) k1_wave<PART, 1>(x, wqkv, ws, lds, mt, nt); else k1_wave<PART, 0>(x, wqkv, ws, lds, mt, nt);
}
__global__ __launch_bounds__(512, 2) void k1_qkv(const char* x, const char* wqkv, const char* wo, char* ws) {
    extern __shared__ __attribute__((aligned(16))) char lds[];
    const int b = blockIdx.x;
    if (b >= 192) {
        const int lane = threadIdx.x & 63, wv = rfl(threadIdx.x >> 6), gw = (b - 192) * 8 + wv;
        float sink = 0.f;
        if (b - 192 < 8) { const char *c0, *c1; CODE_ADDR(c0, preln_k2_begin); CODE_ADDR(c1, preln_ffn_end); touch_code(sink, c0, c1, wv + 8 * (b - 192), lane); }
        if (threadIdx.x == 511) ((unsigned*)(ws + WS_CNT))[(b - 192) * 64] = 0u;
        if (threadIdx.x == 510 && b - 192 < 40) ((unsigned*)(ws + WS_CNT))[(64 + b - 192) * 64] = 0u;
        if (gw < 162) retile_wo(gw, lane, wo, ws, lds + wv * 4096);
        for (int c = gw; c < D_; c += 512) {
            const char* row = wo + (size_t)wo_row(c) * (D_ * 2);
            const u32x4 a = *(const u32x4*)(row + lane * 16); float s = 0.f;
#pragma unroll
            for (int e = 0; e < 4; ++e) s = dot2(a[e], 0x3f803f80u, s);
            if (lane < 8) { const u32x4 bq = *(const u32x4*)(row + 1024 + lane * 16);
#pragma unroll
                for (int e = 0; e < 4; ++e) s = dot2(bq[e], 0x3f803f80u, s); }
#pragma unroll
            for (int o = 1; o < 64; o <<= 1) s += __shfl_xor(s, o);
            if (lane == 0) { const float w = s * (1.f / D_); const unsigned hi = cvtpk(w, 0.f) & 0xffffu; const unsigned lo = cvtpk(w - bflo(hi), 0.f) & 0xffffu;
                ((unsigned short*)(ws + WS_WBAR))[c] = (unsigned short)hi; ((unsigned short*)(ws + WS_WBAR))[D_ + c] = (unsigned short)lo; }
        }
        asm volatile("s_waitcnt vmcnt(0)" : "+v"(sink));
        return;
    }
    const int xcd = b & 7, idx = b >> 3, mt = (xcd & 3) * 4 + (idx & 3), nt = (xcd >> 2) * 6 + (idx >> 2);
    if (nt >= 8) k1_body<2>(x, wqkv, ws, lds, mt, nt); else if (nt >= 4) k1_body<1>(x, wqkv, ws, lds, mt, nt); else k1_body<0>(x, wqkv, ws, lds, mt, nt);
}

DI unsigned* counter_ptr(char* ws, int counter) { return (unsigned*)(ws + WS_CNT) + counter * 64; }
DI void arrive(char* ws, int counter) { if (threadIdx.x == 0) __hip_atomic_fetch_add(counter_ptr(ws, counter), 1u, __ATOMIC_RELAXED, __HIP_MEMORY_SCOPE_AGENT); }
template <bool ADD, int POLLER = 0, bool DRAIN = false> DI bool await(char* ws, int counter, unsigned want, LAS unsigned* flag) {
    if (threadIdx.x == POLLER) { unsigned* cnt = counter_ptr(ws, counter); unsigned bad = 0;
        unsigned seen = ADD ? __hip_atomic_fetch_add(cnt, 1u, __ATOMIC_RELAXED, __HIP_MEMORY_SCOPE_AGENT) + 1u : __hip_atomic_load(cnt, __ATOMIC_RELAXED, __HIP_MEMORY_SCOPE_AGENT);
        const unsigned long long t0 = __builtin_amdgcn_s_memrealtime();
        while (seen < want) {
            if (__builtin_amdgcn_s_memrealtime() - t0 > 2000000ull) { bad = 1; break; }
            seen = __hip_atomic_load(cnt, __ATOMIC_RELAXED, __HIP_MEMORY_SCOPE_AGENT); }
        *flag = bad; }
    if (DRAIN) WAIT_BAR(0); else LBAR();
    return *flag != 0;
}
constexpr int K2_LDS = 160 * 1024;
struct RtJob { unsigned so[4], dst[4]; };
struct RtTable { RtJob j[8][486];
    constexpr RtTable() : j{} {
        for (int xg = 0; xg < 8; ++xg) for (int slot = 0; slot < 486; ++slot) { RtJob& e = j[xg][slot];
            if (slot < 324) { const int gu = slot >= 162, r = slot - 162 * gu, ks = r / 9, cb = 9 * xg + r % 9;
                for (int p = 0; p < 4; ++p) { const int T = 4 * cb + p, jj = T / 9, tt = T % 9;
                    e.so[p] = (ks * 32 + p * 8) * (F_ * 2) + cb * 128;
                    e.dst[p] = (unsigned)WS_W1 + (jj * 324 + (tt < 4 ? ks * 8 + tt * 2 : tt < 8 ? (tt >> 1) * 72 + ks * 4 + (tt & 1) * 2 : 288 + ks * 2) + gu) * 1024; }
            } else { const int r2 = slot - 324, q = r2 / 9, cb = r2 % 9, ks = 18 * xg + q, a = ks / 9, qi = ks % 9;
                const int t0 = qi == 8 ? 18 * a + 8 : 18 * a + 9 * (qi >> 2) + 2 * (qi & 3), t1 = qi == 8 ? t0 + 9 : t0 + 1;
                for (int p = 0; p < 4; ++p) { const int T = 4 * cb + p, n4 = T / 9, tt = T % 9;
                    e.so[p] = (16 * (p >> 1 ? t1 : t0) + 8 * (p & 1)) * (D_ * 2) + cb * 128;
                    e.dst[p] = (unsigned)WS_W2 + (((xg >> 1) * 4 + n4) * 324 + (((q >> 1) * 2 + (xg & 1)) * 2 + (q & 1)) * 9 + tt) * 1024; } } }
    } };
__device__ const RtTable rt_table{};
#define EXPS(S, a, b) _Pragma("unroll") for (int r_ = a; r_ < b; ++r_) S[r_] = __builtin_amdgcn_exp2f(S[r_]); SBAR()
#define PLO(S) __builtin_bit_cast(bf16x16, (f32x8)__builtin_shufflevector(S, S, 0, 1, 2, 3, 4, 5, 6, 7))
#define PHI(S) __builtin_bit_cast(bf16x16, (f32x8)__builtin_shufflevector(S, S, 8, 9, 10, 11, 12, 13, 14, 15))
#define KTAIL(p) __builtin_bit_cast(s16x4, (u32x2){*(LAS const unsigned*)(p), kc})
#define QK1(q) __builtin_amdgcn_mfma_f32_32x32x16_bf16(kf, q, (f32x16){}, 0, 0, 0)
#define QK2(q, s) __builtin_amdgcn_mfma_f32_32x32x8bf16_1k(kt, q, s, 0, 0, 0)
#define PV(v, p, o) __builtin_amdgcn_smfmac_f32_32x32x32_bf16(v, p, o, 0xDDDD, 0, 0)
template <class Hook>
DI void attn_fast(LAS const char* kp, LAS const char* tp, LAS const char* vp, const bf16x8 (&qm)[2], const s16x4 (&qt)[2], f32x16 (&o)[2], unsigned kc, Hook hook) {
    bf16x8 kf = *(LAS const bf16x8*)kp; s16x4 kt = KTAIL(tp); bf16x8 v0, v1;
    f32x16 s0 = QK1(qm[0]), s1; s0 = QK2(qt[0], s0);
    SBAR();
#pragma unroll
    for (int it = 0; it < 8; ++it) {
        { if (it == 0) __builtin_amdgcn_s_setprio(3); if (it == 2) __builtin_amdgcn_s_setprio(2); if (it == 4) __builtin_amdgcn_s_setprio(1); if (it == 6) __builtin_amdgcn_s_setprio(0); }
        EXPS(s0, 0, 2);
        if (it > 0) { o[1] = PV(v0, PLO(s1), o[1]); SBAR(); }
        EXPS(s0, 2, 6);
        if (it > 0) { o[1] = PV(v1, PHI(s1), o[1]); SBAR(); }
        v0 = *(LAS const bf16x8*)(vp + it * 1280); v1 = *(LAS const bf16x8*)(vp + it * 1280 + 640); SBAR();
        hook(it);
        EXPS(s0, 6, 9);
        s1 = QK1(qm[1]); SBAR();
        EXPS(s0, 9, 12);
        s1 = QK2(qt[1], s1); SBAR();
        if (it + 1 < 8) { kf = *(LAS const bf16x8*)(kp + (it + 1) * 512); kt = KTAIL(tp + (it + 1) * 128); SBAR(); }
        EXPS(s0, 12, 16);
        EXPS(s1, 0, 2);
        o[0] = PV(v0, PLO(s0), o[0]); SBAR();
        EXPS(s1, 2, 6);
        o[0] = PV(v1, PHI(s0), o[0]); SBAR();
        EXPS(s1, 6, 9);
        if (it + 1 < 8) { s0 = QK1(qm[0]); SBAR(); }
        EXPS(s1, 9, 12);
        if (it + 1 < 8) { s0 = QK2(qt[0], s0); SBAR(); }
        EXPS(s1, 12, 16);
    }
    o[1] = PV(v0, PLO(s1), o[1]); o[1] = PV(v1, PHI(s1), o[1]);
}
DI void attn_slow(LAS const char* kp, LAS const char* tp, LAS const char* vp, const bf16x8 (&qm)[2], const s16x4 (&qt)[2], f32x16 (&o)[2], float (&mrun)[2], unsigned kc) {
#pragma unroll 1
    for (int it = 0; it < 8; ++it) {
        const bf16x8 kf = *(LAS const bf16x8*)(kp + it * 512); const s16x4 kt = KTAIL(tp + it * 128);
        const bf16x8 v0 = *(LAS const bf16x8*)(vp + it * 1280), v1 = *(LAS const bf16x8*)(vp + it * 1280 + 640);
#pragma unroll
        for (int u = 0; u < 2; ++u) {
            f32x16 s = QK1(qm[u]); s = QK2(qt[u], s);
            float mx = s[0];
#pragma unroll
            for (int r = 1; r < 16; ++r) mx = fmaxf(mx, s[r]);
            mx = fmaxf(mx, xor32(mx));
            const float mn = fmaxf(mrun[u], mx), al = __builtin_amdgcn_exp2f(mrun[u] - mn);
            mrun[u] = mn;
#pragma unroll
            for (int r = 0; r < 16; ++r) { s[r] = __builtin_amdgcn_exp2f(s[r] - mn); o[u][r] *= al; }
            o[u] = PV(v0, PLO(s), o[u]); o[u] = PV(v1, PHI(s), o[u]);
        }
    }
}
constexpr int K3_X = 108 * 1024, K3_WB = K3_X + 12 * 1024, K3_Z = K3_WB + 2304, K3_FLAG = K3_WB + 4096;
DI void k3_request(const char* x, char* ws, char* lds_generic, int mt32, int n6, float& mux) {
    LAS char* lds = (LAS char*)lds_generic; const unsigned ldsb = (unsigned)(uintptr_t)lds_generic;
    const int tid = threadIdx.x, lane = tid & 63, wid = rfl(tid >> 6);
    const srd_t srd = make_srd(ws, (unsigned)WS_END), sx = make_srd(x, S_ * D_ * 2);
    const unsigned l16 = lane * 16, wo0 = (unsigned)WS_WO + n6 * (108 * 1024), f0 = (wid < 13 ? 8 * wid : 100) * 1024;
    if (wid < 14) { bdma4(srd, l16, wo0 + f0, ldsb + f0); bdma4(srd, l16, wo0 + f0 + 4096, ldsb + f0 + 4096); }
    if (wid < 12) { const int q = wid * 64 + lane, row = (q * 5462) >> 16;
        bdma1(sx, row * (D_ * 2) + (q - 12 * row) * 16, (64 * mt32) * (D_ * 2) + n6 * 192, ldsb + K3_X + wid * 1024); }
    if (wid == 12 || wid == 13) bdma1(srd, l16, (unsigned)WS_WBAR + (wid - 12) * 1024, ldsb + K3_WB + (wid - 12) * 1024);
    if (wid == 14 && lane < 16) bdma1(srd, l16, (unsigned)WS_WBAR + 2048, ldsb + K3_WB + 2048);
    mux = 0.f;
    if (wid < 8) bload4<0>(mux, srd, (lane & 15) * 4, (unsigned)WS_MUX + (4 * mt32 + (wid & 3)) * 64);
    if (tid < 37) *(LAS u32x4*)(lds + K3_Z + tid * 16) = (u32x4){0, 0, 0, 0};
}
DI void k3_compute(char* ws, char* lds_generic, int mt32, int n6, float mux, bool bad) {
    LAS char* lds = (LAS char*)lds_generic;
    int tid = threadIdx.x; asm volatile("" : "+v"(tid));
    const int lane = tid & 63, wid = rfl(tid >> 6), mi = wid & 3, kj = (wid >> 2) & 1, m = lane & 15, g = lane >> 4, mt16 = 4 * mt32 + mi;
    if (wid >= 8) { LBAR(); LBAR(); return; }
    const srd_t srd = make_srd(ws, (unsigned)WS_END);
    const unsigned l16 = lane * 16, ob = (unsigned)WS_OF + (mt16 * 18 + 9 * kj) * 1024;
    bf16x8 of[9];
    bload16_sc1<0>(of[0], srd, l16, ob); bload16_sc1<1024>(of[1], srd, l16, ob); bload16_sc1<2048>(of[2], srd, l16, ob); bload16_sc1<3072>(of[3], srd, l16, ob);
    bload16_sc1<0>(of[4], srd, l16, ob + 4096); bload16_sc1<1024>(of[5], srd, l16, ob + 4096); bload16_sc1<2048>(of[6], srd, l16, ob + 4096); bload16_sc1<3072>(of[7], srd, l16, ob + 4096);
    bload16_sc1<0>(of[8], srd, l16, ob + 8192);
    f32x4 acc[6], accw;
    LAS const char* ab = lds + kj * (54 * 1024) + lane * 16;
    LAS const char* wbp = lds + (m < 2 ? K3_WB + m * (D_ * 2) + kj * 576 + g * 16 : K3_Z);
    bf16x8 wr[7];
    auto rd = [&](int s) {
#pragma unroll
        for (int t = 0; t < 6; ++t) wr[t] = lds_frag(ab + (s * 6 + t) * 1024);
        wr[6] = lds_frag(wbp + s * 64); SBAR(); };
    rd(0);
    u32x2 xr[4];
#pragma unroll
    for (int t = 0; t < 4; ++t) xr[t] = *(LAS const u32x2*)(lds + K3_X + (16 * mi + m) * 192 + (4 * kj + (kj ? (t & 1) : t)) * 32 + g * 8);
#pragma unroll
    for (int s = 0; s < 9; ++s) {
        if (s == 0) asm volatile("s_waitcnt vmcnt(5)" : "+v"(of[0]), "+v"(of[1]), "+v"(of[2]), "+v"(of[3]));
        if (s == 4) asm volatile("s_waitcnt vmcnt(0)" : "+v"(of[4]), "+v"(of[5]), "+v"(of[6]), "+v"(of[7]), "+v"(of[8]));
#pragma unroll
        for (int t = 0; t < 6; ++t) { if (s == 0) mfma16_new(acc[t], wr[t], of[s]); else mfma16_acc(acc[t], wr[t], of[s]); }
        if (s == 0) mfma16_new(accw, wr[6], of[s]); else mfma16_acc(accw, wr[6], of[s]);
        SBAR();
        if (s + 1 < 9) rd(s + 1); }
    asm volatile("s_nop 7" : "+v"(acc[5]), "+v"(acc[4]), "+v"(accw), "+v"(xr[0]), "+v"(xr[1]), "+v"(xr[2]), "+v"(xr[3]));
    LBAR();
    LAS char* ex = lds + mi * 8192 + lane * 16;
    if (kj == 0) { *(LAS f32x4*)(ex + 4 * 1024) = acc[4]; *(LAS f32x4*)(ex + 5 * 1024) = acc[5]; *(LAS f32x4*)(ex + 6 * 1024) = accw; }
    else {
#pragma unroll
        for (int t = 0; t < 4; ++t) *(LAS f32x4*)(ex + t * 1024) = acc[t];
        *(LAS f32x4*)(ex + 7 * 1024) = accw; }
    LBAR();
    accw += *(LAS const f32x4*)(ex + (kj ? 6 : 7) * 1024);
    const float mu = mux + __shfl(accw[0] + accw[1], m);
    float ssq = 0.f;
    auto fin = [&](int t, u32x2 xv, unsigned (&pk)[2]) {
        const f32x4 a = acc[t] + *(LAS const f32x4*)(ex + t * 1024);
        const unsigned h0 = cvtpk(a[0] + bflo(xv[0]), a[1] + bfhi(xv[0])), h1 = cvtpk(a[2] + bflo(xv[1]), a[3] + bfhi(xv[1]));
        pk[0] = cvtpk(bflo(h0) - mu, bfhi(h0) - mu); pk[1] = cvtpk(bflo(h1) - mu, bfhi(h1) - mu);
        ssq = dot2(pk[0], pk[0], ssq); ssq = dot2(pk[1], pk[1], ssq);
        if (bad) { pk[0] = 0x7fc07fc0u; pk[1] = 0x7fc07fc0u; } };
    auto pair = [&](int t, u32x2 xa, u32x2 xb2) { unsigned p0[2], p1[2]; fin(t, xa, p0); fin(t + 1, xb2, p1);
        bstore16<1>((u32x4){p0[0], p0[1], p1[0], p1[1]}, srd, l16, rfl((unsigned)WS_HCF + (mt16 * 18 + 3 * n6 + (t >> 1)) * 1024)); };
    if (kj == 0) { pair(0, xr[0], xr[1]); pair(2, xr[2], xr[3]); } else pair(4, xr[0], xr[1]);
    ssq += xor16(ssq); ssq += xor32(ssq);
    if (g == 0) { ((float*)(ws + WS_SSQ))[(mt16 * 16 + 2 * n6 + kj) * 16 + m] = ssq;
        if (n6 == 0 && kj == 0) ((float*)(ws + WS_MUH))[16 * mt16 + m] = mu; }
}
__global__ __launch_bounds__(1024, 4) void k2_attn(const char* x, const char* wg, const char* wu, const char* wd, char* ws) {
    extern __shared__ __attribute__((aligned(16))) char lds_generic[];
    LAS char* lds = (LAS char*)lds_generic; const unsigned ldsb = (unsigned)(uintptr_t)lds_generic;
    const int tid = threadIdx.x, lane = tid & 63, wid = rfl(tid >> 6), qg = wid & 3, kq = wid >> 2, r32 = lane & 31, h = lane >> 5;
    CODE_LABEL(preln_k2_begin);
    const int b = blockIdx.x, xcd = b & 7, idx = b >> 3, head = xcd * 4 + (idx & 3), qb = idx >> 2;
    const srd_t srd = make_srd(ws, (unsigned)WS_END);
    const int c0 = (2 * kq + (qb & 1)) * KVCHUNK, c1 = (2 * kq + ((qb & 1) ^ 1)) * KVCHUNK;
    const unsigned kvb = (unsigned)WS_KV + head * KVIMG, l16 = lane * 16;
    const int q0 = 256 * qb + 64 * qg;
    bf16x8 qm[2]; float qtl[2]; float km;
    { const unsigned so = (unsigned)WS_QM + (head * S_ + q0) * 32, vo = r32 * 32 + h * 16; bload16<0>(qm[0], srd, vo, so); bload16<1024>(qm[1], srd, vo, so);
      const unsigned st = (unsigned)WS_QT + (head * S_ + q0) * 4; bload4<0>(qtl[0], srd, r32 * 4, st); bload4<128>(qtl[1], srd, r32 * 4, st);
      bload4<0>(km, srd, (lane & 15) * 4, (unsigned)WS_KMAX + head * 64); }
    bdma4(srd, l16, kvb + c0 + qg * 5120, ldsb + c0 + qg * 5120); if (qg < 3) bdma1(srd, l16, kvb + c0 + qg * 5120 + 4096, ldsb + c0 + qg * 5120 + 4096);
    const int slot = wid * 32 + idx; const bool rt = slot < 486;
    bf16x8 wb[4]; unsigned rdst[4], rso[4];
    { const RtJob& J = rt_table.j[xcd][rt ? slot : 0];
#pragma unroll
      for (int p = 0; p < 4; ++p) { rso[p] = J.so[p]; rdst[p] = J.dst[p]; } }
    const char* rsrc = slot < 162 ? wg : slot < 324 ? wu : wd;
    const unsigned rvo = (lane >> 3) * (slot < 324 ? F_ * 2 : D_ * 2) + (lane & 7) * 16;
    asm volatile("s_waitcnt vmcnt(4)" : "+v"(qm[0]), "+v"(qm[1]), "+v"(qtl[0]), "+v"(qtl[1]), "+v"(km) :: "memory");
    km = fmaxf(km, row_ror<8>(km)); km = fmaxf(km, row_ror<4>(km)); km = fmaxf(km, row_ror<2>(km)); km = fmaxf(km, row_ror<1>(km));
    s16x4 qt[2]; bool unsafe = false;
#pragma unroll
    for (int u = 0; u < 2; ++u) { const u32x4 qv = __builtin_bit_cast(u32x4, qm[u]); const unsigned tl = __float_as_uint(qtl[u]);
        float n2 = h ? 0.f : dot2(tl, tl, 0.f);
#pragma unroll
        for (int e = 0; e < 4; ++e) n2 = dot2(qv[e], qv[e], n2);
        n2 += xor32(n2);
        const float bound = __builtin_amdgcn_sqrtf(n2 * km) * 1.0001f + 1e-6f;
        unsafe |= !(bound <= SAFE_B);
        const unsigned cb = (__float_as_uint(bound) + 0xffffu) >> 16;
        const u32x2 w = h ? (u32x2){0u, 0u} : (u32x2){tl, cb | 0x8000u};
        qt[u] = __builtin_bit_cast(s16x4, w); }
    const bool slow = __any(unsafe);
    if (slow) {
#pragma unroll
        for (int u = 0; u < 2; ++u) { u32x2 w = __builtin_bit_cast(u32x2, qt[u]); w[1] = 0u; qt[u] = __builtin_bit_cast(s16x4, w); } }
    f32x16 o[2] = {(f32x16){}, (f32x16){}}; float mrun[2] = {-INFINITY, -INFINITY};
    const int vrow = r32 >= 19 ? 4 : r32 + (r32 >= 4 ? 5 : 0) - (r32 >= 12 ? 12 : 0) + (r32 >= 16 ? 8 : 0);
    const unsigned kc = h ? 0u : 0x00003f80u;
    unsigned kp = ldsb + c0 + h * 4096 + r32 * 16, tp = ldsb + c0 + 8192 + r32 * 4, vp = ldsb + c0 + 9216 + h * 320 + vrow * 16;
#define KP(o) ((LAS const char*)(uintptr_t)kp + (o))
#define TP(o) ((LAS const char*)(uintptr_t)tp + (o))
#define VP(o) ((LAS const char*)(uintptr_t)vp + (o))

    WAIT_BAR(0);
    const srd_t sw = make_srd(rsrc, D_ * F_ * 2);
    auto chunk1 = [&](int part) { if (part == 0) bdma4(srd, l16, kvb + c1 + qg * 5120, ldsb + c1 + qg * 5120);
                                  else if (qg < 3) bdma1(srd, l16, kvb + c1 + qg * 5120 + 4096, ldsb + c1 + qg * 5120 + 4096); };
    if (slow) { chunk1(0); chunk1(1);
        if (rt) {
#pragma unroll
            for (int p = 0; p < 4; ++p) bload16<0>(wb[p], sw, rvo, rso[p]); }
        attn_slow(KP(0), TP(0), VP(0), qm, qt, o, mrun, kc); }
    else attn_fast(KP(0), TP(0), VP(0), qm, qt, o, kc, [&](int it) {
        if (it < 2) { chunk1(it); SBAR(); }
        if (it >= 2 && it < 6 && rt) { bload16<0>(wb[it - 2], sw, rvo, rso[it - 2]); SBAR(); } });
    if (rt) WAIT_BAR(4); else WAIT_BAR(0);
    if (rt) { asm volatile("s_waitcnt vmcnt(0)" : "+v"(wb[0]), "+v"(wb[1]), "+v"(wb[2]), "+v"(wb[3]) :: "memory");
        int ty = threadIdx.x; asm volatile("" : "+v"(ty)); LAS char* scr = lds + c0 + qg * 4864 + (ty & 63) * 16;
#pragma unroll
        for (int p = 0; p < 4; ++p) *(LAS bf16x8*)(scr + p * 1024) = wb[p]; }
    auto rt_step = [&](int it) {
        if (it < 1 || it > 4 || !rt) return;
        int ty = threadIdx.x; asm volatile("" : "+v"(ty)); const int ln = ty & 63;
        LAS const char* base = lds + c0 + qg * 4864 + (4 * (ln >> 4) + ((ln >> 2) & 3)) * 128 + (ln & 3) * 8;
        bstore16<1>(__builtin_bit_cast(u32x4, tr_frag(base + 32 * (it - 1), 16 * 128)), srd, ln * 16, rdst[it - 1]);
        SBAR(); };
    { const int dc = c1 - c0; kp += dc; tp += dc; vp += dc; PINV(kp); PINV(tp); PINV(vp); }
    if (slow) {
#pragma unroll
        for (int it = 1; it < 5; ++it) rt_step(it);
        attn_slow(KP(0), TP(0), VP(0), qm, qt, o, mrun, kc); }
    else attn_fast(KP(0), TP(0), VP(0), qm, qt, o, kc, rt_step);
    int tz = threadIdx.x; asm volatile("" : "+v"(tz));
    const int lane2 = tz & 63, r2 = tz & 31, h2 = (tz >> 5) & 1;
    LAS float* mx = (LAS float*)(lds + KVIMG);
#pragma unroll
    for (int u = 0; u < 2; ++u) { LAS char* pb = lds + c0 + (qg * 2 + u) * 2432;
        *(LAS f32x4*)(pb + lane2 * 16) = (f32x4){o[u][0], o[u][1], o[u][2], o[u][3]};
        *(LAS f32x4*)(pb + 1024 + lane2 * 16) = (f32x4){o[u][4], o[u][5], o[u][6], o[u][7]};
        if (h2 == 0) { *(LAS f32x2*)(pb + 2048 + r2 * 8) = (f32x2){o[u][8], o[u][9]}; *(LAS float*)(pb + 2304 + r2 * 4) = o[u][10];
                      if (slow) mx[wid * 64 + 32 * u + r2] = mrun[u]; } }
    LBAR();
    { const int u = kq & 1, hf = kq >> 1;
      LAS const char* pb = lds + (qb & 1) * KVCHUNK + (qg * 2 + u) * 2432;
      float l = 0.f; f32x4 a = {0, 0, 0, 0}; f32x2 c = {0, 0};
      if (slow) { float mk[4], M = -INFINITY;
#pragma unroll
          for (int k = 0; k < 4; ++k) { mk[k] = mx[(4 * k + qg) * 64 + 32 * u + r2]; M = fmaxf(M, mk[k]); }
#pragma unroll
          for (int k = 0; k < 4; ++k) { LAS const char* p = pb + k * 2 * KVCHUNK; const float f = __builtin_amdgcn_exp2f(mk[k] - M);
              l += f * *(LAS const float*)(p + 2304 + r2 * 4); a += *(LAS const f32x4*)(p + hf * 1024 + lane2 * 16) * f; c += *(LAS const f32x2*)(p + 2048 + r2 * 8) * f; }
      } else {
#pragma unroll
          for (int k = 0; k < 4; ++k) { LAS const char* p = pb + k * 2 * KVCHUNK;
              l += *(LAS const float*)(p + 2304 + r2 * 4); a += *(LAS const f32x4*)(p + hf * 1024 + lane2 * 16); c += *(LAS const f32x2*)(p + 2048 + r2 * 8); } }
      const float rl = __builtin_amdgcn_rcpf(l);
      const int token = q0 + 32 * u + r2; const unsigned ov = (token >> 4) * (18 * 1024) + (token & 15) * 16;
      bstore8<1>((u32x2){cvtpk(a[0] * rl, a[1] * rl), cvtpk(a[2] * rl, a[3] * rl)}, srd, ov + h2 * 8, rfl((unsigned)WS_OF + (head >> 1) * 1024 + (2 * (head & 1) + hf) * 256));
      if (hf == 0 && h2 == 0) __hip_atomic_store((unsigned*)(ws + WS_OF + ov + (16 + (head >> 4)) * 1024 + ((head >> 2) & 3) * 256 + (head & 3) * 4), cvtpk(c[0] * rl, c[1] * rl), __ATOMIC_RELAXED, __HIP_MEMORY_SCOPE_AGENT);
    }
    asm volatile("s_waitcnt vmcnt(0) lgkmcnt(0)\n\ts_barrier" ::: "memory");
    arrive(ws, 96 + qb);
    if (idx < 24) { const int mt32 = 4 * xcd + (idx & 3), n6 = idx >> 2; float mux;
        k3_request(x, ws, lds_generic, mt32, n6, mux);
        const bool bad = await<false, 960, true>(ws, 96 + xcd, 32u, (LAS unsigned*)(lds + K3_FLAG));
        PINV(mux);
        k3_compute(ws, lds_generic, mt32, n6, mux, bad); }
}

constexpr int RING_SLOT = 36 * 1024, G_LDS = 4 * RING_SLOT;

struct Ev { int type, arg, n; };
enum { E_SSQ, E_A, E_B, E_WAITB, E_BAR, E_READ, E_MMA, E_GP, E_RS, E_ST, E_NOP, E_A2 };
constexpr int G1_PD = 4;
constexpr int g1_grp(int f) { return f < 144 ? 0 : f < 216 ? 1 : f < 288 ? 2 : 3; }
constexpr int g1_ks(int f) { return f < 144 ? f / 8 : f < 288 ? ((f - 144) % 72) / 4 : (f - 288) / 2; }
constexpr int g1_tile(int f) { return f < 288 ? (f >> 1) & 1 : 0; }
constexpr int g1_set(int f) { return f < 144 ? (f >> 2) & 1 : f < 216 ? 0 : f < 288 ? 1 : 0; }
template <bool LATE> struct G1Prog {
    Ev ev[1600]; int n;
    constexpr void add(int t, int a) { ev[n].type = t; ev[n].arg = a; ev[n].n = 0; ++n; }
    static constexpr int vm(const Ev& e) { return e.type == E_SSQ ? 6 : (e.type == E_A || e.type == E_B || e.type == E_ST || e.type == E_A2) ? 1 : 0; }
    static constexpr int NP = LATE ? 4 : 5;
    constexpr void addB(int s, int u) { add(E_B, s * 2 + u); }
    static constexpr int jset(int q) { return q == 0 ? 0 : q == 1 ? 1 : q == 2 ? 0 : q == 3 ? 1 : 0; }
    constexpr G1Prog() : ev{}, n(0) {
        for (int p = 0; p < NP; ++p) add(E_A, p);
        addB(0, 0); addB(0, 1); addB(1, 0); addB(1, 1);
        add(E_BAR, 0);
        for (int f = 0; f < G1_PD; ++f) add(E_READ, f);
        for (int f = 0; f < 324; ++f) {
            const int st = f / 36, i = f % 36, g = g1_grp(f);
            if (g == 0) { const int s = f / 8, q = f % 8;
                if (q == 0) add(E_WAITB, s);
                if (f == 0) { addB(2, 0); addB(2, 1); }
                if (s + 3 < 18 && q == 2) addB(s + 3, 0);
                if (s + 3 < 18 && q == 6) addB(s + 3, 1);
                if (f == 100) add(E_SSQ, 0);
                if (f == 136) add(E_RS, 0); }
            if (st == 0 && i >= 1 && i < 1 + 3 * NP && (i - 1) % 3 == 0) add(E_A, 8 + (i - 1) / 3);
            if (st + 2 < 9) { const int i0 = st == 0 ? 17 : LATE ? 17 : 1, d = 4;
                if (i >= i0 && (i - i0) % d == 0 && (i - i0) / d < NP) add(E_A, (st + 2) * 8 + (i - i0) / d); }
            if (i == 36 - G1_PD && st + 1 < 9) add(E_BAR, st + 1);
            if (f + G1_PD < 324) add(E_READ, f + G1_PD);
            add(E_MMA, f);
            if (f == 143) { add(E_NOP, 0);
                for (int k = 0; k < 32; ++k) { add(E_GP, 0 * 64 + k); if (k == 15 || k == 31) add(E_ST, 0 * 2 + (k == 31)); } }
            if (g > 0) {
                const int r = f - (g == 1 ? 144 : g == 2 ? 216 : 288), step = g < 3 ? 2 : 1, r0 = 3, k = (r - r0) / step;
                if (r >= r0 && (r - r0) % step == 0 && k < 32) { add(E_GP, g * 64 + k);
                    if (k == (LATE ? 22 : 15)) add(E_ST, g * 2);
                    if (k == 31 && !LATE) add(E_ST, g * 2 + 1); }
                if (LATE && r == (g < 3 ? 71 : 35)) add(E_ST, g * 2 + 1); }
            if (st == 8 && i >= 2 && i % 3 == 2 && (i - 2) / 3 < 2 * NP) add(E_A2, ((i - 2) / 3 / NP) * 8 + ((i - 2) / 3) % NP);
        }
        add(E_NOP, 1);
        for (int k = 0; k < 16; ++k) { add(E_GP, 4 * 64 + k); if (k == 7 || k == 15) add(E_ST, 8 + (k == 15)); }
        for (int i = 0; i < n; ++i) if (ev[i].type == E_WAITB || ev[i].type == E_BAR || ev[i].type == E_RS) {
            int cnt = 0, j = i - 1;
            for (; j >= 0; --j) {
                if (ev[i].type == E_WAITB && ev[j].type == E_B && ev[j].arg == 2 * ev[i].arg + 1) break;
                if (ev[i].type == E_RS && ev[j].type == E_SSQ) break;
                if (ev[i].type == E_BAR && ev[j].type == E_A && ev[j].arg == 8 * ev[i].arg + NP - 1) break;
                cnt += vm(ev[j]); }
            ev[i].n = cnt > 63 ? 63 : cnt; }
    }
};
template <bool LATE> constexpr G1Prog<LATE> g1prog{};
DI float vmul(float a, float b) { float r; asm("v_mul_f32_e32 %0, %1, %2" : "=v"(r) : "v"(a), "v"(b)); return r; }
DI float vfma(float a, float b, float c) { float r; asm("v_fma_f32 %0, %1, %2, %3" : "=v"(r) : "v"(a), "v"(b), "v"(c)); return r; }
struct G1State {
    bf16x8 hf[2][18]; f32x4 acc[2][2][2][2];
    bf16x8 ar[G1_PD + 1]; float sq[2][3]; float a0[2], a1[2], kk[2]; f32x4 z, w; u32x4 o; unsigned soffA2;
    unsigned lb0, lb1, lb2; unsigned l16, ldsb, soffA, soffH, soffS, soffAct, soffAct8, vq; int wid, j, g;
};
template <bool LATE, int PC> DI void g1_exec(G1State& c, const srd_t srd) {
    constexpr Ev e = g1prog<LATE>.ev[PC];
    if constexpr (e.type == E_SSQ) {
        bload4<0>(c.sq[0][0], srd, c.vq, c.soffS); bload4<256>(c.sq[0][1], srd, c.vq, c.soffS); bload4<512>(c.sq[0][2], srd, c.vq, c.soffS);
        bload4<1024>(c.sq[1][0], srd, c.vq, c.soffS); bload4<1280>(c.sq[1][1], srd, c.vq, c.soffS); bload4<1536>(c.sq[1][2], srd, c.vq, c.soffS); }
    else if constexpr (e.type == E_A) { constexpr int st = e.arg >> 3, p = e.arg & 7;
        const unsigned so = c.soffA + st * RING_SLOT, dst = c.ldsb + (st & 3) * RING_SLOT;
        if constexpr (p < 4) bdma_next<p * 1024>(srd, c.l16, so + c.wid * 4096, dst + c.wid * 4096);
        else bdma1(srd, c.l16, so + 32768 + c.wid * 1024, dst + 32768 + c.wid * 1024); }
    else if constexpr (e.type == E_B) { constexpr int s = e.arg >> 1, u = e.arg & 1;
        bload16<((18 * u + s) & 3) * 1024>(c.hf[u][s], srd, c.l16, c.soffH + ((18 * u + s) >> 2) * 4096); }
    else if constexpr (e.type == E_WAITB) { asm volatile("s_waitcnt vmcnt(%2)" : "+v"(c.hf[0][e.arg]), "+v"(c.hf[1][e.arg]) : "n"(e.n)); }
    else if constexpr (e.type == E_BAR) { asm volatile("s_waitcnt vmcnt(%0)\n\ts_barrier" :: "n"(e.n) : "memory"); }
    else if constexpr (e.type == E_RS) { asm volatile("s_waitcnt vmcnt(%6)" : "+v"(c.sq[0][0]), "+v"(c.sq[0][1]), "+v"(c.sq[0][2]), "+v"(c.sq[1][0]), "+v"(c.sq[1][1]), "+v"(c.sq[1][2]) : "n"(e.n));
#pragma unroll
        for (int u = 0; u < 2; ++u) { float t = (c.sq[u][0] + c.sq[u][1]) + c.sq[u][2]; t += xor16(t); t += xor32(t);
            const float v = t * (1.f / D_) + EPS, rs = __builtin_amdgcn_rsqf(v);
            c.a0[u] = -2.30220818f * rs; c.a1[u] = -0.10294324f * rs * rs * rs; c.kk[u] = v; }
        SBAR(); }
    else if constexpr (e.type == E_A2) { constexpr int st = e.arg >> 3, p = e.arg & 7;
        const unsigned so = c.soffA2 + st * RING_SLOT, dst = c.ldsb + ((st + 1) & 3) * RING_SLOT;
        if constexpr (p < 4) bdma_next<p * 1024>(srd, c.l16, so + c.wid * 4096, dst + c.wid * 4096);
        else bdma1(srd, c.l16, so + 32768 + c.wid * 1024, dst + 32768 + c.wid * 1024); }
    else if constexpr (e.type == E_NOP) {
        asm volatile("s_nop 7" : "+v"(c.acc[0][0][0][0]), "+v"(c.acc[0][0][0][1]), "+v"(c.acc[0][0][1][0]), "+v"(c.acc[0][0][1][1]));
        if constexpr (e.arg == 0) asm volatile("" : "+v"(c.acc[0][1][0][0]), "+v"(c.acc[0][1][0][1]), "+v"(c.acc[0][1][1][0]), "+v"(c.acc[0][1][1][1])); }
    else if constexpr (e.type == E_READ) { constexpr int f = e.arg, off = ((f / 36) & 3) * RING_SLOT + (f % 36) * 1024;
        if constexpr ((off >> 16) == 0) c.ar[f % (G1_PD + 1)] = lds_frag((LAS const char*)(uintptr_t)c.lb0 + (off & 65535));
        else if constexpr ((off >> 16) == 1) c.ar[f % (G1_PD + 1)] = lds_frag((LAS const char*)(uintptr_t)c.lb1 + (off & 65535));
        else c.ar[f % (G1_PD + 1)] = lds_frag((LAS const char*)(uintptr_t)c.lb2 + (off & 65535));
        SBAR(); }
    else if constexpr (e.type == E_MMA) { constexpr int f = e.arg, s = g1_ks(f), t = g1_tile(f), q = f & 1, set = g1_set(f);
        if constexpr (s == 0) { mfma16_new(c.acc[set][t][q][0], c.ar[f % (G1_PD + 1)], c.hf[0][s]); mfma16_new(c.acc[set][t][q][1], c.ar[f % (G1_PD + 1)], c.hf[1][s]); }
        else { mfma16_acc(c.acc[set][t][q][0], c.ar[f % (G1_PD + 1)], c.hf[0][s]); mfma16_acc(c.acc[set][t][q][1], c.ar[f % (G1_PD + 1)], c.hf[1][s]); }
        SBAR(); }
    else if constexpr (e.type == E_GP) {
        constexpr int grp = e.arg >> 6, k = e.arg & 63, quad = k >> 3, ph = k & 7, set = G1Prog<LATE>::jset(grp);
        constexpr int u = grp < 4 ? quad >> 1 : quad, t = grp < 4 ? quad & 1 : 0;
        const f32x4 g = c.acc[set][t][0][u];
        if constexpr ((grp == 0 || grp == 4) && (ph == 4 || ph == 6)) asm volatile("s_nop 0" : "+v"(c.z));
#pragma unroll
        for (int r = 0; r < 4; ++r) {
            if constexpr (ph == 0) c.z[r] = vmul(g[r], g[r]);
            if constexpr (ph == 1) c.z[r] = vfma(c.z[r], c.a1[u], c.a0[u]);
            if constexpr (ph == 2) c.z[r] = vmul(c.z[r], g[r]);
            if constexpr (ph == 3) c.z[r] = __builtin_amdgcn_exp2f(c.z[r]);
            if constexpr (ph == 4) c.z[r] = vfma(c.z[r], c.kk[u], c.kk[u]);
            if constexpr (ph == 5) c.z[r] = __builtin_amdgcn_rcpf(c.z[r]);
            if constexpr (ph == 6) c.w[r] = vmul(vmul(g[r], c.acc[set][t][1][u][r]), c.z[r]); }
        if constexpr (ph == 7) { c.o[2 * t] = cvtpk(c.w[0], c.w[1]); c.o[2 * t + 1] = cvtpk(c.w[2], c.w[3]); }
        SBAR(); }
    else if constexpr (e.type == E_ST) { constexpr int grp = e.arg >> 1, u = e.arg & 1;
        if constexpr (grp < 4) bstore16<1>(c.o, srd, c.l16, c.soffAct + u * (144 * 1024) + grp * 1024);
        else bstore8<1>((u32x2){c.o[0], c.o[1]}, srd, c.l16 + (c.j & 1) * 8, c.soffAct8 + u * (144 * 1024));
        SBAR(); }
}
template <bool LATE, int... I> DI void g1_run(G1State& c, const srd_t srd, std::integer_sequence<int, I...>) { (g1_exec<LATE, I>(c, srd), ...); }

constexpr int G2_PD = 4;
template <bool LATE> struct G2Prog {
    Ev ev[700]; int n;
    constexpr void add(int t, int a) { ev[n].type = t; ev[n].arg = a; ev[n].n = 0; ++n; }
    static constexpr int vm(const Ev& e) { return (e.type == E_A || e.type == E_B) ? 1 : 0; }
    static constexpr int NP = LATE ? 4 : 5;
    constexpr G2Prog() : ev{}, n(0) {
        add(E_B, 0); add(E_B, 1); add(E_B, 2); add(E_B, 3);
        for (int f = 0; f < G2_PD; ++f) add(E_READ, f);
        for (int f = 0; f < 162; ++f) {
            const int st = f / 18, i = f % 18;
            if (i % 9 == 0) add(E_WAITB, st * 2 + i / 9);
            const int ia = LATE ? 3 : 1, ib = LATE ? 1 : 3;
            if (st == 0) {
                if (i >= 1 && i < 5) add(E_B, 4 + i - 1);
                if (i >= 6 && i < 6 + NP) add(E_A, 16 + i - 6);
                if (i >= 12 && i < 16) add(E_B, 8 + i - 12);
            } else {
                if (st == 1 && i >= 14) add(E_B, 16 + i - 14);
                if (st + 2 < 9 && i >= ia && (i - ia) % 4 == 0 && (i - ia) / 4 < 4) add(E_A, (st + 2) * 8 + (i - ia) / 4);
                if (st + 2 < 9 && NP == 5 && i == 12) add(E_A, (st + 2) * 8 + 4);
                { const int la = st == 1 ? 2 : 3; if (st + la < 9 && (st == 1 || st + la > 4) && i >= ib && (i - ib) % 4 == 0 && (i - ib) / 4 < 4) add(E_B, (st + la) * 4 + (i - ib) / 4); }
            }
            if (i == 18 - G2_PD && st + 1 < 9) add(E_BAR, st + 1);
            if (f + G2_PD < 162) add(E_READ, f + G2_PD);
            add(E_MMA, f);
        }
        for (int i = 0; i < n; ++i) if (ev[i].type == E_WAITB || ev[i].type == E_BAR) {
            int cnt = 0, j = i - 1;
            for (; j >= 0; --j) {
                if (ev[i].type == E_WAITB && ev[j].type == E_B && ev[j].arg == 2 * ev[i].arg + 1) break;
                if (ev[i].type == E_BAR && ev[j].type == E_A && (ev[j].arg >> 3) == ev[i].arg) break;
                cnt += vm(ev[j]); }
            ev[i].n = cnt > 63 ? 63 : cnt; }
    }
};
template <bool LATE> constexpr G2Prog<LATE> g2prog{};
struct G2State {
    bf16x8 af[4][2][2]; f32x4 acc[9][2]; bf16x8 ar[G2_PD + 1];
    unsigned lb0, lb1, lb2; unsigned l16, ldsb, soffA, soffB; int wid;
};
template <bool LATE, int PC> DI void g2_exec(G2State& c, const srd_t srd) {
    constexpr Ev e = g2prog<LATE>.ev[PC];
    if constexpr (e.type == E_A) { constexpr int st = e.arg >> 3, p = e.arg & 7;
        const unsigned so = c.soffA + st * RING_SLOT, dst = c.ldsb + ((st + 1) & 3) * RING_SLOT;
        if constexpr (p < 4) bdma_next<p * 1024>(srd, c.l16, so + c.wid * 4096, dst + c.wid * 4096);
        else bdma1(srd, c.l16, so + 32768 + c.wid * 1024, dst + 32768 + c.wid * 1024); }
    else if constexpr (e.type == E_B) { constexpr int st = e.arg >> 2, s2 = (e.arg >> 1) & 1, u = e.arg & 1;
        bload16_sc1<((2 * st + s2) & 3) * 1024>(c.af[st & 3][s2][u], srd, c.l16, c.soffB + u * (144 * 1024) + ((2 * st + s2) >> 2) * 4096); }
    else if constexpr (e.type == E_WAITB) { asm volatile("s_waitcnt vmcnt(%2)" : "+v"(c.af[(e.arg >> 1) & 3][e.arg & 1][0]), "+v"(c.af[(e.arg >> 1) & 3][e.arg & 1][1]) : "n"(e.n)); }
    else if constexpr (e.type == E_BAR) { asm volatile("s_waitcnt vmcnt(%0)\n\ts_barrier" :: "n"(e.n) : "memory"); }
    else if constexpr (e.type == E_READ) { constexpr int f = e.arg, st = f / 18, off = ((st + 1) & 3) * RING_SLOT + (f % 18) * 1024;
        if constexpr ((off >> 16) == 0) c.ar[f % (G2_PD + 1)] = lds_frag((LAS const char*)(uintptr_t)c.lb0 + (off & 65535));
        else c.ar[f % (G2_PD + 1)] = lds_frag((LAS const char*)(uintptr_t)c.lb1 + (off & 65535));
        SBAR(); }
    else if constexpr (e.type == E_MMA) { constexpr int f = e.arg, st = f / 18, s2 = (f % 18) / 9, t = f % 9;
        if constexpr (f < 9) { mfma16_new(c.acc[t][0], c.ar[f % (G2_PD + 1)], c.af[st & 3][s2][0]); mfma16_new(c.acc[t][1], c.ar[f % (G2_PD + 1)], c.af[st & 3][s2][1]); }
        else { mfma16_acc(c.acc[t][0], c.ar[f % (G2_PD + 1)], c.af[st & 3][s2][0]); mfma16_acc(c.acc[t][1], c.ar[f % (G2_PD + 1)], c.af[st & 3][s2][1]); }
        SBAR(); }
}
template <bool LATE, int... I> DI void g2_run(G2State& c, const srd_t srd, std::integer_sequence<int, I...>) { (g2_exec<LATE, I>(c, srd), ...); }
constexpr int FFN_LDS = G_LDS + 1024;
__global__ __launch_bounds__(512, 2) void ffn_kernel(char* ws, char* out) {
    extern __shared__ __attribute__((aligned(16))) char lds_generic[];
    LAS char* lds = (LAS char*)lds_generic;
    CODE_LABEL(preln_ffn_begin);
    const srd_t srd = make_srd(ws, (unsigned)WS_END);
    const int b = blockIdx.x, xcd = b & 7, idx = b >> 3;
    LAS unsigned* flag = (LAS unsigned*)(lds + G_LDS);
    float csink = 0.f;
    if (idx == 0) { const char *c0, *c1; CODE_ADDR(c0, preln_ffn_begin); CODE_ADDR(c1, preln_ffn_end); touch_code(csink, c0, c1, rfl(threadIdx.x >> 6), threadIdx.x & 63); }
    {
        int tid = threadIdx.x; asm volatile("" : "+v"(tid));
        const int lane = tid & 63, wid = rfl(tid >> 6);
        const int j = (xcd >> 1) * 8 + (idx & 7), mt8 = (xcd & 1) * 4 + (idx >> 3), mt16 = 16 * mt8 + 2 * wid;
        G1State c;
        c.l16 = lane * 16; c.ldsb = (unsigned)(uintptr_t)lds_generic; c.wid = wid; c.j = j; c.g = lane >> 4;
        c.lb0 = c.ldsb + lane * 16; c.lb1 = c.lb0 + 65536; c.lb2 = c.lb0 + 131072; PINV(c.lb0); PINV(c.lb1); PINV(c.lb2);
        c.soffA = (unsigned)WS_W1 + j * (324 * 1024); c.soffH = (unsigned)WS_HCF + mt16 * (18 * 1024); c.soffS = (unsigned)WS_SSQ + mt16 * 1024;
        c.soffAct8 = rfl((unsigned)WS_ACT + (mt16 * 144 + 9 * (j >> 1) + 8) * 1024);
        c.soffAct = rfl((unsigned)WS_ACT + (mt16 * 144 + 9 * (j >> 1) + 4 * (j & 1)) * 1024); c.vq = (lane >> 4) * 64 + (lane & 15) * 4;
        c.soffA2 = (unsigned)WS_W2 + ((xcd >> 1) * 4 + (idx >> 3)) * (324 * 1024);
        if (wid >= 4) g1_run<true>(c, srd, std::make_integer_sequence<int, g1prog<true>.n>{});
        else g1_run<false>(c, srd, std::make_integer_sequence<int, g1prog<false>.n>{});
        asm volatile("s_waitcnt vmcnt(0) lgkmcnt(0)\n\ts_barrier" : "+v"(csink) :: "memory");
        arrive(ws, 64 + (j >> 3) * 8 + mt8);
    }
    int tz = threadIdx.x; asm volatile("" : "+v"(tz));
    const int lane = tz & 63, wid = rfl(tz >> 6), mi = wid & 3, kj = wid >> 2;
    const int ksl = xcd >> 1, mt = (xcd & 1) * 8 + (idx & 7), n4 = idx >> 3;
    const int mt16 = 8 * mt + 2 * mi;
    G2State c;
    c.l16 = lane * 16; c.ldsb = (unsigned)(uintptr_t)lds_generic; c.wid = wid;
    c.lb0 = c.ldsb + kj * (18 * 1024) + lane * 16; c.lb1 = c.lb0 + 65536; PINV(c.lb0); PINV(c.lb1);
    c.soffA = (unsigned)WS_W2 + (ksl * 4 + n4) * (324 * 1024); c.soffB = (unsigned)WS_ACT + (mt16 * 144 + 36 * ksl + 18 * kj) * 1024;
    bool bad = await<false>(ws, 64 + ksl * 8 + (mt >> 1), 8u, flag);
    if (wid >= 4) g2_run<true>(c, srd, std::make_integer_sequence<int, g2prog<true>.n>{});
    else g2_run<false>(c, srd, std::make_integer_sequence<int, g2prog<false>.n>{});
    asm volatile("s_nop 7" : "+v"(c.acc[8][0]), "+v"(c.acc[8][1]), "+v"(c.acc[7][0]), "+v"(c.acc[7][1]));
    u32x2 hv[3]; u32x4 pv[3]; u32x2 pw[3]; float mu[2];
    { const int T0 = 9 * n4 + wid, T8 = 9 * n4 + 8; const unsigned hb = rfl((unsigned)WS_HCF + (8 * mt + 2 * ksl) * (18 * 1024));
      bload8<0>(hv[0], srd, lane * 16 + (T0 & 1) * 8, hb + (T0 >> 1) * 1024); bload8<0>(hv[1], srd, lane * 16 + (T0 & 1) * 8, hb + (18 + (T0 >> 1)) * 1024);
      bload8<0>(hv[2], srd, lane * 16 + (T8 & 1) * 8, hb + ((wid & 1) * 18 + (T8 >> 1)) * 1024);
      const unsigned mb = rfl((unsigned)WS_MUH + (128 * mt + 32 * ksl) * 4); bload4<0>(mu[0], srd, (lane & 15) * 4, mb); bload4<64>(mu[1], srd, (lane & 15) * 4, mb); }
    LBAR();
    LAS char* ex = lds + mi * 18432 + lane * 16;
    if (kj == 0) {
#pragma unroll
        for (int t = 5; t < 9; ++t) { *(LAS f32x4*)(ex + (2 * t) * 1024) = c.acc[t][0]; *(LAS f32x4*)(ex + (2 * t + 1) * 1024) = c.acc[t][1]; }
    } else {
#pragma unroll
        for (int t = 0; t < 5; ++t) { *(LAS f32x4*)(ex + (2 * t) * 1024) = c.acc[t][0]; *(LAS f32x4*)(ex + (2 * t + 1) * 1024) = c.acc[t][1]; }
    }
    LBAR();
    const int tile = mt * 4 + n4; const unsigned xb = rfl((unsigned)WS_PART + tile * (16 * 9 * 1024));
    const unsigned xs0 = rfl(xb + (mi * 4 + ksl) * (9 * 1024));
    auto fin = [&](int t) {
        const f32x4 a0 = c.acc[t][0] + *(LAS const f32x4*)(ex + (2 * t) * 1024), a1 = c.acc[t][1] + *(LAS const f32x4*)(ex + (2 * t + 1) * 1024);
        if (mi == ksl) { *(LAS f32x4*)(ex + (2 * t) * 1024) = a0; *(LAS f32x4*)(ex + (2 * t + 1) * 1024) = a1; }
        else bstore16<1>((u32x4){cvtpk(a0[0], a0[1]), cvtpk(a0[2], a0[3]), cvtpk(a1[0], a1[1]), cvtpk(a1[2], a1[3])}, srd, lane * 16, xs0 + t * 1024); };
    if (kj == 0) { fin(0); fin(1); fin(2); fin(3); fin(4); } else { fin(5); fin(6); fin(7); fin(8); }
    asm volatile("s_waitcnt vmcnt(0) lgkmcnt(0)\n\ts_barrier" : "+v"(hv[0]), "+v"(hv[1]), "+v"(hv[2]), "+v"(mu[0]), "+v"(mu[1]) :: "memory");
    bad |= await<true>(ws, tile, 4u, flag);

#pragma unroll
    for (int k = 0; k < 3; ++k) { const unsigned sb = rfl(xb + (ksl * 4 + (k + (k >= ksl))) * (9 * 1024));
        bload16u_sc1<0>(pv[k], srd, lane * 16, sb + wid * 1024); bload8_sc1<0>(pw[k], srd, lane * 16 + (wid & 1) * 8, sb + 8 * 1024); }
    asm volatile("s_waitcnt vmcnt(0)" : "+v"(pv[0]), "+v"(pv[1]), "+v"(pv[2]), "+v"(pw[0]), "+v"(pw[1]), "+v"(pw[2]) :: "memory");
    const int m = lane & 15, g = lane >> 4;
    auto put = [&](int t, int u, u32x2 h2, u32x2 p0, u32x2 p1, u32x2 p2) {
        f32x4 a = *(LAS const f32x4*)(lds + ksl * 18432 + (2 * t + u) * 1024 + lane * 16);
        a += (f32x4){bflo(h2[0]), bfhi(h2[0]), bflo(h2[1]), bfhi(h2[1])} + (u ? mu[1] : mu[0]); a += (f32x4){bflo(p0[0]), bfhi(p0[0]), bflo(p0[1]), bfhi(p0[1])};
        a += (f32x4){bflo(p1[0]), bfhi(p1[0]), bflo(p1[1]), bfhi(p1[1])}; a += (f32x4){bflo(p2[0]), bfhi(p2[0]), bflo(p2[1]), bfhi(p2[1])};
        u32x2 o = {cvtpk(a[0], a[1]), cvtpk(a[2], a[3])};
        if (bad) o = (u32x2){0x7fc07fc0u, 0x7fc07fc0u};
        *(u32x2*)(out + (size_t)(128 * mt + 32 * ksl + 16 * u + m) * (D_ * 2) + (144 * n4 + 16 * t + 4 * g) * 2) = o; };
    put(wid, 0, hv[0], (u32x2){pv[0][0], pv[0][1]}, (u32x2){pv[1][0], pv[1][1]}, (u32x2){pv[2][0], pv[2][1]});
    put(wid, 1, hv[1], (u32x2){pv[0][2], pv[0][3]}, (u32x2){pv[1][2], pv[1][3]}, (u32x2){pv[2][2], pv[2][3]});
    if (wid < 2) put(8, wid, hv[2], pw[0], pw[1], pw[2]);
    CODE_LABEL(preln_ffn_end);
}

}

extern "C" void kernel_launch(void* const* d_in, const int* in_sizes, int n_in, void* d_out, int out_size, void* d_ws, size_t ws_size, hipStream_t stream) {
    const char* x = (const char*)d_in[0]; const char* wqkv = (const char*)d_in[1]; const char* wo = (const char*)d_in[2];
    const char* wg = (const char*)d_in[4]; const char* wu = (const char*)d_in[5]; const char* wd = (const char*)d_in[6];
    char* ws = (char*)d_ws;
    (void)hipFuncSetAttribute((const void*)k1_qkv, hipFuncAttributeMaxDynamicSharedMemorySize, K1_LDS);
    (void)hipFuncSetAttribute((const void*)k2_attn, hipFuncAttributeMaxDynamicSharedMemorySize, K2_LDS);
    (void)hipFuncSetAttribute((const void*)ffn_kernel, hipFuncAttributeMaxDynamicSharedMemorySize, FFN_LDS);
    k1_qkv<<<256, 512, K1_LDS, stream>>>(x, wqkv, wo, ws);
    k2_attn<<<256, 1024, K2_LDS, stream>>>(x, wg, wu, wd, ws);
    ffn_kernel<<<256, 512, FFN_LDS, stream>>>(ws, (char*)d_out);
}
```
